```python
import torch

def _softmax(x, axis=-1):
    return torch.softmax(x, dim=axis)

def _gelu(x):
    return torch.nn.functional.gelu(x, approximate='tanh')

def _silu(x):
    return torch.nn.functional.silu(x)

def _sigmoid(x):
    return torch.sigmoid(x)

def _layernorm(x, axis=-1, eps=1e-05):
    m = x.mean(dim=axis, keepdim=True)
    v = x.var(dim=axis, keepdim=True, unbiased=False)
    return (x - m) / torch.sqrt(v + eps)

def _scatter_add(tgt, idx, src, axis=0):
    return tgt.clone().index_add_(axis, idx, src)

def _segment_sum(src, seg, n):
    out = torch.zeros((n,) + src.shape[1:], dtype=src.dtype)
    return out.index_add_(0, seg, src)

def _pad(x, pw):
    flat = tuple((v for pair in reversed(pw) for v in pair))
    return torch.nn.functional.pad(x, flat)
kernel_name = 'adaLN_cond__mha_block_cached__geglu_ffn__prompt'

def setup_inputs():

    _w = lambda a: (lambda t: t.to(torch.bfloat16) if t.is_floating_point() else t)(torch.as_tensor(a))

    def _A_setup():
        B, Sq, D = (1, 2048, 512)
        H, Sk, Dh = (32, 2048, 128)
        return {'x': _w((lambda *s: torch.randn(*s, dtype=torch.bfloat16) * 0.1)(B, Sq, D)), 'k_cache': _w((lambda *s: torch.randn(*s, dtype=torch.bfloat16) * 0.8)(B, H, Sk, Dh)), 'v_cache': _w((lambda *s: torch.randn(*s, dtype=torch.bfloat16) * 1.6)(B, H, Sk, Dh)), 'wq': _w((lambda *s: torch.randn(*s, dtype=torch.bfloat16) * 0.8)(D, H * Dh) * D ** (-0.5)), 'wo': _w((lambda *s: torch.randn(*s, dtype=torch.bfloat16) * 1.6)(H * Dh, D) * (H * Dh) ** (-0.5))}
    _A_in = _A_setup()

    def _B_setup():
        B, S, D, F = (1, 2048, 512, 1792)
        return {'x': _w((lambda *s: torch.randn(*s, dtype=torch.bfloat16) * 0.1)(B, S, D)), 'w_gate': _w((lambda *s: torch.randn(*s, dtype=torch.bfloat16) * 0.1)(D, F)), 'w_up': _w((lambda *s: torch.randn(*s, dtype=torch.bfloat16) * 0.1)(D, F)), 'w_down': _w((lambda *s: torch.randn(*s, dtype=torch.bfloat16) * 0.1)(F, D))}
    _B_in = _B_setup()
    return {'A_x': _A_in['x'], 'A_k_cache': _A_in['k_cache'], 'A_v_cache': _A_in['v_cache'], 'A_wq': _A_in['wq'], 'A_wo': _A_in['wo'], 'B_x': _B_in['x'], 'B_w_gate': _B_in['w_gate'], 'B_w_up': _B_in['w_up'], 'B_w_down': _B_in['w_down'], 'c': _w((lambda *s: torch.randn(*s, dtype=torch.bfloat16) * 0.8)(1, 512) * 8.0), 'w_mod': _w((lambda *s: torch.randn(*s, dtype=torch.bfloat16) * 0.8)(512, 6 * 512) * 512 ** (-0.5) * 8.0)}

def reference(A_x, A_k_cache, A_v_cache, A_wq, A_wo, B_x, B_w_gate, B_w_up, B_w_down, c, w_mod):

    def _A_ref(x, k_cache, v_cache, wq, wo):
        B, Sq, D = x.shape
        H, Dh = (32, 128)
        q = (lambda x, *d: x.permute(*d))((x @ wq).reshape(B, Sq, H, Dh), 0, 2, 1, 3)
        scores = torch.einsum('bhqd,bhkd->bhqk', q, k_cache) * Dh ** (-0.5)
        p = _softmax(scores, axis=-1)
        o = torch.einsum('bhqk,bhkd->bhqd', p, v_cache)
        return (lambda x, *d: x.permute(*d))(o, 0, 2, 1, 3).reshape(B, Sq, H * Dh) @ wo

    def _B_ref(x, w_gate, w_up, w_down):
        return _gelu(x @ w_gate) * (x @ w_up) @ w_down
    x = A_x
    mod = (_silu(c) @ w_mod).reshape(c.shape[0], 6, 1, -1)
    sa, ca, ga, sb, cb, gb = (mod[:, 0].clone(), mod[:, 1].clone(), mod[:, 2].clone(), mod[:, 3].clone(), mod[:, 4].clone(), mod[:, 5].clone())
    h = _layernorm(x, axis=-1) * (1 + ca) + sa
    x = x + ga * _A_ref(h, A_k_cache, A_v_cache, A_wq, A_wo)
    h = _layernorm(x, axis=-1) * (1 + cb) + sb
    x = x + gb * _B_ref(h, B_w_gate, B_w_up, B_w_down)
    return x
```

```cpp
#include <hip/hip_runtime.h>
#include <hip/hip_bf16.h>
#include <cstdio>
typedef unsigned short u16;
typedef short bf16x8 __attribute__((ext_vector_type(8)));
typedef float f32x4 __attribute__((ext_vector_type(4)));
typedef float f32x16 __attribute__((ext_vector_type(16)));
__device__ __forceinline__ float bf2f(u16 x) { return __uint_as_float(((unsigned)x) << 16); }
__device__ __forceinline__ u16 f2bf(float f) { __hip_bfloat16 b = __float2bfloat16(f); return *(u16*)&b; }
__device__ __forceinline__ float rbf(float f) { return bf2f(f2bf(f)); }

constexpr int S = 2048, D = 512, H = 32, DH = 128, F = 1792;

__global__ void k_mod(const u16* c, const u16* wmod, float* mod) {
  __shared__ float sc[512];
  __shared__ float red[4][64];
  int t = threadIdx.x;
  for (int i = t; i < 512; i += 256) { float v = bf2f(c[i]); sc[i] = rbf(v / (1.f + __expf(-v))); }
  __syncthreads();
  int col = blockIdx.x * 64 + (t & 63), g = t >> 6;
  float acc = 0.f;
  for (int k = g * 128; k < g * 128 + 128; ++k) acc += sc[k] * bf2f(wmod[k * 3072 + col]);
  red[g][t & 63] = acc;
  __syncthreads();
  if (t < 64) mod[col] = rbf(red[0][t] + red[1][t] + red[2][t] + red[3][t]);
}

template <typename T> __global__ void k_ln(const T* x, const float* sh, const float* cs, u16* h) {
  int row = blockIdx.x * 4 + (threadIdx.x >> 6), lane = threadIdx.x & 63;
  float v[8];
  for (int j = 0; j < 8; ++j) { if constexpr (sizeof(T) == 2) v[j] = bf2f(x[row * D + lane * 8 + j]); else v[j] = x[row * D + lane * 8 + j]; }
  float s = 0; for (int j = 0; j < 8; ++j) s += v[j];
  for (int o = 32; o; o >>= 1) s += __shfl_xor(s, o);
  float m = s / D, q = 0;
  for (int j = 0; j < 8; ++j) { float d = v[j] - m; q += d * d; }
  for (int o = 32; o; o >>= 1) q += __shfl_xor(q, o);
  float r = rsqrtf(q / D + 1e-5f);
  for (int j = 0; j < 8; ++j) { int k = lane * 8 + j; h[row * D + k] = f2bf((v[j] - m) * r * (1.f + cs[k]) + sh[k]); }
}

template <int EPI> __global__ __launch_bounds__(256) void k_gemm(const u16* A, const u16* B, const u16* B2, int M, int N, int K, void* Cout, const void* res, const float* g) {
  __shared__ u16 As[64][40], Bs[64][40], Bs2[64][40];
  int t = threadIdx.x, lane = t & 63, w = t >> 6, wr = w >> 1, wc = w & 1;
  int m0 = blockIdx.y * 64, n0 = blockIdx.x * 64;
  f32x16 acc = {}, acc2 = {};
  for (int k0 = 0; k0 < K; k0 += 32) {
    { int r = t >> 2, c = (t & 3) * 8; *(bf16x8*)&As[r][c] = *(const bf16x8*)&A[(size_t)(m0 + r) * K + k0 + c]; }
    { int kk = t >> 3, c = (t & 7) * 8; bf16x8 v = *(const bf16x8*)&B[(size_t)(k0 + kk) * N + n0 + c]; for (int j = 0; j < 8; ++j) Bs[c + j][kk] = v[j];
      if (EPI == 3) { bf16x8 v2 = *(const bf16x8*)&B2[(size_t)(k0 + kk) * N + n0 + c]; for (int j = 0; j < 8; ++j) Bs2[c + j][kk] = v2[j]; } }
    __syncthreads();
    for (int ks = 0; ks < 2; ++ks) {
      bf16x8 a = *(bf16x8*)&As[wr * 32 + (lane & 31)][ks * 16 + (lane >> 5) * 8];
      bf16x8 b = *(bf16x8*)&Bs[wc * 32 + (lane & 31)][ks * 16 + (lane >> 5) * 8];
      acc = __builtin_amdgcn_mfma_f32_32x32x16_bf16(a, b, acc, 0, 0, 0);
      if (EPI == 3) { bf16x8 b2 = *(bf16x8*)&Bs2[wc * 32 + (lane & 31)][ks * 16 + (lane >> 5) * 8]; acc2 = __builtin_amdgcn_mfma_f32_32x32x16_bf16(a, b2, acc2, 0, 0, 0); }
    }
    __syncthreads();
  }
  for (int r = 0; r < 16; ++r) {
    int row = m0 + wr * 32 + (r & 3) + 8 * (r >> 2) + 4 * (lane >> 5), col = n0 + wc * 32 + (lane & 31);
    size_t idx = (size_t)row * N + col;
    float v = acc[r];
    if (EPI == 0) ((u16*)Cout)[idx] = f2bf(v);
    if (EPI == 1) ((float*)Cout)[idx] = bf2f(((const u16*)res)[idx]) + g[col] * v;
    if (EPI == 2) ((u16*)Cout)[idx] = f2bf(((const float*)res)[idx] + g[col] * v);
    if (EPI == 3) { float u = 0.7978845608028654f * (v + 0.044715f * v * v * v); float ge = 0.5f * v * (1.f + tanhf(u)); ((u16*)Cout)[idx] = f2bf(ge * acc2[r]); }
  }
}

__global__ __launch_bounds__(64) void k_attn(const u16* Q, const u16* Kc, const u16* Vc, u16* O) {
  __shared__ u16 Ps[32][40];
  __shared__ u16 Vt[128][40];
  int lane = threadIdx.x, r32 = lane & 31, hi = lane >> 5;
  int h = blockIdx.y, q0 = blockIdx.x * 32;
  const u16* Kh = Kc + (size_t)h * S * DH; const u16* Vh = Vc + (size_t)h * S * DH;
  bf16x8 qf[8];
  for (int d0 = 0; d0 < 8; ++d0) qf[d0] = *(const bf16x8*)&Q[(size_t)(q0 + r32) * (H * DH) + h * DH + d0 * 16 + hi * 8];
  f32x16 o[4] = {};
  float m = -1e30f, l = 0.f;
  const float C = 0.088388347648318440f * 1.4426950408889634f;
  for (int k0 = 0; k0 < S; k0 += 32) {
    f32x16 s = {};
    for (int d0 = 0; d0 < 8; ++d0) { bf16x8 kf = *(const bf16x8*)&Kh[(size_t)(k0 + r32) * DH + d0 * 16 + hi * 8]; s = __builtin_amdgcn_mfma_f32_32x32x16_bf16(kf, qf[d0], s, 0, 0, 0); }
    float mx = s[0]; for (int r = 1; r < 16; ++r) mx = fmaxf(mx, s[r]);
    mx = fmaxf(mx, __shfl_xor(mx, 32));
    float mn = fmaxf(m, mx), al = exp2f((m - mn) * C); m = mn;
    float ps = 0;
    for (int r = 0; r < 16; ++r) { float p = exp2f((s[r] - mn) * C); ps += p; Ps[r32][(r & 3) + 8 * (r >> 2) + 4 * hi] = f2bf(p); }
    ps += __shfl_xor(ps, 32);
    l = l * al + ps;
    for (int i = lane; i < 32 * 16; i += 64) { int kk = i >> 4, c = (i & 15) * 8; bf16x8 v = *(const bf16x8*)&Vh[(size_t)(k0 + kk) * DH + c]; for (int j = 0; j < 8; ++j) Vt[c + j][kk] = v[j]; }
    __syncthreads();
    for (int db = 0; db < 4; ++db) {
      for (int r = 0; r < 16; ++r) o[db][r] *= al;
      for (int ks = 0; ks < 2; ++ks) {
        bf16x8 a = *(bf16x8*)&Vt[db * 32 + r32][ks * 16 + hi * 8];
        bf16x8 b = *(bf16x8*)&Ps[r32][ks * 16 + hi * 8];
        o[db] = __builtin_amdgcn_mfma_f32_32x32x16_bf16(a, b, o[db], 0, 0, 0);
      }
    }
    __syncthreads();
  }
  float il = 1.f / l;
  for (int db = 0; db < 4; ++db) for (int r = 0; r < 16; ++r) {
    int d = db * 32 + (r & 3) + 8 * (r >> 2) + 4 * hi;
    O[(size_t)(q0 + r32) * (H * DH) + h * DH + d] = f2bf(o[db][r] * il);
  }
}

extern "C" void kernel_launch(void* const* d_in, const int* in_sizes, int n_in, void* d_out, int out_size, void* d_ws, size_t ws_size, hipStream_t stream) {
  const u16* x = (const u16*)d_in[0]; const u16* kc = (const u16*)d_in[1]; const u16* vc = (const u16*)d_in[2];
  const u16* wq = (const u16*)d_in[3]; const u16* wo = (const u16*)d_in[4];
  const u16* wg = (const u16*)d_in[6]; const u16* wu = (const u16*)d_in[7]; const u16* wd = (const u16*)d_in[8];
  const u16* c = (const u16*)d_in[9]; const u16* wmod = (const u16*)d_in[10];
  char* ws = (char*)d_ws;
  float* mod = (float*)ws;
  u16* h = (u16*)(ws + (1 << 20));
  u16* q = (u16*)(ws + (4 << 20));
  u16* o = (u16*)(ws + (24 << 20));
  float* x1 = (float*)(ws + (44 << 20));
  u16* h2 = (u16*)(ws + (50 << 20));
  u16* hid = (u16*)(ws + (54 << 20));
  k_mod<<<48, 256, 0, stream>>>(c, wmod, mod);
  k_ln<u16><<<S / 4, 256, 0, stream>>>(x, mod + 0, mod + 512, h);
  k_gemm<0><<<dim3(4096 / 64, S / 64), 256, 0, stream>>>(h, wq, nullptr, S, 4096, 512, q, nullptr, nullptr);
  k_attn<<<dim3(S / 32, H), 64, 0, stream>>>(q, kc, vc, o);
  k_gemm<1><<<dim3(512 / 64, S / 64), 256, 0, stream>>>(o, wo, nullptr, S, 512, 4096, x1, x, mod + 1024);
  k_ln<float><<<S / 4, 256, 0, stream>>>(x1, mod + 1536, mod + 2048, h2);
  k_gemm<3><<<dim3(1792 / 64, S / 64), 256, 0, stream>>>(h2, wg, wu, S, 1792, 512, hid, nullptr, nullptr);
  k_gemm<2><<<dim3(512 / 64, S / 64), 256, 0, stream>>>(hid, wd, nullptr, S, 512, 1792, d_out, x1, mod + 2560);
}
```

```cpp
#include <hip/hip_runtime.h>
#include <hip/hip_bf16.h>
typedef unsigned short u16;
typedef short bf16x8 __attribute__((ext_vector_type(8)));
typedef short bf16x4 __attribute__((ext_vector_type(4)));
typedef float f32x4 __attribute__((ext_vector_type(4)));
typedef float f32x2 __attribute__((ext_vector_type(2)));
typedef unsigned u32x4 __attribute__((ext_vector_type(4)));
typedef unsigned u32x2 __attribute__((ext_vector_type(2)));
typedef __bf16 bf16x2_t __attribute__((ext_vector_type(2)));
__device__ __forceinline__ float bf2f(u16 x) { return __uint_as_float(((unsigned)x) << 16); }
__device__ __forceinline__ unsigned cvtpk(float lo, float hi) { f32x2 v = {lo, hi}; bf16x2_t b = __builtin_convertvector(v, bf16x2_t); return __builtin_bit_cast(unsigned, b); }
__device__ __forceinline__ float rbf(float f) { return __uint_as_float(cvtpk(f, f) << 16); }

constexpr size_t WS_MOD = 0, WS_TAB = 16384, WS_CNT = 32768, WS_H = 1 << 20, WS_OF = 4 << 20, WS_X1F = 22 << 20, WS_H2F = 24 << 20, WS_HID = 26 << 20, WS_PART = 36 << 20;
constexpr int S = 2048, D = 512, H = 32, DH = 128, F = 1792;

typedef _Float16 f16x2_t __attribute__((ext_vector_type(2)));
__device__ __forceinline__ unsigned cvtpk_h(float lo, float hi) { f16x2_t h = {(_Float16)lo, (_Float16)hi}; return __builtin_bit_cast(unsigned, h); }
__global__ __launch_bounds__(256) void k_mod(const u16* __restrict__ c, const u16* __restrict__ wmod, float* __restrict__ mod, float* __restrict__ modtab,
                                             const u16* __restrict__ x, u16* __restrict__ xnfrag,
                                             const u16* __restrict__ wq, const u16* __restrict__ kc, const u16* __restrict__ vc) {
  __shared__ float red[64][33];
  const int t = threadIdx.x;
  if (blockIdx.x >= 352) {
    const unsigned j = blockIdx.x - 352, xc = blockIdx.x & 7, part = j >> 3;
    unsigned lo, hi, len;
    asm volatile("s_getpc_b64 s[90:91]\n\tLpf_%=:\n\ts_add_u32 %0, s90, (code_first_mark-Lpf_%=)&4294967295\n\ts_addc_u32 %1, s91, (code_first_mark-Lpf_%=)>>32\n\ts_mov_b32 %2, (code_last_mark-code_first_mark)&4294967295"
                 : "=s"(lo), "=s"(hi), "=s"(len) :: "s90", "s91", "scc");
    const char* code = (const char*)(((unsigned long long)hi << 32) | (lo & ~15u));
    u32x4 v[13];
    { const unsigned o = part * 4096 + t * 16; v[0] = *(const u32x4*)(code + (o + 16 <= len ? o : 0u)); }
#pragma unroll
    for (int i = 0; i < 7; ++i) { const unsigned r = min(part * 4 + (t >> 6) + 80u * i, 511u); v[1 + i] = *(const u32x4*)((const char*)wq + (size_t)r * 8192 + xc * 1024 + (t & 63) * 16); }
#pragma unroll
    for (int i = 0; i < 5; ++i) { const unsigned p = min(part + 20u * i, 95u), hd = 4 * xc + p / 24, q = p % 24; const char* b = (const char*)(q < 12 ? kc : vc);
      v[8 + i] = *(const u32x4*)(b + (size_t)hd * 524288 + (q % 12) * 4096 + t * 16); }
    u32x4 acc = v[0];
#pragma unroll
    for (int i = 1; i < 13; ++i) acc ^= v[i];
    asm volatile("" :: "v"(acc));
    return;
  }
  if (blockIdx.x >= 96) {
    if (blockIdx.x < 116) ((unsigned*)((char*)mod + WS_CNT))[(blockIdx.x - 96) * 256 + t] = 0u;
    const int w = t >> 6, lane = t & 63, i = lane >> 3, p = lane & 7, row = (blockIdx.x - 96) * 8 + i;
    const bf16x8 t0 = *(const bf16x8*)&x[row * D + (16 * w + p) * 8], t1 = *(const bf16x8*)&x[row * D + (16 * w + p + 8) * 8];
    float v[16];
#pragma unroll
    for (int j = 0; j < 8; ++j) { v[j] = bf2f((u16)t0[j]); v[8 + j] = bf2f((u16)t1[j]); }
    float s = 0;
#pragma unroll
    for (int j = 0; j < 16; ++j) s += v[j];
    s += __shfl_xor(s, 1); s += __shfl_xor(s, 2); s += __shfl_xor(s, 4);
    const float ml = s * (1.f / 128); float q = 0;
#pragma unroll
    for (int j = 0; j < 16; ++j) { const float d = v[j] - ml; q += d * d; }
    q += __shfl_xor(q, 1); q += __shfl_xor(q, 2); q += __shfl_xor(q, 4);
    if (p == 0) { red[w][2 * i] = ml; red[w][2 * i + 1] = q; }
    __syncthreads();
    const float m0 = red[0][2 * i], m1 = red[1][2 * i], m2 = red[2][2 * i], m3 = red[3][2 * i];
    const float mean = 0.25f * ((m0 + m1) + (m2 + m3));
    const float M2 = (red[0][2 * i + 1] + red[1][2 * i + 1]) + (red[2][2 * i + 1] + red[3][2 * i + 1])
                   + 128.f * ((m0 - mean) * (m0 - mean) + (m1 - mean) * (m1 - mean) + (m2 - mean) * (m2 - mean) + (m3 - mean) * (m3 - mean));
    const float r = rsqrtf(M2 * (1.f / D) + 1e-5f);
#pragma unroll
    for (int u = 0; u < 2; ++u) {
      const int ch = 16 * w + p + 8 * u;
      u32x4 o;
#pragma unroll
      for (int e = 0; e < 4; ++e) o[e] = cvtpk_h((v[8 * u + 2 * e] - mean) * r, (v[8 * u + 2 * e + 1] - mean) * r);
      *(u32x4*)&xnfrag[(size_t)((((row >> 4) * 16 + (ch >> 2)) * 64) + (ch & 3) * 16 + (row & 15)) * 8] = o;
    }
    return;
  }
  const int ch = t & 3, kg = t >> 2, col0 = blockIdx.x * 32 + ch * 8;
  bf16x8 w[8];
#pragma unroll
  for (int i = 0; i < 8; ++i) w[i] = *(const bf16x8*)&wmod[(size_t)(kg * 8 + i) * 3072 + col0];
  const bf16x8 cv = *(const bf16x8*)&c[kg * 8];
  float acc[8];
#pragma unroll
  for (int j = 0; j < 8; ++j) acc[j] = 0.f;
#pragma unroll
  for (int i = 0; i < 8; ++i) {
    const float v = bf2f((u16)cv[i]); const float sc = rbf(v / (1.f + __expf(-v)));
#pragma unroll
    for (int j = 0; j < 8; ++j) acc[j] += sc * bf2f((u16)w[i][j]);
  }
#pragma unroll
  for (int j = 0; j < 8; ++j) red[kg][ch * 8 + j] = acc[j];
  __syncthreads();
  const int col = t & 31, part = t >> 5;
  float s = 0.f;
#pragma unroll
  for (int i = 0; i < 8; ++i) s += red[part * 8 + i][col];
  s += __shfl_xor(s, 32);
  __syncthreads();
  if ((t & 32) == 0) red[t >> 6][col] = s;
  __syncthreads();
  if (t < 32) {
    const float r = rbf((red[0][t] + red[1][t]) + (red[2][t] + red[3][t]));
    const int n = blockIdx.x * 32 + t;
    mod[n] = r;
    if (n < 1024) {
      const float cscale = 0.12751743082459868f;
      const int k = n & 511;
      modtab[(k >> 3) * 16 + (n < 512 ? 8 : 0) + (k & 7)] = (n < 512 ? r : rbf(1.f + r)) * cscale;
    }
  }
}

#define XACQ false
__device__ __forceinline__ unsigned lane_id() { unsigned l; asm volatile("v_mbcnt_lo_u32_b32 %0, -1, 0\n\tv_mbcnt_hi_u32_b32 %0, -1, %0" : "=v"(l)); return l; }
__device__ __forceinline__ void arrive(unsigned* cnt, unsigned wid) {
  if (wid == 0 && lane_id() == 0) __hip_atomic_fetch_add(cnt, 1u, __ATOMIC_RELAXED, __HIP_MEMORY_SCOPE_AGENT);
}
template <bool ACQ> __device__ __forceinline__ void wait_ge(unsigned* cnt, unsigned need, unsigned wid) {
  if (wid == 0) {
    for (unsigned spins = 0; spins < (1u << 16); ++spins) {
      if ((unsigned)__builtin_amdgcn_readfirstlane(__hip_atomic_load(cnt, __ATOMIC_RELAXED, __HIP_MEMORY_SCOPE_AGENT)) >= need) break;
      __builtin_amdgcn_s_sleep(1);
    }
  }
  asm volatile("" ::: "memory"); __builtin_amdgcn_s_barrier(); asm volatile("" ::: "memory");
}
__device__ __forceinline__ unsigned arrive_ret(unsigned* cnt, unsigned wid) {
  unsigned r = 0;
  if (wid == 0) { if (lane_id() == 0) r = __hip_atomic_fetch_add(cnt, 1u, __ATOMIC_RELAXED, __HIP_MEMORY_SCOPE_AGENT) + 1u; r = __builtin_amdgcn_readfirstlane(r); }
  return r;
}
__device__ __forceinline__ void wait_ge2(unsigned* cnt, unsigned need, unsigned wid, unsigned seen) {
  if (wid == 0 && seen < need) {
    for (unsigned spins = 0; spins < (1u << 16); ++spins) {
      if ((unsigned)__builtin_amdgcn_readfirstlane(__hip_atomic_load(cnt, __ATOMIC_RELAXED, __HIP_MEMORY_SCOPE_AGENT)) >= need) break;
      __builtin_amdgcn_s_sleep(1);
    }
  }
  asm volatile("" ::: "memory"); __builtin_amdgcn_s_barrier(); asm volatile("" ::: "memory");
}
#define G1P0 \
    "v_mbcnt_lo_u32_b32 v196, -1, 0\n\t" \
    "v_mbcnt_hi_u32_b32 v196, -1, v196\n\t" \
    "s_lshl_b32 s60, %5, 8\n\t" \
    "s_lshl_b32 s61, %6, 20\n\t" \
    "s_add_u32 s60, s60, s61\n\t" \
    "s_mov_b64 s[40:41], %1\n\t" \
    "s_add_u32 s40, s40, s60\n\t" \
    "s_addc_u32 s41, s41, 0\n\t" \
    "s_and_b32 s41, s41, 0xffff\n\t" \
    "s_mov_b32 s42, 1047808\n\t" \
    "s_mov_b32 s43, 0x20000\n\t" \
    "v_lshrrev_b32 v220, 3, v196\n\t" \
    "s_lshl_b32 s60, %7, 4\n\t" \
    "v_add_u32 v220, s60, v220\n\t" \
    "s_mov_b32 s60, 1024\n\t" \
    "v_mul_lo_u32 v198, v220, s60\n\t" \
    "v_bfe_u32 v221, v196, 4, 1\n\t" \
    "v_lshlrev_b32 v221, 2, v221\n\t" \
    "v_bfe_u32 v222, v196, 5, 1\n\t" \
    "v_lshl_add_u32 v221, v222, 1, v221\n\t" \
    "v_and_b32 v222, 7, v196\n\t" \
    "v_xor_b32 v222, v222, v221\n\t" \
    "v_lshl_add_u32 v198, v222, 4, v198\n\t" \
    "s_lshl_b32 s68, %7, 11\n\t" \
    "s_add_u32 s68, s68, %8\n\t" \
    "s_add_u32 m0, s68, 0\n\t" \
    "s_mov_b32 s69, 0\n\t" \
    "buffer_load_dwordx4 v198, s[40:43], s69 offen lds\n\t" \
    "s_add_u32 m0, s68, 1024\n\t" \
    "s_mov_b32 s69, 8192\n\t" \
    "buffer_load_dwordx4 v198, s[40:43], s69 offen lds\n\t" \
    "s_add_u32 m0, s68, 8192\n\t" \
    "s_mov_b32 s69, 128\n\t" \
    "buffer_load_dwordx4 v198, s[40:43], s69 offen lds\n\t" \
    "s_add_u32 m0, s68, 9216\n\t" \
    "s_mov_b32 s69, 8320\n\t" \
    "buffer_load_dwordx4 v198, s[40:43], s69 offen lds\n\t" \
    "s_add_u32 m0, s68, 16384\n\t" \
    "s_mov_b32 s69, 65536\n\t" \
    "buffer_load_dwordx4 v198, s[40:43], s69 offen lds\n\t" \
    "s_add_u32 m0, s68, 17408\n\t" \
    "s_mov_b32 s69, 73728\n\t" \
    "buffer_load_dwordx4 v198, s[40:43], s69 offen lds\n\t" \
    "s_add_u32 m0, s68, 24576\n\t" \
    "s_mov_b32 s69, 65664\n\t" \
    "buffer_load_dwordx4 v198, s[40:43], s69 offen lds\n\t" \
    "s_add_u32 m0, s68, 25600\n\t" \
    "s_mov_b32 s69, 73856\n\t" \
    "buffer_load_dwordx4 v198, s[40:43], s69 offen lds\n\t"
#define G1P1 \
    "v_mbcnt_lo_u32_b32 v196, -1, 0\n\t" \
    "v_mbcnt_hi_u32_b32 v196, -1, v196\n\t" \
    "s_lshl_b32 s60, %5, 8\n\t" \
    "s_lshl_b32 s61, %6, 20\n\t" \
    "s_add_u32 s60, s60, s61\n\t" \
    "s_mov_b64 s[40:41], %1\n\t" \
    "s_add_u32 s40, s40, s60\n\t" \
    "s_addc_u32 s41, s41, 0\n\t" \
    "s_and_b32 s41, s41, 0xffff\n\t" \
    "s_mov_b32 s42, 1047808\n\t" \
    "s_mov_b32 s43, 0x20000\n\t" \
    "v_lshrrev_b32 v220, 3, v196\n\t" \
    "s_lshl_b32 s60, %7, 4\n\t" \
    "v_add_u32 v220, s60, v220\n\t" \
    "s_mov_b32 s60, 1024\n\t" \
    "v_mul_lo_u32 v198, v220, s60\n\t" \
    "v_bfe_u32 v221, v196, 4, 1\n\t" \
    "v_lshlrev_b32 v221, 2, v221\n\t" \
    "v_bfe_u32 v222, v196, 5, 1\n\t" \
    "v_lshl_add_u32 v221, v222, 1, v221\n\t" \
    "v_and_b32 v222, 7, v196\n\t" \
    "v_xor_b32 v222, v222, v221\n\t" \
    "v_lshl_add_u32 v198, v222, 4, v198\n\t" \
    "s_lshl_b32 s68, %7, 11\n\t" \
    "s_add_u32 s68, s68, %8\n\t" \
    "s_add_u32 m0, s68, 32768\n\t" \
    "s_mov_b32 s69, 131072\n\t" \
    "buffer_load_dwordx4 v198, s[40:43], s69 offen lds\n\t" \
    "s_add_u32 m0, s68, 33792\n\t" \
    "s_mov_b32 s69, 139264\n\t" \
    "buffer_load_dwordx4 v198, s[40:43], s69 offen lds\n\t" \
    "s_add_u32 m0, s68, 40960\n\t" \
    "s_mov_b32 s69, 131200\n\t" \
    "buffer_load_dwordx4 v198, s[40:43], s69 offen lds\n\t" \
    "s_add_u32 m0, s68, 41984\n\t" \
    "s_mov_b32 s69, 139392\n\t" \
    "buffer_load_dwordx4 v198, s[40:43], s69 offen lds\n\t" \
    "s_add_u32 m0, s68, 49152\n\t" \
    "s_mov_b32 s69, 196608\n\t" \
    "buffer_load_dwordx4 v198, s[40:43], s69 offen lds\n\t" \
    "s_add_u32 m0, s68, 50176\n\t" \
    "s_mov_b32 s69, 204800\n\t" \
    "buffer_load_dwordx4 v198, s[40:43], s69 offen lds\n\t" \
    "s_add_u32 m0, s68, 57344\n\t" \
    "s_mov_b32 s69, 196736\n\t" \
    "buffer_load_dwordx4 v198, s[40:43], s69 offen lds\n\t" \
    "s_add_u32 m0, s68, 58368\n\t" \
    "s_mov_b32 s69, 204928\n\t" \
    "buffer_load_dwordx4 v198, s[40:43], s69 offen lds\n\t" \
    "s_add_u32 m0, s68, 65536\n\t" \
    "s_mov_b32 s69, 262144\n\t" \
    "buffer_load_dwordx4 v198, s[40:43], s69 offen lds\n\t" \
    "s_add_u32 m0, s68, 66560\n\t" \
    "s_mov_b32 s69, 270336\n\t" \
    "buffer_load_dwordx4 v198, s[40:43], s69 offen lds\n\t" \
    "s_add_u32 m0, s68, 73728\n\t" \
    "s_mov_b32 s69, 262272\n\t" \
    "buffer_load_dwordx4 v198, s[40:43], s69 offen lds\n\t" \
    "s_add_u32 m0, s68, 74752\n\t" \
    "s_mov_b32 s69, 270464\n\t" \
    "buffer_load_dwordx4 v198, s[40:43], s69 offen lds\n\t" \
    "s_add_u32 m0, s68, 81920\n\t" \
    "s_mov_b32 s69, 327680\n\t" \
    "buffer_load_dwordx4 v198, s[40:43], s69 offen lds\n\t" \
    "s_add_u32 m0, s68, 82944\n\t" \
    "s_mov_b32 s69, 335872\n\t" \
    "buffer_load_dwordx4 v198, s[40:43], s69 offen lds\n\t" \
    "s_add_u32 m0, s68, 90112\n\t" \
    "s_mov_b32 s69, 327808\n\t" \
    "buffer_load_dwordx4 v198, s[40:43], s69 offen lds\n\t" \
    "s_add_u32 m0, s68, 91136\n\t" \
    "s_mov_b32 s69, 336000\n\t" \
    "buffer_load_dwordx4 v198, s[40:43], s69 offen lds\n\t" \
    "s_add_u32 m0, s68, 98304\n\t" \
    "s_mov_b32 s69, 393216\n\t" \
    "buffer_load_dwordx4 v198, s[40:43], s69 offen lds\n\t" \
    "s_add_u32 m0, s68, 99328\n\t" \
    "s_mov_b32 s69, 401408\n\t" \
    "buffer_load_dwordx4 v198, s[40:43], s69 offen lds\n\t" \
    "s_add_u32 m0, s68, 106496\n\t" \
    "s_mov_b32 s69, 393344\n\t" \
    "buffer_load_dwordx4 v198, s[40:43], s69 offen lds\n\t" \
    "s_add_u32 m0, s68, 107520\n\t" \
    "s_mov_b32 s69, 401536\n\t" \
    "buffer_load_dwordx4 v198, s[40:43], s69 offen lds\n\t"
#define G2P0 \
    "v_mbcnt_lo_u32_b32 v196, -1, 0\n\t" \
    "v_mbcnt_hi_u32_b32 v196, -1, v196\n\t" \
    "s_lshl_b32 s60, %5, 8\n\t" \
    "s_mov_b64 s[40:41], %1\n\t" \
    "s_add_u32 s40, s40, s60\n\t" \
    "s_addc_u32 s41, s41, 0\n\t" \
    "s_and_b32 s41, s41, 0xffff\n\t" \
    "s_mov_b32 s42, 1831680\n\t" \
    "s_mov_b32 s43, 0x20000\n\t" \
    "s_mov_b64 s[44:45], %2\n\t" \
    "s_add_u32 s44, s44, s60\n\t" \
    "s_addc_u32 s45, s45, 0\n\t" \
    "s_and_b32 s45, s45, 0xffff\n\t" \
    "s_mov_b32 s46, 1831680\n\t" \
    "s_mov_b32 s47, 0x20000\n\t" \
    "v_lshrrev_b32 v220, 3, v196\n\t" \
    "s_lshl_b32 s60, %7, 4\n\t" \
    "v_add_u32 v220, s60, v220\n\t" \
    "s_mov_b32 s60, 3584\n\t" \
    "v_mul_lo_u32 v198, v220, s60\n\t" \
    "v_bfe_u32 v221, v196, 4, 1\n\t" \
    "v_lshlrev_b32 v221, 2, v221\n\t" \
    "v_bfe_u32 v222, v196, 5, 1\n\t" \
    "v_lshl_add_u32 v221, v222, 1, v221\n\t" \
    "v_and_b32 v222, 7, v196\n\t" \
    "v_xor_b32 v222, v222, v221\n\t" \
    "v_lshl_add_u32 v198, v222, 4, v198\n\t" \
    "s_lshl_b32 s68, %7, 11\n\t" \
    "s_add_u32 s68, s68, %8\n\t" \
    "s_add_u32 m0, s68, 114688\n\t" \
    "s_mov_b32 s69, 0\n\t" \
    "buffer_load_dwordx4 v198, s[40:43], s69 offen lds\n\t" \
    "s_add_u32 m0, s68, 115712\n\t" \
    "s_mov_b32 s69, 28672\n\t" \
    "buffer_load_dwordx4 v198, s[40:43], s69 offen lds\n\t" \
    "s_add_u32 m0, s68, 122880\n\t" \
    "s_mov_b32 s69, 0\n\t" \
    "buffer_load_dwordx4 v198, s[44:47], s69 offen lds\n\t" \
    "s_add_u32 m0, s68, 123904\n\t" \
    "s_mov_b32 s69, 28672\n\t" \
    "buffer_load_dwordx4 v198, s[44:47], s69 offen lds\n\t" \
    "s_add_u32 m0, s68, 131072\n\t" \
    "s_mov_b32 s69, 229376\n\t" \
    "buffer_load_dwordx4 v198, s[40:43], s69 offen lds\n\t" \
    "s_add_u32 m0, s68, 132096\n\t" \
    "s_mov_b32 s69, 258048\n\t" \
    "buffer_load_dwordx4 v198, s[40:43], s69 offen lds\n\t" \
    "s_add_u32 m0, s68, 139264\n\t" \
    "s_mov_b32 s69, 229376\n\t" \
    "buffer_load_dwordx4 v198, s[44:47], s69 offen lds\n\t" \
    "s_add_u32 m0, s68, 140288\n\t" \
    "s_mov_b32 s69, 258048\n\t" \
    "buffer_load_dwordx4 v198, s[44:47], s69 offen lds\n\t" \
    "s_add_u32 m0, s68, 0\n\t" \
    "s_mov_b32 s69, 458752\n\t" \
    "buffer_load_dwordx4 v198, s[40:43], s69 offen lds\n\t" \
    "s_add_u32 m0, s68, 1024\n\t" \
    "s_mov_b32 s69, 487424\n\t" \
    "buffer_load_dwordx4 v198, s[40:43], s69 offen lds\n\t" \
    "s_add_u32 m0, s68, 8192\n\t" \
    "s_mov_b32 s69, 458752\n\t" \
    "buffer_load_dwordx4 v198, s[44:47], s69 offen lds\n\t" \
    "s_add_u32 m0, s68, 9216\n\t" \
    "s_mov_b32 s69, 487424\n\t" \
    "buffer_load_dwordx4 v198, s[44:47], s69 offen lds\n\t" \
    "s_add_u32 m0, s68, 16384\n\t" \
    "s_mov_b32 s69, 688128\n\t" \
    "buffer_load_dwordx4 v198, s[40:43], s69 offen lds\n\t" \
    "s_add_u32 m0, s68, 17408\n\t" \
    "s_mov_b32 s69, 716800\n\t" \
    "buffer_load_dwordx4 v198, s[40:43], s69 offen lds\n\t" \
    "s_add_u32 m0, s68, 24576\n\t" \
    "s_mov_b32 s69, 688128\n\t" \
    "buffer_load_dwordx4 v198, s[44:47], s69 offen lds\n\t" \
    "s_add_u32 m0, s68, 25600\n\t" \
    "s_mov_b32 s69, 716800\n\t" \
    "buffer_load_dwordx4 v198, s[44:47], s69 offen lds\n\t" \
    "s_add_u32 m0, s68, 32768\n\t" \
    "s_mov_b32 s69, 917504\n\t" \
    "buffer_load_dwordx4 v198, s[40:43], s69 offen lds\n\t" \
    "s_add_u32 m0, s68, 33792\n\t" \
    "s_mov_b32 s69, 946176\n\t" \
    "buffer_load_dwordx4 v198, s[40:43], s69 offen lds\n\t" \
    "s_add_u32 m0, s68, 40960\n\t" \
    "s_mov_b32 s69, 917504\n\t" \
    "buffer_load_dwordx4 v198, s[44:47], s69 offen lds\n\t" \
    "s_add_u32 m0, s68, 41984\n\t" \
    "s_mov_b32 s69, 946176\n\t" \
    "buffer_load_dwordx4 v198, s[44:47], s69 offen lds\n\t" \
    "s_add_u32 m0, s68, 49152\n\t" \
    "s_mov_b32 s69, 1146880\n\t" \
    "buffer_load_dwordx4 v198, s[40:43], s69 offen lds\n\t" \
    "s_add_u32 m0, s68, 50176\n\t" \
    "s_mov_b32 s69, 1175552\n\t" \
    "buffer_load_dwordx4 v198, s[40:43], s69 offen lds\n\t" \
    "s_add_u32 m0, s68, 57344\n\t" \
    "s_mov_b32 s69, 1146880\n\t" \
    "buffer_load_dwordx4 v198, s[44:47], s69 offen lds\n\t" \
    "s_add_u32 m0, s68, 58368\n\t" \
    "s_mov_b32 s69, 1175552\n\t" \
    "buffer_load_dwordx4 v198, s[44:47], s69 offen lds\n\t" \
    "s_add_u32 m0, s68, 65536\n\t" \
    "s_mov_b32 s69, 1376256\n\t" \
    "buffer_load_dwordx4 v198, s[40:43], s69 offen lds\n\t" \
    "s_add_u32 m0, s68, 66560\n\t" \
    "s_mov_b32 s69, 1404928\n\t" \
    "buffer_load_dwordx4 v198, s[40:43], s69 offen lds\n\t" \
    "s_add_u32 m0, s68, 73728\n\t" \
    "s_mov_b32 s69, 1376256\n\t" \
    "buffer_load_dwordx4 v198, s[44:47], s69 offen lds\n\t" \
    "s_add_u32 m0, s68, 74752\n\t" \
    "s_mov_b32 s69, 1404928\n\t" \
    "buffer_load_dwordx4 v198, s[44:47], s69 offen lds\n\t"
#define G2P1 \
    "v_mbcnt_lo_u32_b32 v196, -1, 0\n\t" \
    "v_mbcnt_hi_u32_b32 v196, -1, v196\n\t" \
    "s_lshl_b32 s60, %5, 8\n\t" \
    "s_mov_b64 s[40:41], %1\n\t" \
    "s_add_u32 s40, s40, s60\n\t" \
    "s_addc_u32 s41, s41, 0\n\t" \
    "s_and_b32 s41, s41, 0xffff\n\t" \
    "s_mov_b32 s42, 1831680\n\t" \
    "s_mov_b32 s43, 0x20000\n\t" \
    "s_mov_b64 s[44:45], %2\n\t" \
    "s_add_u32 s44, s44, s60\n\t" \
    "s_addc_u32 s45, s45, 0\n\t" \
    "s_and_b32 s45, s45, 0xffff\n\t" \
    "s_mov_b32 s46, 1831680\n\t" \
    "s_mov_b32 s47, 0x20000\n\t" \
    "v_lshrrev_b32 v220, 3, v196\n\t" \
    "s_lshl_b32 s60, %7, 4\n\t" \
    "v_add_u32 v220, s60, v220\n\t" \
    "s_mov_b32 s60, 3584\n\t" \
    "v_mul_lo_u32 v198, v220, s60\n\t" \
    "v_bfe_u32 v221, v196, 4, 1\n\t" \
    "v_lshlrev_b32 v221, 2, v221\n\t" \
    "v_bfe_u32 v222, v196, 5, 1\n\t" \
    "v_lshl_add_u32 v221, v222, 1, v221\n\t" \
    "v_and_b32 v222, 7, v196\n\t" \
    "v_xor_b32 v222, v222, v221\n\t" \
    "v_lshl_add_u32 v198, v222, 4, v198\n\t" \
    "s_lshl_b32 s68, %7, 11\n\t" \
    "s_add_u32 s68, s68, %8\n\t" \
    "s_add_u32 m0, s68, 81920\n\t" \
    "s_mov_b32 s69, 1605632\n\t" \
    "buffer_load_dwordx4 v198, s[40:43], s69 offen lds\n\t" \
    "s_add_u32 m0, s68, 82944\n\t" \
    "s_mov_b32 s69, 1634304\n\t" \
    "buffer_load_dwordx4 v198, s[40:43], s69 offen lds\n\t" \
    "s_add_u32 m0, s68, 90112\n\t" \
    "s_mov_b32 s69, 1605632\n\t" \
    "buffer_load_dwordx4 v198, s[44:47], s69 offen lds\n\t" \
    "s_add_u32 m0, s68, 91136\n\t" \
    "s_mov_b32 s69, 1634304\n\t" \
    "buffer_load_dwordx4 v198, s[44:47], s69 offen lds\n\t" \
    "s_add_u32 m0, s68, 98304\n\t" \
    "s_mov_b32 s69, 128\n\t" \
    "buffer_load_dwordx4 v198, s[40:43], s69 offen lds\n\t" \
    "s_add_u32 m0, s68, 99328\n\t" \
    "s_mov_b32 s69, 28800\n\t" \
    "buffer_load_dwordx4 v198, s[40:43], s69 offen lds\n\t" \
    "s_add_u32 m0, s68, 106496\n\t" \
    "s_mov_b32 s69, 128\n\t" \
    "buffer_load_dwordx4 v198, s[44:47], s69 offen lds\n\t" \
    "s_add_u32 m0, s68, 107520\n\t" \
    "s_mov_b32 s69, 28800\n\t" \
    "buffer_load_dwordx4 v198, s[44:47], s69 offen lds\n\t"
#define G3P0 \
    "v_mbcnt_lo_u32_b32 v96, -1, 0\n\t" \
    "v_mbcnt_hi_u32_b32 v96, -1, v96\n\t" \
    "v_lshlrev_b32 v97, 4, v96\n\t" \
    "s_mul_i32 s44, %2, 224\n\t" \
    "s_mul_i32 s45, %4, 14\n\t" \
    "s_add_u32 s44, s44, s45\n\t" \
    "s_lshl_b32 s44, s44, 10\n\t" \
    "s_mov_b64 s[36:37], %0\n\t" \
    "s_add_u32 s36, s36, s44\n\t" \
    "s_addc_u32 s37, s37, 0\n\t" \
    "s_and_b32 s37, s37, 0xffff\n\t" \
    "s_mov_b32 s38, 186368\n\t" \
    "s_mov_b32 s39, 0x20000\n\t" \
    "s_mul_i32 s44, %4, 458752\n\t" \
    "s_lshl_b32 s45, %3, 7\n\t" \
    "s_add_u32 s44, s44, s45\n\t" \
    "s_mov_b64 s[40:41], %1\n\t" \
    "s_add_u32 s40, s40, s44\n\t" \
    "s_addc_u32 s41, s41, 0\n\t" \
    "s_and_b32 s41, s41, 0xffff\n\t" \
    "s_mov_b32 s42, 457856\n\t" \
    "s_mov_b32 s43, 0x20000\n\t" \
    "v_lshrrev_b32 v101, 3, v96\n\t" \
    "s_mov_b32 s44, 1024\n\t" \
    "v_mul_lo_u32 v98, v101, s44\n\t" \
    "v_bfe_u32 v102, v96, 4, 1\n\t" \
    "v_lshlrev_b32 v102, 2, v102\n\t" \
    "v_bfe_u32 v103, v96, 5, 1\n\t" \
    "v_lshl_add_u32 v102, v103, 1, v102\n\t" \
    "v_and_b32 v103, 7, v96\n\t" \
    "v_xor_b32 v103, v103, v102\n\t" \
    "v_lshl_add_u32 v98, v103, 4, v98\n\t" \
    "s_mul_i32 s52, %4, 4096\n\t" \
    "s_add_u32 s52, s52, %5\n\t" \
    "s_add_u32 m0, s52, 81920\n\t" \
    "s_mov_b32 s53, 0\n\t" \
    "buffer_load_dwordx4 v98, s[40:43], s53 offen lds\n\t" \
    "s_add_u32 m0, s52, 82944\n\t" \
    "s_mov_b32 s53, 8192\n\t" \
    "buffer_load_dwordx4 v98, s[40:43], s53 offen lds\n\t" \
    "s_add_u32 m0, s52, 83968\n\t" \
    "s_mov_b32 s53, 16384\n\t" \
    "buffer_load_dwordx4 v98, s[40:43], s53 offen lds\n\t" \
    "s_add_u32 m0, s52, 84992\n\t" \
    "s_mov_b32 s53, 24576\n\t" \
    "buffer_load_dwordx4 v98, s[40:43], s53 offen lds\n\t" \
    "s_add_u32 m0, s52, 98304\n\t" \
    "s_mov_b32 s53, 32768\n\t" \
    "buffer_load_dwordx4 v98, s[40:43], s53 offen lds\n\t" \
    "s_add_u32 m0, s52, 99328\n\t" \
    "s_mov_b32 s53, 40960\n\t" \
    "buffer_load_dwordx4 v98, s[40:43], s53 offen lds\n\t" \
    "s_add_u32 m0, s52, 100352\n\t" \
    "s_mov_b32 s53, 49152\n\t" \
    "buffer_load_dwordx4 v98, s[40:43], s53 offen lds\n\t" \
    "s_add_u32 m0, s52, 101376\n\t" \
    "s_mov_b32 s53, 57344\n\t" \
    "buffer_load_dwordx4 v98, s[40:43], s53 offen lds\n\t" \
    "s_add_u32 m0, s52, 114688\n\t" \
    "s_mov_b32 s53, 65536\n\t" \
    "buffer_load_dwordx4 v98, s[40:43], s53 offen lds\n\t" \
    "s_add_u32 m0, s52, 115712\n\t" \
    "s_mov_b32 s53, 73728\n\t" \
    "buffer_load_dwordx4 v98, s[40:43], s53 offen lds\n\t" \
    "s_add_u32 m0, s52, 116736\n\t" \
    "s_mov_b32 s53, 81920\n\t" \
    "buffer_load_dwordx4 v98, s[40:43], s53 offen lds\n\t" \
    "s_add_u32 m0, s52, 117760\n\t" \
    "s_mov_b32 s53, 90112\n\t" \
    "buffer_load_dwordx4 v98, s[40:43], s53 offen lds\n\t" \
    "s_add_u32 m0, s52, 131072\n\t" \
    "s_mov_b32 s53, 98304\n\t" \
    "buffer_load_dwordx4 v98, s[40:43], s53 offen lds\n\t" \
    "s_add_u32 m0, s52, 132096\n\t" \
    "s_mov_b32 s53, 106496\n\t" \
    "buffer_load_dwordx4 v98, s[40:43], s53 offen lds\n\t" \
    "s_add_u32 m0, s52, 133120\n\t" \
    "s_mov_b32 s53, 114688\n\t" \
    "buffer_load_dwordx4 v98, s[40:43], s53 offen lds\n\t" \
    "s_add_u32 m0, s52, 134144\n\t" \
    "s_mov_b32 s53, 122880\n\t" \
    "buffer_load_dwordx4 v98, s[40:43], s53 offen lds\n\t" \
    "s_add_u32 m0, s52, 0\n\t" \
    "s_mov_b32 s53, 131072\n\t" \
    "buffer_load_dwordx4 v98, s[40:43], s53 offen lds\n\t" \
    "s_add_u32 m0, s52, 1024\n\t" \
    "s_mov_b32 s53, 139264\n\t" \
    "buffer_load_dwordx4 v98, s[40:43], s53 offen lds\n\t" \
    "s_add_u32 m0, s52, 2048\n\t" \
    "s_mov_b32 s53, 147456\n\t" \
    "buffer_load_dwordx4 v98, s[40:43], s53 offen lds\n\t" \
    "s_add_u32 m0, s52, 3072\n\t" \
    "s_mov_b32 s53, 155648\n\t" \
    "buffer_load_dwordx4 v98, s[40:43], s53 offen lds\n\t" \
    "s_add_u32 m0, s52, 16384\n\t" \
    "s_mov_b32 s53, 163840\n\t" \
    "buffer_load_dwordx4 v98, s[40:43], s53 offen lds\n\t" \
    "s_add_u32 m0, s52, 17408\n\t" \
    "s_mov_b32 s53, 172032\n\t" \
    "buffer_load_dwordx4 v98, s[40:43], s53 offen lds\n\t" \
    "s_add_u32 m0, s52, 18432\n\t" \
    "s_mov_b32 s53, 180224\n\t" \
    "buffer_load_dwordx4 v98, s[40:43], s53 offen lds\n\t" \
    "s_add_u32 m0, s52, 19456\n\t" \
    "s_mov_b32 s53, 188416\n\t" \
    "buffer_load_dwordx4 v98, s[40:43], s53 offen lds\n\t" \
    "s_add_u32 m0, s52, 32768\n\t" \
    "s_mov_b32 s53, 196608\n\t" \
    "buffer_load_dwordx4 v98, s[40:43], s53 offen lds\n\t" \
    "s_add_u32 m0, s52, 33792\n\t" \
    "s_mov_b32 s53, 204800\n\t" \
    "buffer_load_dwordx4 v98, s[40:43], s53 offen lds\n\t" \
    "s_add_u32 m0, s52, 34816\n\t" \
    "s_mov_b32 s53, 212992\n\t" \
    "buffer_load_dwordx4 v98, s[40:43], s53 offen lds\n\t" \
    "s_add_u32 m0, s52, 35840\n\t" \
    "s_mov_b32 s53, 221184\n\t" \
    "buffer_load_dwordx4 v98, s[40:43], s53 offen lds\n\t" \
    "s_waitcnt vmcnt(0)\n\t"
#define G3P1 \
    "v_mbcnt_lo_u32_b32 v96, -1, 0\n\t" \
    "v_mbcnt_hi_u32_b32 v96, -1, v96\n\t" \
    "v_lshlrev_b32 v97, 4, v96\n\t" \
    "s_mul_i32 s44, %2, 224\n\t" \
    "s_mul_i32 s45, %4, 14\n\t" \
    "s_add_u32 s44, s44, s45\n\t" \
    "s_lshl_b32 s44, s44, 10\n\t" \
    "s_mov_b64 s[36:37], %0\n\t" \
    "s_add_u32 s36, s36, s44\n\t" \
    "s_addc_u32 s37, s37, 0\n\t" \
    "s_and_b32 s37, s37, 0xffff\n\t" \
    "s_mov_b32 s38, 186368\n\t" \
    "s_mov_b32 s39, 0x20000\n\t" \
    "s_mul_i32 s44, %4, 458752\n\t" \
    "s_lshl_b32 s45, %3, 7\n\t" \
    "s_add_u32 s44, s44, s45\n\t" \
    "s_mov_b64 s[40:41], %1\n\t" \
    "s_add_u32 s40, s40, s44\n\t" \
    "s_addc_u32 s41, s41, 0\n\t" \
    "s_and_b32 s41, s41, 0xffff\n\t" \
    "s_mov_b32 s42, 457856\n\t" \
    "s_mov_b32 s43, 0x20000\n\t" \
    "v_lshrrev_b32 v101, 3, v96\n\t" \
    "s_mov_b32 s44, 1024\n\t" \
    "v_mul_lo_u32 v98, v101, s44\n\t" \
    "v_bfe_u32 v102, v96, 4, 1\n\t" \
    "v_lshlrev_b32 v102, 2, v102\n\t" \
    "v_bfe_u32 v103, v96, 5, 1\n\t" \
    "v_lshl_add_u32 v102, v103, 1, v102\n\t" \
    "v_and_b32 v103, 7, v96\n\t" \
    "v_xor_b32 v103, v103, v102\n\t" \
    "v_lshl_add_u32 v98, v103, 4, v98\n\t" \
    "s_mul_i32 s52, %4, 4096\n\t" \
    "s_add_u32 s52, s52, %5\n\t" \
    "s_add_u32 m0, s52, 49152\n\t" \
    "s_mov_b32 s53, 229376\n\t" \
    "buffer_load_dwordx4 v98, s[40:43], s53 offen lds\n\t" \
    "s_add_u32 m0, s52, 50176\n\t" \
    "s_mov_b32 s53, 237568\n\t" \
    "buffer_load_dwordx4 v98, s[40:43], s53 offen lds\n\t" \
    "s_add_u32 m0, s52, 51200\n\t" \
    "s_mov_b32 s53, 245760\n\t" \
    "buffer_load_dwordx4 v98, s[40:43], s53 offen lds\n\t" \
    "s_add_u32 m0, s52, 52224\n\t" \
    "s_mov_b32 s53, 253952\n\t" \
    "buffer_load_dwordx4 v98, s[40:43], s53 offen lds\n\t" \
    "s_add_u32 m0, s52, 65536\n\t" \
    "s_mov_b32 s53, 262144\n\t" \
    "buffer_load_dwordx4 v98, s[40:43], s53 offen lds\n\t" \
    "s_add_u32 m0, s52, 66560\n\t" \
    "s_mov_b32 s53, 270336\n\t" \
    "buffer_load_dwordx4 v98, s[40:43], s53 offen lds\n\t" \
    "s_add_u32 m0, s52, 67584\n\t" \
    "s_mov_b32 s53, 278528\n\t" \
    "buffer_load_dwordx4 v98, s[40:43], s53 offen lds\n\t" \
    "s_add_u32 m0, s52, 68608\n\t" \
    "s_mov_b32 s53, 286720\n\t" \
    "buffer_load_dwordx4 v98, s[40:43], s53 offen lds\n\t"

#define GW_CLOBBERS \
"v196", "v197", "v198", "v199", "v200", "v201", "v202", "v203", "v204", "v205", "v206", "v207", "v208", "v209", "v210", "v211", "v212", "v213", "v214", "v215", "v216", "v217", "v218", "v219", "v220", "v221", "v222", "v223", "s36", "s37", "s38", "s39", "s40", "s41", "s42", "s43", "s44", "s45", "s46", "s47", "s48", "s49", "s50", "s51", "s52", "s53", "s54", "s55", "s56", "s57", "s58", "s59", "s60", "s61", "s62", "s63", "s64", "s65", "s66", "s67", "s68", "s69", "s70", "s71", "scc", "memory"
#define SK_CLOBBERS \
"v96", "v97", "v98", "v99", "v100", "v101", "v102", "v103", "v104", "v105", "v106", "v107", "v108", "v109", "v110", "v111", "s36", "s37", "s38", "s39", "s40", "s41", "s42", "s43", "s44", "s45", "s46", "s47", "s48", "s49", "s50", "s51", "s52", "s53", "s54", "s55", "scc", "memory"
#define PRE_G1(STR) asm volatile(STR : : "s"(ws), "s"(wo), "s"(wo), "s"(ws), "s"(mtp), "s"(nt128), "s"(kq), "s"(wid), "s"(ldsb) : GW_CLOBBERS)
#define PRE_G2(STR) asm volatile(STR : : "s"(ws), "s"(wg), "s"(wu), "s"(ws), "s"(mt128), "s"(idx), "s"(0u), "s"(wid), "s"(ldsb) : GW_CLOBBERS)
#define PRE_G3(STR) asm volatile(STR : : "s"(ws), "s"(wd), "s"(mt64), "s"(nt64), "s"(wid), "s"(ldsb) : SK_CLOBBERS)
__global__ __launch_bounds__(256, 1) void k_main(char* ws, const u16* wo, const u16* __restrict__ x, const u16* wg, const u16* wu, const u16* wd, u16* __restrict__ out,
                                                 const u16* wq, const u16* kc, const u16* vc) {
  __shared__ __attribute__((aligned(1024))) char lds[147456 + 1024];
  const unsigned wid = __builtin_amdgcn_readfirstlane(threadIdx.x >> 6);
  const unsigned ldsb = (unsigned)(size_t)lds;
  const unsigned bid = blockIdx.x, xc = bid & 7, i = bid >> 3;
  const unsigned kq = xc >> 1, mtp = (xc & 1) * 8 + (i >> 2), nt128 = i & 3;
  const unsigned mt128 = 2 * xc + (i >> 4), idx = i & 15;
  const unsigned mt64 = 2 * mt128 + (idx & 1), nt64 = idx >> 1;
  unsigned* cnt = (unsigned*)(ws + WS_CNT) + mt128 * 192;
  const float* mod = (const float*)(ws + WS_MOD);
  unsigned seen;
  asm volatile(
    "code_first_mark:\n\t"
    "s_mov_b32 s80, %6\n\t"
    "s_mov_b32 s83, %7\n\t"
    "s_and_b32 s70, %5, 7\n\t"
    "s_lshl_b32 s70, s70, 2\n\t"
    "s_lshr_b32 s71, %5, 3\n\t"
    "s_and_b32 s71, s71, 3\n\t"
    "s_add_u32 s81, s70, s71\n\t"
    "s_lshr_b32 s82, %5, 5\n\t"
    "v_mbcnt_lo_u32_b32 v240, -1, 0\n\t"
    "v_mbcnt_hi_u32_b32 v240, -1, v240\n\t"
    "v_lshlrev_b32 v226, 4, v240\n\t"
    "s_lshl_b32 s70, s81, 8\n\t"
    "s_lshl_b32 s71, s80, 17\n\t"
    "s_add_u32 s70, s70, s71\n\t"
    "s_sub_u32 s73, 4194304, s70\n\t"
    "s_mov_b64 s[44:45], %1\n\t"
    "s_add_u32 s44, s44, s70\n\t"
    "s_addc_u32 s45, s45, 0\n\t"
    "s_and_b32 s45, s45, 0xffff\n\t"
    "s_mov_b32 s46, 0\n\t"
    "s_mov_b32 s47, 0x20000\n\t"
    "s_mov_b32 s46, s73\n\t"
    "v_lshrrev_b32 v241, 4, v240\n\t"
    "v_lshlrev_b32 v242, 1, v241\n\t"
    "v_and_b32 v243, 15, v240\n\t"
    "v_xor_b32 v243, v243, v242\n\t"
    "v_lshlrev_b32 v242, 13, v241\n\t"
    "v_lshl_add_u32 v246, v243, 4, v242\n\t"
    "v_xor_b32 v247, 0x80, v246\n\t"
    "s_lshl_b32 s70, s80, 12\n\t"
    "s_add_u32 s70, s70, s83\n\t"
    "s_add_u32 s58, s70, 0\n\t"
    "s_add_u32 s62, s70, 65536\n\t"
    "s_add_u32 s59, s70, 16384\n\t"
    "s_add_u32 s63, s70, 81920\n\t"
    "s_add_u32 s60, s70, 32768\n\t"
    "s_add_u32 s64, s70, 98304\n\t"
    "s_add_u32 s61, s70, 49152\n\t"
    "s_add_u32 s65, s70, 114688\n\t"
    "s_add_u32 m0, s58, 0\n\t"
    "s_mov_b32 s74, 0\n\t"
    "buffer_load_dwordx4 v246, s[44:47], s74 offen lds\n\t"
    "s_add_u32 m0, s58, 1024\n\t"
    "s_mov_b32 s74, 32768\n\t"
    "buffer_load_dwordx4 v246, s[44:47], s74 offen lds\n\t"
    "s_add_u32 m0, s58, 2048\n\t"
    "s_mov_b32 s74, 65536\n\t"
    "buffer_load_dwordx4 v247, s[44:47], s74 offen lds\n\t"
    "s_add_u32 m0, s58, 3072\n\t"
    "s_mov_b32 s74, 98304\n\t"
    "buffer_load_dwordx4 v247, s[44:47], s74 offen lds\n\t"
    "s_lshl_b32 s70, s80, 10\n\t"
    "s_mov_b64 s[52:53], %13\n\t"
    "s_add_u32 s52, s52, s70\n\t"
    "s_addc_u32 s53, s53, 0\n\t"
    "s_and_b32 s53, s53, 0xffff\n\t"
    "s_mov_b32 s54, 1024\n\t"
    "s_mov_b32 s55, 0x20000\n\t"
    "s_add_u32 s70, s70, s83\n\t"
    "s_add_u32 m0, s70, 131072\n\t"
    "s_nop 0\n\t"
    "buffer_load_dwordx4 v226, s[52:55], 0 offen lds\n\t"
    "s_lshl_b32 s70, s82, 4\n\t"
    "s_lshl_b32 s71, s80, 2\n\t"
    "s_add_u32 s72, s70, s71\n\t"
    "s_lshl_b32 s70, s72, 14\n\t"
    "s_mov_b64 s[48:49], %0\n\t"
    "s_add_u32 s48, s48, s70\n\t"
    "s_addc_u32 s49, s49, 0\n\t"
    "s_and_b32 s49, s49, 0xffff\n\t"
    "s_mov_b32 s50, 65536\n\t"
    "s_mov_b32 s51, 0x20000\n\t"
    "s_mov_b32 s75, 0\n\t"
    "buffer_load_dwordx4 v[128:131], v226, s[48:51], s75 offen offset:0\n\t"
    "s_mov_b32 s75, 16384\n\t"
    "buffer_load_dwordx4 v[132:135], v226, s[48:51], s75 offen offset:0\n\t"
    "s_mov_b32 s75, 32768\n\t"
    "buffer_load_dwordx4 v[136:139], v226, s[48:51], s75 offen offset:0\n\t"
    "s_mov_b32 s75, 49152\n\t"
    "buffer_load_dwordx4 v[140:143], v226, s[48:51], s75 offen offset:0\n\t"
    "s_mov_b32 s75, 0\n\t"
    "buffer_load_dwordx4 v[144:147], v226, s[48:51], s75 offen offset:1024\n\t"
    "s_mov_b32 s75, 16384\n\t"
    "buffer_load_dwordx4 v[148:151], v226, s[48:51], s75 offen offset:1024\n\t"
    "s_mov_b32 s75, 32768\n\t"
    "buffer_load_dwordx4 v[152:155], v226, s[48:51], s75 offen offset:1024\n\t"
    "s_mov_b32 s75, 49152\n\t"
    "buffer_load_dwordx4 v[156:159], v226, s[48:51], s75 offen offset:1024\n\t"
    "s_add_u32 m0, s62, 0\n\t"
    "s_mov_b32 s74, 524288\n\t"
    "buffer_load_dwordx4 v246, s[44:47], s74 offen lds\n\t"
    "s_add_u32 m0, s62, 1024\n\t"
    "s_mov_b32 s74, 557056\n\t"
    "buffer_load_dwordx4 v246, s[44:47], s74 offen lds\n\t"
    "s_add_u32 m0, s62, 2048\n\t"
    "s_mov_b32 s74, 589824\n\t"
    "buffer_load_dwordx4 v247, s[44:47], s74 offen lds\n\t"
    "s_add_u32 m0, s62, 3072\n\t"
    "s_mov_b32 s74, 622592\n\t"
    "buffer_load_dwordx4 v247, s[44:47], s74 offen lds\n\t"
    "s_mov_b32 s75, 0\n\t"
    "buffer_load_dwordx4 v[160:163], v226, s[48:51], s75 offen offset:2048\n\t"
    "s_mov_b32 s75, 16384\n\t"
    "buffer_load_dwordx4 v[164:167], v226, s[48:51], s75 offen offset:2048\n\t"
    "s_mov_b32 s75, 32768\n\t"
    "buffer_load_dwordx4 v[168:171], v226, s[48:51], s75 offen offset:2048\n\t"
    "s_mov_b32 s75, 49152\n\t"
    "buffer_load_dwordx4 v[172:175], v226, s[48:51], s75 offen offset:2048\n\t"
    "s_add_u32 m0, s59, 0\n\t"
    "s_mov_b32 s74, 1048576\n\t"
    "buffer_load_dwordx4 v246, s[44:47], s74 offen lds\n\t"
    "s_add_u32 m0, s59, 1024\n\t"
    "s_mov_b32 s74, 1081344\n\t"
    "buffer_load_dwordx4 v246, s[44:47], s74 offen lds\n\t"
    "s_add_u32 m0, s59, 2048\n\t"
    "s_mov_b32 s74, 1114112\n\t"
    "buffer_load_dwordx4 v247, s[44:47], s74 offen lds\n\t"
    "s_add_u32 m0, s59, 3072\n\t"
    "s_mov_b32 s74, 1146880\n\t"
    "buffer_load_dwordx4 v247, s[44:47], s74 offen lds\n\t"
    "s_lshl_b32 s70, s81, 19\n\t"
    "s_mov_b64 s[36:37], %2\n\t"
    "s_add_u32 s36, s36, s70\n\t"
    "s_addc_u32 s37, s37, 0\n\t"
    "s_and_b32 s37, s37, 0xffff\n\t"
    "s_mov_b32 s38, 524288\n\t"
    "s_mov_b32 s39, 0x20000\n\t"
    "s_mov_b64 s[40:41], %3\n\t"
    "s_add_u32 s40, s40, s70\n\t"
    "s_addc_u32 s41, s41, 0\n\t"
    "s_and_b32 s41, s41, 0xffff\n\t"
    "s_mov_b32 s42, 524288\n\t"
    "s_mov_b32 s43, 0x20000\n\t"
    "s_lshl_b32 s70, s72, 17\n\t"
    "s_lshl_b32 s71, s81, 12\n\t"
    "s_add_u32 s70, s70, s71\n\t"
    "s_mov_b64 s[52:53], %4\n\t"
    "s_add_u32 s52, s52, s70\n\t"
    "s_addc_u32 s53, s53, 0\n\t"
    "s_and_b32 s53, s53, 0xffff\n\t"
    "s_mov_b32 s54, 397312\n\t"
    "s_mov_b32 s55, 0x20000\n\t"
    "s_mov_b32 s68, 0x55800000\n\t"
    "s_mov_b32 s69, 0x42a00000\n\t"
    "s_and_b32 s70, %5, 7\n\t"
    "s_lshr_b32 s70, s70, 1\n\t"
    "s_lshl_b32 s70, s70, 20\n\t"
    "s_lshr_b32 s71, %5, 3\n\t"
    "s_min_u32 s71, s71, 30\n\t"
    "s_lshl_b32 s71, s71, 15\n\t"
    "s_add_u32 s70, s70, s71\n\t"
    "s_mov_b64 s[76:77], %9\n\t"
    "s_add_u32 s76, s76, s70\n\t"
    "s_addc_u32 s77, s77, 0\n\t"
    "s_getpc_b64 s[78:79]\n\t"
    "L_pc_%=:\n\t"
    "s_add_u32 s78, s78, (code_last_mark-L_pc_%=-32768)&4294967295\n\t"
    "s_addc_u32 s79, s79, (code_last_mark-L_pc_%=-32768)>>32\n\t"
    "s_and_b32 s78, s78, 0xffffff00\n\t"
    "s_and_b32 s77, s77, 0xffff\n\t"
    "s_and_b32 s79, s79, 0xffff\n\t"
    "s_mov_b32 s56, 0\n\t"
    "s_mov_b32 s57, 0\n\t"
    "v_and_b32 v241, 15, v240\n\t"
    "v_lshrrev_b32 v242, 4, v240\n\t"
    "v_xor_b32 v243, v241, v242\n\t"
    "v_lshlrev_b32 v241, 8, v241\n\t"
    "v_add_u32 v241, s83, v241\n\t"
    "v_lshrrev_b32 v241, 2, v240\n\t"
    "v_and_b32 v243, 7, v241\n\t"
    "v_lshlrev_b32 v243, 1, v243\n\t"
    "v_lshlrev_b32 v241, 8, v241\n\t"
    "v_add_u32 v241, s83, v241\n\t"
    "v_lshl_add_u32 v248, v242, 10, v241\n\t"
    "v_and_b32 v244, 3, v240\n\t"
    "v_xor_b32 v244, v244, v243\n\t"
    "v_xor_b32 v245, 0, v244\n\t"
    "v_lshl_add_u32 v192, v245, 4, v248\n\t"
    "v_add_u32 v196, 0x10000, v192\n\t"
    "v_xor_b32 v245, 4, v244\n\t"
    "v_lshl_add_u32 v193, v245, 4, v248\n\t"
    "v_add_u32 v197, 0x10000, v193\n\t"
    "v_xor_b32 v245, 8, v244\n\t"
    "v_lshl_add_u32 v194, v245, 4, v248\n\t"
    "v_add_u32 v198, 0x10000, v194\n\t"
    "v_xor_b32 v245, 12, v244\n\t"
    "v_lshl_add_u32 v195, v245, 4, v248\n\t"
    "v_add_u32 v199, 0x10000, v195\n\t"
    "v_lshrrev_b32 v241, 4, v240\n\t"
    "v_and_b32 v243, 15, v240\n\t"
    "v_lshlrev_b32 v245, 1, v241\n\t"
    "v_xor_b32 v245, v245, v243\n\t"
    "v_xor_b32 v243, v243, v241\n\t"
    "s_lshl_b32 s70, s80, 12\n\t"
    "v_lshlrev_b32 v241, 8, v241\n\t"
    "v_add_u32 v241, s70, v241\n\t"
    "v_xor_b32 v244, 0, v243\n\t"
    "v_lshl_add_u32 v220, v244, 4, v241\n\t"
    "v_xor_b32 v244, 4, v243\n\t"
    "v_lshl_add_u32 v221, v244, 4, v241\n\t"
    "v_xor_b32 v244, 8, v243\n\t"
    "v_lshl_add_u32 v222, v244, 4, v241\n\t"
    "v_xor_b32 v244, 12, v243\n\t"
    "v_lshl_add_u32 v223, v244, 4, v241\n\t"
    "v_lshl_add_u32 v224, v245, 4, v241\n\t"
    "v_xor_b32 v225, 0x80, v224\n\t"
    "v_lshlrev_b32 v200, 6, v242\n\t"
    "v_add_u32 v200, s83, v200\n\t"
    "v_add_u32 v200, 0x20000, v200\n\t"
    "v_mov_b32 v252, v246\n\t"
    "v_mov_b32 v253, v247\n\t"
    "s_waitcnt vmcnt(20)\n\t"
    "s_barrier\n\t"
    "ds_read_b128 v[228:231], v200 offset:0\n\t"
    "ds_read_b128 v[232:235], v200 offset:16\n\t"
    "ds_read_b128 v[236:239], v200 offset:32\n\t"
    "ds_read_b128 v[240:243], v200 offset:48\n\t"
    "ds_read_b64_tr_b16 a[64:65], v192 offset:0\n\t"
    "ds_read_b64_tr_b16 a[66:67], v192 offset:1024\n\t"
    "ds_read_b64_tr_b16 a[68:69], v192 offset:8\n\t"
    "ds_read_b64_tr_b16 a[70:71], v192 offset:1032\n\t"
    "ds_read_b64_tr_b16 a[72:73], v193 offset:0\n\t"
    "ds_read_b64_tr_b16 a[74:75], v193 offset:1024\n\t"
    "ds_read_b64_tr_b16 a[76:77], v193 offset:8\n\t"
    "ds_read_b64_tr_b16 a[78:79], v193 offset:1032\n\t"
    "ds_read_b64_tr_b16 a[80:81], v194 offset:0\n\t"
    "ds_read_b64_tr_b16 a[82:83], v194 offset:1024\n\t"
    "ds_read_b64_tr_b16 a[84:85], v194 offset:8\n\t"
    "ds_read_b64_tr_b16 a[86:87], v194 offset:1032\n\t"
    "s_waitcnt lgkmcnt(12)\n\t"
    "s_waitcnt vmcnt(19)\n\t"
    "v_fma_mix_f32 v244, v128, v228, v236 op_sel:[0,0,0] op_sel_hi:[1,0,0]\n\t"
    "v_fma_mix_f32 v245, v128, v229, v237 op_sel:[1,0,0] op_sel_hi:[1,0,0]\n\t"
    "v_fma_mix_f32 v246, v129, v230, v238 op_sel:[0,0,0] op_sel_hi:[1,0,0]\n\t"
    "v_fma_mix_f32 v247, v129, v231, v239 op_sel:[1,0,0] op_sel_hi:[1,0,0]\n\t"
    "v_fma_mix_f32 v248, v130, v232, v240 op_sel:[0,0,0] op_sel_hi:[1,0,0]\n\t"
    "v_fma_mix_f32 v249, v130, v233, v241 op_sel:[1,0,0] op_sel_hi:[1,0,0]\n\t"
    "v_fma_mix_f32 v250, v131, v234, v242 op_sel:[0,0,0] op_sel_hi:[1,0,0]\n\t"
    "v_fma_mix_f32 v251, v131, v235, v243 op_sel:[1,0,0] op_sel_hi:[1,0,0]\n\t"
    "v_cvt_pk_bf16_f32 v128, v244, v245\n\t"
    "v_cvt_pk_bf16_f32 v129, v246, v247\n\t"
    "v_cvt_pk_bf16_f32 v130, v248, v249\n\t"
    "v_cvt_pk_bf16_f32 v131, v250, v251\n\t"
    "s_waitcnt vmcnt(18)\n\t"
    "v_fma_mix_f32 v244, v132, v228, v236 op_sel:[0,0,0] op_sel_hi:[1,0,0]\n\t"
    "v_fma_mix_f32 v245, v132, v229, v237 op_sel:[1,0,0] op_sel_hi:[1,0,0]\n\t"
    "v_fma_mix_f32 v246, v133, v230, v238 op_sel:[0,0,0] op_sel_hi:[1,0,0]\n\t"
    "v_fma_mix_f32 v247, v133, v231, v239 op_sel:[1,0,0] op_sel_hi:[1,0,0]\n\t"
    "v_fma_mix_f32 v248, v134, v232, v240 op_sel:[0,0,0] op_sel_hi:[1,0,0]\n\t"
    "v_fma_mix_f32 v249, v134, v233, v241 op_sel:[1,0,0] op_sel_hi:[1,0,0]\n\t"
    "v_fma_mix_f32 v250, v135, v234, v242 op_sel:[0,0,0] op_sel_hi:[1,0,0]\n\t"
    "v_fma_mix_f32 v251, v135, v235, v243 op_sel:[1,0,0] op_sel_hi:[1,0,0]\n\t"
    "v_cvt_pk_bf16_f32 v132, v244, v245\n\t"
    "v_cvt_pk_bf16_f32 v133, v246, v247\n\t"
    "v_cvt_pk_bf16_f32 v134, v248, v249\n\t"
    "v_cvt_pk_bf16_f32 v135, v250, v251\n\t"
    "s_waitcnt vmcnt(17)\n\t"
    "v_fma_mix_f32 v244, v136, v228, v236 op_sel:[0,0,0] op_sel_hi:[1,0,0]\n\t"
    "v_fma_mix_f32 v245, v136, v229, v237 op_sel:[1,0,0] op_sel_hi:[1,0,0]\n\t"
    "v_fma_mix_f32 v246, v137, v230, v238 op_sel:[0,0,0] op_sel_hi:[1,0,0]\n\t"
    "v_fma_mix_f32 v247, v137, v231, v239 op_sel:[1,0,0] op_sel_hi:[1,0,0]\n\t"
    "v_fma_mix_f32 v248, v138, v232, v240 op_sel:[0,0,0] op_sel_hi:[1,0,0]\n\t"
    "v_fma_mix_f32 v249, v138, v233, v241 op_sel:[1,0,0] op_sel_hi:[1,0,0]\n\t"
    "v_fma_mix_f32 v250, v139, v234, v242 op_sel:[0,0,0] op_sel_hi:[1,0,0]\n\t"
    "v_fma_mix_f32 v251, v139, v235, v243 op_sel:[1,0,0] op_sel_hi:[1,0,0]\n\t"
    "v_cvt_pk_bf16_f32 v136, v244, v245\n\t"
    "v_cvt_pk_bf16_f32 v137, v246, v247\n\t"
    "v_cvt_pk_bf16_f32 v138, v248, v249\n\t"
    "v_cvt_pk_bf16_f32 v139, v250, v251\n\t"
    "s_waitcnt vmcnt(16)\n\t"
    "v_fma_mix_f32 v244, v140, v228, v236 op_sel:[0,0,0] op_sel_hi:[1,0,0]\n\t"
    "v_fma_mix_f32 v245, v140, v229, v237 op_sel:[1,0,0] op_sel_hi:[1,0,0]\n\t"
    "v_fma_mix_f32 v246, v141, v230, v238 op_sel:[0,0,0] op_sel_hi:[1,0,0]\n\t"
    "v_fma_mix_f32 v247, v141, v231, v239 op_sel:[1,0,0] op_sel_hi:[1,0,0]\n\t"
    "v_fma_mix_f32 v248, v142, v232, v240 op_sel:[0,0,0] op_sel_hi:[1,0,0]\n\t"
    "v_fma_mix_f32 v249, v142, v233, v241 op_sel:[1,0,0] op_sel_hi:[1,0,0]\n\t"
    "v_fma_mix_f32 v250, v143, v234, v242 op_sel:[0,0,0] op_sel_hi:[1,0,0]\n\t"
    "v_fma_mix_f32 v251, v143, v235, v243 op_sel:[1,0,0] op_sel_hi:[1,0,0]\n\t"
    "v_cvt_pk_bf16_f32 v140, v244, v245\n\t"
    "v_cvt_pk_bf16_f32 v141, v246, v247\n\t"
    "v_cvt_pk_bf16_f32 v142, v248, v249\n\t"
    "v_cvt_pk_bf16_f32 v143, v250, v251\n\t"
    "s_waitcnt lgkmcnt(10)\n\t"
    "v_mfma_f32_16x16x32_bf16 v[0:3], a[64:67], v[128:131], 0\n\t"
    "ds_read_b64_tr_b16 a[88:89], v195 offset:0\n\t"
    "ds_read_b128 v[228:231], v200 offset:256\n\t"
    "ds_read_b128 v[232:235], v200 offset:272\n\t"
    "v_mfma_f32_16x16x32_bf16 v[4:7], a[64:67], v[132:135], 0\n\t"
    "ds_read_b64_tr_b16 a[90:91], v195 offset:1024\n\t"
    "ds_read_b128 v[236:239], v200 offset:288\n\t"
    "ds_read_b128 v[240:243], v200 offset:304\n\t"
    "s_mov_b32 s75, 0\n\t"
    "v_mfma_f32_16x16x32_bf16 v[8:11], a[64:67], v[136:139], 0\n\t"
    "buffer_load_dwordx4 v[176:179], v226, s[48:51], s75 offen offset:3072\n\t"
    "v_mfma_f32_16x16x32_bf16 v[12:15], a[64:67], v[140:143], 0\n\t"
    "s_waitcnt lgkmcnt(14)\n\t"
    "v_mfma_f32_16x16x32_bf16 v[16:19], a[68:71], v[128:131], 0\n\t"
    "ds_read_b64_tr_b16 a[92:93], v195 offset:8\n\t"
    "v_mfma_f32_16x16x32_bf16 v[20:23], a[68:71], v[132:135], 0\n\t"
    "ds_read_b64_tr_b16 a[94:95], v195 offset:1032\n\t"
    "s_waitcnt lgkmcnt(2)\n\t"
    "s_waitcnt vmcnt(16)\n\t"
    "v_fma_mix_f32 v244, v144, v228, v236 op_sel:[0,0,0] op_sel_hi:[1,0,0]\n\t"
    "v_fma_mix_f32 v245, v144, v229, v237 op_sel:[1,0,0] op_sel_hi:[1,0,0]\n\t"
    "s_mov_b32 s75, 16384\n\t"
    "v_mfma_f32_16x16x32_bf16 v[24:27], a[68:71], v[136:139], 0\n\t"
    "buffer_load_dwordx4 v[180:183], v226, s[48:51], s75 offen offset:3072\n\t"
    "v_fma_mix_f32 v246, v145, v230, v238 op_sel:[0,0,0] op_sel_hi:[1,0,0]\n\t"
    "v_fma_mix_f32 v247, v145, v231, v239 op_sel:[1,0,0] op_sel_hi:[1,0,0]\n\t"
    "v_mfma_f32_16x16x32_bf16 v[28:31], a[68:71], v[140:143], 0\n\t"
    "v_fma_mix_f32 v248, v146, v232, v240 op_sel:[0,0,0] op_sel_hi:[1,0,0]\n\t"
    "v_fma_mix_f32 v249, v146, v233, v241 op_sel:[1,0,0] op_sel_hi:[1,0,0]\n\t"
    "v_mfma_f32_16x16x32_bf16 v[32:35], a[72:75], v[128:131], 0\n\t"
    "ds_read_b64_tr_b16 a[96:97], v192 offset:8192\n\t"
    "v_fma_mix_f32 v250, v147, v234, v242 op_sel:[0,0,0] op_sel_hi:[1,0,0]\n\t"
    "v_fma_mix_f32 v251, v147, v235, v243 op_sel:[1,0,0] op_sel_hi:[1,0,0]\n\t"
    "v_mfma_f32_16x16x32_bf16 v[36:39], a[72:75], v[132:135], 0\n\t"
    "ds_read_b64_tr_b16 a[98:99], v192 offset:9216\n\t"
    "v_cvt_pk_bf16_f32 v144, v244, v245\n\t"
    "v_cvt_pk_bf16_f32 v145, v246, v247\n\t"
    "s_mov_b32 s75, 32768\n\t"
    "v_mfma_f32_16x16x32_bf16 v[40:43], a[72:75], v[136:139], 0\n\t"
    "buffer_load_dwordx4 v[184:187], v226, s[48:51], s75 offen offset:3072\n\t"
    "v_cvt_pk_bf16_f32 v146, v248, v249\n\t"
    "v_cvt_pk_bf16_f32 v147, v250, v251\n\t"
    "v_mfma_f32_16x16x32_bf16 v[44:47], a[72:75], v[140:143], 0\n\t"
    "s_waitcnt vmcnt(17)\n\t"
    "v_fma_mix_f32 v244, v148, v228, v236 op_sel:[0,0,0] op_sel_hi:[1,0,0]\n\t"
    "v_fma_mix_f32 v245, v148, v229, v237 op_sel:[1,0,0] op_sel_hi:[1,0,0]\n\t"
    "v_mfma_f32_16x16x32_bf16 v[48:51], a[76:79], v[128:131], 0\n\t"
    "ds_read_b64_tr_b16 a[100:101], v192 offset:8200\n\t"
    "v_fma_mix_f32 v246, v149, v230, v238 op_sel:[0,0,0] op_sel_hi:[1,0,0]\n\t"
    "v_fma_mix_f32 v247, v149, v231, v239 op_sel:[1,0,0] op_sel_hi:[1,0,0]\n\t"
    "v_mfma_f32_16x16x32_bf16 v[52:55], a[76:79], v[132:135], 0\n\t"
    "ds_read_b64_tr_b16 a[102:103], v192 offset:9224\n\t"
    "v_fma_mix_f32 v248, v150, v232, v240 op_sel:[0,0,0] op_sel_hi:[1,0,0]\n\t"
    "v_fma_mix_f32 v249, v150, v233, v241 op_sel:[1,0,0] op_sel_hi:[1,0,0]\n\t"
    "s_mov_b32 s75, 49152\n\t"
    "v_mfma_f32_16x16x32_bf16 v[56:59], a[76:79], v[136:139], 0\n\t"
    "buffer_load_dwordx4 v[188:191], v226, s[48:51], s75 offen offset:3072\n\t"
    "v_fma_mix_f32 v250, v151, v234, v242 op_sel:[0,0,0] op_sel_hi:[1,0,0]\n\t"
    "v_fma_mix_f32 v251, v151, v235, v243 op_sel:[1,0,0] op_sel_hi:[1,0,0]\n\t"
    "v_mfma_f32_16x16x32_bf16 v[60:63], a[76:79], v[140:143], 0\n\t"
    "v_cvt_pk_bf16_f32 v148, v244, v245\n\t"
    "v_cvt_pk_bf16_f32 v149, v246, v247\n\t"
    "v_mfma_f32_16x16x32_bf16 v[64:67], a[80:83], v[128:131], 0\n\t"
    "ds_read_b64_tr_b16 a[104:105], v193 offset:8192\n\t"
    "v_cvt_pk_bf16_f32 v150, v248, v249\n\t"
    "v_cvt_pk_bf16_f32 v151, v250, v251\n\t"
    "v_mfma_f32_16x16x32_bf16 v[68:71], a[80:83], v[132:135], 0\n\t"
    "ds_read_b64_tr_b16 a[106:107], v193 offset:9216\n\t"
    "s_waitcnt vmcnt(17)\n\t"
    "v_fma_mix_f32 v244, v152, v228, v236 op_sel:[0,0,0] op_sel_hi:[1,0,0]\n\t"
    "v_fma_mix_f32 v245, v152, v229, v237 op_sel:[1,0,0] op_sel_hi:[1,0,0]\n\t"
    "s_add_u32 m0, s63, 0\n\t"
    "s_mov_b32 s74, 1572864\n\t"
    "v_mfma_f32_16x16x32_bf16 v[72:75], a[80:83], v[136:139], 0\n\t"
    "buffer_load_dwordx4 v252, s[44:47], s74 offen lds\n\t"
    "v_fma_mix_f32 v246, v153, v230, v238 op_sel:[0,0,0] op_sel_hi:[1,0,0]\n\t"
    "v_fma_mix_f32 v247, v153, v231, v239 op_sel:[1,0,0] op_sel_hi:[1,0,0]\n\t"
    "v_mfma_f32_16x16x32_bf16 v[76:79], a[80:83], v[140:143], 0\n\t"
    "v_fma_mix_f32 v248, v154, v232, v240 op_sel:[0,0,0] op_sel_hi:[1,0,0]\n\t"
    "v_fma_mix_f32 v249, v154, v233, v241 op_sel:[1,0,0] op_sel_hi:[1,0,0]\n\t"
    "v_mfma_f32_16x16x32_bf16 v[80:83], a[84:87], v[128:131], 0\n\t"
    "ds_read_b64_tr_b16 a[108:109], v193 offset:8200\n\t"
    "v_fma_mix_f32 v250, v155, v234, v242 op_sel:[0,0,0] op_sel_hi:[1,0,0]\n\t"
    "v_fma_mix_f32 v251, v155, v235, v243 op_sel:[1,0,0] op_sel_hi:[1,0,0]\n\t"
    "v_mfma_f32_16x16x32_bf16 v[84:87], a[84:87], v[132:135], 0\n\t"
    "ds_read_b64_tr_b16 a[110:111], v193 offset:9224\n\t"
    "v_cvt_pk_bf16_f32 v152, v244, v245\n\t"
    "v_cvt_pk_bf16_f32 v153, v246, v247\n\t"
    "s_add_u32 m0, s63, 1024\n\t"
    "s_mov_b32 s74, 1605632\n\t"
    "v_mfma_f32_16x16x32_bf16 v[88:91], a[84:87], v[136:139], 0\n\t"
    "buffer_load_dwordx4 v252, s[44:47], s74 offen lds\n\t"
    "v_cvt_pk_bf16_f32 v154, v248, v249\n\t"
    "v_cvt_pk_bf16_f32 v155, v250, v251\n\t"
    "v_mfma_f32_16x16x32_bf16 v[92:95], a[84:87], v[140:143], 0\n\t"
    "s_waitcnt vmcnt(18)\n\t"
    "v_fma_mix_f32 v244, v156, v228, v236 op_sel:[0,0,0] op_sel_hi:[1,0,0]\n\t"
    "v_fma_mix_f32 v245, v156, v229, v237 op_sel:[1,0,0] op_sel_hi:[1,0,0]\n\t"
    "s_waitcnt vmcnt(14)\n\t"
    "s_barrier\n\t"
    "v_mfma_f32_16x16x32_bf16 v[96:99], a[88:91], v[128:131], 0\n\t"
    "ds_read_b64_tr_b16 a[112:113], v194 offset:8192\n\t"
    "v_fma_mix_f32 v246, v157, v230, v238 op_sel:[0,0,0] op_sel_hi:[1,0,0]\n\t"
    "v_fma_mix_f32 v247, v157, v231, v239 op_sel:[1,0,0] op_sel_hi:[1,0,0]\n\t"
    "v_mfma_f32_16x16x32_bf16 v[100:103], a[88:91], v[132:135], 0\n\t"
    "ds_read_b64_tr_b16 a[114:115], v194 offset:9216\n\t"
    "v_fma_mix_f32 v248, v158, v232, v240 op_sel:[0,0,0] op_sel_hi:[1,0,0]\n\t"
    "v_fma_mix_f32 v249, v158, v233, v241 op_sel:[1,0,0] op_sel_hi:[1,0,0]\n\t"
    "s_add_u32 m0, s63, 2048\n\t"
    "s_mov_b32 s74, 1638400\n\t"
    "v_mfma_f32_16x16x32_bf16 v[104:107], a[88:91], v[136:139], 0\n\t"
    "buffer_load_dwordx4 v253, s[44:47], s74 offen lds\n\t"
    "v_fma_mix_f32 v250, v159, v234, v242 op_sel:[0,0,0] op_sel_hi:[1,0,0]\n\t"
    "v_fma_mix_f32 v251, v159, v235, v243 op_sel:[1,0,0] op_sel_hi:[1,0,0]\n\t"
    "v_mfma_f32_16x16x32_bf16 v[108:111], a[88:91], v[140:143], 0\n\t"
    "v_cvt_pk_bf16_f32 v156, v244, v245\n\t"
    "v_cvt_pk_bf16_f32 v157, v246, v247\n\t"
    "s_waitcnt lgkmcnt(10)\n\t"
    "v_mfma_f32_16x16x32_bf16 v[112:115], a[92:95], v[128:131], 0\n\t"
    "ds_read_b64_tr_b16 a[116:117], v194 offset:8200\n\t"
    "v_cvt_pk_bf16_f32 v158, v248, v249\n\t"
    "v_cvt_pk_bf16_f32 v159, v250, v251\n\t"
    "v_mfma_f32_16x16x32_bf16 v[116:119], a[92:95], v[132:135], 0\n\t"
    "ds_read_b64_tr_b16 a[118:119], v194 offset:9224\n\t"
    "s_add_u32 m0, s63, 3072\n\t"
    "s_mov_b32 s74, 1671168\n\t"
    "v_mfma_f32_16x16x32_bf16 v[120:123], a[92:95], v[136:139], 0\n\t"
    "buffer_load_dwordx4 v253, s[44:47], s74 offen lds\n\t"
    "v_mfma_f32_16x16x32_bf16 v[124:127], a[92:95], v[140:143], 0\n\t"
    "s_waitcnt lgkmcnt(10)\n\t"
    "v_mfma_f32_16x16x32_bf16 v[0:3], a[96:99], v[144:147], v[0:3]\n\t"
    "ds_read_b64_tr_b16 a[120:121], v195 offset:8192\n\t"
    "ds_read_b128 v[228:231], v200 offset:512\n\t"
    "ds_read_b128 v[232:235], v200 offset:528\n\t"
    "v_mfma_f32_16x16x32_bf16 v[4:7], a[96:99], v[148:151], v[4:7]\n\t"
    "ds_read_b64_tr_b16 a[122:123], v195 offset:9216\n\t"
    "ds_read_b128 v[236:239], v200 offset:544\n\t"
    "ds_read_b128 v[240:243], v200 offset:560\n\t"
    "s_mov_b32 s75, 4096\n\t"
    "v_mfma_f32_16x16x32_bf16 v[8:11], a[96:99], v[152:155], v[8:11]\n\t"
    "buffer_load_dwordx4 v[128:131], v226, s[48:51], s75 offen offset:0\n\t"
    "v_mfma_f32_16x16x32_bf16 v[12:15], a[96:99], v[156:159], v[12:15]\n\t"
    "s_waitcnt lgkmcnt(14)\n\t"
    "v_mfma_f32_16x16x32_bf16 v[16:19], a[100:103], v[144:147], v[16:19]\n\t"
    "ds_read_b64_tr_b16 a[124:125], v195 offset:8200\n\t"
    "v_mfma_f32_16x16x32_bf16 v[20:23], a[100:103], v[148:151], v[20:23]\n\t"
    "ds_read_b64_tr_b16 a[126:127], v195 offset:9224\n\t"
    "s_waitcnt lgkmcnt(2)\n\t"
    "s_waitcnt vmcnt(16)\n\t"
    "v_fma_mix_f32 v244, v160, v228, v236 op_sel:[0,0,0] op_sel_hi:[1,0,0]\n\t"
    "v_fma_mix_f32 v245, v160, v229, v237 op_sel:[1,0,0] op_sel_hi:[1,0,0]\n\t"
    "s_mov_b32 s75, 20480\n\t"
    "v_mfma_f32_16x16x32_bf16 v[24:27], a[100:103], v[152:155], v[24:27]\n\t"
    "buffer_load_dwordx4 v[132:135], v226, s[48:51], s75 offen offset:0\n\t"
    "v_fma_mix_f32 v246, v161, v230, v238 op_sel:[0,0,0] op_sel_hi:[1,0,0]\n\t"
    "v_fma_mix_f32 v247, v161, v231, v239 op_sel:[1,0,0] op_sel_hi:[1,0,0]\n\t"
    "v_mfma_f32_16x16x32_bf16 v[28:31], a[100:103], v[156:159], v[28:31]\n\t"
    "v_fma_mix_f32 v248, v162, v232, v240 op_sel:[0,0,0] op_sel_hi:[1,0,0]\n\t"
    "v_fma_mix_f32 v249, v162, v233, v241 op_sel:[1,0,0] op_sel_hi:[1,0,0]\n\t"
    "v_mfma_f32_16x16x32_bf16 v[32:35], a[104:107], v[144:147], v[32:35]\n\t"
    "ds_read_b64_tr_b16 a[128:129], v196 offset:0\n\t"
    "v_fma_mix_f32 v250, v163, v234, v242 op_sel:[0,0,0] op_sel_hi:[1,0,0]\n\t"
    "v_fma_mix_f32 v251, v163, v235, v243 op_sel:[1,0,0] op_sel_hi:[1,0,0]\n\t"
    "v_mfma_f32_16x16x32_bf16 v[36:39], a[104:107], v[148:151], v[36:39]\n\t"
    "ds_read_b64_tr_b16 a[130:131], v196 offset:1024\n\t"
    "v_cvt_pk_bf16_f32 v160, v244, v245\n\t"
    "v_cvt_pk_bf16_f32 v161, v246, v247\n\t"
    "s_mov_b32 s75, 36864\n\t"
    "v_mfma_f32_16x16x32_bf16 v[40:43], a[104:107], v[152:155], v[40:43]\n\t"
    "buffer_load_dwordx4 v[136:139], v226, s[48:51], s75 offen offset:0\n\t"
    "v_cvt_pk_bf16_f32 v162, v248, v249\n\t"
    "v_cvt_pk_bf16_f32 v163, v250, v251\n\t"
    "v_mfma_f32_16x16x32_bf16 v[44:47], a[104:107], v[156:159], v[44:47]\n\t"
    "s_waitcnt vmcnt(17)\n\t"
    "v_fma_mix_f32 v244, v164, v228, v236 op_sel:[0,0,0] op_sel_hi:[1,0,0]\n\t"
    "v_fma_mix_f32 v245, v164, v229, v237 op_sel:[1,0,0] op_sel_hi:[1,0,0]\n\t"
    "v_mfma_f32_16x16x32_bf16 v[48:51], a[108:111], v[144:147], v[48:51]\n\t"
    "ds_read_b64_tr_b16 a[132:133], v196 offset:8\n\t"
    "v_fma_mix_f32 v246, v165, v230, v238 op_sel:[0,0,0] op_sel_hi:[1,0,0]\n\t"
    "v_fma_mix_f32 v247, v165, v231, v239 op_sel:[1,0,0] op_sel_hi:[1,0,0]\n\t"
    "v_mfma_f32_16x16x32_bf16 v[52:55], a[108:111], v[148:151], v[52:55]\n\t"
    "ds_read_b64_tr_b16 a[134:135], v196 offset:1032\n\t"
    "v_fma_mix_f32 v248, v166, v232, v240 op_sel:[0,0,0] op_sel_hi:[1,0,0]\n\t"
    "v_fma_mix_f32 v249, v166, v233, v241 op_sel:[1,0,0] op_sel_hi:[1,0,0]\n\t"
    "s_mov_b32 s75, 53248\n\t"
    "v_mfma_f32_16x16x32_bf16 v[56:59], a[108:111], v[152:155], v[56:59]\n\t"
    "buffer_load_dwordx4 v[140:143], v226, s[48:51], s75 offen offset:0\n\t"
    "v_fma_mix_f32 v250, v167, v234, v242 op_sel:[0,0,0] op_sel_hi:[1,0,0]\n\t"
    "v_fma_mix_f32 v251, v167, v235, v243 op_sel:[1,0,0] op_sel_hi:[1,0,0]\n\t"
    "v_mfma_f32_16x16x32_bf16 v[60:63], a[108:111], v[156:159], v[60:63]\n\t"
    "v_cvt_pk_bf16_f32 v164, v244, v245\n\t"
    "v_cvt_pk_bf16_f32 v165, v246, v247\n\t"
    "v_mfma_f32_16x16x32_bf16 v[64:67], a[112:115], v[144:147], v[64:67]\n\t"
    "ds_read_b64_tr_b16 a[136:137], v197 offset:0\n\t"
    "v_cvt_pk_bf16_f32 v166, v248, v249\n\t"
    "v_cvt_pk_bf16_f32 v167, v250, v251\n\t"
    "v_mfma_f32_16x16x32_bf16 v[68:71], a[112:115], v[148:151], v[68:71]\n\t"
    "ds_read_b64_tr_b16 a[138:139], v197 offset:1024\n\t"
    "s_waitcnt vmcnt(17)\n\t"
    "v_fma_mix_f32 v244, v168, v228, v236 op_sel:[0,0,0] op_sel_hi:[1,0,0]\n\t"
    "v_fma_mix_f32 v245, v168, v229, v237 op_sel:[1,0,0] op_sel_hi:[1,0,0]\n\t"
    "s_add_u32 m0, s60, 0\n\t"
    "s_mov_b32 s74, 2097152\n\t"
    "v_mfma_f32_16x16x32_bf16 v[72:75], a[112:115], v[152:155], v[72:75]\n\t"
    "buffer_load_dwordx4 v252, s[44:47], s74 offen lds\n\t"
    "v_fma_mix_f32 v246, v169, v230, v238 op_sel:[0,0,0] op_sel_hi:[1,0,0]\n\t"
    "v_fma_mix_f32 v247, v169, v231, v239 op_sel:[1,0,0] op_sel_hi:[1,0,0]\n\t"
    "v_mfma_f32_16x16x32_bf16 v[76:79], a[112:115], v[156:159], v[76:79]\n\t"
    "v_fma_mix_f32 v248, v170, v232, v240 op_sel:[0,0,0] op_sel_hi:[1,0,0]\n\t"
    "v_fma_mix_f32 v249, v170, v233, v241 op_sel:[1,0,0] op_sel_hi:[1,0,0]\n\t"
    "v_mfma_f32_16x16x32_bf16 v[80:83], a[116:119], v[144:147], v[80:83]\n\t"
    "ds_read_b64_tr_b16 a[140:141], v197 offset:8\n\t"
    "v_fma_mix_f32 v250, v171, v234, v242 op_sel:[0,0,0] op_sel_hi:[1,0,0]\n\t"
    "v_fma_mix_f32 v251, v171, v235, v243 op_sel:[1,0,0] op_sel_hi:[1,0,0]\n\t"
    "v_mfma_f32_16x16x32_bf16 v[84:87], a[116:119], v[148:151], v[84:87]\n\t"
    "ds_read_b64_tr_b16 a[142:143], v197 offset:1032\n\t"
    "v_cvt_pk_bf16_f32 v168, v244, v245\n\t"
    "v_cvt_pk_bf16_f32 v169, v246, v247\n\t"
    "s_add_u32 m0, s60, 1024\n\t"
    "s_mov_b32 s74, 2129920\n\t"
    "v_mfma_f32_16x16x32_bf16 v[88:91], a[116:119], v[152:155], v[88:91]\n\t"
    "buffer_load_dwordx4 v252, s[44:47], s74 offen lds\n\t"
    "v_cvt_pk_bf16_f32 v170, v248, v249\n\t"
    "v_cvt_pk_bf16_f32 v171, v250, v251\n\t"
    "v_mfma_f32_16x16x32_bf16 v[92:95], a[116:119], v[156:159], v[92:95]\n\t"
    "s_waitcnt vmcnt(18)\n\t"
    "v_fma_mix_f32 v244, v172, v228, v236 op_sel:[0,0,0] op_sel_hi:[1,0,0]\n\t"
    "v_fma_mix_f32 v245, v172, v229, v237 op_sel:[1,0,0] op_sel_hi:[1,0,0]\n\t"
    "v_mfma_f32_16x16x32_bf16 v[96:99], a[120:123], v[144:147], v[96:99]\n\t"
    "ds_read_b64_tr_b16 a[144:145], v198 offset:0\n\t"
    "v_fma_mix_f32 v246, v173, v230, v238 op_sel:[0,0,0] op_sel_hi:[1,0,0]\n\t"
    "v_fma_mix_f32 v247, v173, v231, v239 op_sel:[1,0,0] op_sel_hi:[1,0,0]\n\t"
    "v_mfma_f32_16x16x32_bf16 v[100:103], a[120:123], v[148:151], v[100:103]\n\t"
    "ds_read_b64_tr_b16 a[146:147], v198 offset:1024\n\t"
    "v_fma_mix_f32 v248, v174, v232, v240 op_sel:[0,0,0] op_sel_hi:[1,0,0]\n\t"
    "v_fma_mix_f32 v249, v174, v233, v241 op_sel:[1,0,0] op_sel_hi:[1,0,0]\n\t"
    "s_add_u32 m0, s60, 2048\n\t"
    "s_mov_b32 s74, 2162688\n\t"
    "v_mfma_f32_16x16x32_bf16 v[104:107], a[120:123], v[152:155], v[104:107]\n\t"
    "buffer_load_dwordx4 v253, s[44:47], s74 offen lds\n\t"
    "v_fma_mix_f32 v250, v175, v234, v242 op_sel:[0,0,0] op_sel_hi:[1,0,0]\n\t"
    "v_fma_mix_f32 v251, v175, v235, v243 op_sel:[1,0,0] op_sel_hi:[1,0,0]\n\t"
    "v_mfma_f32_16x16x32_bf16 v[108:111], a[120:123], v[156:159], v[108:111]\n\t"
    "v_cvt_pk_bf16_f32 v172, v244, v245\n\t"
    "v_cvt_pk_bf16_f32 v173, v246, v247\n\t"
    "s_waitcnt lgkmcnt(10)\n\t"
    "v_mfma_f32_16x16x32_bf16 v[112:115], a[124:127], v[144:147], v[112:115]\n\t"
    "ds_read_b64_tr_b16 a[148:149], v198 offset:8\n\t"
    "v_cvt_pk_bf16_f32 v174, v248, v249\n\t"
    "v_cvt_pk_bf16_f32 v175, v250, v251\n\t"
    "v_mfma_f32_16x16x32_bf16 v[116:119], a[124:127], v[148:151], v[116:119]\n\t"
    "ds_read_b64_tr_b16 a[150:151], v198 offset:1032\n\t"
    "s_add_u32 m0, s60, 3072\n\t"
    "s_mov_b32 s74, 2195456\n\t"
    "v_mfma_f32_16x16x32_bf16 v[120:123], a[124:127], v[152:155], v[120:123]\n\t"
    "buffer_load_dwordx4 v253, s[44:47], s74 offen lds\n\t"
    "v_mfma_f32_16x16x32_bf16 v[124:127], a[124:127], v[156:159], v[124:127]\n\t"
    "s_waitcnt lgkmcnt(10)\n\t"
    "v_mfma_f32_16x16x32_bf16 v[0:3], a[128:131], v[160:163], v[0:3]\n\t"
    "ds_read_b64_tr_b16 a[152:153], v199 offset:0\n\t"
    "ds_read_b128 v[228:231], v200 offset:768\n\t"
    "ds_read_b128 v[232:235], v200 offset:784\n\t"
    "v_mfma_f32_16x16x32_bf16 v[4:7], a[128:131], v[164:167], v[4:7]\n\t"
    "ds_read_b64_tr_b16 a[154:155], v199 offset:1024\n\t"
    "ds_read_b128 v[236:239], v200 offset:800\n\t"
    "ds_read_b128 v[240:243], v200 offset:816\n\t"
    "s_mov_b32 s75, 4096\n\t"
    "v_mfma_f32_16x16x32_bf16 v[8:11], a[128:131], v[168:171], v[8:11]\n\t"
    "buffer_load_dwordx4 v[144:147], v226, s[48:51], s75 offen offset:1024\n\t"
    "v_mfma_f32_16x16x32_bf16 v[12:15], a[128:131], v[172:175], v[12:15]\n\t"
    "s_waitcnt lgkmcnt(14)\n\t"
    "v_mfma_f32_16x16x32_bf16 v[16:19], a[132:135], v[160:163], v[16:19]\n\t"
    "ds_read_b64_tr_b16 a[156:157], v199 offset:8\n\t"
    "v_mfma_f32_16x16x32_bf16 v[20:23], a[132:135], v[164:167], v[20:23]\n\t"
    "ds_read_b64_tr_b16 a[158:159], v199 offset:1032\n\t"
    "s_waitcnt lgkmcnt(2)\n\t"
    "s_waitcnt vmcnt(16)\n\t"
    "v_fma_mix_f32 v244, v176, v228, v236 op_sel:[0,0,0] op_sel_hi:[1,0,0]\n\t"
    "v_fma_mix_f32 v245, v176, v229, v237 op_sel:[1,0,0] op_sel_hi:[1,0,0]\n\t"
    "s_mov_b32 s75, 20480\n\t"
    "v_mfma_f32_16x16x32_bf16 v[24:27], a[132:135], v[168:171], v[24:27]\n\t"
    "buffer_load_dwordx4 v[148:151], v226, s[48:51], s75 offen offset:1024\n\t"
    "v_fma_mix_f32 v246, v177, v230, v238 op_sel:[0,0,0] op_sel_hi:[1,0,0]\n\t"
    "v_fma_mix_f32 v247, v177, v231, v239 op_sel:[1,0,0] op_sel_hi:[1,0,0]\n\t"
    "v_mfma_f32_16x16x32_bf16 v[28:31], a[132:135], v[172:175], v[28:31]\n\t"
    "v_fma_mix_f32 v248, v178, v232, v240 op_sel:[0,0,0] op_sel_hi:[1,0,0]\n\t"
    "v_fma_mix_f32 v249, v178, v233, v241 op_sel:[1,0,0] op_sel_hi:[1,0,0]\n\t"
    "v_mfma_f32_16x16x32_bf16 v[32:35], a[136:139], v[160:163], v[32:35]\n\t"
    "ds_read_b64_tr_b16 a[160:161], v196 offset:8192\n\t"
    "v_fma_mix_f32 v250, v179, v234, v242 op_sel:[0,0,0] op_sel_hi:[1,0,0]\n\t"
    "v_fma_mix_f32 v251, v179, v235, v243 op_sel:[1,0,0] op_sel_hi:[1,0,0]\n\t"
    "v_mfma_f32_16x16x32_bf16 v[36:39], a[136:139], v[164:167], v[36:39]\n\t"
    "ds_read_b64_tr_b16 a[162:163], v196 offset:9216\n\t"
    "v_cvt_pk_bf16_f32 v176, v244, v245\n\t"
    "v_cvt_pk_bf16_f32 v177, v246, v247\n\t"
    "s_mov_b32 s75, 36864\n\t"
    "v_mfma_f32_16x16x32_bf16 v[40:43], a[136:139], v[168:171], v[40:43]\n\t"
    "buffer_load_dwordx4 v[152:155], v226, s[48:51], s75 offen offset:1024\n\t"
    "v_cvt_pk_bf16_f32 v178, v248, v249\n\t"
    "v_cvt_pk_bf16_f32 v179, v250, v251\n\t"
    "v_mfma_f32_16x16x32_bf16 v[44:47], a[136:139], v[172:175], v[44:47]\n\t"
    "s_waitcnt vmcnt(17)\n\t"
    "v_fma_mix_f32 v244, v180, v228, v236 op_sel:[0,0,0] op_sel_hi:[1,0,0]\n\t"
    "v_fma_mix_f32 v245, v180, v229, v237 op_sel:[1,0,0] op_sel_hi:[1,0,0]\n\t"
    "v_mfma_f32_16x16x32_bf16 v[48:51], a[140:143], v[160:163], v[48:51]\n\t"
    "ds_read_b64_tr_b16 a[164:165], v196 offset:8200\n\t"
    "v_fma_mix_f32 v246, v181, v230, v238 op_sel:[0,0,0] op_sel_hi:[1,0,0]\n\t"
    "v_fma_mix_f32 v247, v181, v231, v239 op_sel:[1,0,0] op_sel_hi:[1,0,0]\n\t"
    "v_mfma_f32_16x16x32_bf16 v[52:55], a[140:143], v[164:167], v[52:55]\n\t"
    "ds_read_b64_tr_b16 a[166:167], v196 offset:9224\n\t"
    "v_fma_mix_f32 v248, v182, v232, v240 op_sel:[0,0,0] op_sel_hi:[1,0,0]\n\t"
    "v_fma_mix_f32 v249, v182, v233, v241 op_sel:[1,0,0] op_sel_hi:[1,0,0]\n\t"
    "s_mov_b32 s75, 53248\n\t"
    "v_mfma_f32_16x16x32_bf16 v[56:59], a[140:143], v[168:171], v[56:59]\n\t"
    "buffer_load_dwordx4 v[156:159], v226, s[48:51], s75 offen offset:1024\n\t"
    "v_fma_mix_f32 v250, v183, v234, v242 op_sel:[0,0,0] op_sel_hi:[1,0,0]\n\t"
    "v_fma_mix_f32 v251, v183, v235, v243 op_sel:[1,0,0] op_sel_hi:[1,0,0]\n\t"
    "v_mfma_f32_16x16x32_bf16 v[60:63], a[140:143], v[172:175], v[60:63]\n\t"
    "v_cvt_pk_bf16_f32 v180, v244, v245\n\t"
    "v_cvt_pk_bf16_f32 v181, v246, v247\n\t"
    "v_mfma_f32_16x16x32_bf16 v[64:67], a[144:147], v[160:163], v[64:67]\n\t"
    "ds_read_b64_tr_b16 a[168:169], v197 offset:8192\n\t"
    "v_cvt_pk_bf16_f32 v182, v248, v249\n\t"
    "v_cvt_pk_bf16_f32 v183, v250, v251\n\t"
    "v_mfma_f32_16x16x32_bf16 v[68:71], a[144:147], v[164:167], v[68:71]\n\t"
    "ds_read_b64_tr_b16 a[170:171], v197 offset:9216\n\t"
    "s_waitcnt vmcnt(17)\n\t"
    "v_fma_mix_f32 v244, v184, v228, v236 op_sel:[0,0,0] op_sel_hi:[1,0,0]\n\t"
    "v_fma_mix_f32 v245, v184, v229, v237 op_sel:[1,0,0] op_sel_hi:[1,0,0]\n\t"
    "s_add_u32 m0, s64, 0\n\t"
    "s_mov_b32 s74, 2621440\n\t"
    "v_mfma_f32_16x16x32_bf16 v[72:75], a[144:147], v[168:171], v[72:75]\n\t"
    "buffer_load_dwordx4 v252, s[44:47], s74 offen lds\n\t"
    "v_fma_mix_f32 v246, v185, v230, v238 op_sel:[0,0,0] op_sel_hi:[1,0,0]\n\t"
    "v_fma_mix_f32 v247, v185, v231, v239 op_sel:[1,0,0] op_sel_hi:[1,0,0]\n\t"
    "v_mfma_f32_16x16x32_bf16 v[76:79], a[144:147], v[172:175], v[76:79]\n\t"
    "v_fma_mix_f32 v248, v186, v232, v240 op_sel:[0,0,0] op_sel_hi:[1,0,0]\n\t"
    "v_fma_mix_f32 v249, v186, v233, v241 op_sel:[1,0,0] op_sel_hi:[1,0,0]\n\t"
    "v_mfma_f32_16x16x32_bf16 v[80:83], a[148:151], v[160:163], v[80:83]\n\t"
    "ds_read_b64_tr_b16 a[172:173], v197 offset:8200\n\t"
    "v_fma_mix_f32 v250, v187, v234, v242 op_sel:[0,0,0] op_sel_hi:[1,0,0]\n\t"
    "v_fma_mix_f32 v251, v187, v235, v243 op_sel:[1,0,0] op_sel_hi:[1,0,0]\n\t"
    "v_mfma_f32_16x16x32_bf16 v[84:87], a[148:151], v[164:167], v[84:87]\n\t"
    "ds_read_b64_tr_b16 a[174:175], v197 offset:9224\n\t"
    "v_cvt_pk_bf16_f32 v184, v244, v245\n\t"
    "v_cvt_pk_bf16_f32 v185, v246, v247\n\t"
    "s_add_u32 m0, s64, 1024\n\t"
    "s_mov_b32 s74, 2654208\n\t"
    "v_mfma_f32_16x16x32_bf16 v[88:91], a[148:151], v[168:171], v[88:91]\n\t"
    "buffer_load_dwordx4 v252, s[44:47], s74 offen lds\n\t"
    "v_cvt_pk_bf16_f32 v186, v248, v249\n\t"
    "v_cvt_pk_bf16_f32 v187, v250, v251\n\t"
    "v_mfma_f32_16x16x32_bf16 v[92:95], a[148:151], v[172:175], v[92:95]\n\t"
    "s_waitcnt vmcnt(18)\n\t"
    "v_fma_mix_f32 v244, v188, v228, v236 op_sel:[0,0,0] op_sel_hi:[1,0,0]\n\t"
    "v_fma_mix_f32 v245, v188, v229, v237 op_sel:[1,0,0] op_sel_hi:[1,0,0]\n\t"
    "s_barrier\n\t"
    "v_mfma_f32_16x16x32_bf16 v[96:99], a[152:155], v[160:163], v[96:99]\n\t"
    "ds_read_b64_tr_b16 a[176:177], v198 offset:8192\n\t"
    "v_fma_mix_f32 v246, v189, v230, v238 op_sel:[0,0,0] op_sel_hi:[1,0,0]\n\t"
    "v_fma_mix_f32 v247, v189, v231, v239 op_sel:[1,0,0] op_sel_hi:[1,0,0]\n\t"
    "v_mfma_f32_16x16x32_bf16 v[100:103], a[152:155], v[164:167], v[100:103]\n\t"
    "ds_read_b64_tr_b16 a[178:179], v198 offset:9216\n\t"
    "v_fma_mix_f32 v248, v190, v232, v240 op_sel:[0,0,0] op_sel_hi:[1,0,0]\n\t"
    "v_fma_mix_f32 v249, v190, v233, v241 op_sel:[1,0,0] op_sel_hi:[1,0,0]\n\t"
    "s_mov_b32 m0, s58\n\t"
    "v_mfma_f32_16x16x32_bf16 v[104:107], a[152:155], v[168:171], v[104:107]\n\t"
    "buffer_load_dwordx4 v220, s[36:39], s56 offen offset:0 lds\n\t"
    "v_fma_mix_f32 v250, v191, v234, v242 op_sel:[0,0,0] op_sel_hi:[1,0,0]\n\t"
    "v_fma_mix_f32 v251, v191, v235, v243 op_sel:[1,0,0] op_sel_hi:[1,0,0]\n\t"
    "v_mfma_f32_16x16x32_bf16 v[108:111], a[152:155], v[172:175], v[108:111]\n\t"
    "v_cvt_pk_bf16_f32 v188, v244, v245\n\t"
    "v_cvt_pk_bf16_f32 v189, v246, v247\n\t"
    "s_waitcnt lgkmcnt(10)\n\t"
    "v_mfma_f32_16x16x32_bf16 v[112:115], a[156:159], v[160:163], v[112:115]\n\t"
    "ds_read_b64_tr_b16 a[180:181], v198 offset:8200\n\t"
    "v_cvt_pk_bf16_f32 v190, v248, v249\n\t"
    "v_cvt_pk_bf16_f32 v191, v250, v251\n\t"
    "v_mfma_f32_16x16x32_bf16 v[116:119], a[156:159], v[164:167], v[116:119]\n\t"
    "ds_read_b64_tr_b16 a[182:183], v198 offset:9224\n\t"
    "s_mov_b32 m0, s58\n\t"
    "v_mfma_f32_16x16x32_bf16 v[120:123], a[156:159], v[168:171], v[120:123]\n\t"
    "buffer_load_dwordx4 v221, s[36:39], s56 offen offset:1024 lds\n\t"
    "v_mfma_f32_16x16x32_bf16 v[124:127], a[156:159], v[172:175], v[124:127]\n\t"
    "s_waitcnt lgkmcnt(10)\n\t"
    "v_mfma_f32_16x16x32_bf16 v[0:3], a[160:163], v[176:179], v[0:3]\n\t"
    "ds_read_b64_tr_b16 a[184:185], v199 offset:8192\n\t"
    "ds_read_b128 v[228:231], v200 offset:1024\n\t"
    "ds_read_b128 v[232:235], v200 offset:1040\n\t"
    "v_mfma_f32_16x16x32_bf16 v[4:7], a[160:163], v[180:183], v[4:7]\n\t"
    "ds_read_b64_tr_b16 a[186:187], v199 offset:9216\n\t"
    "ds_read_b128 v[236:239], v200 offset:1056\n\t"
    "ds_read_b128 v[240:243], v200 offset:1072\n\t"
    "s_mov_b32 s75, 4096\n\t"
    "v_mfma_f32_16x16x32_bf16 v[8:11], a[160:163], v[184:187], v[8:11]\n\t"
    "buffer_load_dwordx4 v[160:163], v226, s[48:51], s75 offen offset:2048\n\t"
    "v_mfma_f32_16x16x32_bf16 v[12:15], a[160:163], v[188:191], v[12:15]\n\t"
    "s_waitcnt lgkmcnt(14)\n\t"
    "v_mfma_f32_16x16x32_bf16 v[16:19], a[164:167], v[176:179], v[16:19]\n\t"
    "ds_read_b64_tr_b16 a[188:189], v199 offset:8200\n\t"
    "v_mfma_f32_16x16x32_bf16 v[20:23], a[164:167], v[180:183], v[20:23]\n\t"
    "ds_read_b64_tr_b16 a[190:191], v199 offset:9224\n\t"
    "s_waitcnt lgkmcnt(2)\n\t"
    "s_waitcnt vmcnt(16)\n\t"
    "v_fma_mix_f32 v244, v128, v228, v236 op_sel:[0,0,0] op_sel_hi:[1,0,0]\n\t"
    "v_fma_mix_f32 v245, v128, v229, v237 op_sel:[1,0,0] op_sel_hi:[1,0,0]\n\t"
    "s_mov_b32 s75, 20480\n\t"
    "v_mfma_f32_16x16x32_bf16 v[24:27], a[164:167], v[184:187], v[24:27]\n\t"
    "buffer_load_dwordx4 v[164:167], v226, s[48:51], s75 offen offset:2048\n\t"
    "v_fma_mix_f32 v246, v129, v230, v238 op_sel:[0,0,0] op_sel_hi:[1,0,0]\n\t"
    "v_fma_mix_f32 v247, v129, v231, v239 op_sel:[1,0,0] op_sel_hi:[1,0,0]\n\t"
    "v_mfma_f32_16x16x32_bf16 v[28:31], a[164:167], v[188:191], v[28:31]\n\t"
    "v_fma_mix_f32 v248, v130, v232, v240 op_sel:[0,0,0] op_sel_hi:[1,0,0]\n\t"
    "v_fma_mix_f32 v249, v130, v233, v241 op_sel:[1,0,0] op_sel_hi:[1,0,0]\n\t"
    "v_mfma_f32_16x16x32_bf16 v[32:35], a[168:171], v[176:179], v[32:35]\n\t"
    "ds_read_b64_tr_b16 a[64:65], v192 offset:16384\n\t"
    "v_fma_mix_f32 v250, v131, v234, v242 op_sel:[0,0,0] op_sel_hi:[1,0,0]\n\t"
    "v_fma_mix_f32 v251, v131, v235, v243 op_sel:[1,0,0] op_sel_hi:[1,0,0]\n\t"
    "v_mfma_f32_16x16x32_bf16 v[36:39], a[168:171], v[180:183], v[36:39]\n\t"
    "ds_read_b64_tr_b16 a[66:67], v192 offset:17408\n\t"
    "v_cvt_pk_bf16_f32 v128, v244, v245\n\t"
    "v_cvt_pk_bf16_f32 v129, v246, v247\n\t"
    "s_mov_b32 s75, 36864\n\t"
    "v_mfma_f32_16x16x32_bf16 v[40:43], a[168:171], v[184:187], v[40:43]\n\t"
    "buffer_load_dwordx4 v[168:171], v226, s[48:51], s75 offen offset:2048\n\t"
    "v_cvt_pk_bf16_f32 v130, v248, v249\n\t"
    "v_cvt_pk_bf16_f32 v131, v250, v251\n\t"
    "v_mfma_f32_16x16x32_bf16 v[44:47], a[168:171], v[188:191], v[44:47]\n\t"
    "s_waitcnt vmcnt(17)\n\t"
    "v_fma_mix_f32 v244, v132, v228, v236 op_sel:[0,0,0] op_sel_hi:[1,0,0]\n\t"
    "v_fma_mix_f32 v245, v132, v229, v237 op_sel:[1,0,0] op_sel_hi:[1,0,0]\n\t"
    "v_mfma_f32_16x16x32_bf16 v[48:51], a[172:175], v[176:179], v[48:51]\n\t"
    "ds_read_b64_tr_b16 a[68:69], v192 offset:16392\n\t"
    "v_fma_mix_f32 v246, v133, v230, v238 op_sel:[0,0,0] op_sel_hi:[1,0,0]\n\t"
    "v_fma_mix_f32 v247, v133, v231, v239 op_sel:[1,0,0] op_sel_hi:[1,0,0]\n\t"
    "v_mfma_f32_16x16x32_bf16 v[52:55], a[172:175], v[180:183], v[52:55]\n\t"
    "ds_read_b64_tr_b16 a[70:71], v192 offset:17416\n\t"
    "v_fma_mix_f32 v248, v134, v232, v240 op_sel:[0,0,0] op_sel_hi:[1,0,0]\n\t"
    "v_fma_mix_f32 v249, v134, v233, v241 op_sel:[1,0,0] op_sel_hi:[1,0,0]\n\t"
    "s_mov_b32 s75, 53248\n\t"
    "v_mfma_f32_16x16x32_bf16 v[56:59], a[172:175], v[184:187], v[56:59]\n\t"
    "buffer_load_dwordx4 v[172:175], v226, s[48:51], s75 offen offset:2048\n\t"
    "v_fma_mix_f32 v250, v135, v234, v242 op_sel:[0,0,0] op_sel_hi:[1,0,0]\n\t"
    "v_fma_mix_f32 v251, v135, v235, v243 op_sel:[1,0,0] op_sel_hi:[1,0,0]\n\t"
    "v_mfma_f32_16x16x32_bf16 v[60:63], a[172:175], v[188:191], v[60:63]\n\t"
    "v_cvt_pk_bf16_f32 v132, v244, v245\n\t"
    "v_cvt_pk_bf16_f32 v133, v246, v247\n\t"
    "v_mfma_f32_16x16x32_bf16 v[64:67], a[176:179], v[176:179], v[64:67]\n\t"
    "ds_read_b64_tr_b16 a[72:73], v193 offset:16384\n\t"
    "v_cvt_pk_bf16_f32 v134, v248, v249\n\t"
    "v_cvt_pk_bf16_f32 v135, v250, v251\n\t"
    "v_mfma_f32_16x16x32_bf16 v[68:71], a[176:179], v[180:183], v[68:71]\n\t"
    "ds_read_b64_tr_b16 a[74:75], v193 offset:17408\n\t"
    "s_waitcnt vmcnt(17)\n\t"
    "v_fma_mix_f32 v244, v136, v228, v236 op_sel:[0,0,0] op_sel_hi:[1,0,0]\n\t"
    "v_fma_mix_f32 v245, v136, v229, v237 op_sel:[1,0,0] op_sel_hi:[1,0,0]\n\t"
    "s_mov_b32 m0, s58\n\t"
    "v_mfma_f32_16x16x32_bf16 v[72:75], a[176:179], v[184:187], v[72:75]\n\t"
    "buffer_load_dwordx4 v222, s[36:39], s56 offen offset:2048 lds\n\t"
    "v_fma_mix_f32 v246, v137, v230, v238 op_sel:[0,0,0] op_sel_hi:[1,0,0]\n\t"
    "v_fma_mix_f32 v247, v137, v231, v239 op_sel:[1,0,0] op_sel_hi:[1,0,0]\n\t"
    "v_mfma_f32_16x16x32_bf16 v[76:79], a[176:179], v[188:191], v[76:79]\n\t"
    "v_fma_mix_f32 v248, v138, v232, v240 op_sel:[0,0,0] op_sel_hi:[1,0,0]\n\t"
    "v_fma_mix_f32 v249, v138, v233, v241 op_sel:[1,0,0] op_sel_hi:[1,0,0]\n\t"
    "v_mfma_f32_16x16x32_bf16 v[80:83], a[180:183], v[176:179], v[80:83]\n\t"
    "ds_read_b64_tr_b16 a[76:77], v193 offset:16392\n\t"
    "v_fma_mix_f32 v250, v139, v234, v242 op_sel:[0,0,0] op_sel_hi:[1,0,0]\n\t"
    "v_fma_mix_f32 v251, v139, v235, v243 op_sel:[1,0,0] op_sel_hi:[1,0,0]\n\t"
    "v_mfma_f32_16x16x32_bf16 v[84:87], a[180:183], v[180:183], v[84:87]\n\t"
    "ds_read_b64_tr_b16 a[78:79], v193 offset:17416\n\t"
    "v_cvt_pk_bf16_f32 v136, v244, v245\n\t"
    "v_cvt_pk_bf16_f32 v137, v246, v247\n\t"
    "s_mov_b32 m0, s58\n\t"
    "v_mfma_f32_16x16x32_bf16 v[88:91], a[180:183], v[184:187], v[88:91]\n\t"
    "buffer_load_dwordx4 v223, s[36:39], s56 offen offset:3072 lds\n\t"
    "s_add_u32 s56, s56, 0x4000\n\t"
    "v_cvt_pk_bf16_f32 v138, v248, v249\n\t"
    "v_cvt_pk_bf16_f32 v139, v250, v251\n\t"
    "v_mfma_f32_16x16x32_bf16 v[92:95], a[180:183], v[188:191], v[92:95]\n\t"
    "s_waitcnt vmcnt(18)\n\t"
    "v_fma_mix_f32 v244, v140, v228, v236 op_sel:[0,0,0] op_sel_hi:[1,0,0]\n\t"
    "v_fma_mix_f32 v245, v140, v229, v237 op_sel:[1,0,0] op_sel_hi:[1,0,0]\n\t"
    "v_mfma_f32_16x16x32_bf16 v[96:99], a[184:187], v[176:179], v[96:99]\n\t"
    "ds_read_b64_tr_b16 a[80:81], v194 offset:16384\n\t"
    "v_fma_mix_f32 v246, v141, v230, v238 op_sel:[0,0,0] op_sel_hi:[1,0,0]\n\t"
    "v_fma_mix_f32 v247, v141, v231, v239 op_sel:[1,0,0] op_sel_hi:[1,0,0]\n\t"
    "v_mfma_f32_16x16x32_bf16 v[100:103], a[184:187], v[180:183], v[100:103]\n\t"
    "ds_read_b64_tr_b16 a[82:83], v194 offset:17408\n\t"
    "v_fma_mix_f32 v248, v142, v232, v240 op_sel:[0,0,0] op_sel_hi:[1,0,0]\n\t"
    "v_fma_mix_f32 v249, v142, v233, v241 op_sel:[1,0,0] op_sel_hi:[1,0,0]\n\t"
    "s_add_u32 m0, s64, 2048\n\t"
    "s_mov_b32 s74, 2686976\n\t"
    "v_mfma_f32_16x16x32_bf16 v[104:107], a[184:187], v[184:187], v[104:107]\n\t"
    "buffer_load_dwordx4 v253, s[44:47], s74 offen lds\n\t"
    "v_fma_mix_f32 v250, v143, v234, v242 op_sel:[0,0,0] op_sel_hi:[1,0,0]\n\t"
    "v_fma_mix_f32 v251, v143, v235, v243 op_sel:[1,0,0] op_sel_hi:[1,0,0]\n\t"
    "v_mfma_f32_16x16x32_bf16 v[108:111], a[184:187], v[188:191], v[108:111]\n\t"
    "v_cvt_pk_bf16_f32 v140, v244, v245\n\t"
    "v_cvt_pk_bf16_f32 v141, v246, v247\n\t"
    "s_waitcnt lgkmcnt(10)\n\t"
    "v_mfma_f32_16x16x32_bf16 v[112:115], a[188:191], v[176:179], v[112:115]\n\t"
    "ds_read_b64_tr_b16 a[84:85], v194 offset:16392\n\t"
    "v_cvt_pk_bf16_f32 v142, v248, v249\n\t"
    "v_cvt_pk_bf16_f32 v143, v250, v251\n\t"
    "v_mfma_f32_16x16x32_bf16 v[116:119], a[188:191], v[180:183], v[116:119]\n\t"
    "ds_read_b64_tr_b16 a[86:87], v194 offset:17416\n\t"
    "s_add_u32 m0, s64, 3072\n\t"
    "s_mov_b32 s74, 2719744\n\t"
    "v_mfma_f32_16x16x32_bf16 v[120:123], a[188:191], v[184:187], v[120:123]\n\t"
    "buffer_load_dwordx4 v253, s[44:47], s74 offen lds\n\t"
    "v_mfma_f32_16x16x32_bf16 v[124:127], a[188:191], v[188:191], v[124:127]\n\t"
    "s_waitcnt lgkmcnt(10)\n\t"
    "v_mfma_f32_16x16x32_bf16 v[0:3], a[64:67], v[128:131], v[0:3]\n\t"
    "ds_read_b64_tr_b16 a[88:89], v195 offset:16384\n\t"
    "ds_read_b128 v[228:231], v200 offset:1280\n\t"
    "ds_read_b128 v[232:235], v200 offset:1296\n\t"
    "v_mfma_f32_16x16x32_bf16 v[4:7], a[64:67], v[132:135], v[4:7]\n\t"
    "ds_read_b64_tr_b16 a[90:91], v195 offset:17408\n\t"
    "ds_read_b128 v[236:239], v200 offset:1312\n\t"
    "ds_read_b128 v[240:243], v200 offset:1328\n\t"
    "s_mov_b32 s75, 4096\n\t"
    "v_mfma_f32_16x16x32_bf16 v[8:11], a[64:67], v[136:139], v[8:11]\n\t"
    "buffer_load_dwordx4 v[176:179], v226, s[48:51], s75 offen offset:3072\n\t"
    "v_mfma_f32_16x16x32_bf16 v[12:15], a[64:67], v[140:143], v[12:15]\n\t"
    "s_waitcnt lgkmcnt(14)\n\t"
    "v_mfma_f32_16x16x32_bf16 v[16:19], a[68:71], v[128:131], v[16:19]\n\t"
    "ds_read_b64_tr_b16 a[92:93], v195 offset:16392\n\t"
    "v_mfma_f32_16x16x32_bf16 v[20:23], a[68:71], v[132:135], v[20:23]\n\t"
    "ds_read_b64_tr_b16 a[94:95], v195 offset:17416\n\t"
    "s_waitcnt lgkmcnt(2)\n\t"
    "s_waitcnt vmcnt(16)\n\t"
    "v_fma_mix_f32 v244, v144, v228, v236 op_sel:[0,0,0] op_sel_hi:[1,0,0]\n\t"
    "v_fma_mix_f32 v245, v144, v229, v237 op_sel:[1,0,0] op_sel_hi:[1,0,0]\n\t"
    "s_mov_b32 s75, 20480\n\t"
    "v_mfma_f32_16x16x32_bf16 v[24:27], a[68:71], v[136:139], v[24:27]\n\t"
    "buffer_load_dwordx4 v[180:183], v226, s[48:51], s75 offen offset:3072\n\t"
    "v_fma_mix_f32 v246, v145, v230, v238 op_sel:[0,0,0] op_sel_hi:[1,0,0]\n\t"
    "v_fma_mix_f32 v247, v145, v231, v239 op_sel:[1,0,0] op_sel_hi:[1,0,0]\n\t"
    "v_mfma_f32_16x16x32_bf16 v[28:31], a[68:71], v[140:143], v[28:31]\n\t"
    "v_fma_mix_f32 v248, v146, v232, v240 op_sel:[0,0,0] op_sel_hi:[1,0,0]\n\t"
    "v_fma_mix_f32 v249, v146, v233, v241 op_sel:[1,0,0] op_sel_hi:[1,0,0]\n\t"
    "v_mfma_f32_16x16x32_bf16 v[32:35], a[72:75], v[128:131], v[32:35]\n\t"
    "ds_read_b64_tr_b16 a[96:97], v192 offset:24576\n\t"
    "v_fma_mix_f32 v250, v147, v234, v242 op_sel:[0,0,0] op_sel_hi:[1,0,0]\n\t"
    "v_fma_mix_f32 v251, v147, v235, v243 op_sel:[1,0,0] op_sel_hi:[1,0,0]\n\t"
    "v_mfma_f32_16x16x32_bf16 v[36:39], a[72:75], v[132:135], v[36:39]\n\t"
    "ds_read_b64_tr_b16 a[98:99], v192 offset:25600\n\t"
    "v_cvt_pk_bf16_f32 v144, v244, v245\n\t"
    "v_cvt_pk_bf16_f32 v145, v246, v247\n\t"
    "s_mov_b32 s75, 36864\n\t"
    "v_mfma_f32_16x16x32_bf16 v[40:43], a[72:75], v[136:139], v[40:43]\n\t"
    "buffer_load_dwordx4 v[184:187], v226, s[48:51], s75 offen offset:3072\n\t"
    "v_cvt_pk_bf16_f32 v146, v248, v249\n\t"
    "v_cvt_pk_bf16_f32 v147, v250, v251\n\t"
    "v_mfma_f32_16x16x32_bf16 v[44:47], a[72:75], v[140:143], v[44:47]\n\t"
    "s_waitcnt vmcnt(17)\n\t"
    "v_fma_mix_f32 v244, v148, v228, v236 op_sel:[0,0,0] op_sel_hi:[1,0,0]\n\t"
    "v_fma_mix_f32 v245, v148, v229, v237 op_sel:[1,0,0] op_sel_hi:[1,0,0]\n\t"
    "v_mfma_f32_16x16x32_bf16 v[48:51], a[76:79], v[128:131], v[48:51]\n\t"
    "ds_read_b64_tr_b16 a[100:101], v192 offset:24584\n\t"
    "v_fma_mix_f32 v246, v149, v230, v238 op_sel:[0,0,0] op_sel_hi:[1,0,0]\n\t"
    "v_fma_mix_f32 v247, v149, v231, v239 op_sel:[1,0,0] op_sel_hi:[1,0,0]\n\t"
    "v_mfma_f32_16x16x32_bf16 v[52:55], a[76:79], v[132:135], v[52:55]\n\t"
    "ds_read_b64_tr_b16 a[102:103], v192 offset:25608\n\t"
    "v_fma_mix_f32 v248, v150, v232, v240 op_sel:[0,0,0] op_sel_hi:[1,0,0]\n\t"
    "v_fma_mix_f32 v249, v150, v233, v241 op_sel:[1,0,0] op_sel_hi:[1,0,0]\n\t"
    "s_mov_b32 s75, 53248\n\t"
    "v_mfma_f32_16x16x32_bf16 v[56:59], a[76:79], v[136:139], v[56:59]\n\t"
    "buffer_load_dwordx4 v[188:191], v226, s[48:51], s75 offen offset:3072\n\t"
    "v_fma_mix_f32 v250, v151, v234, v242 op_sel:[0,0,0] op_sel_hi:[1,0,0]\n\t"
    "v_fma_mix_f32 v251, v151, v235, v243 op_sel:[1,0,0] op_sel_hi:[1,0,0]\n\t"
    "v_mfma_f32_16x16x32_bf16 v[60:63], a[76:79], v[140:143], v[60:63]\n\t"
    "v_cvt_pk_bf16_f32 v148, v244, v245\n\t"
    "v_cvt_pk_bf16_f32 v149, v246, v247\n\t"
    "v_mfma_f32_16x16x32_bf16 v[64:67], a[80:83], v[128:131], v[64:67]\n\t"
    "ds_read_b64_tr_b16 a[104:105], v193 offset:24576\n\t"
    "v_cvt_pk_bf16_f32 v150, v248, v249\n\t"
    "v_cvt_pk_bf16_f32 v151, v250, v251\n\t"
    "v_mfma_f32_16x16x32_bf16 v[68:71], a[80:83], v[132:135], v[68:71]\n\t"
    "ds_read_b64_tr_b16 a[106:107], v193 offset:25600\n\t"
    "s_waitcnt vmcnt(17)\n\t"
    "v_fma_mix_f32 v244, v152, v228, v236 op_sel:[0,0,0] op_sel_hi:[1,0,0]\n\t"
    "v_fma_mix_f32 v245, v152, v229, v237 op_sel:[1,0,0] op_sel_hi:[1,0,0]\n\t"
    "s_add_u32 m0, s61, 0\n\t"
    "s_mov_b32 s74, 3145728\n\t"
    "v_mfma_f32_16x16x32_bf16 v[72:75], a[80:83], v[136:139], v[72:75]\n\t"
    "buffer_load_dwordx4 v252, s[44:47], s74 offen lds\n\t"
    "v_fma_mix_f32 v246, v153, v230, v238 op_sel:[0,0,0] op_sel_hi:[1,0,0]\n\t"
    "v_fma_mix_f32 v247, v153, v231, v239 op_sel:[1,0,0] op_sel_hi:[1,0,0]\n\t"
    "v_mfma_f32_16x16x32_bf16 v[76:79], a[80:83], v[140:143], v[76:79]\n\t"
    "v_fma_mix_f32 v248, v154, v232, v240 op_sel:[0,0,0] op_sel_hi:[1,0,0]\n\t"
    "v_fma_mix_f32 v249, v154, v233, v241 op_sel:[1,0,0] op_sel_hi:[1,0,0]\n\t"
    "v_mfma_f32_16x16x32_bf16 v[80:83], a[84:87], v[128:131], v[80:83]\n\t"
    "ds_read_b64_tr_b16 a[108:109], v193 offset:24584\n\t"
    "v_fma_mix_f32 v250, v155, v234, v242 op_sel:[0,0,0] op_sel_hi:[1,0,0]\n\t"
    "v_fma_mix_f32 v251, v155, v235, v243 op_sel:[1,0,0] op_sel_hi:[1,0,0]\n\t"
    "v_mfma_f32_16x16x32_bf16 v[84:87], a[84:87], v[132:135], v[84:87]\n\t"
    "ds_read_b64_tr_b16 a[110:111], v193 offset:25608\n\t"
    "v_cvt_pk_bf16_f32 v152, v244, v245\n\t"
    "v_cvt_pk_bf16_f32 v153, v246, v247\n\t"
    "s_add_u32 m0, s61, 1024\n\t"
    "s_mov_b32 s74, 3178496\n\t"
    "v_mfma_f32_16x16x32_bf16 v[88:91], a[84:87], v[136:139], v[88:91]\n\t"
    "buffer_load_dwordx4 v252, s[44:47], s74 offen lds\n\t"
    "v_cvt_pk_bf16_f32 v154, v248, v249\n\t"
    "v_cvt_pk_bf16_f32 v155, v250, v251\n\t"
    "v_mfma_f32_16x16x32_bf16 v[92:95], a[84:87], v[140:143], v[92:95]\n\t"
    "s_waitcnt vmcnt(18)\n\t"
    "v_fma_mix_f32 v244, v156, v228, v236 op_sel:[0,0,0] op_sel_hi:[1,0,0]\n\t"
    "v_fma_mix_f32 v245, v156, v229, v237 op_sel:[1,0,0] op_sel_hi:[1,0,0]\n\t"
    "s_barrier\n\t"
    "v_mfma_f32_16x16x32_bf16 v[96:99], a[88:91], v[128:131], v[96:99]\n\t"
    "ds_read_b64_tr_b16 a[112:113], v194 offset:24576\n\t"
    "v_fma_mix_f32 v246, v157, v230, v238 op_sel:[0,0,0] op_sel_hi:[1,0,0]\n\t"
    "v_fma_mix_f32 v247, v157, v231, v239 op_sel:[1,0,0] op_sel_hi:[1,0,0]\n\t"
    "v_mfma_f32_16x16x32_bf16 v[100:103], a[88:91], v[132:135], v[100:103]\n\t"
    "ds_read_b64_tr_b16 a[114:115], v194 offset:25600\n\t"
    "v_fma_mix_f32 v248, v158, v232, v240 op_sel:[0,0,0] op_sel_hi:[1,0,0]\n\t"
    "v_fma_mix_f32 v249, v158, v233, v241 op_sel:[1,0,0] op_sel_hi:[1,0,0]\n\t"
    "s_mov_b32 m0, s62\n\t"
    "v_mfma_f32_16x16x32_bf16 v[104:107], a[88:91], v[136:139], v[104:107]\n\t"
    "buffer_load_dwordx4 v224, s[40:43], s57 offen offset:0 lds\n\t"
    "v_fma_mix_f32 v250, v159, v234, v242 op_sel:[0,0,0] op_sel_hi:[1,0,0]\n\t"
    "v_fma_mix_f32 v251, v159, v235, v243 op_sel:[1,0,0] op_sel_hi:[1,0,0]\n\t"
    "v_mfma_f32_16x16x32_bf16 v[108:111], a[88:91], v[140:143], v[108:111]\n\t"
    "v_cvt_pk_bf16_f32 v156, v244, v245\n\t"
    "v_cvt_pk_bf16_f32 v157, v246, v247\n\t"
    "s_waitcnt lgkmcnt(10)\n\t"
    "v_mfma_f32_16x16x32_bf16 v[112:115], a[92:95], v[128:131], v[112:115]\n\t"
    "ds_read_b64_tr_b16 a[116:117], v194 offset:24584\n\t"
    "v_cvt_pk_bf16_f32 v158, v248, v249\n\t"
    "v_cvt_pk_bf16_f32 v159, v250, v251\n\t"
    "v_mfma_f32_16x16x32_bf16 v[116:119], a[92:95], v[132:135], v[116:119]\n\t"
    "ds_read_b64_tr_b16 a[118:119], v194 offset:25608\n\t"
    "s_mov_b32 m0, s62\n\t"
    "v_mfma_f32_16x16x32_bf16 v[120:123], a[92:95], v[136:139], v[120:123]\n\t"
    "buffer_load_dwordx4 v225, s[40:43], s57 offen offset:1024 lds\n\t"
    "v_mfma_f32_16x16x32_bf16 v[124:127], a[92:95], v[140:143], v[124:127]\n\t"
    "s_waitcnt lgkmcnt(10)\n\t"
    "v_mfma_f32_16x16x32_bf16 v[0:3], a[96:99], v[144:147], v[0:3]\n\t"
    "ds_read_b64_tr_b16 a[120:121], v195 offset:24576\n\t"
    "ds_read_b128 v[228:231], v200 offset:1536\n\t"
    "ds_read_b128 v[232:235], v200 offset:1552\n\t"
    "v_mfma_f32_16x16x32_bf16 v[4:7], a[96:99], v[148:151], v[4:7]\n\t"
    "ds_read_b64_tr_b16 a[122:123], v195 offset:25600\n\t"
    "ds_read_b128 v[236:239], v200 offset:1568\n\t"
    "ds_read_b128 v[240:243], v200 offset:1584\n\t"
    "s_mov_b32 s75, 8192\n\t"
    "v_mfma_f32_16x16x32_bf16 v[8:11], a[96:99], v[152:155], v[8:11]\n\t"
    "buffer_load_dwordx4 v[128:131], v226, s[48:51], s75 offen offset:0\n\t"
    "v_mfma_f32_16x16x32_bf16 v[12:15], a[96:99], v[156:159], v[12:15]\n\t"
    "s_waitcnt lgkmcnt(14)\n\t"
    "v_mfma_f32_16x16x32_bf16 v[16:19], a[100:103], v[144:147], v[16:19]\n\t"
    "ds_read_b64_tr_b16 a[124:125], v195 offset:24584\n\t"
    "v_mfma_f32_16x16x32_bf16 v[20:23], a[100:103], v[148:151], v[20:23]\n\t"
    "ds_read_b64_tr_b16 a[126:127], v195 offset:25608\n\t"
    "s_waitcnt lgkmcnt(2)\n\t"
    "s_waitcnt vmcnt(16)\n\t"
    "v_fma_mix_f32 v244, v160, v228, v236 op_sel:[0,0,0] op_sel_hi:[1,0,0]\n\t"
    "v_fma_mix_f32 v245, v160, v229, v237 op_sel:[1,0,0] op_sel_hi:[1,0,0]\n\t"
    "s_mov_b32 s75, 24576\n\t"
    "v_mfma_f32_16x16x32_bf16 v[24:27], a[100:103], v[152:155], v[24:27]\n\t"
    "buffer_load_dwordx4 v[132:135], v226, s[48:51], s75 offen offset:0\n\t"
    "v_fma_mix_f32 v246, v161, v230, v238 op_sel:[0,0,0] op_sel_hi:[1,0,0]\n\t"
    "v_fma_mix_f32 v247, v161, v231, v239 op_sel:[1,0,0] op_sel_hi:[1,0,0]\n\t"
    "v_mfma_f32_16x16x32_bf16 v[28:31], a[100:103], v[156:159], v[28:31]\n\t"
    "v_fma_mix_f32 v248, v162, v232, v240 op_sel:[0,0,0] op_sel_hi:[1,0,0]\n\t"
    "v_fma_mix_f32 v249, v162, v233, v241 op_sel:[1,0,0] op_sel_hi:[1,0,0]\n\t"
    "v_mfma_f32_16x16x32_bf16 v[32:35], a[104:107], v[144:147], v[32:35]\n\t"
    "ds_read_b64_tr_b16 a[128:129], v196 offset:16384\n\t"
    "v_fma_mix_f32 v250, v163, v234, v242 op_sel:[0,0,0] op_sel_hi:[1,0,0]\n\t"
    "v_fma_mix_f32 v251, v163, v235, v243 op_sel:[1,0,0] op_sel_hi:[1,0,0]\n\t"
    "v_mfma_f32_16x16x32_bf16 v[36:39], a[104:107], v[148:151], v[36:39]\n\t"
    "ds_read_b64_tr_b16 a[130:131], v196 offset:17408\n\t"
    "v_cvt_pk_bf16_f32 v160, v244, v245\n\t"
    "v_cvt_pk_bf16_f32 v161, v246, v247\n\t"
    "s_mov_b32 s75, 40960\n\t"
    "v_mfma_f32_16x16x32_bf16 v[40:43], a[104:107], v[152:155], v[40:43]\n\t"
    "buffer_load_dwordx4 v[136:139], v226, s[48:51], s75 offen offset:0\n\t"
    "v_cvt_pk_bf16_f32 v162, v248, v249\n\t"
    "v_cvt_pk_bf16_f32 v163, v250, v251\n\t"
    "v_mfma_f32_16x16x32_bf16 v[44:47], a[104:107], v[156:159], v[44:47]\n\t"
    "s_waitcnt vmcnt(17)\n\t"
    "v_fma_mix_f32 v244, v164, v228, v236 op_sel:[0,0,0] op_sel_hi:[1,0,0]\n\t"
    "v_fma_mix_f32 v245, v164, v229, v237 op_sel:[1,0,0] op_sel_hi:[1,0,0]\n\t"
    "v_mfma_f32_16x16x32_bf16 v[48:51], a[108:111], v[144:147], v[48:51]\n\t"
    "ds_read_b64_tr_b16 a[132:133], v196 offset:16392\n\t"
    "v_fma_mix_f32 v246, v165, v230, v238 op_sel:[0,0,0] op_sel_hi:[1,0,0]\n\t"
    "v_fma_mix_f32 v247, v165, v231, v239 op_sel:[1,0,0] op_sel_hi:[1,0,0]\n\t"
    "v_mfma_f32_16x16x32_bf16 v[52:55], a[108:111], v[148:151], v[52:55]\n\t"
    "ds_read_b64_tr_b16 a[134:135], v196 offset:17416\n\t"
    "v_fma_mix_f32 v248, v166, v232, v240 op_sel:[0,0,0] op_sel_hi:[1,0,0]\n\t"
    "v_fma_mix_f32 v249, v166, v233, v241 op_sel:[1,0,0] op_sel_hi:[1,0,0]\n\t"
    "s_mov_b32 s75, 57344\n\t"
    "v_mfma_f32_16x16x32_bf16 v[56:59], a[108:111], v[152:155], v[56:59]\n\t"
    "buffer_load_dwordx4 v[140:143], v226, s[48:51], s75 offen offset:0\n\t"
    "v_fma_mix_f32 v250, v167, v234, v242 op_sel:[0,0,0] op_sel_hi:[1,0,0]\n\t"
    "v_fma_mix_f32 v251, v167, v235, v243 op_sel:[1,0,0] op_sel_hi:[1,0,0]\n\t"
    "v_mfma_f32_16x16x32_bf16 v[60:63], a[108:111], v[156:159], v[60:63]\n\t"
    "v_cvt_pk_bf16_f32 v164, v244, v245\n\t"
    "v_cvt_pk_bf16_f32 v165, v246, v247\n\t"
    "v_mfma_f32_16x16x32_bf16 v[64:67], a[112:115], v[144:147], v[64:67]\n\t"
    "ds_read_b64_tr_b16 a[136:137], v197 offset:16384\n\t"
    "v_cvt_pk_bf16_f32 v166, v248, v249\n\t"
    "v_cvt_pk_bf16_f32 v167, v250, v251\n\t"
    "v_mfma_f32_16x16x32_bf16 v[68:71], a[112:115], v[148:151], v[68:71]\n\t"
    "ds_read_b64_tr_b16 a[138:139], v197 offset:17408\n\t"
    "s_waitcnt vmcnt(17)\n\t"
    "v_fma_mix_f32 v244, v168, v228, v236 op_sel:[0,0,0] op_sel_hi:[1,0,0]\n\t"
    "v_fma_mix_f32 v245, v168, v229, v237 op_sel:[1,0,0] op_sel_hi:[1,0,0]\n\t"
    "s_mov_b32 m0, s62\n\t"
    "v_mfma_f32_16x16x32_bf16 v[72:75], a[112:115], v[152:155], v[72:75]\n\t"
    "buffer_load_dwordx4 v224, s[40:43], s57 offen offset:2048 lds\n\t"
    "v_fma_mix_f32 v246, v169, v230, v238 op_sel:[0,0,0] op_sel_hi:[1,0,0]\n\t"
    "v_fma_mix_f32 v247, v169, v231, v239 op_sel:[1,0,0] op_sel_hi:[1,0,0]\n\t"
    "v_mfma_f32_16x16x32_bf16 v[76:79], a[112:115], v[156:159], v[76:79]\n\t"
    "v_fma_mix_f32 v248, v170, v232, v240 op_sel:[0,0,0] op_sel_hi:[1,0,0]\n\t"
    "v_fma_mix_f32 v249, v170, v233, v241 op_sel:[1,0,0] op_sel_hi:[1,0,0]\n\t"
    "v_mfma_f32_16x16x32_bf16 v[80:83], a[116:119], v[144:147], v[80:83]\n\t"
    "ds_read_b64_tr_b16 a[140:141], v197 offset:16392\n\t"
    "v_fma_mix_f32 v250, v171, v234, v242 op_sel:[0,0,0] op_sel_hi:[1,0,0]\n\t"
    "v_fma_mix_f32 v251, v171, v235, v243 op_sel:[1,0,0] op_sel_hi:[1,0,0]\n\t"
    "v_mfma_f32_16x16x32_bf16 v[84:87], a[116:119], v[148:151], v[84:87]\n\t"
    "ds_read_b64_tr_b16 a[142:143], v197 offset:17416\n\t"
    "v_cvt_pk_bf16_f32 v168, v244, v245\n\t"
    "v_cvt_pk_bf16_f32 v169, v246, v247\n\t"
    "s_mov_b32 m0, s62\n\t"
    "v_mfma_f32_16x16x32_bf16 v[88:91], a[116:119], v[152:155], v[88:91]\n\t"
    "buffer_load_dwordx4 v225, s[40:43], s57 offen offset:3072 lds\n\t"
    "s_add_u32 s57, s57, 0x4000\n\t"
    "v_cvt_pk_bf16_f32 v170, v248, v249\n\t"
    "v_cvt_pk_bf16_f32 v171, v250, v251\n\t"
    "v_mfma_f32_16x16x32_bf16 v[92:95], a[116:119], v[156:159], v[92:95]\n\t"
    "s_waitcnt vmcnt(18)\n\t"
    "v_fma_mix_f32 v244, v172, v228, v236 op_sel:[0,0,0] op_sel_hi:[1,0,0]\n\t"
    "v_fma_mix_f32 v245, v172, v229, v237 op_sel:[1,0,0] op_sel_hi:[1,0,0]\n\t"
    "v_mfma_f32_16x16x32_bf16 v[96:99], a[120:123], v[144:147], v[96:99]\n\t"
    "ds_read_b64_tr_b16 a[144:145], v198 offset:16384\n\t"
    "v_fma_mix_f32 v246, v173, v230, v238 op_sel:[0,0,0] op_sel_hi:[1,0,0]\n\t"
    "v_fma_mix_f32 v247, v173, v231, v239 op_sel:[1,0,0] op_sel_hi:[1,0,0]\n\t"
    "v_mfma_f32_16x16x32_bf16 v[100:103], a[120:123], v[148:151], v[100:103]\n\t"
    "ds_read_b64_tr_b16 a[146:147], v198 offset:17408\n\t"
    "v_fma_mix_f32 v248, v174, v232, v240 op_sel:[0,0,0] op_sel_hi:[1,0,0]\n\t"
    "v_fma_mix_f32 v249, v174, v233, v241 op_sel:[1,0,0] op_sel_hi:[1,0,0]\n\t"
    "s_add_u32 m0, s61, 2048\n\t"
    "s_mov_b32 s74, 3211264\n\t"
    "v_mfma_f32_16x16x32_bf16 v[104:107], a[120:123], v[152:155], v[104:107]\n\t"
    "buffer_load_dwordx4 v253, s[44:47], s74 offen lds\n\t"
    "v_fma_mix_f32 v250, v175, v234, v242 op_sel:[0,0,0] op_sel_hi:[1,0,0]\n\t"
    "v_fma_mix_f32 v251, v175, v235, v243 op_sel:[1,0,0] op_sel_hi:[1,0,0]\n\t"
    "v_mfma_f32_16x16x32_bf16 v[108:111], a[120:123], v[156:159], v[108:111]\n\t"
    "v_cvt_pk_bf16_f32 v172, v244, v245\n\t"
    "v_cvt_pk_bf16_f32 v173, v246, v247\n\t"
    "s_waitcnt lgkmcnt(10)\n\t"
    "v_mfma_f32_16x16x32_bf16 v[112:115], a[124:127], v[144:147], v[112:115]\n\t"
    "ds_read_b64_tr_b16 a[148:149], v198 offset:16392\n\t"
    "v_cvt_pk_bf16_f32 v174, v248, v249\n\t"
    "v_cvt_pk_bf16_f32 v175, v250, v251\n\t"
    "v_mfma_f32_16x16x32_bf16 v[116:119], a[124:127], v[148:151], v[116:119]\n\t"
    "ds_read_b64_tr_b16 a[150:151], v198 offset:17416\n\t"
    "s_add_u32 m0, s61, 3072\n\t"
    "s_mov_b32 s74, 3244032\n\t"
    "v_mfma_f32_16x16x32_bf16 v[120:123], a[124:127], v[152:155], v[120:123]\n\t"
    "buffer_load_dwordx4 v253, s[44:47], s74 offen lds\n\t"
    "v_mfma_f32_16x16x32_bf16 v[124:127], a[124:127], v[156:159], v[124:127]\n\t"
    "s_waitcnt lgkmcnt(10)\n\t"
    "v_mfma_f32_16x16x32_bf16 v[0:3], a[128:131], v[160:163], v[0:3]\n\t"
    "ds_read_b64_tr_b16 a[152:153], v199 offset:16384\n\t"
    "ds_read_b128 v[228:231], v200 offset:1792\n\t"
    "ds_read_b128 v[232:235], v200 offset:1808\n\t"
    "v_mfma_f32_16x16x32_bf16 v[4:7], a[128:131], v[164:167], v[4:7]\n\t"
    "ds_read_b64_tr_b16 a[154:155], v199 offset:17408\n\t"
    "ds_read_b128 v[236:239], v200 offset:1824\n\t"
    "ds_read_b128 v[240:243], v200 offset:1840\n\t"
    "s_mov_b32 s75, 8192\n\t"
    "v_mfma_f32_16x16x32_bf16 v[8:11], a[128:131], v[168:171], v[8:11]\n\t"
    "buffer_load_dwordx4 v[144:147], v226, s[48:51], s75 offen offset:1024\n\t"
    "v_mfma_f32_16x16x32_bf16 v[12:15], a[128:131], v[172:175], v[12:15]\n\t"
    "s_waitcnt lgkmcnt(14)\n\t"
    "v_mfma_f32_16x16x32_bf16 v[16:19], a[132:135], v[160:163], v[16:19]\n\t"
    "ds_read_b64_tr_b16 a[156:157], v199 offset:16392\n\t"
    "v_mfma_f32_16x16x32_bf16 v[20:23], a[132:135], v[164:167], v[20:23]\n\t"
    "ds_read_b64_tr_b16 a[158:159], v199 offset:17416\n\t"
    "s_waitcnt lgkmcnt(2)\n\t"
    "s_waitcnt vmcnt(16)\n\t"
    "v_fma_mix_f32 v244, v176, v228, v236 op_sel:[0,0,0] op_sel_hi:[1,0,0]\n\t"
    "v_fma_mix_f32 v245, v176, v229, v237 op_sel:[1,0,0] op_sel_hi:[1,0,0]\n\t"
    "s_mov_b32 s75, 24576\n\t"
    "v_mfma_f32_16x16x32_bf16 v[24:27], a[132:135], v[168:171], v[24:27]\n\t"
    "buffer_load_dwordx4 v[148:151], v226, s[48:51], s75 offen offset:1024\n\t"
    "v_fma_mix_f32 v246, v177, v230, v238 op_sel:[0,0,0] op_sel_hi:[1,0,0]\n\t"
    "v_fma_mix_f32 v247, v177, v231, v239 op_sel:[1,0,0] op_sel_hi:[1,0,0]\n\t"
    "v_mfma_f32_16x16x32_bf16 v[28:31], a[132:135], v[172:175], v[28:31]\n\t"
    "v_fma_mix_f32 v248, v178, v232, v240 op_sel:[0,0,0] op_sel_hi:[1,0,0]\n\t"
    "v_fma_mix_f32 v249, v178, v233, v241 op_sel:[1,0,0] op_sel_hi:[1,0,0]\n\t"
    "v_mfma_f32_16x16x32_bf16 v[32:35], a[136:139], v[160:163], v[32:35]\n\t"
    "ds_read_b64_tr_b16 a[160:161], v196 offset:24576\n\t"
    "v_fma_mix_f32 v250, v179, v234, v242 op_sel:[0,0,0] op_sel_hi:[1,0,0]\n\t"
    "v_fma_mix_f32 v251, v179, v235, v243 op_sel:[1,0,0] op_sel_hi:[1,0,0]\n\t"
    "v_mfma_f32_16x16x32_bf16 v[36:39], a[136:139], v[164:167], v[36:39]\n\t"
    "ds_read_b64_tr_b16 a[162:163], v196 offset:25600\n\t"
    "v_cvt_pk_bf16_f32 v176, v244, v245\n\t"
    "v_cvt_pk_bf16_f32 v177, v246, v247\n\t"
    "s_mov_b32 s75, 40960\n\t"
    "v_mfma_f32_16x16x32_bf16 v[40:43], a[136:139], v[168:171], v[40:43]\n\t"
    "buffer_load_dwordx4 v[152:155], v226, s[48:51], s75 offen offset:1024\n\t"
    "v_cvt_pk_bf16_f32 v178, v248, v249\n\t"
    "v_cvt_pk_bf16_f32 v179, v250, v251\n\t"
    "v_mfma_f32_16x16x32_bf16 v[44:47], a[136:139], v[172:175], v[44:47]\n\t"
    "s_waitcnt vmcnt(17)\n\t"
    "v_fma_mix_f32 v244, v180, v228, v236 op_sel:[0,0,0] op_sel_hi:[1,0,0]\n\t"
    "v_fma_mix_f32 v245, v180, v229, v237 op_sel:[1,0,0] op_sel_hi:[1,0,0]\n\t"
    "v_mfma_f32_16x16x32_bf16 v[48:51], a[140:143], v[160:163], v[48:51]\n\t"
    "ds_read_b64_tr_b16 a[164:165], v196 offset:24584\n\t"
    "v_fma_mix_f32 v246, v181, v230, v238 op_sel:[0,0,0] op_sel_hi:[1,0,0]\n\t"
    "v_fma_mix_f32 v247, v181, v231, v239 op_sel:[1,0,0] op_sel_hi:[1,0,0]\n\t"
    "v_mfma_f32_16x16x32_bf16 v[52:55], a[140:143], v[164:167], v[52:55]\n\t"
    "ds_read_b64_tr_b16 a[166:167], v196 offset:25608\n\t"
    "v_fma_mix_f32 v248, v182, v232, v240 op_sel:[0,0,0] op_sel_hi:[1,0,0]\n\t"
    "v_fma_mix_f32 v249, v182, v233, v241 op_sel:[1,0,0] op_sel_hi:[1,0,0]\n\t"
    "s_mov_b32 s75, 57344\n\t"
    "v_mfma_f32_16x16x32_bf16 v[56:59], a[140:143], v[168:171], v[56:59]\n\t"
    "buffer_load_dwordx4 v[156:159], v226, s[48:51], s75 offen offset:1024\n\t"
    "v_fma_mix_f32 v250, v183, v234, v242 op_sel:[0,0,0] op_sel_hi:[1,0,0]\n\t"
    "v_fma_mix_f32 v251, v183, v235, v243 op_sel:[1,0,0] op_sel_hi:[1,0,0]\n\t"
    "v_mfma_f32_16x16x32_bf16 v[60:63], a[140:143], v[172:175], v[60:63]\n\t"
    "v_cvt_pk_bf16_f32 v180, v244, v245\n\t"
    "v_cvt_pk_bf16_f32 v181, v246, v247\n\t"
    "v_mfma_f32_16x16x32_bf16 v[64:67], a[144:147], v[160:163], v[64:67]\n\t"
    "ds_read_b64_tr_b16 a[168:169], v197 offset:24576\n\t"
    "v_cvt_pk_bf16_f32 v182, v248, v249\n\t"
    "v_cvt_pk_bf16_f32 v183, v250, v251\n\t"
    "v_mfma_f32_16x16x32_bf16 v[68:71], a[144:147], v[164:167], v[68:71]\n\t"
    "ds_read_b64_tr_b16 a[170:171], v197 offset:25600\n\t"
    "s_waitcnt vmcnt(17)\n\t"
    "v_fma_mix_f32 v244, v184, v228, v236 op_sel:[0,0,0] op_sel_hi:[1,0,0]\n\t"
    "v_fma_mix_f32 v245, v184, v229, v237 op_sel:[1,0,0] op_sel_hi:[1,0,0]\n\t"
    "s_add_u32 m0, s65, 0\n\t"
    "s_mov_b32 s74, 3670016\n\t"
    "v_mfma_f32_16x16x32_bf16 v[72:75], a[144:147], v[168:171], v[72:75]\n\t"
    "buffer_load_dwordx4 v252, s[44:47], s74 offen lds\n\t"
    "v_fma_mix_f32 v246, v185, v230, v238 op_sel:[0,0,0] op_sel_hi:[1,0,0]\n\t"
    "v_fma_mix_f32 v247, v185, v231, v239 op_sel:[1,0,0] op_sel_hi:[1,0,0]\n\t"
    "v_mfma_f32_16x16x32_bf16 v[76:79], a[144:147], v[172:175], v[76:79]\n\t"
    "v_fma_mix_f32 v248, v186, v232, v240 op_sel:[0,0,0] op_sel_hi:[1,0,0]\n\t"
    "v_fma_mix_f32 v249, v186, v233, v241 op_sel:[1,0,0] op_sel_hi:[1,0,0]\n\t"
    "v_mfma_f32_16x16x32_bf16 v[80:83], a[148:151], v[160:163], v[80:83]\n\t"
    "ds_read_b64_tr_b16 a[172:173], v197 offset:24584\n\t"
    "v_fma_mix_f32 v250, v187, v234, v242 op_sel:[0,0,0] op_sel_hi:[1,0,0]\n\t"
    "v_fma_mix_f32 v251, v187, v235, v243 op_sel:[1,0,0] op_sel_hi:[1,0,0]\n\t"
    "v_mfma_f32_16x16x32_bf16 v[84:87], a[148:151], v[164:167], v[84:87]\n\t"
    "ds_read_b64_tr_b16 a[174:175], v197 offset:25608\n\t"
    "v_cvt_pk_bf16_f32 v184, v244, v245\n\t"
    "v_cvt_pk_bf16_f32 v185, v246, v247\n\t"
    "s_add_u32 m0, s65, 1024\n\t"
    "s_mov_b32 s74, 3702784\n\t"
    "v_mfma_f32_16x16x32_bf16 v[88:91], a[148:151], v[168:171], v[88:91]\n\t"
    "buffer_load_dwordx4 v252, s[44:47], s74 offen lds\n\t"
    "v_cvt_pk_bf16_f32 v186, v248, v249\n\t"
    "v_cvt_pk_bf16_f32 v187, v250, v251\n\t"
    "v_mfma_f32_16x16x32_bf16 v[92:95], a[148:151], v[172:175], v[92:95]\n\t"
    "s_waitcnt vmcnt(18)\n\t"
    "v_fma_mix_f32 v244, v188, v228, v236 op_sel:[0,0,0] op_sel_hi:[1,0,0]\n\t"
    "v_fma_mix_f32 v245, v188, v229, v237 op_sel:[1,0,0] op_sel_hi:[1,0,0]\n\t"
    "s_barrier\n\t"
    "v_mfma_f32_16x16x32_bf16 v[96:99], a[152:155], v[160:163], v[96:99]\n\t"
    "ds_read_b64_tr_b16 a[176:177], v198 offset:24576\n\t"
    "v_fma_mix_f32 v246, v189, v230, v238 op_sel:[0,0,0] op_sel_hi:[1,0,0]\n\t"
    "v_fma_mix_f32 v247, v189, v231, v239 op_sel:[1,0,0] op_sel_hi:[1,0,0]\n\t"
    "v_mfma_f32_16x16x32_bf16 v[100:103], a[152:155], v[164:167], v[100:103]\n\t"
    "ds_read_b64_tr_b16 a[178:179], v198 offset:25600\n\t"
    "v_fma_mix_f32 v248, v190, v232, v240 op_sel:[0,0,0] op_sel_hi:[1,0,0]\n\t"
    "v_fma_mix_f32 v249, v190, v233, v241 op_sel:[1,0,0] op_sel_hi:[1,0,0]\n\t"
    "s_mov_b32 m0, s59\n\t"
    "v_mfma_f32_16x16x32_bf16 v[104:107], a[152:155], v[168:171], v[104:107]\n\t"
    "buffer_load_dwordx4 v220, s[36:39], s56 offen offset:0 lds\n\t"
    "v_fma_mix_f32 v250, v191, v234, v242 op_sel:[0,0,0] op_sel_hi:[1,0,0]\n\t"
    "v_fma_mix_f32 v251, v191, v235, v243 op_sel:[1,0,0] op_sel_hi:[1,0,0]\n\t"
    "v_mfma_f32_16x16x32_bf16 v[108:111], a[152:155], v[172:175], v[108:111]\n\t"
    "v_cvt_pk_bf16_f32 v188, v244, v245\n\t"
    "v_cvt_pk_bf16_f32 v189, v246, v247\n\t"
    "s_waitcnt lgkmcnt(10)\n\t"
    "v_mfma_f32_16x16x32_bf16 v[112:115], a[156:159], v[160:163], v[112:115]\n\t"
    "ds_read_b64_tr_b16 a[180:181], v198 offset:24584\n\t"
    "v_cvt_pk_bf16_f32 v190, v248, v249\n\t"
    "v_cvt_pk_bf16_f32 v191, v250, v251\n\t"
    "v_mfma_f32_16x16x32_bf16 v[116:119], a[156:159], v[164:167], v[116:119]\n\t"
    "ds_read_b64_tr_b16 a[182:183], v198 offset:25608\n\t"
    "s_mov_b32 m0, s59\n\t"
    "v_mfma_f32_16x16x32_bf16 v[120:123], a[156:159], v[168:171], v[120:123]\n\t"
    "buffer_load_dwordx4 v221, s[36:39], s56 offen offset:1024 lds\n\t"
    "v_mfma_f32_16x16x32_bf16 v[124:127], a[156:159], v[172:175], v[124:127]\n\t"
    "s_waitcnt lgkmcnt(10)\n\t"
    "v_mfma_f32_16x16x32_bf16 v[0:3], a[160:163], v[176:179], v[0:3]\n\t"
    "ds_read_b64_tr_b16 a[184:185], v199 offset:24576\n\t"
    "ds_read_b128 v[228:231], v200 offset:2048\n\t"
    "ds_read_b128 v[232:235], v200 offset:2064\n\t"
    "v_mfma_f32_16x16x32_bf16 v[4:7], a[160:163], v[180:183], v[4:7]\n\t"
    "ds_read_b64_tr_b16 a[186:187], v199 offset:25600\n\t"
    "ds_read_b128 v[236:239], v200 offset:2080\n\t"
    "ds_read_b128 v[240:243], v200 offset:2096\n\t"
    "s_mov_b32 s75, 8192\n\t"
    "v_mfma_f32_16x16x32_bf16 v[8:11], a[160:163], v[184:187], v[8:11]\n\t"
    "buffer_load_dwordx4 v[160:163], v226, s[48:51], s75 offen offset:2048\n\t"
    "v_mfma_f32_16x16x32_bf16 v[12:15], a[160:163], v[188:191], v[12:15]\n\t"
    "s_waitcnt lgkmcnt(14)\n\t"
    "v_mfma_f32_16x16x32_bf16 v[16:19], a[164:167], v[176:179], v[16:19]\n\t"
    "ds_read_b64_tr_b16 a[188:189], v199 offset:24584\n\t"
    "v_mfma_f32_16x16x32_bf16 v[20:23], a[164:167], v[180:183], v[20:23]\n\t"
    "ds_read_b64_tr_b16 a[190:191], v199 offset:25608\n\t"
    "s_waitcnt lgkmcnt(2)\n\t"
    "s_waitcnt vmcnt(16)\n\t"
    "v_fma_mix_f32 v244, v128, v228, v236 op_sel:[0,0,0] op_sel_hi:[1,0,0]\n\t"
    "v_fma_mix_f32 v245, v128, v229, v237 op_sel:[1,0,0] op_sel_hi:[1,0,0]\n\t"
    "s_mov_b32 s75, 24576\n\t"
    "v_mfma_f32_16x16x32_bf16 v[24:27], a[164:167], v[184:187], v[24:27]\n\t"
    "buffer_load_dwordx4 v[164:167], v226, s[48:51], s75 offen offset:2048\n\t"
    "v_fma_mix_f32 v246, v129, v230, v238 op_sel:[0,0,0] op_sel_hi:[1,0,0]\n\t"
    "v_fma_mix_f32 v247, v129, v231, v239 op_sel:[1,0,0] op_sel_hi:[1,0,0]\n\t"
    "v_mfma_f32_16x16x32_bf16 v[28:31], a[164:167], v[188:191], v[28:31]\n\t"
    "v_fma_mix_f32 v248, v130, v232, v240 op_sel:[0,0,0] op_sel_hi:[1,0,0]\n\t"
    "v_fma_mix_f32 v249, v130, v233, v241 op_sel:[1,0,0] op_sel_hi:[1,0,0]\n\t"
    "v_mfma_f32_16x16x32_bf16 v[32:35], a[168:171], v[176:179], v[32:35]\n\t"
    "ds_read_b64_tr_b16 a[64:65], v192 offset:32768\n\t"
    "v_fma_mix_f32 v250, v131, v234, v242 op_sel:[0,0,0] op_sel_hi:[1,0,0]\n\t"
    "v_fma_mix_f32 v251, v131, v235, v243 op_sel:[1,0,0] op_sel_hi:[1,0,0]\n\t"
    "v_mfma_f32_16x16x32_bf16 v[36:39], a[168:171], v[180:183], v[36:39]\n\t"
    "ds_read_b64_tr_b16 a[66:67], v192 offset:33792\n\t"
    "v_cvt_pk_bf16_f32 v128, v244, v245\n\t"
    "v_cvt_pk_bf16_f32 v129, v246, v247\n\t"
    "s_mov_b32 s75, 40960\n\t"
    "v_mfma_f32_16x16x32_bf16 v[40:43], a[168:171], v[184:187], v[40:43]\n\t"
    "buffer_load_dwordx4 v[168:171], v226, s[48:51], s75 offen offset:2048\n\t"
    "v_cvt_pk_bf16_f32 v130, v248, v249\n\t"
    "v_cvt_pk_bf16_f32 v131, v250, v251\n\t"
    "v_mfma_f32_16x16x32_bf16 v[44:47], a[168:171], v[188:191], v[44:47]\n\t"
    "s_waitcnt vmcnt(17)\n\t"
    "v_fma_mix_f32 v244, v132, v228, v236 op_sel:[0,0,0] op_sel_hi:[1,0,0]\n\t"
    "v_fma_mix_f32 v245, v132, v229, v237 op_sel:[1,0,0] op_sel_hi:[1,0,0]\n\t"
    "v_mfma_f32_16x16x32_bf16 v[48:51], a[172:175], v[176:179], v[48:51]\n\t"
    "ds_read_b64_tr_b16 a[68:69], v192 offset:32776\n\t"
    "v_fma_mix_f32 v246, v133, v230, v238 op_sel:[0,0,0] op_sel_hi:[1,0,0]\n\t"
    "v_fma_mix_f32 v247, v133, v231, v239 op_sel:[1,0,0] op_sel_hi:[1,0,0]\n\t"
    "v_mfma_f32_16x16x32_bf16 v[52:55], a[172:175], v[180:183], v[52:55]\n\t"
    "ds_read_b64_tr_b16 a[70:71], v192 offset:33800\n\t"
    "v_fma_mix_f32 v248, v134, v232, v240 op_sel:[0,0,0] op_sel_hi:[1,0,0]\n\t"
    "v_fma_mix_f32 v249, v134, v233, v241 op_sel:[1,0,0] op_sel_hi:[1,0,0]\n\t"
    "s_mov_b32 s75, 57344\n\t"
    "v_mfma_f32_16x16x32_bf16 v[56:59], a[172:175], v[184:187], v[56:59]\n\t"
    "buffer_load_dwordx4 v[172:175], v226, s[48:51], s75 offen offset:2048\n\t"
    "v_fma_mix_f32 v250, v135, v234, v242 op_sel:[0,0,0] op_sel_hi:[1,0,0]\n\t"
    "v_fma_mix_f32 v251, v135, v235, v243 op_sel:[1,0,0] op_sel_hi:[1,0,0]\n\t"
    "v_mfma_f32_16x16x32_bf16 v[60:63], a[172:175], v[188:191], v[60:63]\n\t"
    "v_cvt_pk_bf16_f32 v132, v244, v245\n\t"
    "v_cvt_pk_bf16_f32 v133, v246, v247\n\t"
    "v_mfma_f32_16x16x32_bf16 v[64:67], a[176:179], v[176:179], v[64:67]\n\t"
    "ds_read_b64_tr_b16 a[72:73], v193 offset:32768\n\t"
    "v_cvt_pk_bf16_f32 v134, v248, v249\n\t"
    "v_cvt_pk_bf16_f32 v135, v250, v251\n\t"
    "v_mfma_f32_16x16x32_bf16 v[68:71], a[176:179], v[180:183], v[68:71]\n\t"
    "ds_read_b64_tr_b16 a[74:75], v193 offset:33792\n\t"
    "s_waitcnt vmcnt(17)\n\t"
    "v_fma_mix_f32 v244, v136, v228, v236 op_sel:[0,0,0] op_sel_hi:[1,0,0]\n\t"
    "v_fma_mix_f32 v245, v136, v229, v237 op_sel:[1,0,0] op_sel_hi:[1,0,0]\n\t"
    "s_mov_b32 m0, s59\n\t"
    "v_mfma_f32_16x16x32_bf16 v[72:75], a[176:179], v[184:187], v[72:75]\n\t"
    "buffer_load_dwordx4 v222, s[36:39], s56 offen offset:2048 lds\n\t"
    "v_fma_mix_f32 v246, v137, v230, v238 op_sel:[0,0,0] op_sel_hi:[1,0,0]\n\t"
    "v_fma_mix_f32 v247, v137, v231, v239 op_sel:[1,0,0] op_sel_hi:[1,0,0]\n\t"
    "v_mfma_f32_16x16x32_bf16 v[76:79], a[176:179], v[188:191], v[76:79]\n\t"
    "v_fma_mix_f32 v248, v138, v232, v240 op_sel:[0,0,0] op_sel_hi:[1,0,0]\n\t"
    "v_fma_mix_f32 v249, v138, v233, v241 op_sel:[1,0,0] op_sel_hi:[1,0,0]\n\t"
    "v_mfma_f32_16x16x32_bf16 v[80:83], a[180:183], v[176:179], v[80:83]\n\t"
    "ds_read_b64_tr_b16 a[76:77], v193 offset:32776\n\t"
    "v_fma_mix_f32 v250, v139, v234, v242 op_sel:[0,0,0] op_sel_hi:[1,0,0]\n\t"
    "v_fma_mix_f32 v251, v139, v235, v243 op_sel:[1,0,0] op_sel_hi:[1,0,0]\n\t"
    "v_mfma_f32_16x16x32_bf16 v[84:87], a[180:183], v[180:183], v[84:87]\n\t"
    "ds_read_b64_tr_b16 a[78:79], v193 offset:33800\n\t"
    "v_cvt_pk_bf16_f32 v136, v244, v245\n\t"
    "v_cvt_pk_bf16_f32 v137, v246, v247\n\t"
    "s_mov_b32 m0, s59\n\t"
    "v_mfma_f32_16x16x32_bf16 v[88:91], a[180:183], v[184:187], v[88:91]\n\t"
    "buffer_load_dwordx4 v223, s[36:39], s56 offen offset:3072 lds\n\t"
    "s_add_u32 s56, s56, 0x4000\n\t"
    "v_cvt_pk_bf16_f32 v138, v248, v249\n\t"
    "v_cvt_pk_bf16_f32 v139, v250, v251\n\t"
    "v_mfma_f32_16x16x32_bf16 v[92:95], a[180:183], v[188:191], v[92:95]\n\t"
    "s_waitcnt vmcnt(18)\n\t"
    "v_fma_mix_f32 v244, v140, v228, v236 op_sel:[0,0,0] op_sel_hi:[1,0,0]\n\t"
    "v_fma_mix_f32 v245, v140, v229, v237 op_sel:[1,0,0] op_sel_hi:[1,0,0]\n\t"
    "v_mfma_f32_16x16x32_bf16 v[96:99], a[184:187], v[176:179], v[96:99]\n\t"
    "ds_read_b64_tr_b16 a[80:81], v194 offset:32768\n\t"
    "v_fma_mix_f32 v246, v141, v230, v238 op_sel:[0,0,0] op_sel_hi:[1,0,0]\n\t"
    "v_fma_mix_f32 v247, v141, v231, v239 op_sel:[1,0,0] op_sel_hi:[1,0,0]\n\t"
    "v_mfma_f32_16x16x32_bf16 v[100:103], a[184:187], v[180:183], v[100:103]\n\t"
    "ds_read_b64_tr_b16 a[82:83], v194 offset:33792\n\t"
    "v_fma_mix_f32 v248, v142, v232, v240 op_sel:[0,0,0] op_sel_hi:[1,0,0]\n\t"
    "v_fma_mix_f32 v249, v142, v233, v241 op_sel:[1,0,0] op_sel_hi:[1,0,0]\n\t"
    "s_add_u32 m0, s65, 2048\n\t"
    "s_mov_b32 s74, 3735552\n\t"
    "v_mfma_f32_16x16x32_bf16 v[104:107], a[184:187], v[184:187], v[104:107]\n\t"
    "buffer_load_dwordx4 v253, s[44:47], s74 offen lds\n\t"
    "v_fma_mix_f32 v250, v143, v234, v242 op_sel:[0,0,0] op_sel_hi:[1,0,0]\n\t"
    "v_fma_mix_f32 v251, v143, v235, v243 op_sel:[1,0,0] op_sel_hi:[1,0,0]\n\t"
    "v_mfma_f32_16x16x32_bf16 v[108:111], a[184:187], v[188:191], v[108:111]\n\t"
    "v_cvt_pk_bf16_f32 v140, v244, v245\n\t"
    "v_cvt_pk_bf16_f32 v141, v246, v247\n\t"
    "s_waitcnt lgkmcnt(10)\n\t"
    "v_mfma_f32_16x16x32_bf16 v[112:115], a[188:191], v[176:179], v[112:115]\n\t"
    "ds_read_b64_tr_b16 a[84:85], v194 offset:32776\n\t"
    "v_cvt_pk_bf16_f32 v142, v248, v249\n\t"
    "v_cvt_pk_bf16_f32 v143, v250, v251\n\t"
    "v_mfma_f32_16x16x32_bf16 v[116:119], a[188:191], v[180:183], v[116:119]\n\t"
    "ds_read_b64_tr_b16 a[86:87], v194 offset:33800\n\t"
    "s_add_u32 m0, s65, 3072\n\t"
    "s_mov_b32 s74, 3768320\n\t"
    "v_mfma_f32_16x16x32_bf16 v[120:123], a[188:191], v[184:187], v[120:123]\n\t"
    "buffer_load_dwordx4 v253, s[44:47], s74 offen lds\n\t"
    "v_mfma_f32_16x16x32_bf16 v[124:127], a[188:191], v[188:191], v[124:127]\n\t"
    "s_waitcnt lgkmcnt(10)\n\t"
    "v_mfma_f32_16x16x32_bf16 v[0:3], a[64:67], v[128:131], v[0:3]\n\t"
    "ds_read_b64_tr_b16 a[88:89], v195 offset:32768\n\t"
    "ds_read_b128 v[228:231], v200 offset:2304\n\t"
    "ds_read_b128 v[232:235], v200 offset:2320\n\t"
    "v_mfma_f32_16x16x32_bf16 v[4:7], a[64:67], v[132:135], v[4:7]\n\t"
    "ds_read_b64_tr_b16 a[90:91], v195 offset:33792\n\t"
    "ds_read_b128 v[236:239], v200 offset:2336\n\t"
    "ds_read_b128 v[240:243], v200 offset:2352\n\t"
    "s_mov_b32 s75, 8192\n\t"
    "v_mfma_f32_16x16x32_bf16 v[8:11], a[64:67], v[136:139], v[8:11]\n\t"
    "buffer_load_dwordx4 v[176:179], v226, s[48:51], s75 offen offset:3072\n\t"
    "v_mfma_f32_16x16x32_bf16 v[12:15], a[64:67], v[140:143], v[12:15]\n\t"
    "s_waitcnt lgkmcnt(14)\n\t"
    "v_mfma_f32_16x16x32_bf16 v[16:19], a[68:71], v[128:131], v[16:19]\n\t"
    "ds_read_b64_tr_b16 a[92:93], v195 offset:32776\n\t"
    "v_mfma_f32_16x16x32_bf16 v[20:23], a[68:71], v[132:135], v[20:23]\n\t"
    "ds_read_b64_tr_b16 a[94:95], v195 offset:33800\n\t"
    "s_waitcnt lgkmcnt(2)\n\t"
    "s_waitcnt vmcnt(16)\n\t"
    "v_fma_mix_f32 v244, v144, v228, v236 op_sel:[0,0,0] op_sel_hi:[1,0,0]\n\t"
    "v_fma_mix_f32 v245, v144, v229, v237 op_sel:[1,0,0] op_sel_hi:[1,0,0]\n\t"
    "s_mov_b32 s75, 24576\n\t"
    "v_mfma_f32_16x16x32_bf16 v[24:27], a[68:71], v[136:139], v[24:27]\n\t"
    "buffer_load_dwordx4 v[180:183], v226, s[48:51], s75 offen offset:3072\n\t"
    "v_fma_mix_f32 v246, v145, v230, v238 op_sel:[0,0,0] op_sel_hi:[1,0,0]\n\t"
    "v_fma_mix_f32 v247, v145, v231, v239 op_sel:[1,0,0] op_sel_hi:[1,0,0]\n\t"
    "v_mfma_f32_16x16x32_bf16 v[28:31], a[68:71], v[140:143], v[28:31]\n\t"
    "v_fma_mix_f32 v248, v146, v232, v240 op_sel:[0,0,0] op_sel_hi:[1,0,0]\n\t"
    "v_fma_mix_f32 v249, v146, v233, v241 op_sel:[1,0,0] op_sel_hi:[1,0,0]\n\t"
    "v_mfma_f32_16x16x32_bf16 v[32:35], a[72:75], v[128:131], v[32:35]\n\t"
    "ds_read_b64_tr_b16 a[96:97], v192 offset:40960\n\t"
    "v_fma_mix_f32 v250, v147, v234, v242 op_sel:[0,0,0] op_sel_hi:[1,0,0]\n\t"
    "v_fma_mix_f32 v251, v147, v235, v243 op_sel:[1,0,0] op_sel_hi:[1,0,0]\n\t"
    "v_mfma_f32_16x16x32_bf16 v[36:39], a[72:75], v[132:135], v[36:39]\n\t"
    "ds_read_b64_tr_b16 a[98:99], v192 offset:41984\n\t"
    "v_cvt_pk_bf16_f32 v144, v244, v245\n\t"
    "v_cvt_pk_bf16_f32 v145, v246, v247\n\t"
    "s_mov_b32 s75, 40960\n\t"
    "v_mfma_f32_16x16x32_bf16 v[40:43], a[72:75], v[136:139], v[40:43]\n\t"
    "buffer_load_dwordx4 v[184:187], v226, s[48:51], s75 offen offset:3072\n\t"
    "v_cvt_pk_bf16_f32 v146, v248, v249\n\t"
    "v_cvt_pk_bf16_f32 v147, v250, v251\n\t"
    "v_mfma_f32_16x16x32_bf16 v[44:47], a[72:75], v[140:143], v[44:47]\n\t"
    "s_waitcnt vmcnt(17)\n\t"
    "v_fma_mix_f32 v244, v148, v228, v236 op_sel:[0,0,0] op_sel_hi:[1,0,0]\n\t"
    "v_fma_mix_f32 v245, v148, v229, v237 op_sel:[1,0,0] op_sel_hi:[1,0,0]\n\t"
    "v_mfma_f32_16x16x32_bf16 v[48:51], a[76:79], v[128:131], v[48:51]\n\t"
    "ds_read_b64_tr_b16 a[100:101], v192 offset:40968\n\t"
    "v_fma_mix_f32 v246, v149, v230, v238 op_sel:[0,0,0] op_sel_hi:[1,0,0]\n\t"
    "v_fma_mix_f32 v247, v149, v231, v239 op_sel:[1,0,0] op_sel_hi:[1,0,0]\n\t"
    "v_mfma_f32_16x16x32_bf16 v[52:55], a[76:79], v[132:135], v[52:55]\n\t"
    "ds_read_b64_tr_b16 a[102:103], v192 offset:41992\n\t"
    "v_fma_mix_f32 v248, v150, v232, v240 op_sel:[0,0,0] op_sel_hi:[1,0,0]\n\t"
    "v_fma_mix_f32 v249, v150, v233, v241 op_sel:[1,0,0] op_sel_hi:[1,0,0]\n\t"
    "s_mov_b32 s75, 57344\n\t"
    "v_mfma_f32_16x16x32_bf16 v[56:59], a[76:79], v[136:139], v[56:59]\n\t"
    "buffer_load_dwordx4 v[188:191], v226, s[48:51], s75 offen offset:3072\n\t"
    "v_fma_mix_f32 v250, v151, v234, v242 op_sel:[0,0,0] op_sel_hi:[1,0,0]\n\t"
    "v_fma_mix_f32 v251, v151, v235, v243 op_sel:[1,0,0] op_sel_hi:[1,0,0]\n\t"
    "v_mfma_f32_16x16x32_bf16 v[60:63], a[76:79], v[140:143], v[60:63]\n\t"
    "v_cvt_pk_bf16_f32 v148, v244, v245\n\t"
    "v_cvt_pk_bf16_f32 v149, v246, v247\n\t"
    "v_mfma_f32_16x16x32_bf16 v[64:67], a[80:83], v[128:131], v[64:67]\n\t"
    "ds_read_b64_tr_b16 a[104:105], v193 offset:40960\n\t"
    "v_cvt_pk_bf16_f32 v150, v248, v249\n\t"
    "v_cvt_pk_bf16_f32 v151, v250, v251\n\t"
    "v_mfma_f32_16x16x32_bf16 v[68:71], a[80:83], v[132:135], v[68:71]\n\t"
    "ds_read_b64_tr_b16 a[106:107], v193 offset:41984\n\t"
    "s_waitcnt vmcnt(17)\n\t"
    "v_fma_mix_f32 v244, v152, v228, v236 op_sel:[0,0,0] op_sel_hi:[1,0,0]\n\t"
    "v_fma_mix_f32 v245, v152, v229, v237 op_sel:[1,0,0] op_sel_hi:[1,0,0]\n\t"
    "v_mfma_f32_16x16x32_bf16 v[72:75], a[80:83], v[136:139], v[72:75]\n\t"
    "v_fma_mix_f32 v246, v153, v230, v238 op_sel:[0,0,0] op_sel_hi:[1,0,0]\n\t"
    "v_fma_mix_f32 v247, v153, v231, v239 op_sel:[1,0,0] op_sel_hi:[1,0,0]\n\t"
    "v_mfma_f32_16x16x32_bf16 v[76:79], a[80:83], v[140:143], v[76:79]\n\t"
    "v_fma_mix_f32 v248, v154, v232, v240 op_sel:[0,0,0] op_sel_hi:[1,0,0]\n\t"
    "v_fma_mix_f32 v249, v154, v233, v241 op_sel:[1,0,0] op_sel_hi:[1,0,0]\n\t"
    "v_mfma_f32_16x16x32_bf16 v[80:83], a[84:87], v[128:131], v[80:83]\n\t"
    "ds_read_b64_tr_b16 a[108:109], v193 offset:40968\n\t"
    "v_fma_mix_f32 v250, v155, v234, v242 op_sel:[0,0,0] op_sel_hi:[1,0,0]\n\t"
    "v_fma_mix_f32 v251, v155, v235, v243 op_sel:[1,0,0] op_sel_hi:[1,0,0]\n\t"
    "v_mfma_f32_16x16x32_bf16 v[84:87], a[84:87], v[132:135], v[84:87]\n\t"
    "ds_read_b64_tr_b16 a[110:111], v193 offset:41992\n\t"
    "v_cvt_pk_bf16_f32 v152, v244, v245\n\t"
    "v_cvt_pk_bf16_f32 v153, v246, v247\n\t"
    "v_mfma_f32_16x16x32_bf16 v[88:91], a[84:87], v[136:139], v[88:91]\n\t"
    "v_cvt_pk_bf16_f32 v154, v248, v249\n\t"
    "v_cvt_pk_bf16_f32 v155, v250, v251\n\t"
    "v_mfma_f32_16x16x32_bf16 v[92:95], a[84:87], v[140:143], v[92:95]\n\t"
    "s_waitcnt vmcnt(16)\n\t"
    "v_fma_mix_f32 v244, v156, v228, v236 op_sel:[0,0,0] op_sel_hi:[1,0,0]\n\t"
    "v_fma_mix_f32 v245, v156, v229, v237 op_sel:[1,0,0] op_sel_hi:[1,0,0]\n\t"
    "s_barrier\n\t"
    "v_mfma_f32_16x16x32_bf16 v[96:99], a[88:91], v[128:131], v[96:99]\n\t"
    "ds_read_b64_tr_b16 a[112:113], v194 offset:40960\n\t"
    "v_fma_mix_f32 v246, v157, v230, v238 op_sel:[0,0,0] op_sel_hi:[1,0,0]\n\t"
    "v_fma_mix_f32 v247, v157, v231, v239 op_sel:[1,0,0] op_sel_hi:[1,0,0]\n\t"
    "v_mfma_f32_16x16x32_bf16 v[100:103], a[88:91], v[132:135], v[100:103]\n\t"
    "ds_read_b64_tr_b16 a[114:115], v194 offset:41984\n\t"
    "v_fma_mix_f32 v248, v158, v232, v240 op_sel:[0,0,0] op_sel_hi:[1,0,0]\n\t"
    "v_fma_mix_f32 v249, v158, v233, v241 op_sel:[1,0,0] op_sel_hi:[1,0,0]\n\t"
    "s_mov_b32 m0, s63\n\t"
    "v_mfma_f32_16x16x32_bf16 v[104:107], a[88:91], v[136:139], v[104:107]\n\t"
    "buffer_load_dwordx4 v224, s[40:43], s57 offen offset:0 lds\n\t"
    "v_fma_mix_f32 v250, v159, v234, v242 op_sel:[0,0,0] op_sel_hi:[1,0,0]\n\t"
    "v_fma_mix_f32 v251, v159, v235, v243 op_sel:[1,0,0] op_sel_hi:[1,0,0]\n\t"
    "v_mfma_f32_16x16x32_bf16 v[108:111], a[88:91], v[140:143], v[108:111]\n\t"
    "v_cvt_pk_bf16_f32 v156, v244, v245\n\t"
    "v_cvt_pk_bf16_f32 v157, v246, v247\n\t"
    "s_waitcnt lgkmcnt(10)\n\t"
    "v_mfma_f32_16x16x32_bf16 v[112:115], a[92:95], v[128:131], v[112:115]\n\t"
    "ds_read_b64_tr_b16 a[116:117], v194 offset:40968\n\t"
    "v_cvt_pk_bf16_f32 v158, v248, v249\n\t"
    "v_cvt_pk_bf16_f32 v159, v250, v251\n\t"
    "v_mfma_f32_16x16x32_bf16 v[116:119], a[92:95], v[132:135], v[116:119]\n\t"
    "ds_read_b64_tr_b16 a[118:119], v194 offset:41992\n\t"
    "s_mov_b32 m0, s63\n\t"
    "v_mfma_f32_16x16x32_bf16 v[120:123], a[92:95], v[136:139], v[120:123]\n\t"
    "buffer_load_dwordx4 v225, s[40:43], s57 offen offset:1024 lds\n\t"
    "v_mfma_f32_16x16x32_bf16 v[124:127], a[92:95], v[140:143], v[124:127]\n\t"
    "s_waitcnt lgkmcnt(10)\n\t"
    "v_mfma_f32_16x16x32_bf16 v[0:3], a[96:99], v[144:147], v[0:3]\n\t"
    "ds_read_b64_tr_b16 a[120:121], v195 offset:40960\n\t"
    "ds_read_b128 v[228:231], v200 offset:2560\n\t"
    "ds_read_b128 v[232:235], v200 offset:2576\n\t"
    "v_mfma_f32_16x16x32_bf16 v[4:7], a[96:99], v[148:151], v[4:7]\n\t"
    "ds_read_b64_tr_b16 a[122:123], v195 offset:41984\n\t"
    "ds_read_b128 v[236:239], v200 offset:2592\n\t"
    "ds_read_b128 v[240:243], v200 offset:2608\n\t"
    "s_mov_b32 s75, 12288\n\t"
    "v_mfma_f32_16x16x32_bf16 v[8:11], a[96:99], v[152:155], v[8:11]\n\t"
    "buffer_load_dwordx4 v[128:131], v226, s[48:51], s75 offen offset:0\n\t"
    "v_mfma_f32_16x16x32_bf16 v[12:15], a[96:99], v[156:159], v[12:15]\n\t"
    "s_waitcnt lgkmcnt(14)\n\t"
    "v_mfma_f32_16x16x32_bf16 v[16:19], a[100:103], v[144:147], v[16:19]\n\t"
    "ds_read_b64_tr_b16 a[124:125], v195 offset:40968\n\t"
    "v_mfma_f32_16x16x32_bf16 v[20:23], a[100:103], v[148:151], v[20:23]\n\t"
    "ds_read_b64_tr_b16 a[126:127], v195 offset:41992\n\t"
    "s_waitcnt lgkmcnt(2)\n\t"
    "s_waitcnt vmcnt(14)\n\t"
    "v_fma_mix_f32 v244, v160, v228, v236 op_sel:[0,0,0] op_sel_hi:[1,0,0]\n\t"
    "v_fma_mix_f32 v245, v160, v229, v237 op_sel:[1,0,0] op_sel_hi:[1,0,0]\n\t"
    "s_mov_b32 s75, 28672\n\t"
    "v_mfma_f32_16x16x32_bf16 v[24:27], a[100:103], v[152:155], v[24:27]\n\t"
    "buffer_load_dwordx4 v[132:135], v226, s[48:51], s75 offen offset:0\n\t"
    "v_fma_mix_f32 v246, v161, v230, v238 op_sel:[0,0,0] op_sel_hi:[1,0,0]\n\t"
    "v_fma_mix_f32 v247, v161, v231, v239 op_sel:[1,0,0] op_sel_hi:[1,0,0]\n\t"
    "v_mfma_f32_16x16x32_bf16 v[28:31], a[100:103], v[156:159], v[28:31]\n\t"
    "v_fma_mix_f32 v248, v162, v232, v240 op_sel:[0,0,0] op_sel_hi:[1,0,0]\n\t"
    "v_fma_mix_f32 v249, v162, v233, v241 op_sel:[1,0,0] op_sel_hi:[1,0,0]\n\t"
    "v_mfma_f32_16x16x32_bf16 v[32:35], a[104:107], v[144:147], v[32:35]\n\t"
    "ds_read_b64_tr_b16 a[128:129], v196 offset:32768\n\t"
    "v_fma_mix_f32 v250, v163, v234, v242 op_sel:[0,0,0] op_sel_hi:[1,0,0]\n\t"
    "v_fma_mix_f32 v251, v163, v235, v243 op_sel:[1,0,0] op_sel_hi:[1,0,0]\n\t"
    "v_mfma_f32_16x16x32_bf16 v[36:39], a[104:107], v[148:151], v[36:39]\n\t"
    "ds_read_b64_tr_b16 a[130:131], v196 offset:33792\n\t"
    "v_cvt_pk_bf16_f32 v160, v244, v245\n\t"
    "v_cvt_pk_bf16_f32 v161, v246, v247\n\t"
    "s_mov_b32 s75, 45056\n\t"
    "v_mfma_f32_16x16x32_bf16 v[40:43], a[104:107], v[152:155], v[40:43]\n\t"
    "buffer_load_dwordx4 v[136:139], v226, s[48:51], s75 offen offset:0\n\t"
    "v_cvt_pk_bf16_f32 v162, v248, v249\n\t"
    "v_cvt_pk_bf16_f32 v163, v250, v251\n\t"
    "v_mfma_f32_16x16x32_bf16 v[44:47], a[104:107], v[156:159], v[44:47]\n\t"
    "s_waitcnt vmcnt(15)\n\t"
    "v_fma_mix_f32 v244, v164, v228, v236 op_sel:[0,0,0] op_sel_hi:[1,0,0]\n\t"
    "v_fma_mix_f32 v245, v164, v229, v237 op_sel:[1,0,0] op_sel_hi:[1,0,0]\n\t"
    "v_mfma_f32_16x16x32_bf16 v[48:51], a[108:111], v[144:147], v[48:51]\n\t"
    "ds_read_b64_tr_b16 a[132:133], v196 offset:32776\n\t"
    "v_fma_mix_f32 v246, v165, v230, v238 op_sel:[0,0,0] op_sel_hi:[1,0,0]\n\t"
    "v_fma_mix_f32 v247, v165, v231, v239 op_sel:[1,0,0] op_sel_hi:[1,0,0]\n\t"
    "v_mfma_f32_16x16x32_bf16 v[52:55], a[108:111], v[148:151], v[52:55]\n\t"
    "ds_read_b64_tr_b16 a[134:135], v196 offset:33800\n\t"
    "v_fma_mix_f32 v248, v166, v232, v240 op_sel:[0,0,0] op_sel_hi:[1,0,0]\n\t"
    "v_fma_mix_f32 v249, v166, v233, v241 op_sel:[1,0,0] op_sel_hi:[1,0,0]\n\t"
    "s_mov_b32 s75, 61440\n\t"
    "v_mfma_f32_16x16x32_bf16 v[56:59], a[108:111], v[152:155], v[56:59]\n\t"
    "buffer_load_dwordx4 v[140:143], v226, s[48:51], s75 offen offset:0\n\t"
    "v_fma_mix_f32 v250, v167, v234, v242 op_sel:[0,0,0] op_sel_hi:[1,0,0]\n\t"
    "v_fma_mix_f32 v251, v167, v235, v243 op_sel:[1,0,0] op_sel_hi:[1,0,0]\n\t"
    "v_mfma_f32_16x16x32_bf16 v[60:63], a[108:111], v[156:159], v[60:63]\n\t"
    "v_cvt_pk_bf16_f32 v164, v244, v245\n\t"
    "v_cvt_pk_bf16_f32 v165, v246, v247\n\t"
    "v_mfma_f32_16x16x32_bf16 v[64:67], a[112:115], v[144:147], v[64:67]\n\t"
    "ds_read_b64_tr_b16 a[136:137], v197 offset:32768\n\t"
    "v_cvt_pk_bf16_f32 v166, v248, v249\n\t"
    "v_cvt_pk_bf16_f32 v167, v250, v251\n\t"
    "v_mfma_f32_16x16x32_bf16 v[68:71], a[112:115], v[148:151], v[68:71]\n\t"
    "ds_read_b64_tr_b16 a[138:139], v197 offset:33792\n\t"
    "s_waitcnt vmcnt(15)\n\t"
    "v_fma_mix_f32 v244, v168, v228, v236 op_sel:[0,0,0] op_sel_hi:[1,0,0]\n\t"
    "v_fma_mix_f32 v245, v168, v229, v237 op_sel:[1,0,0] op_sel_hi:[1,0,0]\n\t"
    "s_mov_b32 m0, s63\n\t"
    "v_mfma_f32_16x16x32_bf16 v[72:75], a[112:115], v[152:155], v[72:75]\n\t"
    "buffer_load_dwordx4 v224, s[40:43], s57 offen offset:2048 lds\n\t"
    "v_fma_mix_f32 v246, v169, v230, v238 op_sel:[0,0,0] op_sel_hi:[1,0,0]\n\t"
    "v_fma_mix_f32 v247, v169, v231, v239 op_sel:[1,0,0] op_sel_hi:[1,0,0]\n\t"
    "v_mfma_f32_16x16x32_bf16 v[76:79], a[112:115], v[156:159], v[76:79]\n\t"
    "v_fma_mix_f32 v248, v170, v232, v240 op_sel:[0,0,0] op_sel_hi:[1,0,0]\n\t"
    "v_fma_mix_f32 v249, v170, v233, v241 op_sel:[1,0,0] op_sel_hi:[1,0,0]\n\t"
    "v_mfma_f32_16x16x32_bf16 v[80:83], a[116:119], v[144:147], v[80:83]\n\t"
    "ds_read_b64_tr_b16 a[140:141], v197 offset:32776\n\t"
    "v_fma_mix_f32 v250, v171, v234, v242 op_sel:[0,0,0] op_sel_hi:[1,0,0]\n\t"
    "v_fma_mix_f32 v251, v171, v235, v243 op_sel:[1,0,0] op_sel_hi:[1,0,0]\n\t"
    "v_mfma_f32_16x16x32_bf16 v[84:87], a[116:119], v[148:151], v[84:87]\n\t"
    "ds_read_b64_tr_b16 a[142:143], v197 offset:33800\n\t"
    "v_cvt_pk_bf16_f32 v168, v244, v245\n\t"
    "v_cvt_pk_bf16_f32 v169, v246, v247\n\t"
    "s_mov_b32 m0, s63\n\t"
    "v_mfma_f32_16x16x32_bf16 v[88:91], a[116:119], v[152:155], v[88:91]\n\t"
    "buffer_load_dwordx4 v225, s[40:43], s57 offen offset:3072 lds\n\t"
    "s_add_u32 s57, s57, 0x4000\n\t"
    "v_cvt_pk_bf16_f32 v170, v248, v249\n\t"
    "v_cvt_pk_bf16_f32 v171, v250, v251\n\t"
    "v_mfma_f32_16x16x32_bf16 v[92:95], a[116:119], v[156:159], v[92:95]\n\t"
    "s_waitcnt vmcnt(16)\n\t"
    "v_fma_mix_f32 v244, v172, v228, v236 op_sel:[0,0,0] op_sel_hi:[1,0,0]\n\t"
    "v_fma_mix_f32 v245, v172, v229, v237 op_sel:[1,0,0] op_sel_hi:[1,0,0]\n\t"
    "v_mfma_f32_16x16x32_bf16 v[96:99], a[120:123], v[144:147], v[96:99]\n\t"
    "ds_read_b64_tr_b16 a[144:145], v198 offset:32768\n\t"
    "v_fma_mix_f32 v246, v173, v230, v238 op_sel:[0,0,0] op_sel_hi:[1,0,0]\n\t"
    "v_fma_mix_f32 v247, v173, v231, v239 op_sel:[1,0,0] op_sel_hi:[1,0,0]\n\t"
    "v_mfma_f32_16x16x32_bf16 v[100:103], a[120:123], v[148:151], v[100:103]\n\t"
    "ds_read_b64_tr_b16 a[146:147], v198 offset:33792\n\t"
    "v_fma_mix_f32 v248, v174, v232, v240 op_sel:[0,0,0] op_sel_hi:[1,0,0]\n\t"
    "v_fma_mix_f32 v249, v174, v233, v241 op_sel:[1,0,0] op_sel_hi:[1,0,0]\n\t"
    "v_mfma_f32_16x16x32_bf16 v[104:107], a[120:123], v[152:155], v[104:107]\n\t"
    "v_fma_mix_f32 v250, v175, v234, v242 op_sel:[0,0,0] op_sel_hi:[1,0,0]\n\t"
    "v_fma_mix_f32 v251, v175, v235, v243 op_sel:[1,0,0] op_sel_hi:[1,0,0]\n\t"
    "v_mfma_f32_16x16x32_bf16 v[108:111], a[120:123], v[156:159], v[108:111]\n\t"
    "v_cvt_pk_bf16_f32 v172, v244, v245\n\t"
    "v_cvt_pk_bf16_f32 v173, v246, v247\n\t"
    "s_waitcnt lgkmcnt(10)\n\t"
    "v_mfma_f32_16x16x32_bf16 v[112:115], a[124:127], v[144:147], v[112:115]\n\t"
    "ds_read_b64_tr_b16 a[148:149], v198 offset:32776\n\t"
    "v_cvt_pk_bf16_f32 v174, v248, v249\n\t"
    "v_cvt_pk_bf16_f32 v175, v250, v251\n\t"
    "v_mfma_f32_16x16x32_bf16 v[116:119], a[124:127], v[148:151], v[116:119]\n\t"
    "ds_read_b64_tr_b16 a[150:151], v198 offset:33800\n\t"
    "v_mfma_f32_16x16x32_bf16 v[120:123], a[124:127], v[152:155], v[120:123]\n\t"
    "v_mfma_f32_16x16x32_bf16 v[124:127], a[124:127], v[156:159], v[124:127]\n\t"
    "s_waitcnt lgkmcnt(10)\n\t"
    "v_mfma_f32_16x16x32_bf16 v[0:3], a[128:131], v[160:163], v[0:3]\n\t"
    "ds_read_b64_tr_b16 a[152:153], v199 offset:32768\n\t"
    "ds_read_b128 v[228:231], v200 offset:2816\n\t"
    "ds_read_b128 v[232:235], v200 offset:2832\n\t"
    "v_mfma_f32_16x16x32_bf16 v[4:7], a[128:131], v[164:167], v[4:7]\n\t"
    "ds_read_b64_tr_b16 a[154:155], v199 offset:33792\n\t"
    "ds_read_b128 v[236:239], v200 offset:2848\n\t"
    "ds_read_b128 v[240:243], v200 offset:2864\n\t"
    "s_mov_b32 s75, 12288\n\t"
    "v_mfma_f32_16x16x32_bf16 v[8:11], a[128:131], v[168:171], v[8:11]\n\t"
    "buffer_load_dwordx4 v[144:147], v226, s[48:51], s75 offen offset:1024\n\t"
    "v_mfma_f32_16x16x32_bf16 v[12:15], a[128:131], v[172:175], v[12:15]\n\t"
    "s_waitcnt lgkmcnt(14)\n\t"
    "v_mfma_f32_16x16x32_bf16 v[16:19], a[132:135], v[160:163], v[16:19]\n\t"
    "ds_read_b64_tr_b16 a[156:157], v199 offset:32776\n\t"
    "v_mfma_f32_16x16x32_bf16 v[20:23], a[132:135], v[164:167], v[20:23]\n\t"
    "ds_read_b64_tr_b16 a[158:159], v199 offset:33800\n\t"
    "s_waitcnt lgkmcnt(2)\n\t"
    "s_waitcnt vmcnt(12)\n\t"
    "v_fma_mix_f32 v244, v176, v228, v236 op_sel:[0,0,0] op_sel_hi:[1,0,0]\n\t"
    "v_fma_mix_f32 v245, v176, v229, v237 op_sel:[1,0,0] op_sel_hi:[1,0,0]\n\t"
    "s_mov_b32 s75, 28672\n\t"
    "v_mfma_f32_16x16x32_bf16 v[24:27], a[132:135], v[168:171], v[24:27]\n\t"
    "buffer_load_dwordx4 v[148:151], v226, s[48:51], s75 offen offset:1024\n\t"
    "v_fma_mix_f32 v246, v177, v230, v238 op_sel:[0,0,0] op_sel_hi:[1,0,0]\n\t"
    "v_fma_mix_f32 v247, v177, v231, v239 op_sel:[1,0,0] op_sel_hi:[1,0,0]\n\t"
    "v_mfma_f32_16x16x32_bf16 v[28:31], a[132:135], v[172:175], v[28:31]\n\t"
    "v_fma_mix_f32 v248, v178, v232, v240 op_sel:[0,0,0] op_sel_hi:[1,0,0]\n\t"
    "v_fma_mix_f32 v249, v178, v233, v241 op_sel:[1,0,0] op_sel_hi:[1,0,0]\n\t"
    "v_mfma_f32_16x16x32_bf16 v[32:35], a[136:139], v[160:163], v[32:35]\n\t"
    "ds_read_b64_tr_b16 a[160:161], v196 offset:40960\n\t"
    "v_fma_mix_f32 v250, v179, v234, v242 op_sel:[0,0,0] op_sel_hi:[1,0,0]\n\t"
    "v_fma_mix_f32 v251, v179, v235, v243 op_sel:[1,0,0] op_sel_hi:[1,0,0]\n\t"
    "v_mfma_f32_16x16x32_bf16 v[36:39], a[136:139], v[164:167], v[36:39]\n\t"
    "ds_read_b64_tr_b16 a[162:163], v196 offset:41984\n\t"
    "v_cvt_pk_bf16_f32 v176, v244, v245\n\t"
    "v_cvt_pk_bf16_f32 v177, v246, v247\n\t"
    "s_mov_b32 s75, 45056\n\t"
    "v_mfma_f32_16x16x32_bf16 v[40:43], a[136:139], v[168:171], v[40:43]\n\t"
    "buffer_load_dwordx4 v[152:155], v226, s[48:51], s75 offen offset:1024\n\t"
    "v_cvt_pk_bf16_f32 v178, v248, v249\n\t"
    "v_cvt_pk_bf16_f32 v179, v250, v251\n\t"
    "v_mfma_f32_16x16x32_bf16 v[44:47], a[136:139], v[172:175], v[44:47]\n\t"
    "s_waitcnt vmcnt(13)\n\t"
    "v_fma_mix_f32 v244, v180, v228, v236 op_sel:[0,0,0] op_sel_hi:[1,0,0]\n\t"
    "v_fma_mix_f32 v245, v180, v229, v237 op_sel:[1,0,0] op_sel_hi:[1,0,0]\n\t"
    "v_mfma_f32_16x16x32_bf16 v[48:51], a[140:143], v[160:163], v[48:51]\n\t"
    "ds_read_b64_tr_b16 a[164:165], v196 offset:40968\n\t"
    "v_fma_mix_f32 v246, v181, v230, v238 op_sel:[0,0,0] op_sel_hi:[1,0,0]\n\t"
    "v_fma_mix_f32 v247, v181, v231, v239 op_sel:[1,0,0] op_sel_hi:[1,0,0]\n\t"
    "v_mfma_f32_16x16x32_bf16 v[52:55], a[140:143], v[164:167], v[52:55]\n\t"
    "ds_read_b64_tr_b16 a[166:167], v196 offset:41992\n\t"
    "v_fma_mix_f32 v248, v182, v232, v240 op_sel:[0,0,0] op_sel_hi:[1,0,0]\n\t"
    "v_fma_mix_f32 v249, v182, v233, v241 op_sel:[1,0,0] op_sel_hi:[1,0,0]\n\t"
    "s_mov_b32 s75, 61440\n\t"
    "v_mfma_f32_16x16x32_bf16 v[56:59], a[140:143], v[168:171], v[56:59]\n\t"
    "buffer_load_dwordx4 v[156:159], v226, s[48:51], s75 offen offset:1024\n\t"
    "v_fma_mix_f32 v250, v183, v234, v242 op_sel:[0,0,0] op_sel_hi:[1,0,0]\n\t"
    "v_fma_mix_f32 v251, v183, v235, v243 op_sel:[1,0,0] op_sel_hi:[1,0,0]\n\t"
    "v_mfma_f32_16x16x32_bf16 v[60:63], a[140:143], v[172:175], v[60:63]\n\t"
    "v_cvt_pk_bf16_f32 v180, v244, v245\n\t"
    "v_cvt_pk_bf16_f32 v181, v246, v247\n\t"
    "v_mfma_f32_16x16x32_bf16 v[64:67], a[144:147], v[160:163], v[64:67]\n\t"
    "ds_read_b64_tr_b16 a[168:169], v197 offset:40960\n\t"
    "v_cvt_pk_bf16_f32 v182, v248, v249\n\t"
    "v_cvt_pk_bf16_f32 v183, v250, v251\n\t"
    "v_mfma_f32_16x16x32_bf16 v[68:71], a[144:147], v[164:167], v[68:71]\n\t"
    "ds_read_b64_tr_b16 a[170:171], v197 offset:41984\n\t"
    "s_waitcnt vmcnt(13)\n\t"
    "v_fma_mix_f32 v244, v184, v228, v236 op_sel:[0,0,0] op_sel_hi:[1,0,0]\n\t"
    "v_fma_mix_f32 v245, v184, v229, v237 op_sel:[1,0,0] op_sel_hi:[1,0,0]\n\t"
    "v_mfma_f32_16x16x32_bf16 v[72:75], a[144:147], v[168:171], v[72:75]\n\t"
    "v_fma_mix_f32 v246, v185, v230, v238 op_sel:[0,0,0] op_sel_hi:[1,0,0]\n\t"
    "v_fma_mix_f32 v247, v185, v231, v239 op_sel:[1,0,0] op_sel_hi:[1,0,0]\n\t"
    "v_mfma_f32_16x16x32_bf16 v[76:79], a[144:147], v[172:175], v[76:79]\n\t"
    "v_fma_mix_f32 v248, v186, v232, v240 op_sel:[0,0,0] op_sel_hi:[1,0,0]\n\t"
    "v_fma_mix_f32 v249, v186, v233, v241 op_sel:[1,0,0] op_sel_hi:[1,0,0]\n\t"
    "v_mfma_f32_16x16x32_bf16 v[80:83], a[148:151], v[160:163], v[80:83]\n\t"
    "ds_read_b64_tr_b16 a[172:173], v197 offset:40968\n\t"
    "v_fma_mix_f32 v250, v187, v234, v242 op_sel:[0,0,0] op_sel_hi:[1,0,0]\n\t"
    "v_fma_mix_f32 v251, v187, v235, v243 op_sel:[1,0,0] op_sel_hi:[1,0,0]\n\t"
    "v_mfma_f32_16x16x32_bf16 v[84:87], a[148:151], v[164:167], v[84:87]\n\t"
    "ds_read_b64_tr_b16 a[174:175], v197 offset:41992\n\t"
    "v_cvt_pk_bf16_f32 v184, v244, v245\n\t"
    "v_cvt_pk_bf16_f32 v185, v246, v247\n\t"
    "v_mfma_f32_16x16x32_bf16 v[88:91], a[148:151], v[168:171], v[88:91]\n\t"
    "v_cvt_pk_bf16_f32 v186, v248, v249\n\t"
    "v_cvt_pk_bf16_f32 v187, v250, v251\n\t"
    "v_mfma_f32_16x16x32_bf16 v[92:95], a[148:151], v[172:175], v[92:95]\n\t"
    "s_waitcnt vmcnt(12)\n\t"
    "v_fma_mix_f32 v244, v188, v228, v236 op_sel:[0,0,0] op_sel_hi:[1,0,0]\n\t"
    "v_fma_mix_f32 v245, v188, v229, v237 op_sel:[1,0,0] op_sel_hi:[1,0,0]\n\t"
    "s_barrier\n\t"
    "v_mfma_f32_16x16x32_bf16 v[96:99], a[152:155], v[160:163], v[96:99]\n\t"
    "ds_read_b64_tr_b16 a[176:177], v198 offset:40960\n\t"
    "v_fma_mix_f32 v246, v189, v230, v238 op_sel:[0,0,0] op_sel_hi:[1,0,0]\n\t"
    "v_fma_mix_f32 v247, v189, v231, v239 op_sel:[1,0,0] op_sel_hi:[1,0,0]\n\t"
    "v_mfma_f32_16x16x32_bf16 v[100:103], a[152:155], v[164:167], v[100:103]\n\t"
    "ds_read_b64_tr_b16 a[178:179], v198 offset:41984\n\t"
    "v_fma_mix_f32 v248, v190, v232, v240 op_sel:[0,0,0] op_sel_hi:[1,0,0]\n\t"
    "v_fma_mix_f32 v249, v190, v233, v241 op_sel:[1,0,0] op_sel_hi:[1,0,0]\n\t"
    "s_mov_b32 m0, s60\n\t"
    "v_mfma_f32_16x16x32_bf16 v[104:107], a[152:155], v[168:171], v[104:107]\n\t"
    "buffer_load_dwordx4 v220, s[36:39], s56 offen offset:0 lds\n\t"
    "v_fma_mix_f32 v250, v191, v234, v242 op_sel:[0,0,0] op_sel_hi:[1,0,0]\n\t"
    "v_fma_mix_f32 v251, v191, v235, v243 op_sel:[1,0,0] op_sel_hi:[1,0,0]\n\t"
    "v_mfma_f32_16x16x32_bf16 v[108:111], a[152:155], v[172:175], v[108:111]\n\t"
    "v_cvt_pk_bf16_f32 v188, v244, v245\n\t"
    "v_cvt_pk_bf16_f32 v189, v246, v247\n\t"
    "s_waitcnt lgkmcnt(10)\n\t"
    "v_mfma_f32_16x16x32_bf16 v[112:115], a[156:159], v[160:163], v[112:115]\n\t"
    "ds_read_b64_tr_b16 a[180:181], v198 offset:40968\n\t"
    "v_cvt_pk_bf16_f32 v190, v248, v249\n\t"
    "v_cvt_pk_bf16_f32 v191, v250, v251\n\t"
    "v_mfma_f32_16x16x32_bf16 v[116:119], a[156:159], v[164:167], v[116:119]\n\t"
    "ds_read_b64_tr_b16 a[182:183], v198 offset:41992\n\t"
    "s_mov_b32 m0, s60\n\t"
    "v_mfma_f32_16x16x32_bf16 v[120:123], a[156:159], v[168:171], v[120:123]\n\t"
    "buffer_load_dwordx4 v221, s[36:39], s56 offen offset:1024 lds\n\t"
    "v_mfma_f32_16x16x32_bf16 v[124:127], a[156:159], v[172:175], v[124:127]\n\t"
    "s_waitcnt lgkmcnt(10)\n\t"
    "v_mfma_f32_16x16x32_bf16 v[0:3], a[160:163], v[176:179], v[0:3]\n\t"
    "ds_read_b64_tr_b16 a[184:185], v199 offset:40960\n\t"
    "ds_read_b128 v[228:231], v200 offset:3072\n\t"
    "ds_read_b128 v[232:235], v200 offset:3088\n\t"
    "v_mfma_f32_16x16x32_bf16 v[4:7], a[160:163], v[180:183], v[4:7]\n\t"
    "ds_read_b64_tr_b16 a[186:187], v199 offset:41984\n\t"
    "ds_read_b128 v[236:239], v200 offset:3104\n\t"
    "ds_read_b128 v[240:243], v200 offset:3120\n\t"
    "s_mov_b32 s75, 12288\n\t"
    "v_mfma_f32_16x16x32_bf16 v[8:11], a[160:163], v[184:187], v[8:11]\n\t"
    "buffer_load_dwordx4 v[160:163], v226, s[48:51], s75 offen offset:2048\n\t"
    "v_mfma_f32_16x16x32_bf16 v[12:15], a[160:163], v[188:191], v[12:15]\n\t"
    "s_waitcnt lgkmcnt(14)\n\t"
    "v_mfma_f32_16x16x32_bf16 v[16:19], a[164:167], v[176:179], v[16:19]\n\t"
    "ds_read_b64_tr_b16 a[188:189], v199 offset:40968\n\t"
    "v_mfma_f32_16x16x32_bf16 v[20:23], a[164:167], v[180:183], v[20:23]\n\t"
    "ds_read_b64_tr_b16 a[190:191], v199 offset:41992\n\t"
    "s_waitcnt lgkmcnt(2)\n\t"
    "s_waitcnt vmcnt(12)\n\t"
    "v_fma_mix_f32 v244, v128, v228, v236 op_sel:[0,0,0] op_sel_hi:[1,0,0]\n\t"
    "v_fma_mix_f32 v245, v128, v229, v237 op_sel:[1,0,0] op_sel_hi:[1,0,0]\n\t"
    "s_mov_b32 s75, 28672\n\t"
    "v_mfma_f32_16x16x32_bf16 v[24:27], a[164:167], v[184:187], v[24:27]\n\t"
    "buffer_load_dwordx4 v[164:167], v226, s[48:51], s75 offen offset:2048\n\t"
    "v_fma_mix_f32 v246, v129, v230, v238 op_sel:[0,0,0] op_sel_hi:[1,0,0]\n\t"
    "v_fma_mix_f32 v247, v129, v231, v239 op_sel:[1,0,0] op_sel_hi:[1,0,0]\n\t"
    "v_mfma_f32_16x16x32_bf16 v[28:31], a[164:167], v[188:191], v[28:31]\n\t"
    "v_fma_mix_f32 v248, v130, v232, v240 op_sel:[0,0,0] op_sel_hi:[1,0,0]\n\t"
    "v_fma_mix_f32 v249, v130, v233, v241 op_sel:[1,0,0] op_sel_hi:[1,0,0]\n\t"
    "v_mfma_f32_16x16x32_bf16 v[32:35], a[168:171], v[176:179], v[32:35]\n\t"
    "ds_read_b64_tr_b16 a[64:65], v192 offset:49152\n\t"
    "v_fma_mix_f32 v250, v131, v234, v242 op_sel:[0,0,0] op_sel_hi:[1,0,0]\n\t"
    "v_fma_mix_f32 v251, v131, v235, v243 op_sel:[1,0,0] op_sel_hi:[1,0,0]\n\t"
    "v_mfma_f32_16x16x32_bf16 v[36:39], a[168:171], v[180:183], v[36:39]\n\t"
    "ds_read_b64_tr_b16 a[66:67], v192 offset:50176\n\t"
    "v_cvt_pk_bf16_f32 v128, v244, v245\n\t"
    "v_cvt_pk_bf16_f32 v129, v246, v247\n\t"
    "s_mov_b32 s75, 45056\n\t"
    "v_mfma_f32_16x16x32_bf16 v[40:43], a[168:171], v[184:187], v[40:43]\n\t"
    "buffer_load_dwordx4 v[168:171], v226, s[48:51], s75 offen offset:2048\n\t"
    "v_cvt_pk_bf16_f32 v130, v248, v249\n\t"
    "v_cvt_pk_bf16_f32 v131, v250, v251\n\t"
    "v_mfma_f32_16x16x32_bf16 v[44:47], a[168:171], v[188:191], v[44:47]\n\t"
    "s_waitcnt vmcnt(13)\n\t"
    "v_fma_mix_f32 v244, v132, v228, v236 op_sel:[0,0,0] op_sel_hi:[1,0,0]\n\t"
    "v_fma_mix_f32 v245, v132, v229, v237 op_sel:[1,0,0] op_sel_hi:[1,0,0]\n\t"
    "v_mfma_f32_16x16x32_bf16 v[48:51], a[172:175], v[176:179], v[48:51]\n\t"
    "ds_read_b64_tr_b16 a[68:69], v192 offset:49160\n\t"
    "v_fma_mix_f32 v246, v133, v230, v238 op_sel:[0,0,0] op_sel_hi:[1,0,0]\n\t"
    "v_fma_mix_f32 v247, v133, v231, v239 op_sel:[1,0,0] op_sel_hi:[1,0,0]\n\t"
    "v_mfma_f32_16x16x32_bf16 v[52:55], a[172:175], v[180:183], v[52:55]\n\t"
    "ds_read_b64_tr_b16 a[70:71], v192 offset:50184\n\t"
    "v_fma_mix_f32 v248, v134, v232, v240 op_sel:[0,0,0] op_sel_hi:[1,0,0]\n\t"
    "v_fma_mix_f32 v249, v134, v233, v241 op_sel:[1,0,0] op_sel_hi:[1,0,0]\n\t"
    "s_mov_b32 s75, 61440\n\t"
    "v_mfma_f32_16x16x32_bf16 v[56:59], a[172:175], v[184:187], v[56:59]\n\t"
    "buffer_load_dwordx4 v[172:175], v226, s[48:51], s75 offen offset:2048\n\t"
    "v_fma_mix_f32 v250, v135, v234, v242 op_sel:[0,0,0] op_sel_hi:[1,0,0]\n\t"
    "v_fma_mix_f32 v251, v135, v235, v243 op_sel:[1,0,0] op_sel_hi:[1,0,0]\n\t"
    "v_mfma_f32_16x16x32_bf16 v[60:63], a[172:175], v[188:191], v[60:63]\n\t"
    "v_cvt_pk_bf16_f32 v132, v244, v245\n\t"
    "v_cvt_pk_bf16_f32 v133, v246, v247\n\t"
    "v_mfma_f32_16x16x32_bf16 v[64:67], a[176:179], v[176:179], v[64:67]\n\t"
    "ds_read_b64_tr_b16 a[72:73], v193 offset:49152\n\t"
    "v_cvt_pk_bf16_f32 v134, v248, v249\n\t"
    "v_cvt_pk_bf16_f32 v135, v250, v251\n\t"
    "v_mfma_f32_16x16x32_bf16 v[68:71], a[176:179], v[180:183], v[68:71]\n\t"
    "ds_read_b64_tr_b16 a[74:75], v193 offset:50176\n\t"
    "s_waitcnt vmcnt(13)\n\t"
    "v_fma_mix_f32 v244, v136, v228, v236 op_sel:[0,0,0] op_sel_hi:[1,0,0]\n\t"
    "v_fma_mix_f32 v245, v136, v229, v237 op_sel:[1,0,0] op_sel_hi:[1,0,0]\n\t"
    "s_mov_b32 m0, s60\n\t"
    "v_mfma_f32_16x16x32_bf16 v[72:75], a[176:179], v[184:187], v[72:75]\n\t"
    "buffer_load_dwordx4 v222, s[36:39], s56 offen offset:2048 lds\n\t"
    "v_fma_mix_f32 v246, v137, v230, v238 op_sel:[0,0,0] op_sel_hi:[1,0,0]\n\t"
    "v_fma_mix_f32 v247, v137, v231, v239 op_sel:[1,0,0] op_sel_hi:[1,0,0]\n\t"
    "v_mfma_f32_16x16x32_bf16 v[76:79], a[176:179], v[188:191], v[76:79]\n\t"
    "v_fma_mix_f32 v248, v138, v232, v240 op_sel:[0,0,0] op_sel_hi:[1,0,0]\n\t"
    "v_fma_mix_f32 v249, v138, v233, v241 op_sel:[1,0,0] op_sel_hi:[1,0,0]\n\t"
    "v_mfma_f32_16x16x32_bf16 v[80:83], a[180:183], v[176:179], v[80:83]\n\t"
    "ds_read_b64_tr_b16 a[76:77], v193 offset:49160\n\t"
    "v_fma_mix_f32 v250, v139, v234, v242 op_sel:[0,0,0] op_sel_hi:[1,0,0]\n\t"
    "v_fma_mix_f32 v251, v139, v235, v243 op_sel:[1,0,0] op_sel_hi:[1,0,0]\n\t"
    "v_mfma_f32_16x16x32_bf16 v[84:87], a[180:183], v[180:183], v[84:87]\n\t"
    "ds_read_b64_tr_b16 a[78:79], v193 offset:50184\n\t"
    "v_cvt_pk_bf16_f32 v136, v244, v245\n\t"
    "v_cvt_pk_bf16_f32 v137, v246, v247\n\t"
    "s_mov_b32 m0, s60\n\t"
    "v_mfma_f32_16x16x32_bf16 v[88:91], a[180:183], v[184:187], v[88:91]\n\t"
    "buffer_load_dwordx4 v223, s[36:39], s56 offen offset:3072 lds\n\t"
    "s_add_u32 s56, s56, 0x4000\n\t"
    "v_cvt_pk_bf16_f32 v138, v248, v249\n\t"
    "v_cvt_pk_bf16_f32 v139, v250, v251\n\t"
    "v_mfma_f32_16x16x32_bf16 v[92:95], a[180:183], v[188:191], v[92:95]\n\t"
    "s_waitcnt vmcnt(14)\n\t"
    "v_fma_mix_f32 v244, v140, v228, v236 op_sel:[0,0,0] op_sel_hi:[1,0,0]\n\t"
    "v_fma_mix_f32 v245, v140, v229, v237 op_sel:[1,0,0] op_sel_hi:[1,0,0]\n\t"
    "v_mfma_f32_16x16x32_bf16 v[96:99], a[184:187], v[176:179], v[96:99]\n\t"
    "ds_read_b64_tr_b16 a[80:81], v194 offset:49152\n\t"
    "v_fma_mix_f32 v246, v141, v230, v238 op_sel:[0,0,0] op_sel_hi:[1,0,0]\n\t"
    "v_fma_mix_f32 v247, v141, v231, v239 op_sel:[1,0,0] op_sel_hi:[1,0,0]\n\t"
    "v_mfma_f32_16x16x32_bf16 v[100:103], a[184:187], v[180:183], v[100:103]\n\t"
    "ds_read_b64_tr_b16 a[82:83], v194 offset:50176\n\t"
    "v_fma_mix_f32 v248, v142, v232, v240 op_sel:[0,0,0] op_sel_hi:[1,0,0]\n\t"
    "v_fma_mix_f32 v249, v142, v233, v241 op_sel:[1,0,0] op_sel_hi:[1,0,0]\n\t"
    "v_mfma_f32_16x16x32_bf16 v[104:107], a[184:187], v[184:187], v[104:107]\n\t"
    "v_fma_mix_f32 v250, v143, v234, v242 op_sel:[0,0,0] op_sel_hi:[1,0,0]\n\t"
    "v_fma_mix_f32 v251, v143, v235, v243 op_sel:[1,0,0] op_sel_hi:[1,0,0]\n\t"
    "v_mfma_f32_16x16x32_bf16 v[108:111], a[184:187], v[188:191], v[108:111]\n\t"
    "v_cvt_pk_bf16_f32 v140, v244, v245\n\t"
    "v_cvt_pk_bf16_f32 v141, v246, v247\n\t"
    "s_waitcnt lgkmcnt(10)\n\t"
    "v_mfma_f32_16x16x32_bf16 v[112:115], a[188:191], v[176:179], v[112:115]\n\t"
    "ds_read_b64_tr_b16 a[84:85], v194 offset:49160\n\t"
    "v_cvt_pk_bf16_f32 v142, v248, v249\n\t"
    "v_cvt_pk_bf16_f32 v143, v250, v251\n\t"
    "v_mfma_f32_16x16x32_bf16 v[116:119], a[188:191], v[180:183], v[116:119]\n\t"
    "ds_read_b64_tr_b16 a[86:87], v194 offset:50184\n\t"
    "v_mfma_f32_16x16x32_bf16 v[120:123], a[188:191], v[184:187], v[120:123]\n\t"
    "v_mfma_f32_16x16x32_bf16 v[124:127], a[188:191], v[188:191], v[124:127]\n\t"
    "s_waitcnt lgkmcnt(10)\n\t"
    "v_mfma_f32_16x16x32_bf16 v[0:3], a[64:67], v[128:131], v[0:3]\n\t"
    "ds_read_b64_tr_b16 a[88:89], v195 offset:49152\n\t"
    "ds_read_b128 v[228:231], v200 offset:3328\n\t"
    "ds_read_b128 v[232:235], v200 offset:3344\n\t"
    "v_mfma_f32_16x16x32_bf16 v[4:7], a[64:67], v[132:135], v[4:7]\n\t"
    "ds_read_b64_tr_b16 a[90:91], v195 offset:50176\n\t"
    "ds_read_b128 v[236:239], v200 offset:3360\n\t"
    "ds_read_b128 v[240:243], v200 offset:3376\n\t"
    "s_mov_b32 s75, 12288\n\t"
    "v_mfma_f32_16x16x32_bf16 v[8:11], a[64:67], v[136:139], v[8:11]\n\t"
    "buffer_load_dwordx4 v[176:179], v226, s[48:51], s75 offen offset:3072\n\t"
    "v_mfma_f32_16x16x32_bf16 v[12:15], a[64:67], v[140:143], v[12:15]\n\t"
    "s_waitcnt lgkmcnt(14)\n\t"
    "v_mfma_f32_16x16x32_bf16 v[16:19], a[68:71], v[128:131], v[16:19]\n\t"
    "ds_read_b64_tr_b16 a[92:93], v195 offset:49160\n\t"
    "v_mfma_f32_16x16x32_bf16 v[20:23], a[68:71], v[132:135], v[20:23]\n\t"
    "ds_read_b64_tr_b16 a[94:95], v195 offset:50184\n\t"
    "s_waitcnt lgkmcnt(2)\n\t"
    "s_waitcnt vmcnt(12)\n\t"
    "v_fma_mix_f32 v244, v144, v228, v236 op_sel:[0,0,0] op_sel_hi:[1,0,0]\n\t"
    "v_fma_mix_f32 v245, v144, v229, v237 op_sel:[1,0,0] op_sel_hi:[1,0,0]\n\t"
    "s_mov_b32 s75, 28672\n\t"
    "v_mfma_f32_16x16x32_bf16 v[24:27], a[68:71], v[136:139], v[24:27]\n\t"
    "buffer_load_dwordx4 v[180:183], v226, s[48:51], s75 offen offset:3072\n\t"
    "v_fma_mix_f32 v246, v145, v230, v238 op_sel:[0,0,0] op_sel_hi:[1,0,0]\n\t"
    "v_fma_mix_f32 v247, v145, v231, v239 op_sel:[1,0,0] op_sel_hi:[1,0,0]\n\t"
    "v_mfma_f32_16x16x32_bf16 v[28:31], a[68:71], v[140:143], v[28:31]\n\t"
    "v_fma_mix_f32 v248, v146, v232, v240 op_sel:[0,0,0] op_sel_hi:[1,0,0]\n\t"
    "v_fma_mix_f32 v249, v146, v233, v241 op_sel:[1,0,0] op_sel_hi:[1,0,0]\n\t"
    "v_mfma_f32_16x16x32_bf16 v[32:35], a[72:75], v[128:131], v[32:35]\n\t"
    "ds_read_b64_tr_b16 a[96:97], v192 offset:57344\n\t"
    "v_fma_mix_f32 v250, v147, v234, v242 op_sel:[0,0,0] op_sel_hi:[1,0,0]\n\t"
    "v_fma_mix_f32 v251, v147, v235, v243 op_sel:[1,0,0] op_sel_hi:[1,0,0]\n\t"
    "v_mfma_f32_16x16x32_bf16 v[36:39], a[72:75], v[132:135], v[36:39]\n\t"
    "ds_read_b64_tr_b16 a[98:99], v192 offset:58368\n\t"
    "v_cvt_pk_bf16_f32 v144, v244, v245\n\t"
    "v_cvt_pk_bf16_f32 v145, v246, v247\n\t"
    "s_mov_b32 s75, 45056\n\t"
    "v_mfma_f32_16x16x32_bf16 v[40:43], a[72:75], v[136:139], v[40:43]\n\t"
    "buffer_load_dwordx4 v[184:187], v226, s[48:51], s75 offen offset:3072\n\t"
    "v_cvt_pk_bf16_f32 v146, v248, v249\n\t"
    "v_cvt_pk_bf16_f32 v147, v250, v251\n\t"
    "v_mfma_f32_16x16x32_bf16 v[44:47], a[72:75], v[140:143], v[44:47]\n\t"
    "s_waitcnt vmcnt(13)\n\t"
    "v_fma_mix_f32 v244, v148, v228, v236 op_sel:[0,0,0] op_sel_hi:[1,0,0]\n\t"
    "v_fma_mix_f32 v245, v148, v229, v237 op_sel:[1,0,0] op_sel_hi:[1,0,0]\n\t"
    "v_mfma_f32_16x16x32_bf16 v[48:51], a[76:79], v[128:131], v[48:51]\n\t"
    "ds_read_b64_tr_b16 a[100:101], v192 offset:57352\n\t"
    "v_fma_mix_f32 v246, v149, v230, v238 op_sel:[0,0,0] op_sel_hi:[1,0,0]\n\t"
    "v_fma_mix_f32 v247, v149, v231, v239 op_sel:[1,0,0] op_sel_hi:[1,0,0]\n\t"
    "v_mfma_f32_16x16x32_bf16 v[52:55], a[76:79], v[132:135], v[52:55]\n\t"
    "ds_read_b64_tr_b16 a[102:103], v192 offset:58376\n\t"
    "v_fma_mix_f32 v248, v150, v232, v240 op_sel:[0,0,0] op_sel_hi:[1,0,0]\n\t"
    "v_fma_mix_f32 v249, v150, v233, v241 op_sel:[1,0,0] op_sel_hi:[1,0,0]\n\t"
    "s_mov_b32 s75, 61440\n\t"
    "v_mfma_f32_16x16x32_bf16 v[56:59], a[76:79], v[136:139], v[56:59]\n\t"
    "buffer_load_dwordx4 v[188:191], v226, s[48:51], s75 offen offset:3072\n\t"
    "v_fma_mix_f32 v250, v151, v234, v242 op_sel:[0,0,0] op_sel_hi:[1,0,0]\n\t"
    "v_fma_mix_f32 v251, v151, v235, v243 op_sel:[1,0,0] op_sel_hi:[1,0,0]\n\t"
    "v_mfma_f32_16x16x32_bf16 v[60:63], a[76:79], v[140:143], v[60:63]\n\t"
    "v_cvt_pk_bf16_f32 v148, v244, v245\n\t"
    "v_cvt_pk_bf16_f32 v149, v246, v247\n\t"
    "v_mfma_f32_16x16x32_bf16 v[64:67], a[80:83], v[128:131], v[64:67]\n\t"
    "ds_read_b64_tr_b16 a[104:105], v193 offset:57344\n\t"
    "v_cvt_pk_bf16_f32 v150, v248, v249\n\t"
    "v_cvt_pk_bf16_f32 v151, v250, v251\n\t"
    "v_mfma_f32_16x16x32_bf16 v[68:71], a[80:83], v[132:135], v[68:71]\n\t"
    "ds_read_b64_tr_b16 a[106:107], v193 offset:58368\n\t"
    "s_waitcnt vmcnt(13)\n\t"
    "v_fma_mix_f32 v244, v152, v228, v236 op_sel:[0,0,0] op_sel_hi:[1,0,0]\n\t"
    "v_fma_mix_f32 v245, v152, v229, v237 op_sel:[1,0,0] op_sel_hi:[1,0,0]\n\t"
    "v_mfma_f32_16x16x32_bf16 v[72:75], a[80:83], v[136:139], v[72:75]\n\t"
    "v_fma_mix_f32 v246, v153, v230, v238 op_sel:[0,0,0] op_sel_hi:[1,0,0]\n\t"
    "v_fma_mix_f32 v247, v153, v231, v239 op_sel:[1,0,0] op_sel_hi:[1,0,0]\n\t"
    "v_mfma_f32_16x16x32_bf16 v[76:79], a[80:83], v[140:143], v[76:79]\n\t"
    "v_fma_mix_f32 v248, v154, v232, v240 op_sel:[0,0,0] op_sel_hi:[1,0,0]\n\t"
    "v_fma_mix_f32 v249, v154, v233, v241 op_sel:[1,0,0] op_sel_hi:[1,0,0]\n\t"
    "v_mfma_f32_16x16x32_bf16 v[80:83], a[84:87], v[128:131], v[80:83]\n\t"
    "ds_read_b64_tr_b16 a[108:109], v193 offset:57352\n\t"
    "v_fma_mix_f32 v250, v155, v234, v242 op_sel:[0,0,0] op_sel_hi:[1,0,0]\n\t"
    "v_fma_mix_f32 v251, v155, v235, v243 op_sel:[1,0,0] op_sel_hi:[1,0,0]\n\t"
    "v_mfma_f32_16x16x32_bf16 v[84:87], a[84:87], v[132:135], v[84:87]\n\t"
    "ds_read_b64_tr_b16 a[110:111], v193 offset:58376\n\t"
    "v_cvt_pk_bf16_f32 v152, v244, v245\n\t"
    "v_cvt_pk_bf16_f32 v153, v246, v247\n\t"
    "v_mfma_f32_16x16x32_bf16 v[88:91], a[84:87], v[136:139], v[88:91]\n\t"
    "v_cvt_pk_bf16_f32 v154, v248, v249\n\t"
    "v_cvt_pk_bf16_f32 v155, v250, v251\n\t"
    "v_mfma_f32_16x16x32_bf16 v[92:95], a[84:87], v[140:143], v[92:95]\n\t"
    "s_waitcnt vmcnt(12)\n\t"
    "v_fma_mix_f32 v244, v156, v228, v236 op_sel:[0,0,0] op_sel_hi:[1,0,0]\n\t"
    "v_fma_mix_f32 v245, v156, v229, v237 op_sel:[1,0,0] op_sel_hi:[1,0,0]\n\t"
    "s_barrier\n\t"
    "v_mfma_f32_16x16x32_bf16 v[96:99], a[88:91], v[128:131], v[96:99]\n\t"
    "ds_read_b64_tr_b16 a[112:113], v194 offset:57344\n\t"
    "v_fma_mix_f32 v246, v157, v230, v238 op_sel:[0,0,0] op_sel_hi:[1,0,0]\n\t"
    "v_fma_mix_f32 v247, v157, v231, v239 op_sel:[1,0,0] op_sel_hi:[1,0,0]\n\t"
    "v_mfma_f32_16x16x32_bf16 v[100:103], a[88:91], v[132:135], v[100:103]\n\t"
    "ds_read_b64_tr_b16 a[114:115], v194 offset:58368\n\t"
    "v_fma_mix_f32 v248, v158, v232, v240 op_sel:[0,0,0] op_sel_hi:[1,0,0]\n\t"
    "v_fma_mix_f32 v249, v158, v233, v241 op_sel:[1,0,0] op_sel_hi:[1,0,0]\n\t"
    "v_mfma_f32_16x16x32_bf16 v[104:107], a[88:91], v[136:139], v[104:107]\n\t"
    "v_fma_mix_f32 v250, v159, v234, v242 op_sel:[0,0,0] op_sel_hi:[1,0,0]\n\t"
    "v_fma_mix_f32 v251, v159, v235, v243 op_sel:[1,0,0] op_sel_hi:[1,0,0]\n\t"
    "v_mfma_f32_16x16x32_bf16 v[108:111], a[88:91], v[140:143], v[108:111]\n\t"
    "v_cvt_pk_bf16_f32 v156, v244, v245\n\t"
    "v_cvt_pk_bf16_f32 v157, v246, v247\n\t"
    "s_waitcnt lgkmcnt(10)\n\t"
    "v_mfma_f32_16x16x32_bf16 v[112:115], a[92:95], v[128:131], v[112:115]\n\t"
    "ds_read_b64_tr_b16 a[116:117], v194 offset:57352\n\t"
    "v_cvt_pk_bf16_f32 v158, v248, v249\n\t"
    "v_cvt_pk_bf16_f32 v159, v250, v251\n\t"
    "v_mfma_f32_16x16x32_bf16 v[116:119], a[92:95], v[132:135], v[116:119]\n\t"
    "ds_read_b64_tr_b16 a[118:119], v194 offset:58376\n\t"
    "v_mfma_f32_16x16x32_bf16 v[120:123], a[92:95], v[136:139], v[120:123]\n\t"
    "v_mfma_f32_16x16x32_bf16 v[124:127], a[92:95], v[140:143], v[124:127]\n\t"
    "s_waitcnt lgkmcnt(10)\n\t"
    "v_mfma_f32_16x16x32_bf16 v[0:3], a[96:99], v[144:147], v[0:3]\n\t"
    "ds_read_b64_tr_b16 a[120:121], v195 offset:57344\n\t"
    "ds_read_b128 v[228:231], v200 offset:3584\n\t"
    "ds_read_b128 v[232:235], v200 offset:3600\n\t"
    "v_mfma_f32_16x16x32_bf16 v[4:7], a[96:99], v[148:151], v[4:7]\n\t"
    "ds_read_b64_tr_b16 a[122:123], v195 offset:58368\n\t"
    "ds_read_b128 v[236:239], v200 offset:3616\n\t"
    "ds_read_b128 v[240:243], v200 offset:3632\n\t"
    "v_mfma_f32_16x16x32_bf16 v[8:11], a[96:99], v[152:155], v[8:11]\n\t"
    "v_mfma_f32_16x16x32_bf16 v[12:15], a[96:99], v[156:159], v[12:15]\n\t"
    "s_waitcnt lgkmcnt(14)\n\t"
    "v_mfma_f32_16x16x32_bf16 v[16:19], a[100:103], v[144:147], v[16:19]\n\t"
    "ds_read_b64_tr_b16 a[124:125], v195 offset:57352\n\t"
    "v_mfma_f32_16x16x32_bf16 v[20:23], a[100:103], v[148:151], v[20:23]\n\t"
    "ds_read_b64_tr_b16 a[126:127], v195 offset:58376\n\t"
    "s_waitcnt lgkmcnt(2)\n\t"
    "s_waitcnt vmcnt(9)\n\t"
    "v_fma_mix_f32 v244, v160, v228, v236 op_sel:[0,0,0] op_sel_hi:[1,0,0]\n\t"
    "v_fma_mix_f32 v245, v160, v229, v237 op_sel:[1,0,0] op_sel_hi:[1,0,0]\n\t"
    "v_mfma_f32_16x16x32_bf16 v[24:27], a[100:103], v[152:155], v[24:27]\n\t"
    "v_fma_mix_f32 v246, v161, v230, v238 op_sel:[0,0,0] op_sel_hi:[1,0,0]\n\t"
    "v_fma_mix_f32 v247, v161, v231, v239 op_sel:[1,0,0] op_sel_hi:[1,0,0]\n\t"
    "v_mfma_f32_16x16x32_bf16 v[28:31], a[100:103], v[156:159], v[28:31]\n\t"
    "v_fma_mix_f32 v248, v162, v232, v240 op_sel:[0,0,0] op_sel_hi:[1,0,0]\n\t"
    "v_fma_mix_f32 v249, v162, v233, v241 op_sel:[1,0,0] op_sel_hi:[1,0,0]\n\t"
    "v_mfma_f32_16x16x32_bf16 v[32:35], a[104:107], v[144:147], v[32:35]\n\t"
    "ds_read_b64_tr_b16 a[128:129], v196 offset:49152\n\t"
    "v_fma_mix_f32 v250, v163, v234, v242 op_sel:[0,0,0] op_sel_hi:[1,0,0]\n\t"
    "v_fma_mix_f32 v251, v163, v235, v243 op_sel:[1,0,0] op_sel_hi:[1,0,0]\n\t"
    "v_mfma_f32_16x16x32_bf16 v[36:39], a[104:107], v[148:151], v[36:39]\n\t"
    "ds_read_b64_tr_b16 a[130:131], v196 offset:50176\n\t"
    "v_cvt_pk_bf16_f32 v160, v244, v245\n\t"
    "v_cvt_pk_bf16_f32 v161, v246, v247\n\t"
    "v_mfma_f32_16x16x32_bf16 v[40:43], a[104:107], v[152:155], v[40:43]\n\t"
    "v_cvt_pk_bf16_f32 v162, v248, v249\n\t"
    "v_cvt_pk_bf16_f32 v163, v250, v251\n\t"
    "v_mfma_f32_16x16x32_bf16 v[44:47], a[104:107], v[156:159], v[44:47]\n\t"
    "s_waitcnt vmcnt(8)\n\t"
    "v_fma_mix_f32 v244, v164, v228, v236 op_sel:[0,0,0] op_sel_hi:[1,0,0]\n\t"
    "v_fma_mix_f32 v245, v164, v229, v237 op_sel:[1,0,0] op_sel_hi:[1,0,0]\n\t"
    "v_mfma_f32_16x16x32_bf16 v[48:51], a[108:111], v[144:147], v[48:51]\n\t"
    "ds_read_b64_tr_b16 a[132:133], v196 offset:49160\n\t"
    "v_fma_mix_f32 v246, v165, v230, v238 op_sel:[0,0,0] op_sel_hi:[1,0,0]\n\t"
    "v_fma_mix_f32 v247, v165, v231, v239 op_sel:[1,0,0] op_sel_hi:[1,0,0]\n\t"
    "v_mfma_f32_16x16x32_bf16 v[52:55], a[108:111], v[148:151], v[52:55]\n\t"
    "ds_read_b64_tr_b16 a[134:135], v196 offset:50184\n\t"
    "v_fma_mix_f32 v248, v166, v232, v240 op_sel:[0,0,0] op_sel_hi:[1,0,0]\n\t"
    "v_fma_mix_f32 v249, v166, v233, v241 op_sel:[1,0,0] op_sel_hi:[1,0,0]\n\t"
    "v_mfma_f32_16x16x32_bf16 v[56:59], a[108:111], v[152:155], v[56:59]\n\t"
    "v_fma_mix_f32 v250, v167, v234, v242 op_sel:[0,0,0] op_sel_hi:[1,0,0]\n\t"
    "v_fma_mix_f32 v251, v167, v235, v243 op_sel:[1,0,0] op_sel_hi:[1,0,0]\n\t"
    "v_mfma_f32_16x16x32_bf16 v[60:63], a[108:111], v[156:159], v[60:63]\n\t"
    "v_cvt_pk_bf16_f32 v164, v244, v245\n\t"
    "v_cvt_pk_bf16_f32 v165, v246, v247\n\t"
    "v_mfma_f32_16x16x32_bf16 v[64:67], a[112:115], v[144:147], v[64:67]\n\t"
    "ds_read_b64_tr_b16 a[136:137], v197 offset:49152\n\t"
    "v_cvt_pk_bf16_f32 v166, v248, v249\n\t"
    "v_cvt_pk_bf16_f32 v167, v250, v251\n\t"
    "v_mfma_f32_16x16x32_bf16 v[68:71], a[112:115], v[148:151], v[68:71]\n\t"
    "ds_read_b64_tr_b16 a[138:139], v197 offset:50176\n\t"
    "s_waitcnt vmcnt(7)\n\t"
    "v_fma_mix_f32 v244, v168, v228, v236 op_sel:[0,0,0] op_sel_hi:[1,0,0]\n\t"
    "v_fma_mix_f32 v245, v168, v229, v237 op_sel:[1,0,0] op_sel_hi:[1,0,0]\n\t"
    "v_mfma_f32_16x16x32_bf16 v[72:75], a[112:115], v[152:155], v[72:75]\n\t"
    "v_fma_mix_f32 v246, v169, v230, v238 op_sel:[0,0,0] op_sel_hi:[1,0,0]\n\t"
    "v_fma_mix_f32 v247, v169, v231, v239 op_sel:[1,0,0] op_sel_hi:[1,0,0]\n\t"
    "v_mfma_f32_16x16x32_bf16 v[76:79], a[112:115], v[156:159], v[76:79]\n\t"
    "v_fma_mix_f32 v248, v170, v232, v240 op_sel:[0,0,0] op_sel_hi:[1,0,0]\n\t"
    "v_fma_mix_f32 v249, v170, v233, v241 op_sel:[1,0,0] op_sel_hi:[1,0,0]\n\t"
    "v_mfma_f32_16x16x32_bf16 v[80:83], a[116:119], v[144:147], v[80:83]\n\t"
    "ds_read_b64_tr_b16 a[140:141], v197 offset:49160\n\t"
    "v_fma_mix_f32 v250, v171, v234, v242 op_sel:[0,0,0] op_sel_hi:[1,0,0]\n\t"
    "v_fma_mix_f32 v251, v171, v235, v243 op_sel:[1,0,0] op_sel_hi:[1,0,0]\n\t"
    "v_mfma_f32_16x16x32_bf16 v[84:87], a[116:119], v[148:151], v[84:87]\n\t"
    "ds_read_b64_tr_b16 a[142:143], v197 offset:50184\n\t"
    "v_cvt_pk_bf16_f32 v168, v244, v245\n\t"
    "v_cvt_pk_bf16_f32 v169, v246, v247\n\t"
    "v_mfma_f32_16x16x32_bf16 v[88:91], a[116:119], v[152:155], v[88:91]\n\t"
    "v_cvt_pk_bf16_f32 v170, v248, v249\n\t"
    "v_cvt_pk_bf16_f32 v171, v250, v251\n\t"
    "v_mfma_f32_16x16x32_bf16 v[92:95], a[116:119], v[156:159], v[92:95]\n\t"
    "s_waitcnt vmcnt(6)\n\t"
    "v_fma_mix_f32 v244, v172, v228, v236 op_sel:[0,0,0] op_sel_hi:[1,0,0]\n\t"
    "v_fma_mix_f32 v245, v172, v229, v237 op_sel:[1,0,0] op_sel_hi:[1,0,0]\n\t"
    "v_mfma_f32_16x16x32_bf16 v[96:99], a[120:123], v[144:147], v[96:99]\n\t"
    "ds_read_b64_tr_b16 a[144:145], v198 offset:49152\n\t"
    "v_fma_mix_f32 v246, v173, v230, v238 op_sel:[0,0,0] op_sel_hi:[1,0,0]\n\t"
    "v_fma_mix_f32 v247, v173, v231, v239 op_sel:[1,0,0] op_sel_hi:[1,0,0]\n\t"
    "v_mfma_f32_16x16x32_bf16 v[100:103], a[120:123], v[148:151], v[100:103]\n\t"
    "ds_read_b64_tr_b16 a[146:147], v198 offset:50176\n\t"
    "v_fma_mix_f32 v248, v174, v232, v240 op_sel:[0,0,0] op_sel_hi:[1,0,0]\n\t"
    "v_fma_mix_f32 v249, v174, v233, v241 op_sel:[1,0,0] op_sel_hi:[1,0,0]\n\t"
    "v_mfma_f32_16x16x32_bf16 v[104:107], a[120:123], v[152:155], v[104:107]\n\t"
    "v_fma_mix_f32 v250, v175, v234, v242 op_sel:[0,0,0] op_sel_hi:[1,0,0]\n\t"
    "v_fma_mix_f32 v251, v175, v235, v243 op_sel:[1,0,0] op_sel_hi:[1,0,0]\n\t"
    "v_mfma_f32_16x16x32_bf16 v[108:111], a[120:123], v[156:159], v[108:111]\n\t"
    "v_cvt_pk_bf16_f32 v172, v244, v245\n\t"
    "v_cvt_pk_bf16_f32 v173, v246, v247\n\t"
    "s_waitcnt lgkmcnt(10)\n\t"
    "v_mfma_f32_16x16x32_bf16 v[112:115], a[124:127], v[144:147], v[112:115]\n\t"
    "ds_read_b64_tr_b16 a[148:149], v198 offset:49160\n\t"
    "v_cvt_pk_bf16_f32 v174, v248, v249\n\t"
    "v_cvt_pk_bf16_f32 v175, v250, v251\n\t"
    "v_mfma_f32_16x16x32_bf16 v[116:119], a[124:127], v[148:151], v[116:119]\n\t"
    "ds_read_b64_tr_b16 a[150:151], v198 offset:50184\n\t"
    "v_mfma_f32_16x16x32_bf16 v[120:123], a[124:127], v[152:155], v[120:123]\n\t"
    "v_mfma_f32_16x16x32_bf16 v[124:127], a[124:127], v[156:159], v[124:127]\n\t"
    "s_waitcnt lgkmcnt(10)\n\t"
    "v_mfma_f32_16x16x32_bf16 v[0:3], a[128:131], v[160:163], v[0:3]\n\t"
    "ds_read_b64_tr_b16 a[152:153], v199 offset:49152\n\t"
    "ds_read_b128 v[228:231], v200 offset:3840\n\t"
    "ds_read_b128 v[232:235], v200 offset:3856\n\t"
    "v_mfma_f32_16x16x32_bf16 v[4:7], a[128:131], v[164:167], v[4:7]\n\t"
    "ds_read_b64_tr_b16 a[154:155], v199 offset:50176\n\t"
    "ds_read_b128 v[236:239], v200 offset:3872\n\t"
    "ds_read_b128 v[240:243], v200 offset:3888\n\t"
    "v_mfma_f32_16x16x32_bf16 v[8:11], a[128:131], v[168:171], v[8:11]\n\t"
    "v_mfma_f32_16x16x32_bf16 v[12:15], a[128:131], v[172:175], v[12:15]\n\t"
    "s_waitcnt lgkmcnt(14)\n\t"
    "v_mfma_f32_16x16x32_bf16 v[16:19], a[132:135], v[160:163], v[16:19]\n\t"
    "ds_read_b64_tr_b16 a[156:157], v199 offset:49160\n\t"
    "v_mfma_f32_16x16x32_bf16 v[20:23], a[132:135], v[164:167], v[20:23]\n\t"
    "ds_read_b64_tr_b16 a[158:159], v199 offset:50184\n\t"
    "s_waitcnt lgkmcnt(2)\n\t"
    "s_waitcnt vmcnt(3)\n\t"
    "v_fma_mix_f32 v244, v176, v228, v236 op_sel:[0,0,0] op_sel_hi:[1,0,0]\n\t"
    "v_fma_mix_f32 v245, v176, v229, v237 op_sel:[1,0,0] op_sel_hi:[1,0,0]\n\t"
    "v_mfma_f32_16x16x32_bf16 v[24:27], a[132:135], v[168:171], v[24:27]\n\t"
    "v_fma_mix_f32 v246, v177, v230, v238 op_sel:[0,0,0] op_sel_hi:[1,0,0]\n\t"
    "v_fma_mix_f32 v247, v177, v231, v239 op_sel:[1,0,0] op_sel_hi:[1,0,0]\n\t"
    "v_mfma_f32_16x16x32_bf16 v[28:31], a[132:135], v[172:175], v[28:31]\n\t"
    "v_fma_mix_f32 v248, v178, v232, v240 op_sel:[0,0,0] op_sel_hi:[1,0,0]\n\t"
    "v_fma_mix_f32 v249, v178, v233, v241 op_sel:[1,0,0] op_sel_hi:[1,0,0]\n\t"
    "v_mfma_f32_16x16x32_bf16 v[32:35], a[136:139], v[160:163], v[32:35]\n\t"
    "ds_read_b64_tr_b16 a[160:161], v196 offset:57344\n\t"
    "v_fma_mix_f32 v250, v179, v234, v242 op_sel:[0,0,0] op_sel_hi:[1,0,0]\n\t"
    "v_fma_mix_f32 v251, v179, v235, v243 op_sel:[1,0,0] op_sel_hi:[1,0,0]\n\t"
    "v_mfma_f32_16x16x32_bf16 v[36:39], a[136:139], v[164:167], v[36:39]\n\t"
    "ds_read_b64_tr_b16 a[162:163], v196 offset:58368\n\t"
    "v_cvt_pk_bf16_f32 v176, v244, v245\n\t"
    "v_cvt_pk_bf16_f32 v177, v246, v247\n\t"
    "v_mfma_f32_16x16x32_bf16 v[40:43], a[136:139], v[168:171], v[40:43]\n\t"
    "v_cvt_pk_bf16_f32 v178, v248, v249\n\t"
    "v_cvt_pk_bf16_f32 v179, v250, v251\n\t"
    "v_mfma_f32_16x16x32_bf16 v[44:47], a[136:139], v[172:175], v[44:47]\n\t"
    "s_waitcnt vmcnt(2)\n\t"
    "v_fma_mix_f32 v244, v180, v228, v236 op_sel:[0,0,0] op_sel_hi:[1,0,0]\n\t"
    "v_fma_mix_f32 v245, v180, v229, v237 op_sel:[1,0,0] op_sel_hi:[1,0,0]\n\t"
    "v_mfma_f32_16x16x32_bf16 v[48:51], a[140:143], v[160:163], v[48:51]\n\t"
    "ds_read_b64_tr_b16 a[164:165], v196 offset:57352\n\t"
    "v_fma_mix_f32 v246, v181, v230, v238 op_sel:[0,0,0] op_sel_hi:[1,0,0]\n\t"
    "v_fma_mix_f32 v247, v181, v231, v239 op_sel:[1,0,0] op_sel_hi:[1,0,0]\n\t"
    "v_mfma_f32_16x16x32_bf16 v[52:55], a[140:143], v[164:167], v[52:55]\n\t"
    "ds_read_b64_tr_b16 a[166:167], v196 offset:58376\n\t"
    "v_fma_mix_f32 v248, v182, v232, v240 op_sel:[0,0,0] op_sel_hi:[1,0,0]\n\t"
    "v_fma_mix_f32 v249, v182, v233, v241 op_sel:[1,0,0] op_sel_hi:[1,0,0]\n\t"
    "v_mfma_f32_16x16x32_bf16 v[56:59], a[140:143], v[168:171], v[56:59]\n\t"
    "v_fma_mix_f32 v250, v183, v234, v242 op_sel:[0,0,0] op_sel_hi:[1,0,0]\n\t"
    "v_fma_mix_f32 v251, v183, v235, v243 op_sel:[1,0,0] op_sel_hi:[1,0,0]\n\t"
    "v_mfma_f32_16x16x32_bf16 v[60:63], a[140:143], v[172:175], v[60:63]\n\t"
    "v_cvt_pk_bf16_f32 v180, v244, v245\n\t"
    "v_cvt_pk_bf16_f32 v181, v246, v247\n\t"
    "v_mfma_f32_16x16x32_bf16 v[64:67], a[144:147], v[160:163], v[64:67]\n\t"
    "ds_read_b64_tr_b16 a[168:169], v197 offset:57344\n\t"
    "v_cvt_pk_bf16_f32 v182, v248, v249\n\t"
    "v_cvt_pk_bf16_f32 v183, v250, v251\n\t"
    "v_mfma_f32_16x16x32_bf16 v[68:71], a[144:147], v[164:167], v[68:71]\n\t"
    "ds_read_b64_tr_b16 a[170:171], v197 offset:58368\n\t"
    "s_waitcnt vmcnt(1)\n\t"
    "v_fma_mix_f32 v244, v184, v228, v236 op_sel:[0,0,0] op_sel_hi:[1,0,0]\n\t"
    "v_fma_mix_f32 v245, v184, v229, v237 op_sel:[1,0,0] op_sel_hi:[1,0,0]\n\t"
    "v_mfma_f32_16x16x32_bf16 v[72:75], a[144:147], v[168:171], v[72:75]\n\t"
    "v_fma_mix_f32 v246, v185, v230, v238 op_sel:[0,0,0] op_sel_hi:[1,0,0]\n\t"
    "v_fma_mix_f32 v247, v185, v231, v239 op_sel:[1,0,0] op_sel_hi:[1,0,0]\n\t"
    "v_mfma_f32_16x16x32_bf16 v[76:79], a[144:147], v[172:175], v[76:79]\n\t"
    "v_fma_mix_f32 v248, v186, v232, v240 op_sel:[0,0,0] op_sel_hi:[1,0,0]\n\t"
    "v_fma_mix_f32 v249, v186, v233, v241 op_sel:[1,0,0] op_sel_hi:[1,0,0]\n\t"
    "v_mfma_f32_16x16x32_bf16 v[80:83], a[148:151], v[160:163], v[80:83]\n\t"
    "ds_read_b64_tr_b16 a[172:173], v197 offset:57352\n\t"
    "v_fma_mix_f32 v250, v187, v234, v242 op_sel:[0,0,0] op_sel_hi:[1,0,0]\n\t"
    "v_fma_mix_f32 v251, v187, v235, v243 op_sel:[1,0,0] op_sel_hi:[1,0,0]\n\t"
    "v_mfma_f32_16x16x32_bf16 v[84:87], a[148:151], v[164:167], v[84:87]\n\t"
    "ds_read_b64_tr_b16 a[174:175], v197 offset:58376\n\t"
    "v_cvt_pk_bf16_f32 v184, v244, v245\n\t"
    "v_cvt_pk_bf16_f32 v185, v246, v247\n\t"
    "v_mfma_f32_16x16x32_bf16 v[88:91], a[148:151], v[168:171], v[88:91]\n\t"
    "v_cvt_pk_bf16_f32 v186, v248, v249\n\t"
    "v_cvt_pk_bf16_f32 v187, v250, v251\n\t"
    "v_mfma_f32_16x16x32_bf16 v[92:95], a[148:151], v[172:175], v[92:95]\n\t"
    "s_waitcnt vmcnt(0)\n\t"
    "v_fma_mix_f32 v244, v188, v228, v236 op_sel:[0,0,0] op_sel_hi:[1,0,0]\n\t"
    "v_fma_mix_f32 v245, v188, v229, v237 op_sel:[1,0,0] op_sel_hi:[1,0,0]\n\t"
    "v_mfma_f32_16x16x32_bf16 v[96:99], a[152:155], v[160:163], v[96:99]\n\t"
    "ds_read_b64_tr_b16 a[176:177], v198 offset:57344\n\t"
    "v_fma_mix_f32 v246, v189, v230, v238 op_sel:[0,0,0] op_sel_hi:[1,0,0]\n\t"
    "v_fma_mix_f32 v247, v189, v231, v239 op_sel:[1,0,0] op_sel_hi:[1,0,0]\n\t"
    "v_mfma_f32_16x16x32_bf16 v[100:103], a[152:155], v[164:167], v[100:103]\n\t"
    "ds_read_b64_tr_b16 a[178:179], v198 offset:58368\n\t"
    "v_fma_mix_f32 v248, v190, v232, v240 op_sel:[0,0,0] op_sel_hi:[1,0,0]\n\t"
    "v_fma_mix_f32 v249, v190, v233, v241 op_sel:[1,0,0] op_sel_hi:[1,0,0]\n\t"
    "v_mfma_f32_16x16x32_bf16 v[104:107], a[152:155], v[168:171], v[104:107]\n\t"
    "v_fma_mix_f32 v250, v191, v234, v242 op_sel:[0,0,0] op_sel_hi:[1,0,0]\n\t"
    "v_fma_mix_f32 v251, v191, v235, v243 op_sel:[1,0,0] op_sel_hi:[1,0,0]\n\t"
    "v_mfma_f32_16x16x32_bf16 v[108:111], a[152:155], v[172:175], v[108:111]\n\t"
    "v_cvt_pk_bf16_f32 v188, v244, v245\n\t"
    "v_cvt_pk_bf16_f32 v189, v246, v247\n\t"
    "s_waitcnt lgkmcnt(10)\n\t"
    "v_mfma_f32_16x16x32_bf16 v[112:115], a[156:159], v[160:163], v[112:115]\n\t"
    "ds_read_b64_tr_b16 a[180:181], v198 offset:57352\n\t"
    "v_cvt_pk_bf16_f32 v190, v248, v249\n\t"
    "v_cvt_pk_bf16_f32 v191, v250, v251\n\t"
    "v_mfma_f32_16x16x32_bf16 v[116:119], a[156:159], v[164:167], v[116:119]\n\t"
    "ds_read_b64_tr_b16 a[182:183], v198 offset:58376\n\t"
    "v_mfma_f32_16x16x32_bf16 v[120:123], a[156:159], v[168:171], v[120:123]\n\t"
    "v_mfma_f32_16x16x32_bf16 v[124:127], a[156:159], v[172:175], v[124:127]\n\t"
    "s_waitcnt lgkmcnt(10)\n\t"
    "v_mfma_f32_16x16x32_bf16 v[0:3], a[160:163], v[176:179], v[0:3]\n\t"
    "ds_read_b64_tr_b16 a[184:185], v199 offset:57344\n\t"
    "v_mfma_f32_16x16x32_bf16 v[4:7], a[160:163], v[180:183], v[4:7]\n\t"
    "ds_read_b64_tr_b16 a[186:187], v199 offset:58368\n\t"
    "v_mfma_f32_16x16x32_bf16 v[8:11], a[160:163], v[184:187], v[8:11]\n\t"
    "v_mfma_f32_16x16x32_bf16 v[12:15], a[160:163], v[188:191], v[12:15]\n\t"
    "s_waitcnt lgkmcnt(10)\n\t"
    "v_mfma_f32_16x16x32_bf16 v[16:19], a[164:167], v[176:179], v[16:19]\n\t"
    "ds_read_b64_tr_b16 a[188:189], v199 offset:57352\n\t"
    "v_mfma_f32_16x16x32_bf16 v[20:23], a[164:167], v[180:183], v[20:23]\n\t"
    "ds_read_b64_tr_b16 a[190:191], v199 offset:58376\n\t"
    "v_mfma_f32_16x16x32_bf16 v[24:27], a[164:167], v[184:187], v[24:27]\n\t"
    "v_mfma_f32_16x16x32_bf16 v[28:31], a[164:167], v[188:191], v[28:31]\n\t"
    "s_waitcnt lgkmcnt(10)\n\t"
    "v_mfma_f32_16x16x32_bf16 v[32:35], a[168:171], v[176:179], v[32:35]\n\t"
    "v_mfma_f32_16x16x32_bf16 v[36:39], a[168:171], v[180:183], v[36:39]\n\t"
    "v_mfma_f32_16x16x32_bf16 v[40:43], a[168:171], v[184:187], v[40:43]\n\t"
    "v_mfma_f32_16x16x32_bf16 v[44:47], a[168:171], v[188:191], v[44:47]\n\t"
    "s_waitcnt lgkmcnt(8)\n\t"
    "v_mfma_f32_16x16x32_bf16 v[48:51], a[172:175], v[176:179], v[48:51]\n\t"
    "v_mfma_f32_16x16x32_bf16 v[52:55], a[172:175], v[180:183], v[52:55]\n\t"
    "v_mfma_f32_16x16x32_bf16 v[56:59], a[172:175], v[184:187], v[56:59]\n\t"
    "v_mfma_f32_16x16x32_bf16 v[60:63], a[172:175], v[188:191], v[60:63]\n\t"
    "s_waitcnt lgkmcnt(6)\n\t"
    "v_mfma_f32_16x16x32_bf16 v[64:67], a[176:179], v[176:179], v[64:67]\n\t"
    "v_mfma_f32_16x16x32_bf16 v[68:71], a[176:179], v[180:183], v[68:71]\n\t"
    "v_mfma_f32_16x16x32_bf16 v[72:75], a[176:179], v[184:187], v[72:75]\n\t"
    "v_mfma_f32_16x16x32_bf16 v[76:79], a[176:179], v[188:191], v[76:79]\n\t"
    "s_waitcnt lgkmcnt(4)\n\t"
    "v_mfma_f32_16x16x32_bf16 v[80:83], a[180:183], v[176:179], v[80:83]\n\t"
    "v_mfma_f32_16x16x32_bf16 v[84:87], a[180:183], v[180:183], v[84:87]\n\t"
    "v_mfma_f32_16x16x32_bf16 v[88:91], a[180:183], v[184:187], v[88:91]\n\t"
    "v_mfma_f32_16x16x32_bf16 v[92:95], a[180:183], v[188:191], v[92:95]\n\t"
    "s_waitcnt lgkmcnt(2)\n\t"
    "v_mfma_f32_16x16x32_bf16 v[96:99], a[184:187], v[176:179], v[96:99]\n\t"
    "v_mfma_f32_16x16x32_bf16 v[100:103], a[184:187], v[180:183], v[100:103]\n\t"
    "v_mfma_f32_16x16x32_bf16 v[104:107], a[184:187], v[184:187], v[104:107]\n\t"
    "v_mfma_f32_16x16x32_bf16 v[108:111], a[184:187], v[188:191], v[108:111]\n\t"
    "s_waitcnt lgkmcnt(0)\n\t"
    "v_mfma_f32_16x16x32_bf16 v[112:115], a[188:191], v[176:179], v[112:115]\n\t"
    "v_mfma_f32_16x16x32_bf16 v[116:119], a[188:191], v[180:183], v[116:119]\n\t"
    "v_mfma_f32_16x16x32_bf16 v[120:123], a[188:191], v[184:187], v[120:123]\n\t"
    "v_mfma_f32_16x16x32_bf16 v[124:127], a[188:191], v[188:191], v[124:127]\n\t"
    "v_lshrrev_b32 v240, 4, v226\n\t"
    "v_and_b32 v241, 15, v240\n\t"
    "v_lshrrev_b32 v242, 4, v240\n\t"
    "v_xor_b32 v243, v241, v242\n\t"
    "v_lshlrev_b32 v241, 8, v241\n\t"
    "v_add_u32 v241, s83, v241\n\t"
    "v_xor_b32 v244, 0, v243\n\t"
    "v_lshl_add_u32 v216, v244, 4, v241\n\t"
    "v_xor_b32 v244, 4, v243\n\t"
    "v_lshl_add_u32 v217, v244, 4, v241\n\t"
    "v_xor_b32 v244, 8, v243\n\t"
    "v_lshl_add_u32 v218, v244, 4, v241\n\t"
    "v_xor_b32 v244, 12, v243\n\t"
    "v_lshl_add_u32 v219, v244, 4, v241\n\t"
    "v_lshrrev_b32 v241, 2, v240\n\t"
    "v_and_b32 v243, 7, v241\n\t"
    "v_lshlrev_b32 v243, 1, v243\n\t"
    "v_lshlrev_b32 v241, 8, v241\n\t"
    "v_add_u32 v241, s83, v241\n\t"
    "v_bfe_u32 v244, v240, 1, 1\n\t"
    "v_xor_b32 v243, v243, v244\n\t"
    "v_and_b32 v244, 1, v240\n\t"
    "v_lshl_add_u32 v241, v244, 3, v241\n\t"
    "v_add_u32 v241, 0x10000, v241\n\t"
    "v_xor_b32 v244, 0, v243\n\t"
    "v_lshl_add_u32 v208, v244, 4, v241\n\t"
    "v_xor_b32 v244, 2, v243\n\t"
    "v_lshl_add_u32 v209, v244, 4, v241\n\t"
    "v_xor_b32 v244, 4, v243\n\t"
    "v_lshl_add_u32 v210, v244, 4, v241\n\t"
    "v_xor_b32 v244, 6, v243\n\t"
    "v_lshl_add_u32 v211, v244, 4, v241\n\t"
    "v_xor_b32 v244, 8, v243\n\t"
    "v_lshl_add_u32 v212, v244, 4, v241\n\t"
    "v_xor_b32 v244, 10, v243\n\t"
    "v_lshl_add_u32 v213, v244, 4, v241\n\t"
    "v_xor_b32 v244, 12, v243\n\t"
    "v_lshl_add_u32 v214, v244, 4, v241\n\t"
    "v_xor_b32 v244, 14, v243\n\t"
    "v_lshl_add_u32 v215, v244, 4, v241\n\t"
    "s_barrier\n\t"
    "ds_read_b128 a[64:67], v216 offset:0\n\t"
    "ds_read_b128 a[80:83], v216 offset:4096\n\t"
    "ds_read_b128 a[68:71], v217 offset:0\n\t"
    "ds_read_b128 a[84:87], v217 offset:4096\n\t"
    "ds_read_b128 a[72:75], v218 offset:0\n\t"
    "ds_read_b128 a[88:91], v218 offset:4096\n\t"
    "ds_read_b128 a[76:79], v219 offset:0\n\t"
    "ds_read_b128 a[92:95], v219 offset:4096\n\t"
    "ds_read_b128 a[96:99], v216 offset:8192\n\t"
    "ds_read_b128 a[112:115], v216 offset:12288\n\t"
    "ds_read_b128 a[100:103], v217 offset:8192\n\t"
    "ds_read_b128 a[116:119], v217 offset:12288\n\t"
    "ds_read_b128 a[104:107], v218 offset:8192\n\t"
    "ds_read_b128 a[120:123], v218 offset:12288\n\t"
    "ds_read_b128 a[108:111], v219 offset:8192\n\t"
    "ds_read_b128 a[124:127], v219 offset:12288\n\t"
    "v_cvt_pk_bf16_f32 v228, v0, v1\n\t"
    "v_cvt_pk_bf16_f32 v229, v2, v3\n\t"
    "v_cvt_pk_bf16_f32 v230, v16, v17\n\t"
    "v_cvt_pk_bf16_f32 v231, v18, v19\n\t"
    "v_accvgpr_write_b32 a0, v228\n\t"
    "v_accvgpr_write_b32 a1, v229\n\t"
    "v_accvgpr_write_b32 a2, v230\n\t"
    "v_accvgpr_write_b32 a3, v231\n\t"
    "v_cvt_pk_bf16_f32 v232, v32, v33\n\t"
    "v_cvt_pk_bf16_f32 v233, v34, v35\n\t"
    "v_cvt_pk_bf16_f32 v234, v48, v49\n\t"
    "v_cvt_pk_bf16_f32 v235, v50, v51\n\t"
    "v_accvgpr_write_b32 a4, v232\n\t"
    "v_accvgpr_write_b32 a5, v233\n\t"
    "v_accvgpr_write_b32 a6, v234\n\t"
    "v_accvgpr_write_b32 a7, v235\n\t"
    "v_cvt_pk_bf16_f32 v228, v64, v65\n\t"
    "v_cvt_pk_bf16_f32 v229, v66, v67\n\t"
    "v_cvt_pk_bf16_f32 v230, v80, v81\n\t"
    "v_cvt_pk_bf16_f32 v231, v82, v83\n\t"
    "v_accvgpr_write_b32 a8, v228\n\t"
    "v_accvgpr_write_b32 a9, v229\n\t"
    "v_accvgpr_write_b32 a10, v230\n\t"
    "v_accvgpr_write_b32 a11, v231\n\t"
    "v_cvt_pk_bf16_f32 v232, v96, v97\n\t"
    "v_cvt_pk_bf16_f32 v233, v98, v99\n\t"
    "v_cvt_pk_bf16_f32 v234, v112, v113\n\t"
    "v_cvt_pk_bf16_f32 v235, v114, v115\n\t"
    "v_accvgpr_write_b32 a12, v232\n\t"
    "v_accvgpr_write_b32 a13, v233\n\t"
    "v_accvgpr_write_b32 a14, v234\n\t"
    "v_accvgpr_write_b32 a15, v235\n\t"
    "s_waitcnt lgkmcnt(0)\n\t"
    "v_mfma_f32_16x16x32_bf16 v[128:131], a[64:67], a[0:3], 0\n\t"
    "v_cvt_pk_bf16_f32 v228, v4, v5\n\t"
    "v_cvt_pk_bf16_f32 v229, v6, v7\n\t"
    "v_mfma_f32_16x16x32_bf16 v[128:131], a[68:71], a[4:7], v[128:131]\n\t"
    "v_cvt_pk_bf16_f32 v230, v20, v21\n\t"
    "v_cvt_pk_bf16_f32 v231, v22, v23\n\t"
    "v_mfma_f32_16x16x32_bf16 v[128:131], a[72:75], a[8:11], v[128:131]\n\t"
    "v_accvgpr_write_b32 a16, v228\n\t"
    "v_accvgpr_write_b32 a17, v229\n\t"
    "v_mfma_f32_16x16x32_bf16 v[128:131], a[76:79], a[12:15], v[128:131]\n\t"
    "v_accvgpr_write_b32 a18, v230\n\t"
    "v_accvgpr_write_b32 a19, v231\n\t"
    "v_mfma_f32_16x16x32_bf16 v[132:135], a[80:83], a[0:3], 0\n\t"
    "v_cvt_pk_bf16_f32 v232, v36, v37\n\t"
    "v_cvt_pk_bf16_f32 v233, v38, v39\n\t"
    "v_mfma_f32_16x16x32_bf16 v[132:135], a[84:87], a[4:7], v[132:135]\n\t"
    "v_cvt_pk_bf16_f32 v234, v52, v53\n\t"
    "v_cvt_pk_bf16_f32 v235, v54, v55\n\t"
    "v_mfma_f32_16x16x32_bf16 v[132:135], a[88:91], a[8:11], v[132:135]\n\t"
    "v_accvgpr_write_b32 a20, v232\n\t"
    "v_accvgpr_write_b32 a21, v233\n\t"
    "v_mfma_f32_16x16x32_bf16 v[132:135], a[92:95], a[12:15], v[132:135]\n\t"
    "v_accvgpr_write_b32 a22, v234\n\t"
    "v_accvgpr_write_b32 a23, v235\n\t"
    "v_mfma_f32_16x16x32_bf16 v[160:163], a[96:99], a[0:3], 0\n\t"
    "v_cvt_pk_bf16_f32 v228, v68, v69\n\t"
    "v_cvt_pk_bf16_f32 v229, v70, v71\n\t"
    "v_mfma_f32_16x16x32_bf16 v[160:163], a[100:103], a[4:7], v[160:163]\n\t"
    "v_cvt_pk_bf16_f32 v230, v84, v85\n\t"
    "v_cvt_pk_bf16_f32 v231, v86, v87\n\t"
    "v_mfma_f32_16x16x32_bf16 v[160:163], a[104:107], a[8:11], v[160:163]\n\t"
    "v_accvgpr_write_b32 a24, v228\n\t"
    "v_accvgpr_write_b32 a25, v229\n\t"
    "v_mfma_f32_16x16x32_bf16 v[160:163], a[108:111], a[12:15], v[160:163]\n\t"
    "v_accvgpr_write_b32 a26, v230\n\t"
    "v_accvgpr_write_b32 a27, v231\n\t"
    "v_mfma_f32_16x16x32_bf16 v[164:167], a[112:115], a[0:3], 0\n\t"
    "v_cvt_pk_bf16_f32 v232, v100, v101\n\t"
    "v_cvt_pk_bf16_f32 v233, v102, v103\n\t"
    "v_mfma_f32_16x16x32_bf16 v[164:167], a[116:119], a[4:7], v[164:167]\n\t"
    "v_cvt_pk_bf16_f32 v234, v116, v117\n\t"
    "v_cvt_pk_bf16_f32 v235, v118, v119\n\t"
    "v_mfma_f32_16x16x32_bf16 v[164:167], a[120:123], a[8:11], v[164:167]\n\t"
    "v_accvgpr_write_b32 a28, v232\n\t"
    "v_accvgpr_write_b32 a29, v233\n\t"
    "v_mfma_f32_16x16x32_bf16 v[164:167], a[124:127], a[12:15], v[164:167]\n\t"
    "v_accvgpr_write_b32 a30, v234\n\t"
    "v_accvgpr_write_b32 a31, v235\n\t"
    "v_mfma_f32_16x16x32_bf16 v[136:139], a[64:67], a[16:19], 0\n\t"
    "v_cvt_pk_bf16_f32 v228, v8, v9\n\t"
    "v_cvt_pk_bf16_f32 v229, v10, v11\n\t"
    "v_cvt_pk_bf16_f32 v230, v24, v25\n\t"
    "v_cvt_pk_bf16_f32 v231, v26, v27\n\t"
    "v_accvgpr_write_b32 a32, v228\n\t"
    "v_mfma_f32_16x16x32_bf16 v[136:139], a[68:71], a[20:23], v[136:139]\n\t"
    "v_accvgpr_write_b32 a33, v229\n\t"
    "v_accvgpr_write_b32 a34, v230\n\t"
    "v_accvgpr_write_b32 a35, v231\n\t"
    "v_cvt_pk_bf16_f32 v232, v40, v41\n\t"
    "v_cvt_pk_bf16_f32 v233, v42, v43\n\t"
    "v_mfma_f32_16x16x32_bf16 v[136:139], a[72:75], a[24:27], v[136:139]\n\t"
    "v_cvt_pk_bf16_f32 v234, v56, v57\n\t"
    "v_cvt_pk_bf16_f32 v235, v58, v59\n\t"
    "v_accvgpr_write_b32 a36, v232\n\t"
    "v_accvgpr_write_b32 a37, v233\n\t"
    "v_accvgpr_write_b32 a38, v234\n\t"
    "v_mfma_f32_16x16x32_bf16 v[136:139], a[76:79], a[28:31], v[136:139]\n\t"
    "v_accvgpr_write_b32 a39, v235\n\t"
    "v_cvt_pk_bf16_f32 v228, v72, v73\n\t"
    "v_cvt_pk_bf16_f32 v229, v74, v75\n\t"
    "v_cvt_pk_bf16_f32 v230, v88, v89\n\t"
    "v_cvt_pk_bf16_f32 v231, v90, v91\n\t"
    "v_mfma_f32_16x16x32_bf16 v[140:143], a[80:83], a[16:19], 0\n\t"
    "v_accvgpr_write_b32 a40, v228\n\t"
    "v_accvgpr_write_b32 a41, v229\n\t"
    "v_accvgpr_write_b32 a42, v230\n\t"
    "v_accvgpr_write_b32 a43, v231\n\t"
    "v_cvt_pk_bf16_f32 v232, v104, v105\n\t"
    "v_mfma_f32_16x16x32_bf16 v[140:143], a[84:87], a[20:23], v[140:143]\n\t"
    "v_cvt_pk_bf16_f32 v233, v106, v107\n\t"
    "v_cvt_pk_bf16_f32 v234, v120, v121\n\t"
    "v_cvt_pk_bf16_f32 v235, v122, v123\n\t"
    "v_accvgpr_write_b32 a44, v232\n\t"
    "v_accvgpr_write_b32 a45, v233\n\t"
    "v_mfma_f32_16x16x32_bf16 v[140:143], a[88:91], a[24:27], v[140:143]\n\t"
    "v_accvgpr_write_b32 a46, v234\n\t"
    "v_accvgpr_write_b32 a47, v235\n\t"
    "v_mfma_f32_16x16x32_bf16 v[140:143], a[92:95], a[28:31], v[140:143]\n\t"
    "v_max3_f32 v240, v128, v129, v130\n\t"
    "v_max3_f32 v240, v240, v131, v132\n\t"
    "v_mfma_f32_16x16x32_bf16 v[168:171], a[96:99], a[16:19], 0\n\t"
    "v_max3_f32 v240, v240, v133, v134\n\t"
    "v_max3_f32 v240, v240, v135, v160\n\t"
    "v_max3_f32 v240, v240, v161, v162\n\t"
    "v_max3_f32 v240, v240, v163, v164\n\t"
    "v_max3_f32 v240, v240, v165, v166\n\t"
    "v_mfma_f32_16x16x32_bf16 v[168:171], a[100:103], a[20:23], v[168:171]\n\t"
    "v_max_f32 v240, v240, v167\n\t"
    "v_mov_b32 v241, v240\n\t"
    "s_nop 1\n\t"
    "v_permlane32_swap_b32 v240, v241\n\t"
    "v_max_f32 v240, v240, v241\n\t"
    "v_mfma_f32_16x16x32_bf16 v[168:171], a[104:107], a[24:27], v[168:171]\n\t"
    "v_mov_b32 v241, v240\n\t"
    "s_nop 1\n\t"
    "v_permlane16_swap_b32 v240, v241\n\t"
    "v_max_f32 v240, v240, v241\n\t"
    "v_add_f32 v240, s69, v240\n\t"
    "v_mfma_f32_16x16x32_bf16 v[168:171], a[108:111], a[28:31], v[168:171]\n\t"
    "v_sub_f32 v192, 0, v240\n\t"
    "v_sub_f32 v193, 0, v240\n\t"
    "v_sub_f32 v194, 0, v240\n\t"
    "v_sub_f32 v195, 0, v240\n\t"
    "v_sub_f32 v128, v128, v240\n\t"
    "v_mfma_f32_16x16x32_bf16 v[172:175], a[112:115], a[16:19], 0\n\t"
    "v_sub_f32 v129, v129, v240\n\t"
    "v_sub_f32 v130, v130, v240\n\t"
    "v_sub_f32 v131, v131, v240\n\t"
    "v_sub_f32 v132, v132, v240\n\t"
    "v_sub_f32 v133, v133, v240\n\t"
    "v_mfma_f32_16x16x32_bf16 v[172:175], a[116:119], a[20:23], v[172:175]\n\t"
    "v_sub_f32 v134, v134, v240\n\t"
    "v_sub_f32 v135, v135, v240\n\t"
    "v_mfma_f32_16x16x32_bf16 v[172:175], a[120:123], a[24:27], v[172:175]\n\t"
    "v_mfma_f32_16x16x32_bf16 v[172:175], a[124:127], a[28:31], v[172:175]\n\t"
    "v_mfma_f32_16x16x32_bf16 v[144:147], a[64:67], a[32:35], 0\n\t"
    "v_cvt_pk_bf16_f32 v228, v12, v13\n\t"
    "v_cvt_pk_bf16_f32 v229, v14, v15\n\t"
    "v_cvt_pk_bf16_f32 v230, v28, v29\n\t"
    "v_cvt_pk_bf16_f32 v231, v30, v31\n\t"
    "v_accvgpr_write_b32 a48, v228\n\t"
    "v_mfma_f32_16x16x32_bf16 v[144:147], a[68:71], a[36:39], v[144:147]\n\t"
    "v_accvgpr_write_b32 a49, v229\n\t"
    "v_accvgpr_write_b32 a50, v230\n\t"
    "v_accvgpr_write_b32 a51, v231\n\t"
    "v_cvt_pk_bf16_f32 v232, v44, v45\n\t"
    "v_cvt_pk_bf16_f32 v233, v46, v47\n\t"
    "v_mfma_f32_16x16x32_bf16 v[144:147], a[72:75], a[40:43], v[144:147]\n\t"
    "v_cvt_pk_bf16_f32 v234, v60, v61\n\t"
    "v_cvt_pk_bf16_f32 v235, v62, v63\n\t"
    "v_accvgpr_write_b32 a52, v232\n\t"
    "v_accvgpr_write_b32 a53, v233\n\t"
    "v_accvgpr_write_b32 a54, v234\n\t"
    "v_mfma_f32_16x16x32_bf16 v[144:147], a[76:79], a[44:47], v[144:147]\n\t"
    "v_accvgpr_write_b32 a55, v235\n\t"
    "v_cvt_pk_bf16_f32 v228, v76, v77\n\t"
    "v_cvt_pk_bf16_f32 v229, v78, v79\n\t"
    "v_cvt_pk_bf16_f32 v230, v92, v93\n\t"
    "v_cvt_pk_bf16_f32 v231, v94, v95\n\t"
    "v_mfma_f32_16x16x32_bf16 v[148:151], a[80:83], a[32:35], 0\n\t"
    "v_accvgpr_write_b32 a56, v228\n\t"
    "v_accvgpr_write_b32 a57, v229\n\t"
    "v_accvgpr_write_b32 a58, v230\n\t"
    "v_accvgpr_write_b32 a59, v231\n\t"
    "v_cvt_pk_bf16_f32 v232, v108, v109\n\t"
    "v_mfma_f32_16x16x32_bf16 v[148:151], a[84:87], a[36:39], v[148:151]\n\t"
    "v_cvt_pk_bf16_f32 v233, v110, v111\n\t"
    "v_cvt_pk_bf16_f32 v234, v124, v125\n\t"
    "v_cvt_pk_bf16_f32 v235, v126, v127\n\t"
    "v_accvgpr_write_b32 a60, v232\n\t"
    "v_accvgpr_write_b32 a61, v233\n\t"
    "v_mfma_f32_16x16x32_bf16 v[148:151], a[88:91], a[40:43], v[148:151]\n\t"
    "v_accvgpr_write_b32 a62, v234\n\t"
    "v_accvgpr_write_b32 a63, v235\n\t"
    "v_mfma_f32_16x16x32_bf16 v[148:151], a[92:95], a[44:47], v[148:151]\n\t"
    "v_max3_f32 v240, v136, v137, v138\n\t"
    "v_max3_f32 v240, v240, v139, v140\n\t"
    "v_mfma_f32_16x16x32_bf16 v[176:179], a[96:99], a[32:35], 0\n\t"
    "v_max3_f32 v240, v240, v141, v142\n\t"
    "v_max3_f32 v240, v240, v143, v168\n\t"
    "v_max3_f32 v240, v240, v169, v170\n\t"
    "v_max3_f32 v240, v240, v171, v172\n\t"
    "v_max3_f32 v240, v240, v173, v174\n\t"
    "v_mfma_f32_16x16x32_bf16 v[176:179], a[100:103], a[36:39], v[176:179]\n\t"
    "v_max_f32 v240, v240, v175\n\t"
    "v_mov_b32 v241, v240\n\t"
    "s_nop 1\n\t"
    "v_permlane32_swap_b32 v240, v241\n\t"
    "v_max_f32 v240, v240, v241\n\t"
    "v_mfma_f32_16x16x32_bf16 v[176:179], a[104:107], a[40:43], v[176:179]\n\t"
    "v_mov_b32 v241, v240\n\t"
    "s_nop 1\n\t"
    "v_permlane16_swap_b32 v240, v241\n\t"
    "v_max_f32 v240, v240, v241\n\t"
    "v_add_f32 v240, s69, v240\n\t"
    "v_mfma_f32_16x16x32_bf16 v[176:179], a[108:111], a[44:47], v[176:179]\n\t"
    "v_sub_f32 v196, 0, v240\n\t"
    "v_sub_f32 v197, 0, v240\n\t"
    "v_sub_f32 v198, 0, v240\n\t"
    "v_sub_f32 v199, 0, v240\n\t"
    "v_sub_f32 v136, v136, v240\n\t"
    "v_mfma_f32_16x16x32_bf16 v[180:183], a[112:115], a[32:35], 0\n\t"
    "v_sub_f32 v137, v137, v240\n\t"
    "v_sub_f32 v138, v138, v240\n\t"
    "v_sub_f32 v139, v139, v240\n\t"
    "v_sub_f32 v140, v140, v240\n\t"
    "v_sub_f32 v141, v141, v240\n\t"
    "v_mfma_f32_16x16x32_bf16 v[180:183], a[116:119], a[36:39], v[180:183]\n\t"
    "v_sub_f32 v142, v142, v240\n\t"
    "v_sub_f32 v143, v143, v240\n\t"
    "v_mfma_f32_16x16x32_bf16 v[180:183], a[120:123], a[40:43], v[180:183]\n\t"
    "v_mfma_f32_16x16x32_bf16 v[180:183], a[124:127], a[44:47], v[180:183]\n\t"
    "v_mfma_f32_16x16x32_bf16 v[152:155], a[64:67], a[48:51], 0\n\t"
    "v_mfma_f32_16x16x32_bf16 v[152:155], a[68:71], a[52:55], v[152:155]\n\t"
    "v_mfma_f32_16x16x32_bf16 v[152:155], a[72:75], a[56:59], v[152:155]\n\t"
    "v_max3_f32 v240, v144, v145, v146\n\t"
    "v_max3_f32 v240, v240, v147, v148\n\t"
    "v_max3_f32 v240, v240, v149, v150\n\t"
    "v_mfma_f32_16x16x32_bf16 v[152:155], a[76:79], a[60:63], v[152:155]\n\t"
    "v_max3_f32 v240, v240, v151, v176\n\t"
    "v_max3_f32 v240, v240, v177, v178\n\t"
    "v_max3_f32 v240, v240, v179, v180\n\t"
    "v_mfma_f32_16x16x32_bf16 v[156:159], a[80:83], a[48:51], 0\n\t"
    "v_max3_f32 v240, v240, v181, v182\n\t"
    "v_max_f32 v240, v240, v183\n\t"
    "v_mov_b32 v241, v240\n\t"
    "v_mfma_f32_16x16x32_bf16 v[156:159], a[84:87], a[52:55], v[156:159]\n\t"
    "s_nop 1\n\t"
    "v_permlane32_swap_b32 v240, v241\n\t"
    "v_max_f32 v240, v240, v241\n\t"
    "v_mfma_f32_16x16x32_bf16 v[156:159], a[88:91], a[56:59], v[156:159]\n\t"
    "v_mov_b32 v241, v240\n\t"
    "s_nop 1\n\t"
    "v_permlane16_swap_b32 v240, v241\n\t"
    "v_mfma_f32_16x16x32_bf16 v[156:159], a[92:95], a[60:63], v[156:159]\n\t"
    "v_max_f32 v240, v240, v241\n\t"
    "v_add_f32 v240, s69, v240\n\t"
    "v_sub_f32 v200, 0, v240\n\t"
    "v_mfma_f32_16x16x32_bf16 v[184:187], a[96:99], a[48:51], 0\n\t"
    "v_sub_f32 v201, 0, v240\n\t"
    "v_sub_f32 v202, 0, v240\n\t"
    "v_sub_f32 v203, 0, v240\n\t"
    "v_mfma_f32_16x16x32_bf16 v[184:187], a[100:103], a[52:55], v[184:187]\n\t"
    "v_sub_f32 v144, v144, v240\n\t"
    "v_sub_f32 v145, v145, v240\n\t"
    "v_sub_f32 v146, v146, v240\n\t"
    "v_mfma_f32_16x16x32_bf16 v[184:187], a[104:107], a[56:59], v[184:187]\n\t"
    "v_sub_f32 v147, v147, v240\n\t"
    "v_sub_f32 v148, v148, v240\n\t"
    "v_sub_f32 v149, v149, v240\n\t"
    "v_mfma_f32_16x16x32_bf16 v[184:187], a[108:111], a[60:63], v[184:187]\n\t"
    "v_sub_f32 v150, v150, v240\n\t"
    "v_sub_f32 v151, v151, v240\n\t"
    "v_mfma_f32_16x16x32_bf16 v[188:191], a[112:115], a[48:51], 0\n\t"
    "v_mfma_f32_16x16x32_bf16 v[188:191], a[116:119], a[52:55], v[188:191]\n\t"
    "v_mfma_f32_16x16x32_bf16 v[188:191], a[120:123], a[56:59], v[188:191]\n\t"
    "v_mfma_f32_16x16x32_bf16 v[188:191], a[124:127], a[60:63], v[188:191]\n\t"
    "v_max3_f32 v240, v152, v153, v154\n\t"
    "v_max3_f32 v240, v240, v155, v156\n\t"
    "v_max3_f32 v240, v240, v157, v158\n\t"
    "v_max3_f32 v240, v240, v159, v184\n\t"
    "v_max3_f32 v240, v240, v185, v186\n\t"
    "s_nop 2\n\t"
    "v_max3_f32 v240, v240, v187, v188\n\t"
    "v_max3_f32 v240, v240, v189, v190\n\t"
    "v_max_f32 v240, v240, v191\n\t"
    "v_mov_b32 v241, v240\n\t"
    "s_nop 1\n\t"
    "v_permlane32_swap_b32 v240, v241\n\t"
    "v_max_f32 v240, v240, v241\n\t"
    "v_mov_b32 v241, v240\n\t"
    "s_nop 1\n\t"
    "v_permlane16_swap_b32 v240, v241\n\t"
    "v_max_f32 v240, v240, v241\n\t"
    "v_add_f32 v240, s69, v240\n\t"
    "v_sub_f32 v204, 0, v240\n\t"
    "v_sub_f32 v205, 0, v240\n\t"
    "v_sub_f32 v206, 0, v240\n\t"
    "v_sub_f32 v207, 0, v240\n\t"
    "v_sub_f32 v152, v152, v240\n\t"
    "v_sub_f32 v153, v153, v240\n\t"
    "v_sub_f32 v154, v154, v240\n\t"
    "v_sub_f32 v155, v155, v240\n\t"
    "v_sub_f32 v156, v156, v240\n\t"
    "v_sub_f32 v157, v157, v240\n\t"
    "v_sub_f32 v158, v158, v240\n\t"
    "v_sub_f32 v159, v159, v240\n\t"
    "v_mov_b32 v236, 0\n\t"
    "v_mov_b32 v237, 0\n\t"
    "v_mov_b32 v238, 0\n\t"
    "v_mov_b32 v239, 0\n\t"
    "s_nop 1\n\t"
    "v_mov_b32 v240, 0x3f803f80\n\t"
    "v_accvgpr_write_b32 a252, v240\n\t"
    "v_accvgpr_write_b32 a253, v240\n\t"
    "v_accvgpr_write_b32 a254, v240\n\t"
    "v_accvgpr_write_b32 a255, v240\n\t"
    "ds_read_b128 a[96:99], v216 offset:8192\n\t"
    "ds_read_b128 a[112:115], v216 offset:12288\n\t"
    "ds_read_b128 a[100:103], v217 offset:8192\n\t"
    "s_mov_b32 m0, s64\n\t"
    "ds_read_b128 a[116:119], v217 offset:12288\n\t"
    "buffer_load_dwordx4 v224, s[40:43], s57 offen offset:0 lds\n\t"
    "ds_read_b128 a[104:107], v218 offset:8192\n\t"
    "ds_read_b128 a[120:123], v218 offset:12288\n\t"
    "ds_read_b128 a[108:111], v219 offset:8192\n\t"
    "ds_read_b128 a[124:127], v219 offset:12288\n\t"
    "v_exp_f32 v228, v128\n\t"
    "v_exp_f32 v229, v129\n\t"
    "buffer_load_dwordx4 v225, s[40:43], s57 offen offset:1024 lds\n\t"
    "v_exp_f32 v230, v130\n\t"
    "ds_read_b64_tr_b16 a[128:129], v208 offset:0\n\t"
    "v_exp_f32 v231, v131\n\t"
    "ds_read_b64_tr_b16 a[130:131], v208 offset:4096\n\t"
    "v_cvt_pk_bf16_f32 v160, v228, v229\n\t"
    "v_exp_f32 v228, v132\n\t"
    "ds_read_b64_tr_b16 a[132:133], v209 offset:0\n\t"
    "v_cvt_pk_bf16_f32 v161, v230, v231\n\t"
    "v_exp_f32 v229, v133\n\t"
    "ds_read_b64_tr_b16 a[134:135], v209 offset:4096\n\t"
    "v_exp_f32 v230, v134\n\t"
    "ds_read_b64_tr_b16 a[136:137], v210 offset:0\n\t"
    "v_cvt_pk_bf16_f32 v162, v228, v229\n\t"
    "v_exp_f32 v231, v135\n\t"
    "ds_read_b64_tr_b16 a[138:139], v210 offset:4096\n\t"
    "v_exp_f32 v228, v136\n\t"
    "ds_read_b64_tr_b16 a[140:141], v211 offset:0\n\t"
    "v_cvt_pk_bf16_f32 v163, v230, v231\n\t"
    "v_exp_f32 v229, v137\n\t"
    "ds_read_b64_tr_b16 a[142:143], v211 offset:4096\n\t"
    "v_mfma_f32_16x16x32_bf16 v[252:255], a[252:255], v[160:163], 0\n\t"
    "buffer_load_dwordx4 v224, s[40:43], s57 offen offset:2048 lds\n\t"
    "v_exp_f32 v230, v138\n\t"
    "v_add_f32 v232, v228, v229\n\t"
    "v_cvt_pk_bf16_f32 v164, v228, v229\n\t"
    "v_exp_f32 v231, v139\n\t"
    "ds_read_b64_tr_b16 a[144:145], v212 offset:0\n\t"
    "v_add_f32 v232, v232, v230\n\t"
    "v_exp_f32 v228, v140\n\t"
    "ds_read_b64_tr_b16 a[146:147], v212 offset:4096\n\t"
    "v_add_f32 v232, v232, v231\n\t"
    "v_cvt_pk_bf16_f32 v165, v230, v231\n\t"
    "v_exp_f32 v229, v141\n\t"
    "ds_read_b64_tr_b16 a[148:149], v213 offset:0\n\t"
    "v_add_f32 v232, v232, v228\n\t"
    "v_exp_f32 v230, v142\n\t"
    "v_add_f32 v232, v232, v229\n\t"
    "v_cvt_pk_bf16_f32 v166, v228, v229\n\t"
    "v_exp_f32 v231, v143\n\t"
    "ds_read_b64_tr_b16 a[150:151], v213 offset:4096\n\t"
    "v_add_f32 v232, v232, v230\n\t"
    "v_exp_f32 v228, v144\n\t"
    "v_add_f32 v232, v232, v231\n\t"
    "v_cvt_pk_bf16_f32 v167, v230, v231\n\t"
    "v_exp_f32 v229, v145\n\t"
    "buffer_load_dwordx4 v225, s[40:43], s57 offen offset:3072 lds\n\t"
    "v_max_f32 v248, v232, v252\n\t"
    "v_exp_f32 v230, v146\n\t"
    "ds_read_b64_tr_b16 a[152:153], v214 offset:0\n\t"
    "s_add_u32 s57, s57, 0x4000\n\t"
    "s_and_b32 s57, s57, 0x7ffff\n\t"
    "s_cselect_b64 s[40:41], s[40:41], s[78:79]\n\t"
    "v_cmp_lt_f32 vcc, s68, v248\n\t"
    "v_exp_f32 v231, v147\n\t"
    "ds_read_b64_tr_b16 a[154:155], v214 offset:4096\n\t"
    "v_cvt_pk_bf16_f32 v168, v228, v229\n\t"
    "v_exp_f32 v228, v148\n\t"
    "s_cbranch_vccnz L_rs0_%=\n\t"
    "L_ret0_%=:\n\t"
    "ds_read_b64_tr_b16 a[156:157], v215 offset:0\n\t"
    "v_cvt_pk_bf16_f32 v169, v230, v231\n\t"
    "v_exp_f32 v229, v149\n\t"
    "ds_read_b64_tr_b16 a[158:159], v215 offset:4096\n\t"
    "v_add_f32 v236, v236, v252\n\t"
    "s_waitcnt lgkmcnt(15)\n\t"
    "v_mfma_f32_16x16x32_bf16 v[128:131], a[96:99], a[0:3], v[192:195]\n\t"
    "v_exp_f32 v230, v150\n\t"
    "v_mfma_f32_16x16x32_bf16 v[136:139], a[96:99], a[16:19], v[196:199]\n\t"
    "ds_read_b64_tr_b16 a[160:161], v208 offset:8192\n\t"
    "v_cvt_pk_bf16_f32 v170, v228, v229\n\t"
    "s_barrier\n\t"
    "v_mfma_f32_16x16x32_bf16 v[132:135], a[112:115], a[0:3], v[192:195]\n\t"
    "v_exp_f32 v231, v151\n\t"
    "v_mfma_f32_16x16x32_bf16 v[140:143], a[112:115], a[16:19], v[196:199]\n\t"
    "ds_read_b128 a[64:67], v216 offset:16384\n\t"
    "v_add_f32 v237, v237, v232\n\t"
    "v_mfma_f32_16x16x32_bf16 v[128:131], a[100:103], a[4:7], v[128:131]\n\t"
    "v_exp_f32 v228, v152\n\t"
    "v_mfma_f32_16x16x32_bf16 v[136:139], a[100:103], a[20:23], v[136:139]\n\t"
    "ds_read_b128 a[80:83], v216 offset:20480\n\t"
    "s_mov_b32 m0, s61\n\t"
    "v_cvt_pk_bf16_f32 v171, v230, v231\n\t"
    "v_mfma_f32_16x16x32_bf16 v[132:135], a[116:119], a[4:7], v[132:135]\n\t"
    "v_exp_f32 v229, v153\n\t"
    "v_mfma_f32_16x16x32_bf16 v[140:143], a[116:119], a[20:23], v[140:143]\n\t"
    "ds_read_b128 a[68:71], v217 offset:16384\n\t"
    "v_mfma_f32_16x16x32_bf16 v[252:255], a[252:255], v[168:171], 0\n\t"
    "buffer_load_dwordx4 v220, s[36:39], s56 offen offset:0 lds\n\t"
    "v_mfma_f32_16x16x32_bf16 v[128:131], a[104:107], a[8:11], v[128:131]\n\t"
    "v_exp_f32 v230, v154\n\t"
    "v_mfma_f32_16x16x32_bf16 v[136:139], a[104:107], a[24:27], v[136:139]\n\t"
    "s_lshl_b32 s70, %5, 2\n\t"
    "s_add_u32 s70, s70, s80\n\t"
    "s_lshl_b32 s71, s70, 12\n\t"
    "s_mov_b64 s[48:49], %9\n\t"
    "s_add_u32 s48, s48, s71\n\t"
    "s_addc_u32 s49, s49, 0\n\t"
    "s_and_b32 s49, s49, 0xffff\n\t"
    "s_mov_b32 s50, 4096\n\t"
    "s_mov_b32 s51, 0x20000\n\t"
    "s_mul_i32 s71, s70, 1792\n\t"
    "buffer_load_dwordx4 a[192:195], v226, s[48:51], 0 offen offset:0\n\t"
    "v_add_f32 v233, v228, v229\n\t"
    "v_cvt_pk_bf16_f32 v172, v228, v229\n\t"
    "v_mfma_f32_16x16x32_bf16 v[132:135], a[120:123], a[8:11], v[132:135]\n\t"
    "v_exp_f32 v231, v155\n\t"
    "v_mfma_f32_16x16x32_bf16 v[140:143], a[120:123], a[24:27], v[140:143]\n\t"
    "ds_read_b128 a[84:87], v217 offset:20480\n\t"
    "v_add_f32 v233, v233, v230\n\t"
    "v_mfma_f32_16x16x32_bf16 v[128:131], a[108:111], a[12:15], v[128:131]\n\t"
    "v_exp_f32 v228, v156\n\t"
    "v_mfma_f32_16x16x32_bf16 v[136:139], a[108:111], a[28:31], v[136:139]\n\t"
    "ds_read_b128 a[72:75], v218 offset:16384\n\t"
    "v_add_f32 v233, v233, v231\n\t"
    "v_cvt_pk_bf16_f32 v173, v230, v231\n\t"
    "v_mfma_f32_16x16x32_bf16 v[132:135], a[124:127], a[12:15], v[132:135]\n\t"
    "v_exp_f32 v229, v157\n\t"
    "v_mfma_f32_16x16x32_bf16 v[140:143], a[124:127], a[28:31], v[140:143]\n\t"
    "ds_read_b128 a[88:91], v218 offset:20480\n\t"
    "v_add_f32 v233, v233, v228\n\t"
    "s_waitcnt lgkmcnt(7)\n\t"
    "v_mfma_f32_16x16x32_bf16 v[0:3], a[128:131], v[160:163], 0\n\t"
    "v_exp_f32 v230, v158\n\t"
    "v_mfma_f32_16x16x32_bf16 v[4:7], a[128:131], v[164:167], 0\n\t"
    "buffer_load_dwordx4 a[196:199], v226, s[48:51], 0 offen offset:1024\n\t"
    "v_add_f32 v233, v233, v229\n\t"
    "v_cvt_pk_bf16_f32 v174, v228, v229\n\t"
    "v_mfma_f32_16x16x32_bf16 v[16:19], a[132:135], v[160:163], 0\n\t"
    "v_exp_f32 v231, v159\n\t"
    "v_mfma_f32_16x16x32_bf16 v[20:23], a[132:135], v[164:167], 0\n\t"
    "ds_read_b128 a[76:79], v219 offset:16384\n\t"
    "v_add_f32 v233, v233, v230\n\t"
    "v_mfma_f32_16x16x32_bf16 v[32:35], a[136:139], v[160:163], 0\n\t"
    "v_exp_f32 v228, v128\n\t"
    "v_mfma_f32_16x16x32_bf16 v[36:39], a[136:139], v[164:167], 0\n\t"
    "v_add_f32 v233, v233, v231\n\t"
    "v_cvt_pk_bf16_f32 v175, v230, v231\n\t"
    "v_mfma_f32_16x16x32_bf16 v[48:51], a[140:143], v[160:163], 0\n\t"
    "v_exp_f32 v229, v129\n\t"
    "v_mfma_f32_16x16x32_bf16 v[52:55], a[140:143], v[164:167], 0\n\t"
    "buffer_load_dwordx4 v221, s[36:39], s56 offen offset:1024 lds\n\t"
    "v_max_f32 v248, v233, v252\n\t"
    "v_mfma_f32_16x16x32_bf16 v[64:67], a[144:147], v[160:163], 0\n\t"
    "v_exp_f32 v230, v130\n\t"
    "v_mfma_f32_16x16x32_bf16 v[68:71], a[144:147], v[164:167], 0\n\t"
    "ds_read_b128 a[92:95], v219 offset:20480\n\t"
    "buffer_load_dwordx4 a[200:203], v226, s[48:51], 0 offen offset:2048\n\t"
    "v_cmp_lt_f32 vcc, s68, v248\n\t"
    "v_mfma_f32_16x16x32_bf16 v[80:83], a[148:151], v[160:163], 0\n\t"
    "v_exp_f32 v231, v131\n\t"
    "v_mfma_f32_16x16x32_bf16 v[84:87], a[148:151], v[164:167], 0\n\t"
    "ds_read_b64_tr_b16 a[162:163], v208 offset:12288\n\t"
    "v_cvt_pk_bf16_f32 v176, v228, v229\n\t"
    "v_mfma_f32_16x16x32_bf16 v[96:99], a[152:155], v[160:163], 0\n\t"
    "v_exp_f32 v228, v132\n\t"
    "v_mfma_f32_16x16x32_bf16 v[100:103], a[152:155], v[164:167], 0\n\t"
    "s_cbranch_vccnz L_rs1_%=\n\t"
    "L_ret1_%=:\n\t"
    "ds_read_b64_tr_b16 a[164:165], v209 offset:8192\n\t"
    "v_cvt_pk_bf16_f32 v177, v230, v231\n\t"
    "v_mfma_f32_16x16x32_bf16 v[112:115], a[156:159], v[160:163], 0\n\t"
    "v_exp_f32 v229, v133\n\t"
    "v_mfma_f32_16x16x32_bf16 v[116:119], a[156:159], v[164:167], 0\n\t"
    "ds_read_b64_tr_b16 a[166:167], v209 offset:12288\n\t"
    "v_add_f32 v238, v238, v252\n\t"
    "v_mfma_f32_16x16x32_bf16 v[144:147], a[96:99], a[32:35], v[200:203]\n\t"
    "v_exp_f32 v230, v134\n\t"
    "v_mfma_f32_16x16x32_bf16 v[152:155], a[96:99], a[48:51], v[204:207]\n\t"
    "ds_read_b64_tr_b16 a[168:169], v210 offset:8192\n\t"
    "buffer_load_dwordx4 a[204:207], v226, s[48:51], 0 offen offset:3072\n\t"
    "v_cvt_pk_bf16_f32 v178, v228, v229\n\t"
    "v_mfma_f32_16x16x32_bf16 v[148:151], a[112:115], a[32:35], v[200:203]\n\t"
    "v_exp_f32 v231, v135\n\t"
    "v_mfma_f32_16x16x32_bf16 v[156:159], a[112:115], a[48:51], v[204:207]\n\t"
    "ds_read_b64_tr_b16 a[170:171], v210 offset:12288\n\t"
    "v_add_f32 v239, v239, v233\n\t"
    "v_mfma_f32_16x16x32_bf16 v[144:147], a[100:103], a[36:39], v[144:147]\n\t"
    "v_exp_f32 v228, v136\n\t"
    "v_mfma_f32_16x16x32_bf16 v[152:155], a[100:103], a[52:55], v[152:155]\n\t"
    "ds_read_b64_tr_b16 a[172:173], v211 offset:8192\n\t"
    "v_cvt_pk_bf16_f32 v179, v230, v231\n\t"
    "v_mfma_f32_16x16x32_bf16 v[148:151], a[116:119], a[36:39], v[148:151]\n\t"
    "v_exp_f32 v229, v137\n\t"
    "v_mfma_f32_16x16x32_bf16 v[156:159], a[116:119], a[52:55], v[156:159]\n\t"
    "ds_read_b64_tr_b16 a[174:175], v211 offset:12288\n\t"
    "v_mfma_f32_16x16x32_bf16 v[252:255], a[252:255], v[176:179], 0\n\t"
    "buffer_load_dwordx4 v222, s[36:39], s56 offen offset:2048 lds\n\t"
    "v_mfma_f32_16x16x32_bf16 v[144:147], a[104:107], a[40:43], v[144:147]\n\t"
    "v_exp_f32 v230, v138\n\t"
    "v_mfma_f32_16x16x32_bf16 v[152:155], a[104:107], a[56:59], v[152:155]\n\t"
    "s_mov_b64 s[44:45], %10\n\t"
    "s_add_u32 s44, s44, s71\n\t"
    "s_addc_u32 s45, s45, 0\n\t"
    "s_and_b32 s45, s45, 0xffff\n\t"
    "s_mov_b32 s46, 1792\n\t"
    "s_mov_b32 s47, 0x20000\n\t"
    "buffer_load_dwordx4 a[208:211], v226, s[44:47], 0 offen offset:0\n\t"
    "v_add_f32 v232, v228, v229\n\t"
    "v_cvt_pk_bf16_f32 v180, v228, v229\n\t"
    "v_mfma_f32_16x16x32_bf16 v[148:151], a[120:123], a[40:43], v[148:151]\n\t"
    "v_exp_f32 v231, v139\n\t"
    "v_mfma_f32_16x16x32_bf16 v[156:159], a[120:123], a[56:59], v[156:159]\n\t"
    "ds_read_b64_tr_b16 a[176:177], v212 offset:8192\n\t"
    "v_add_f32 v232, v232, v230\n\t"
    "v_mfma_f32_16x16x32_bf16 v[144:147], a[108:111], a[44:47], v[144:147]\n\t"
    "v_exp_f32 v228, v140\n\t"
    "v_mfma_f32_16x16x32_bf16 v[152:155], a[108:111], a[60:63], v[152:155]\n\t"
    "ds_read_b64_tr_b16 a[178:179], v212 offset:12288\n\t"
    "v_add_f32 v232, v232, v231\n\t"
    "v_cvt_pk_bf16_f32 v181, v230, v231\n\t"
    "v_mfma_f32_16x16x32_bf16 v[148:151], a[124:127], a[44:47], v[148:151]\n\t"
    "v_exp_f32 v229, v141\n\t"
    "v_mfma_f32_16x16x32_bf16 v[156:159], a[124:127], a[60:63], v[156:159]\n\t"
    "ds_read_b64_tr_b16 a[180:181], v213 offset:8192\n\t"
    "v_add_f32 v232, v232, v228\n\t"
    "v_mfma_f32_16x16x32_bf16 v[8:11], a[128:131], v[168:171], 0\n\t"
    "v_exp_f32 v230, v142\n\t"
    "v_mfma_f32_16x16x32_bf16 v[12:15], a[128:131], v[172:175], 0\n\t"
    "v_add_f32 v232, v232, v229\n\t"
    "v_cvt_pk_bf16_f32 v182, v228, v229\n\t"
    "v_mfma_f32_16x16x32_bf16 v[24:27], a[132:135], v[168:171], 0\n\t"
    "v_exp_f32 v231, v143\n\t"
    "v_mfma_f32_16x16x32_bf16 v[28:31], a[132:135], v[172:175], 0\n\t"
    "ds_read_b64_tr_b16 a[182:183], v213 offset:12288\n\t"
    "v_add_f32 v232, v232, v230\n\t"
    "v_mfma_f32_16x16x32_bf16 v[40:43], a[136:139], v[168:171], 0\n\t"
    "v_exp_f32 v228, v144\n\t"
    "v_mfma_f32_16x16x32_bf16 v[44:47], a[136:139], v[172:175], 0\n\t"
    "v_add_f32 v232, v232, v231\n\t"
    "v_cvt_pk_bf16_f32 v183, v230, v231\n\t"
    "v_mfma_f32_16x16x32_bf16 v[56:59], a[140:143], v[168:171], 0\n\t"
    "v_exp_f32 v229, v145\n\t"
    "v_mfma_f32_16x16x32_bf16 v[60:63], a[140:143], v[172:175], 0\n\t"
    "buffer_load_dwordx4 v223, s[36:39], s56 offen offset:3072 lds\n\t"
    "v_max_f32 v248, v232, v252\n\t"
    "v_mfma_f32_16x16x32_bf16 v[72:75], a[144:147], v[168:171], 0\n\t"
    "v_exp_f32 v230, v146\n\t"
    "v_mfma_f32_16x16x32_bf16 v[76:79], a[144:147], v[172:175], 0\n\t"
    "ds_read_b64_tr_b16 a[184:185], v214 offset:8192\n\t"
    "s_add_u32 s56, s56, 0x4000\n\t"
    "s_and_b32 s56, s56, 0x7ffff\n\t"
    "s_cselect_b64 s[36:37], s[36:37], s[76:77]\n\t"
    "v_cmp_lt_f32 vcc, s68, v248\n\t"
    "v_mfma_f32_16x16x32_bf16 v[88:91], a[148:151], v[168:171], 0\n\t"
    "v_exp_f32 v231, v147\n\t"
    "v_mfma_f32_16x16x32_bf16 v[92:95], a[148:151], v[172:175], 0\n\t"
    "ds_read_b64_tr_b16 a[186:187], v214 offset:12288\n\t"
    "v_cvt_pk_bf16_f32 v184, v228, v229\n\t"
    "v_mfma_f32_16x16x32_bf16 v[104:107], a[152:155], v[168:171], 0\n\t"
    "v_exp_f32 v228, v148\n\t"
    "v_mfma_f32_16x16x32_bf16 v[108:111], a[152:155], v[172:175], 0\n\t"
    "s_cbranch_vccnz L_rs2_%=\n\t"
    "L_ret2_%=:\n\t"
    "ds_read_b64_tr_b16 a[188:189], v215 offset:8192\n\t"
    "v_cvt_pk_bf16_f32 v185, v230, v231\n\t"
    "v_mfma_f32_16x16x32_bf16 v[120:123], a[156:159], v[168:171], 0\n\t"
    "v_exp_f32 v229, v149\n\t"
    "v_mfma_f32_16x16x32_bf16 v[124:127], a[156:159], v[172:175], 0\n\t"
    "ds_read_b64_tr_b16 a[190:191], v215 offset:12288\n\t"
    "v_add_f32 v236, v236, v252\n\t"
    "s_waitcnt lgkmcnt(15)\n\t"
    "v_mfma_f32_16x16x32_bf16 v[128:131], a[64:67], a[0:3], v[192:195]\n\t"
    "v_exp_f32 v230, v150\n\t"
    "v_mfma_f32_16x16x32_bf16 v[136:139], a[64:67], a[16:19], v[196:199]\n\t"
    "ds_read_b128 a[96:99], v216 offset:24576\n\t"
    "v_cvt_pk_bf16_f32 v186, v228, v229\n\t"
    "v_mfma_f32_16x16x32_bf16 v[132:135], a[80:83], a[0:3], v[192:195]\n\t"
    "v_exp_f32 v231, v151\n\t"
    "v_mfma_f32_16x16x32_bf16 v[140:143], a[80:83], a[16:19], v[196:199]\n\t"
    "ds_read_b128 a[112:115], v216 offset:28672\n\t"
    "v_add_f32 v237, v237, v232\n\t"
    "v_mfma_f32_16x16x32_bf16 v[128:131], a[68:71], a[4:7], v[128:131]\n\t"
    "v_exp_f32 v228, v152\n\t"
    "v_mfma_f32_16x16x32_bf16 v[136:139], a[68:71], a[20:23], v[136:139]\n\t"
    "ds_read_b128 a[100:103], v217 offset:24576\n\t"
    "s_mov_b32 m0, s65\n\t"
    "v_cvt_pk_bf16_f32 v187, v230, v231\n\t"
    "v_mfma_f32_16x16x32_bf16 v[132:135], a[84:87], a[4:7], v[132:135]\n\t"
    "v_exp_f32 v229, v153\n\t"
    "v_mfma_f32_16x16x32_bf16 v[140:143], a[84:87], a[20:23], v[140:143]\n\t"
    "ds_read_b128 a[116:119], v217 offset:28672\n\t"
    "v_mfma_f32_16x16x32_bf16 v[252:255], a[252:255], v[184:187], 0\n\t"
    "buffer_load_dwordx4 v224, s[40:43], s57 offen offset:0 lds\n\t"
    "v_mfma_f32_16x16x32_bf16 v[128:131], a[72:75], a[8:11], v[128:131]\n\t"
    "v_exp_f32 v230, v154\n\t"
    "v_mfma_f32_16x16x32_bf16 v[136:139], a[72:75], a[24:27], v[136:139]\n\t"
    "v_add_f32 v233, v228, v229\n\t"
    "v_cvt_pk_bf16_f32 v188, v228, v229\n\t"
    "v_mfma_f32_16x16x32_bf16 v[132:135], a[88:91], a[8:11], v[132:135]\n\t"
    "v_exp_f32 v231, v155\n\t"
    "v_mfma_f32_16x16x32_bf16 v[140:143], a[88:91], a[24:27], v[140:143]\n\t"
    "ds_read_b128 a[104:107], v218 offset:24576\n\t"
    "v_add_f32 v233, v233, v230\n\t"
    "v_mfma_f32_16x16x32_bf16 v[128:131], a[76:79], a[12:15], v[128:131]\n\t"
    "v_exp_f32 v228, v156\n\t"
    "v_mfma_f32_16x16x32_bf16 v[136:139], a[76:79], a[28:31], v[136:139]\n\t"
    "ds_read_b128 a[120:123], v218 offset:28672\n\t"
    "v_add_f32 v233, v233, v231\n\t"
    "v_cvt_pk_bf16_f32 v189, v230, v231\n\t"
    "v_mfma_f32_16x16x32_bf16 v[132:135], a[92:95], a[12:15], v[132:135]\n\t"
    "v_exp_f32 v229, v157\n\t"
    "v_mfma_f32_16x16x32_bf16 v[140:143], a[92:95], a[28:31], v[140:143]\n\t"
    "ds_read_b128 a[108:111], v219 offset:24576\n\t"
    "v_add_f32 v233, v233, v228\n\t"
    "s_waitcnt lgkmcnt(7)\n\t"
    "v_mfma_f32_16x16x32_bf16 v[0:3], a[160:163], v[176:179], v[0:3]\n\t"
    "v_exp_f32 v230, v158\n\t"
    "v_mfma_f32_16x16x32_bf16 v[4:7], a[160:163], v[180:183], v[4:7]\n\t"
    "v_add_f32 v233, v233, v229\n\t"
    "v_cvt_pk_bf16_f32 v190, v228, v229\n\t"
    "v_mfma_f32_16x16x32_bf16 v[16:19], a[164:167], v[176:179], v[16:19]\n\t"
    "v_exp_f32 v231, v159\n\t"
    "v_mfma_f32_16x16x32_bf16 v[20:23], a[164:167], v[180:183], v[20:23]\n\t"
    "ds_read_b128 a[124:127], v219 offset:28672\n\t"
    "v_add_f32 v233, v233, v230\n\t"
    "v_mfma_f32_16x16x32_bf16 v[32:35], a[168:171], v[176:179], v[32:35]\n\t"
    "v_exp_f32 v228, v128\n\t"
    "v_mfma_f32_16x16x32_bf16 v[36:39], a[168:171], v[180:183], v[36:39]\n\t"
    "v_add_f32 v233, v233, v231\n\t"
    "v_cvt_pk_bf16_f32 v191, v230, v231\n\t"
    "v_mfma_f32_16x16x32_bf16 v[48:51], a[172:175], v[176:179], v[48:51]\n\t"
    "v_exp_f32 v229, v129\n\t"
    "v_mfma_f32_16x16x32_bf16 v[52:55], a[172:175], v[180:183], v[52:55]\n\t"
    "buffer_load_dwordx4 v225, s[40:43], s57 offen offset:1024 lds\n\t"
    "v_max_f32 v248, v233, v252\n\t"
    "v_mfma_f32_16x16x32_bf16 v[64:67], a[176:179], v[176:179], v[64:67]\n\t"
    "v_exp_f32 v230, v130\n\t"
    "v_mfma_f32_16x16x32_bf16 v[68:71], a[176:179], v[180:183], v[68:71]\n\t"
    "ds_read_b64_tr_b16 a[128:129], v208 offset:16384\n\t"
    "v_cmp_lt_f32 vcc, s68, v248\n\t"
    "v_mfma_f32_16x16x32_bf16 v[80:83], a[180:183], v[176:179], v[80:83]\n\t"
    "v_exp_f32 v231, v131\n\t"
    "v_mfma_f32_16x16x32_bf16 v[84:87], a[180:183], v[180:183], v[84:87]\n\t"
    "ds_read_b64_tr_b16 a[130:131], v208 offset:20480\n\t"
    "v_cvt_pk_bf16_f32 v160, v228, v229\n\t"
    "v_mfma_f32_16x16x32_bf16 v[96:99], a[184:187], v[176:179], v[96:99]\n\t"
    "v_exp_f32 v228, v132\n\t"
    "v_mfma_f32_16x16x32_bf16 v[100:103], a[184:187], v[180:183], v[100:103]\n\t"
    "s_cbranch_vccnz L_rs3_%=\n\t"
    "L_ret3_%=:\n\t"
    "ds_read_b64_tr_b16 a[132:133], v209 offset:16384\n\t"
    "v_cvt_pk_bf16_f32 v161, v230, v231\n\t"
    "v_mfma_f32_16x16x32_bf16 v[112:115], a[188:191], v[176:179], v[112:115]\n\t"
    "v_exp_f32 v229, v133\n\t"
    "v_mfma_f32_16x16x32_bf16 v[116:119], a[188:191], v[180:183], v[116:119]\n\t"
    "ds_read_b64_tr_b16 a[134:135], v209 offset:20480\n\t"
    "v_add_f32 v238, v238, v252\n\t"
    "v_mfma_f32_16x16x32_bf16 v[144:147], a[64:67], a[32:35], v[200:203]\n\t"
    "v_exp_f32 v230, v134\n\t"
    "v_mfma_f32_16x16x32_bf16 v[152:155], a[64:67], a[48:51], v[204:207]\n\t"
    "ds_read_b64_tr_b16 a[136:137], v210 offset:16384\n\t"
    "v_cvt_pk_bf16_f32 v162, v228, v229\n\t"
    "v_mfma_f32_16x16x32_bf16 v[148:151], a[80:83], a[32:35], v[200:203]\n\t"
    "v_exp_f32 v231, v135\n\t"
    "v_mfma_f32_16x16x32_bf16 v[156:159], a[80:83], a[48:51], v[204:207]\n\t"
    "ds_read_b64_tr_b16 a[138:139], v210 offset:20480\n\t"
    "v_add_f32 v239, v239, v233\n\t"
    "v_mfma_f32_16x16x32_bf16 v[144:147], a[68:71], a[36:39], v[144:147]\n\t"
    "v_exp_f32 v228, v136\n\t"
    "v_mfma_f32_16x16x32_bf16 v[152:155], a[68:71], a[52:55], v[152:155]\n\t"
    "ds_read_b64_tr_b16 a[140:141], v211 offset:16384\n\t"
    "v_cvt_pk_bf16_f32 v163, v230, v231\n\t"
    "v_mfma_f32_16x16x32_bf16 v[148:151], a[84:87], a[36:39], v[148:151]\n\t"
    "v_exp_f32 v229, v137\n\t"
    "v_mfma_f32_16x16x32_bf16 v[156:159], a[84:87], a[52:55], v[156:159]\n\t"
    "ds_read_b64_tr_b16 a[142:143], v211 offset:20480\n\t"
    "v_mfma_f32_16x16x32_bf16 v[252:255], a[252:255], v[160:163], 0\n\t"
    "buffer_load_dwordx4 v224, s[40:43], s57 offen offset:2048 lds\n\t"
    "v_mfma_f32_16x16x32_bf16 v[144:147], a[72:75], a[40:43], v[144:147]\n\t"
    "v_exp_f32 v230, v138\n\t"
    "v_mfma_f32_16x16x32_bf16 v[152:155], a[72:75], a[56:59], v[152:155]\n\t"
    "v_add_f32 v232, v228, v229\n\t"
    "v_cvt_pk_bf16_f32 v164, v228, v229\n\t"
    "v_mfma_f32_16x16x32_bf16 v[148:151], a[88:91], a[40:43], v[148:151]\n\t"
    "v_exp_f32 v231, v139\n\t"
    "v_mfma_f32_16x16x32_bf16 v[156:159], a[88:91], a[56:59], v[156:159]\n\t"
    "ds_read_b64_tr_b16 a[144:145], v212 offset:16384\n\t"
    "v_add_f32 v232, v232, v230\n\t"
    "v_mfma_f32_16x16x32_bf16 v[144:147], a[76:79], a[44:47], v[144:147]\n\t"
    "v_exp_f32 v228, v140\n\t"
    "v_mfma_f32_16x16x32_bf16 v[152:155], a[76:79], a[60:63], v[152:155]\n\t"
    "ds_read_b64_tr_b16 a[146:147], v212 offset:20480\n\t"
    "v_add_f32 v232, v232, v231\n\t"
    "v_cvt_pk_bf16_f32 v165, v230, v231\n\t"
    "v_mfma_f32_16x16x32_bf16 v[148:151], a[92:95], a[44:47], v[148:151]\n\t"
    "v_exp_f32 v229, v141\n\t"
    "v_mfma_f32_16x16x32_bf16 v[156:159], a[92:95], a[60:63], v[156:159]\n\t"
    "ds_read_b64_tr_b16 a[148:149], v213 offset:16384\n\t"
    "v_add_f32 v232, v232, v228\n\t"
    "v_mfma_f32_16x16x32_bf16 v[8:11], a[160:163], v[184:187], v[8:11]\n\t"
    "v_exp_f32 v230, v142\n\t"
    "v_mfma_f32_16x16x32_bf16 v[12:15], a[160:163], v[188:191], v[12:15]\n\t"
    "v_add_f32 v232, v232, v229\n\t"
    "v_cvt_pk_bf16_f32 v166, v228, v229\n\t"
    "v_mfma_f32_16x16x32_bf16 v[24:27], a[164:167], v[184:187], v[24:27]\n\t"
    "v_exp_f32 v231, v143\n\t"
    "v_mfma_f32_16x16x32_bf16 v[28:31], a[164:167], v[188:191], v[28:31]\n\t"
    "ds_read_b64_tr_b16 a[150:151], v213 offset:20480\n\t"
    "v_add_f32 v232, v232, v230\n\t"
    "v_mfma_f32_16x16x32_bf16 v[40:43], a[168:171], v[184:187], v[40:43]\n\t"
    "v_exp_f32 v228, v144\n\t"
    "v_mfma_f32_16x16x32_bf16 v[44:47], a[168:171], v[188:191], v[44:47]\n\t"
    "v_add_f32 v232, v232, v231\n\t"
    "v_cvt_pk_bf16_f32 v167, v230, v231\n\t"
    "v_mfma_f32_16x16x32_bf16 v[56:59], a[172:175], v[184:187], v[56:59]\n\t"
    "v_exp_f32 v229, v145\n\t"
    "v_mfma_f32_16x16x32_bf16 v[60:63], a[172:175], v[188:191], v[60:63]\n\t"
    "buffer_load_dwordx4 v225, s[40:43], s57 offen offset:3072 lds\n\t"
    "v_max_f32 v248, v232, v252\n\t"
    "v_mfma_f32_16x16x32_bf16 v[72:75], a[176:179], v[184:187], v[72:75]\n\t"
    "v_exp_f32 v230, v146\n\t"
    "v_mfma_f32_16x16x32_bf16 v[76:79], a[176:179], v[188:191], v[76:79]\n\t"
    "ds_read_b64_tr_b16 a[152:153], v214 offset:16384\n\t"
    "s_add_u32 s57, s57, 0x4000\n\t"
    "s_and_b32 s57, s57, 0x7ffff\n\t"
    "s_cselect_b64 s[40:41], s[40:41], s[78:79]\n\t"
    "v_cmp_lt_f32 vcc, s68, v248\n\t"
    "v_mfma_f32_16x16x32_bf16 v[88:91], a[180:183], v[184:187], v[88:91]\n\t"
    "v_exp_f32 v231, v147\n\t"
    "v_mfma_f32_16x16x32_bf16 v[92:95], a[180:183], v[188:191], v[92:95]\n\t"
    "ds_read_b64_tr_b16 a[154:155], v214 offset:20480\n\t"
    "v_cvt_pk_bf16_f32 v168, v228, v229\n\t"
    "v_mfma_f32_16x16x32_bf16 v[104:107], a[184:187], v[184:187], v[104:107]\n\t"
    "v_exp_f32 v228, v148\n\t"
    "v_mfma_f32_16x16x32_bf16 v[108:111], a[184:187], v[188:191], v[108:111]\n\t"
    "s_cbranch_vccnz L_rs4_%=\n\t"
    "L_ret4_%=:\n\t"
    "ds_read_b64_tr_b16 a[156:157], v215 offset:16384\n\t"
    "v_cvt_pk_bf16_f32 v169, v230, v231\n\t"
    "v_mfma_f32_16x16x32_bf16 v[120:123], a[188:191], v[184:187], v[120:123]\n\t"
    "v_exp_f32 v229, v149\n\t"
    "v_mfma_f32_16x16x32_bf16 v[124:127], a[188:191], v[188:191], v[124:127]\n\t"
    "ds_read_b64_tr_b16 a[158:159], v215 offset:20480\n\t"
    "v_add_f32 v236, v236, v252\n\t"
    "s_waitcnt lgkmcnt(15)\n\t"
    "v_mfma_f32_16x16x32_bf16 v[128:131], a[96:99], a[0:3], v[192:195]\n\t"
    "v_exp_f32 v230, v150\n\t"
    "v_mfma_f32_16x16x32_bf16 v[136:139], a[96:99], a[16:19], v[196:199]\n\t"
    "ds_read_b64_tr_b16 a[160:161], v208 offset:24576\n\t"
    "v_cvt_pk_bf16_f32 v170, v228, v229\n\t"
    "s_waitcnt vmcnt(13)\n\t"
    "s_barrier\n\t"
    "v_mfma_f32_16x16x32_bf16 v[132:135], a[112:115], a[0:3], v[192:195]\n\t"
    "v_exp_f32 v231, v151\n\t"
    "v_mfma_f32_16x16x32_bf16 v[140:143], a[112:115], a[16:19], v[196:199]\n\t"
    "ds_read_b128 a[64:67], v216 offset:32768\n\t"
    "v_add_f32 v237, v237, v232\n\t"
    "v_mfma_f32_16x16x32_bf16 v[128:131], a[100:103], a[4:7], v[128:131]\n\t"
    "v_exp_f32 v228, v152\n\t"
    "v_mfma_f32_16x16x32_bf16 v[136:139], a[100:103], a[20:23], v[136:139]\n\t"
    "ds_read_b128 a[80:83], v216 offset:36864\n\t"
    "s_mov_b32 m0, s58\n\t"
    "v_cvt_pk_bf16_f32 v171, v230, v231\n\t"
    "v_mfma_f32_16x16x32_bf16 v[132:135], a[116:119], a[4:7], v[132:135]\n\t"
    "v_exp_f32 v229, v153\n\t"
    "v_mfma_f32_16x16x32_bf16 v[140:143], a[116:119], a[20:23], v[140:143]\n\t"
    "ds_read_b128 a[68:71], v217 offset:32768\n\t"
    "v_mfma_f32_16x16x32_bf16 v[252:255], a[252:255], v[168:171], 0\n\t"
    "buffer_load_dwordx4 v220, s[36:39], s56 offen offset:0 lds\n\t"
    "v_mfma_f32_16x16x32_bf16 v[128:131], a[104:107], a[8:11], v[128:131]\n\t"
    "v_exp_f32 v230, v154\n\t"
    "v_mfma_f32_16x16x32_bf16 v[136:139], a[104:107], a[24:27], v[136:139]\n\t"
    "buffer_load_dwordx4 a[212:215], v226, s[44:47], 0 offen offset:1024\n\t"
    "v_add_f32 v233, v228, v229\n\t"
    "v_cvt_pk_bf16_f32 v172, v228, v229\n\t"
    "v_mfma_f32_16x16x32_bf16 v[132:135], a[120:123], a[8:11], v[132:135]\n\t"
    "v_exp_f32 v231, v155\n\t"
    "v_mfma_f32_16x16x32_bf16 v[140:143], a[120:123], a[24:27], v[140:143]\n\t"
    "ds_read_b128 a[84:87], v217 offset:36864\n\t"
    "v_add_f32 v233, v233, v230\n\t"
    "v_mfma_f32_16x16x32_bf16 v[128:131], a[108:111], a[12:15], v[128:131]\n\t"
    "v_exp_f32 v228, v156\n\t"
    "v_mfma_f32_16x16x32_bf16 v[136:139], a[108:111], a[28:31], v[136:139]\n\t"
    "ds_read_b128 a[72:75], v218 offset:32768\n\t"
    "v_add_f32 v233, v233, v231\n\t"
    "v_cvt_pk_bf16_f32 v173, v230, v231\n\t"
    "v_mfma_f32_16x16x32_bf16 v[132:135], a[124:127], a[12:15], v[132:135]\n\t"
    "v_exp_f32 v229, v157\n\t"
    "v_mfma_f32_16x16x32_bf16 v[140:143], a[124:127], a[28:31], v[140:143]\n\t"
    "ds_read_b128 a[88:91], v218 offset:36864\n\t"
    "v_add_f32 v233, v233, v228\n\t"
    "s_waitcnt lgkmcnt(7)\n\t"
    "v_mfma_f32_16x16x32_bf16 v[0:3], a[128:131], v[160:163], v[0:3]\n\t"
    "v_exp_f32 v230, v158\n\t"
    "v_mfma_f32_16x16x32_bf16 v[4:7], a[128:131], v[164:167], v[4:7]\n\t"
    "s_mov_b64 s[44:45], %11\n\t"
    "s_add_u32 s44, s44, s71\n\t"
    "s_addc_u32 s45, s45, 0\n\t"
    "s_and_b32 s45, s45, 0xffff\n\t"
    "s_mov_b32 s46, 1792\n\t"
    "s_mov_b32 s47, 0x20000\n\t"
    "buffer_load_dwordx4 a[216:219], v226, s[44:47], 0 offen offset:0\n\t"
    "v_add_f32 v233, v233, v229\n\t"
    "v_cvt_pk_bf16_f32 v174, v228, v229\n\t"
    "v_mfma_f32_16x16x32_bf16 v[16:19], a[132:135], v[160:163], v[16:19]\n\t"
    "v_exp_f32 v231, v159\n\t"
    "v_mfma_f32_16x16x32_bf16 v[20:23], a[132:135], v[164:167], v[20:23]\n\t"
    "ds_read_b128 a[76:79], v219 offset:32768\n\t"
    "v_add_f32 v233, v233, v230\n\t"
    "v_mfma_f32_16x16x32_bf16 v[32:35], a[136:139], v[160:163], v[32:35]\n\t"
    "v_exp_f32 v228, v128\n\t"
    "v_mfma_f32_16x16x32_bf16 v[36:39], a[136:139], v[164:167], v[36:39]\n\t"
    "v_add_f32 v233, v233, v231\n\t"
    "v_cvt_pk_bf16_f32 v175, v230, v231\n\t"
    "v_mfma_f32_16x16x32_bf16 v[48:51], a[140:143], v[160:163], v[48:51]\n\t"
    "v_exp_f32 v229, v129\n\t"
    "v_mfma_f32_16x16x32_bf16 v[52:55], a[140:143], v[164:167], v[52:55]\n\t"
    "buffer_load_dwordx4 v221, s[36:39], s56 offen offset:1024 lds\n\t"
    "v_max_f32 v248, v233, v252\n\t"
    "v_mfma_f32_16x16x32_bf16 v[64:67], a[144:147], v[160:163], v[64:67]\n\t"
    "v_exp_f32 v230, v130\n\t"
    "v_mfma_f32_16x16x32_bf16 v[68:71], a[144:147], v[164:167], v[68:71]\n\t"
    "ds_read_b128 a[92:95], v219 offset:36864\n\t"
    "buffer_load_dwordx4 a[220:223], v226, s[44:47], 0 offen offset:1024\n\t"
    "v_cmp_lt_f32 vcc, s68, v248\n\t"
    "v_mfma_f32_16x16x32_bf16 v[80:83], a[148:151], v[160:163], v[80:83]\n\t"
    "v_exp_f32 v231, v131\n\t"
    "v_mfma_f32_16x16x32_bf16 v[84:87], a[148:151], v[164:167], v[84:87]\n\t"
    "ds_read_b64_tr_b16 a[162:163], v208 offset:28672\n\t"
    "v_cvt_pk_bf16_f32 v176, v228, v229\n\t"
    "v_mfma_f32_16x16x32_bf16 v[96:99], a[152:155], v[160:163], v[96:99]\n\t"
    "v_exp_f32 v228, v132\n\t"
    "v_mfma_f32_16x16x32_bf16 v[100:103], a[152:155], v[164:167], v[100:103]\n\t"
    "s_cbranch_vccnz L_rs5_%=\n\t"
    "L_ret5_%=:\n\t"
    "ds_read_b64_tr_b16 a[164:165], v209 offset:24576\n\t"
    "v_cvt_pk_bf16_f32 v177, v230, v231\n\t"
    "v_mfma_f32_16x16x32_bf16 v[112:115], a[156:159], v[160:163], v[112:115]\n\t"
    "v_exp_f32 v229, v133\n\t"
    "v_mfma_f32_16x16x32_bf16 v[116:119], a[156:159], v[164:167], v[116:119]\n\t"
    "ds_read_b64_tr_b16 a[166:167], v209 offset:28672\n\t"
    "v_add_f32 v238, v238, v252\n\t"
    "v_mfma_f32_16x16x32_bf16 v[144:147], a[96:99], a[32:35], v[200:203]\n\t"
    "v_exp_f32 v230, v134\n\t"
    "v_mfma_f32_16x16x32_bf16 v[152:155], a[96:99], a[48:51], v[204:207]\n\t"
    "ds_read_b64_tr_b16 a[168:169], v210 offset:24576\n\t"
    "s_mov_b64 s[44:45], %12\n\t"
    "s_add_u32 s44, s44, s71\n\t"
    "s_addc_u32 s45, s45, 0\n\t"
    "s_and_b32 s45, s45, 0xffff\n\t"
    "s_mov_b32 s46, 1792\n\t"
    "s_mov_b32 s47, 0x20000\n\t"
    "buffer_load_dwordx4 a[224:227], v226, s[44:47], 0 offen offset:0\n\t"
    "v_cvt_pk_bf16_f32 v178, v228, v229\n\t"
    "v_mfma_f32_16x16x32_bf16 v[148:151], a[112:115], a[32:35], v[200:203]\n\t"
    "v_exp_f32 v231, v135\n\t"
    "v_mfma_f32_16x16x32_bf16 v[156:159], a[112:115], a[48:51], v[204:207]\n\t"
    "ds_read_b64_tr_b16 a[170:171], v210 offset:28672\n\t"
    "v_add_f32 v239, v239, v233\n\t"
    "v_mfma_f32_16x16x32_bf16 v[144:147], a[100:103], a[36:39], v[144:147]\n\t"
    "v_exp_f32 v228, v136\n\t"
    "v_mfma_f32_16x16x32_bf16 v[152:155], a[100:103], a[52:55], v[152:155]\n\t"
    "ds_read_b64_tr_b16 a[172:173], v211 offset:24576\n\t"
    "v_cvt_pk_bf16_f32 v179, v230, v231\n\t"
    "v_mfma_f32_16x16x32_bf16 v[148:151], a[116:119], a[36:39], v[148:151]\n\t"
    "v_exp_f32 v229, v137\n\t"
    "v_mfma_f32_16x16x32_bf16 v[156:159], a[116:119], a[52:55], v[156:159]\n\t"
    "ds_read_b64_tr_b16 a[174:175], v211 offset:28672\n\t"
    "v_mfma_f32_16x16x32_bf16 v[252:255], a[252:255], v[176:179], 0\n\t"
    "buffer_load_dwordx4 v222, s[36:39], s56 offen offset:2048 lds\n\t"
    "v_mfma_f32_16x16x32_bf16 v[144:147], a[104:107], a[40:43], v[144:147]\n\t"
    "v_exp_f32 v230, v138\n\t"
    "v_mfma_f32_16x16x32_bf16 v[152:155], a[104:107], a[56:59], v[152:155]\n\t"
    "buffer_load_dwordx4 a[228:231], v226, s[44:47], 0 offen offset:1024\n\t"
    "v_add_f32 v232, v228, v229\n\t"
    "v_cvt_pk_bf16_f32 v180, v228, v229\n\t"
    "v_mfma_f32_16x16x32_bf16 v[148:151], a[120:123], a[40:43], v[148:151]\n\t"
    "v_exp_f32 v231, v139\n\t"
    "v_mfma_f32_16x16x32_bf16 v[156:159], a[120:123], a[56:59], v[156:159]\n\t"
    "ds_read_b64_tr_b16 a[176:177], v212 offset:24576\n\t"
    "v_add_f32 v232, v232, v230\n\t"
    "v_mfma_f32_16x16x32_bf16 v[144:147], a[108:111], a[44:47], v[144:147]\n\t"
    "v_exp_f32 v228, v140\n\t"
    "v_mfma_f32_16x16x32_bf16 v[152:155], a[108:111], a[60:63], v[152:155]\n\t"
    "ds_read_b64_tr_b16 a[178:179], v212 offset:28672\n\t"
    "v_add_f32 v232, v232, v231\n\t"
    "v_cvt_pk_bf16_f32 v181, v230, v231\n\t"
    "v_mfma_f32_16x16x32_bf16 v[148:151], a[124:127], a[44:47], v[148:151]\n\t"
    "v_exp_f32 v229, v141\n\t"
    "v_mfma_f32_16x16x32_bf16 v[156:159], a[124:127], a[60:63], v[156:159]\n\t"
    "ds_read_b64_tr_b16 a[180:181], v213 offset:24576\n\t"
    "v_add_f32 v232, v232, v228\n\t"
    "v_mfma_f32_16x16x32_bf16 v[8:11], a[128:131], v[168:171], v[8:11]\n\t"
    "v_exp_f32 v230, v142\n\t"
    "v_mfma_f32_16x16x32_bf16 v[12:15], a[128:131], v[172:175], v[12:15]\n\t"
    "v_add_f32 v232, v232, v229\n\t"
    "v_cvt_pk_bf16_f32 v182, v228, v229\n\t"
    "v_mfma_f32_16x16x32_bf16 v[24:27], a[132:135], v[168:171], v[24:27]\n\t"
    "v_exp_f32 v231, v143\n\t"
    "v_mfma_f32_16x16x32_bf16 v[28:31], a[132:135], v[172:175], v[28:31]\n\t"
    "ds_read_b64_tr_b16 a[182:183], v213 offset:28672\n\t"
    "v_add_f32 v232, v232, v230\n\t"
    "v_mfma_f32_16x16x32_bf16 v[40:43], a[136:139], v[168:171], v[40:43]\n\t"
    "v_exp_f32 v228, v144\n\t"
    "v_mfma_f32_16x16x32_bf16 v[44:47], a[136:139], v[172:175], v[44:47]\n\t"
    "v_add_f32 v232, v232, v231\n\t"
    "v_cvt_pk_bf16_f32 v183, v230, v231\n\t"
    "v_mfma_f32_16x16x32_bf16 v[56:59], a[140:143], v[168:171], v[56:59]\n\t"
    "v_exp_f32 v229, v145\n\t"
    "v_mfma_f32_16x16x32_bf16 v[60:63], a[140:143], v[172:175], v[60:63]\n\t"
    "buffer_load_dwordx4 v223, s[36:39], s56 offen offset:3072 lds\n\t"
    "v_max_f32 v248, v232, v252\n\t"
    "v_mfma_f32_16x16x32_bf16 v[72:75], a[144:147], v[168:171], v[72:75]\n\t"
    "v_exp_f32 v230, v146\n\t"
    "v_mfma_f32_16x16x32_bf16 v[76:79], a[144:147], v[172:175], v[76:79]\n\t"
    "ds_read_b64_tr_b16 a[184:185], v214 offset:24576\n\t"
    "s_add_u32 s56, s56, 0x4000\n\t"
    "s_and_b32 s56, s56, 0x7ffff\n\t"
    "s_cselect_b64 s[36:37], s[36:37], s[76:77]\n\t"
    "v_cmp_lt_f32 vcc, s68, v248\n\t"
    "v_mfma_f32_16x16x32_bf16 v[88:91], a[148:151], v[168:171], v[88:91]\n\t"
    "v_exp_f32 v231, v147\n\t"
    "v_mfma_f32_16x16x32_bf16 v[92:95], a[148:151], v[172:175], v[92:95]\n\t"
    "ds_read_b64_tr_b16 a[186:187], v214 offset:28672\n\t"
    "v_cvt_pk_bf16_f32 v184, v228, v229\n\t"
    "v_mfma_f32_16x16x32_bf16 v[104:107], a[152:155], v[168:171], v[104:107]\n\t"
    "v_exp_f32 v228, v148\n\t"
    "v_mfma_f32_16x16x32_bf16 v[108:111], a[152:155], v[172:175], v[108:111]\n\t"
    "s_cbranch_vccnz L_rs6_%=\n\t"
    "L_ret6_%=:\n\t"
    "ds_read_b64_tr_b16 a[188:189], v215 offset:24576\n\t"
    "v_cvt_pk_bf16_f32 v185, v230, v231\n\t"
    "v_mfma_f32_16x16x32_bf16 v[120:123], a[156:159], v[168:171], v[120:123]\n\t"
    "v_exp_f32 v229, v149\n\t"
    "v_mfma_f32_16x16x32_bf16 v[124:127], a[156:159], v[172:175], v[124:127]\n\t"
    "ds_read_b64_tr_b16 a[190:191], v215 offset:28672\n\t"
    "v_add_f32 v236, v236, v252\n\t"
    "s_waitcnt lgkmcnt(15)\n\t"
    "v_mfma_f32_16x16x32_bf16 v[128:131], a[64:67], a[0:3], v[192:195]\n\t"
    "v_exp_f32 v230, v150\n\t"
    "v_mfma_f32_16x16x32_bf16 v[136:139], a[64:67], a[16:19], v[196:199]\n\t"
    "ds_read_b128 a[96:99], v216 offset:40960\n\t"
    "v_cvt_pk_bf16_f32 v186, v228, v229\n\t"
    "v_mfma_f32_16x16x32_bf16 v[132:135], a[80:83], a[0:3], v[192:195]\n\t"
    "v_exp_f32 v231, v151\n\t"
    "v_mfma_f32_16x16x32_bf16 v[140:143], a[80:83], a[16:19], v[196:199]\n\t"
    "ds_read_b128 a[112:115], v216 offset:45056\n\t"
    "v_add_f32 v237, v237, v232\n\t"
    "v_mfma_f32_16x16x32_bf16 v[128:131], a[68:71], a[4:7], v[128:131]\n\t"
    "v_exp_f32 v228, v152\n\t"
    "v_mfma_f32_16x16x32_bf16 v[136:139], a[68:71], a[20:23], v[136:139]\n\t"
    "ds_read_b128 a[100:103], v217 offset:40960\n\t"
    "s_mov_b32 m0, s62\n\t"
    "v_cvt_pk_bf16_f32 v187, v230, v231\n\t"
    "v_mfma_f32_16x16x32_bf16 v[132:135], a[84:87], a[4:7], v[132:135]\n\t"
    "v_exp_f32 v229, v153\n\t"
    "v_mfma_f32_16x16x32_bf16 v[140:143], a[84:87], a[20:23], v[140:143]\n\t"
    "ds_read_b128 a[116:119], v217 offset:45056\n\t"
    "v_mfma_f32_16x16x32_bf16 v[252:255], a[252:255], v[184:187], 0\n\t"
    "buffer_load_dwordx4 v224, s[40:43], s57 offen offset:0 lds\n\t"
    "v_mfma_f32_16x16x32_bf16 v[128:131], a[72:75], a[8:11], v[128:131]\n\t"
    "v_exp_f32 v230, v154\n\t"
    "v_mfma_f32_16x16x32_bf16 v[136:139], a[72:75], a[24:27], v[136:139]\n\t"
    "v_add_f32 v233, v228, v229\n\t"
    "v_cvt_pk_bf16_f32 v188, v228, v229\n\t"
    "v_mfma_f32_16x16x32_bf16 v[132:135], a[88:91], a[8:11], v[132:135]\n\t"
    "v_exp_f32 v231, v155\n\t"
    "v_mfma_f32_16x16x32_bf16 v[140:143], a[88:91], a[24:27], v[140:143]\n\t"
    "ds_read_b128 a[104:107], v218 offset:40960\n\t"
    "v_add_f32 v233, v233, v230\n\t"
    "v_mfma_f32_16x16x32_bf16 v[128:131], a[76:79], a[12:15], v[128:131]\n\t"
    "v_exp_f32 v228, v156\n\t"
    "v_mfma_f32_16x16x32_bf16 v[136:139], a[76:79], a[28:31], v[136:139]\n\t"
    "ds_read_b128 a[120:123], v218 offset:45056\n\t"
    "v_add_f32 v233, v233, v231\n\t"
    "v_cvt_pk_bf16_f32 v189, v230, v231\n\t"
    "v_mfma_f32_16x16x32_bf16 v[132:135], a[92:95], a[12:15], v[132:135]\n\t"
    "v_exp_f32 v229, v157\n\t"
    "v_mfma_f32_16x16x32_bf16 v[140:143], a[92:95], a[28:31], v[140:143]\n\t"
    "ds_read_b128 a[108:111], v219 offset:40960\n\t"
    "v_add_f32 v233, v233, v228\n\t"
    "s_waitcnt lgkmcnt(7)\n\t"
    "v_mfma_f32_16x16x32_bf16 v[0:3], a[160:163], v[176:179], v[0:3]\n\t"
    "v_exp_f32 v230, v158\n\t"
    "v_mfma_f32_16x16x32_bf16 v[4:7], a[160:163], v[180:183], v[4:7]\n\t"
    "v_add_f32 v233, v233, v229\n\t"
    "v_cvt_pk_bf16_f32 v190, v228, v229\n\t"
    "v_mfma_f32_16x16x32_bf16 v[16:19], a[164:167], v[176:179], v[16:19]\n\t"
    "v_exp_f32 v231, v159\n\t"
    "v_mfma_f32_16x16x32_bf16 v[20:23], a[164:167], v[180:183], v[20:23]\n\t"
    "ds_read_b128 a[124:127], v219 offset:45056\n\t"
    "v_add_f32 v233, v233, v230\n\t"
    "v_mfma_f32_16x16x32_bf16 v[32:35], a[168:171], v[176:179], v[32:35]\n\t"
    "v_exp_f32 v228, v128\n\t"
    "v_mfma_f32_16x16x32_bf16 v[36:39], a[168:171], v[180:183], v[36:39]\n\t"
    "v_add_f32 v233, v233, v231\n\t"
    "v_cvt_pk_bf16_f32 v191, v230, v231\n\t"
    "v_mfma_f32_16x16x32_bf16 v[48:51], a[172:175], v[176:179], v[48:51]\n\t"
    "v_exp_f32 v229, v129\n\t"
    "v_mfma_f32_16x16x32_bf16 v[52:55], a[172:175], v[180:183], v[52:55]\n\t"
    "buffer_load_dwordx4 v225, s[40:43], s57 offen offset:1024 lds\n\t"
    "v_max_f32 v248, v233, v252\n\t"
    "v_mfma_f32_16x16x32_bf16 v[64:67], a[176:179], v[176:179], v[64:67]\n\t"
    "v_exp_f32 v230, v130\n\t"
    "v_mfma_f32_16x16x32_bf16 v[68:71], a[176:179], v[180:183], v[68:71]\n\t"
    "ds_read_b64_tr_b16 a[128:129], v208 offset:32768\n\t"
    "v_cmp_lt_f32 vcc, s68, v248\n\t"
    "v_mfma_f32_16x16x32_bf16 v[80:83], a[180:183], v[176:179], v[80:83]\n\t"
    "v_exp_f32 v231, v131\n\t"
    "v_mfma_f32_16x16x32_bf16 v[84:87], a[180:183], v[180:183], v[84:87]\n\t"
    "ds_read_b64_tr_b16 a[130:131], v208 offset:36864\n\t"
    "v_cvt_pk_bf16_f32 v160, v228, v229\n\t"
    "v_mfma_f32_16x16x32_bf16 v[96:99], a[184:187], v[176:179], v[96:99]\n\t"
    "v_exp_f32 v228, v132\n\t"
    "v_mfma_f32_16x16x32_bf16 v[100:103], a[184:187], v[180:183], v[100:103]\n\t"
    "s_cbranch_vccnz L_rs7_%=\n\t"
    "L_ret7_%=:\n\t"
    "ds_read_b64_tr_b16 a[132:133], v209 offset:32768\n\t"
    "v_cvt_pk_bf16_f32 v161, v230, v231\n\t"
    "v_mfma_f32_16x16x32_bf16 v[112:115], a[188:191], v[176:179], v[112:115]\n\t"
    "v_exp_f32 v229, v133\n\t"
    "v_mfma_f32_16x16x32_bf16 v[116:119], a[188:191], v[180:183], v[116:119]\n\t"
    "ds_read_b64_tr_b16 a[134:135], v209 offset:36864\n\t"
    "v_add_f32 v238, v238, v252\n\t"
    "v_mfma_f32_16x16x32_bf16 v[144:147], a[64:67], a[32:35], v[200:203]\n\t"
    "v_exp_f32 v230, v134\n\t"
    "v_mfma_f32_16x16x32_bf16 v[152:155], a[64:67], a[48:51], v[204:207]\n\t"
    "ds_read_b64_tr_b16 a[136:137], v210 offset:32768\n\t"
    "v_cvt_pk_bf16_f32 v162, v228, v229\n\t"
    "v_mfma_f32_16x16x32_bf16 v[148:151], a[80:83], a[32:35], v[200:203]\n\t"
    "v_exp_f32 v231, v135\n\t"
    "v_mfma_f32_16x16x32_bf16 v[156:159], a[80:83], a[48:51], v[204:207]\n\t"
    "ds_read_b64_tr_b16 a[138:139], v210 offset:36864\n\t"
    "v_add_f32 v239, v239, v233\n\t"
    "v_mfma_f32_16x16x32_bf16 v[144:147], a[68:71], a[36:39], v[144:147]\n\t"
    "v_exp_f32 v228, v136\n\t"
    "v_mfma_f32_16x16x32_bf16 v[152:155], a[68:71], a[52:55], v[152:155]\n\t"
    "ds_read_b64_tr_b16 a[140:141], v211 offset:32768\n\t"
    "v_cvt_pk_bf16_f32 v163, v230, v231\n\t"
    "v_mfma_f32_16x16x32_bf16 v[148:151], a[84:87], a[36:39], v[148:151]\n\t"
    "v_exp_f32 v229, v137\n\t"
    "v_mfma_f32_16x16x32_bf16 v[156:159], a[84:87], a[52:55], v[156:159]\n\t"
    "ds_read_b64_tr_b16 a[142:143], v211 offset:36864\n\t"
    "v_mfma_f32_16x16x32_bf16 v[252:255], a[252:255], v[160:163], 0\n\t"
    "buffer_load_dwordx4 v224, s[40:43], s57 offen offset:2048 lds\n\t"
    "v_mfma_f32_16x16x32_bf16 v[144:147], a[72:75], a[40:43], v[144:147]\n\t"
    "v_exp_f32 v230, v138\n\t"
    "v_mfma_f32_16x16x32_bf16 v[152:155], a[72:75], a[56:59], v[152:155]\n\t"
    "v_add_f32 v232, v228, v229\n\t"
    "v_cvt_pk_bf16_f32 v164, v228, v229\n\t"
    "v_mfma_f32_16x16x32_bf16 v[148:151], a[88:91], a[40:43], v[148:151]\n\t"
    "v_exp_f32 v231, v139\n\t"
    "v_mfma_f32_16x16x32_bf16 v[156:159], a[88:91], a[56:59], v[156:159]\n\t"
    "ds_read_b64_tr_b16 a[144:145], v212 offset:32768\n\t"
    "v_add_f32 v232, v232, v230\n\t"
    "v_mfma_f32_16x16x32_bf16 v[144:147], a[76:79], a[44:47], v[144:147]\n\t"
    "v_exp_f32 v228, v140\n\t"
    "v_mfma_f32_16x16x32_bf16 v[152:155], a[76:79], a[60:63], v[152:155]\n\t"
    "ds_read_b64_tr_b16 a[146:147], v212 offset:36864\n\t"
    "v_add_f32 v232, v232, v231\n\t"
    "v_cvt_pk_bf16_f32 v165, v230, v231\n\t"
    "v_mfma_f32_16x16x32_bf16 v[148:151], a[92:95], a[44:47], v[148:151]\n\t"
    "v_exp_f32 v229, v141\n\t"
    "v_mfma_f32_16x16x32_bf16 v[156:159], a[92:95], a[60:63], v[156:159]\n\t"
    "ds_read_b64_tr_b16 a[148:149], v213 offset:32768\n\t"
    "v_add_f32 v232, v232, v228\n\t"
    "v_mfma_f32_16x16x32_bf16 v[8:11], a[160:163], v[184:187], v[8:11]\n\t"
    "v_exp_f32 v230, v142\n\t"
    "v_mfma_f32_16x16x32_bf16 v[12:15], a[160:163], v[188:191], v[12:15]\n\t"
    "v_add_f32 v232, v232, v229\n\t"
    "v_cvt_pk_bf16_f32 v166, v228, v229\n\t"
    "v_mfma_f32_16x16x32_bf16 v[24:27], a[164:167], v[184:187], v[24:27]\n\t"
    "v_exp_f32 v231, v143\n\t"
    "v_mfma_f32_16x16x32_bf16 v[28:31], a[164:167], v[188:191], v[28:31]\n\t"
    "ds_read_b64_tr_b16 a[150:151], v213 offset:36864\n\t"
    "v_add_f32 v232, v232, v230\n\t"
    "v_mfma_f32_16x16x32_bf16 v[40:43], a[168:171], v[184:187], v[40:43]\n\t"
    "v_exp_f32 v228, v144\n\t"
    "v_mfma_f32_16x16x32_bf16 v[44:47], a[168:171], v[188:191], v[44:47]\n\t"
    "v_add_f32 v232, v232, v231\n\t"
    "v_cvt_pk_bf16_f32 v167, v230, v231\n\t"
    "v_mfma_f32_16x16x32_bf16 v[56:59], a[172:175], v[184:187], v[56:59]\n\t"
    "v_exp_f32 v229, v145\n\t"
    "v_mfma_f32_16x16x32_bf16 v[60:63], a[172:175], v[188:191], v[60:63]\n\t"
    "buffer_load_dwordx4 v225, s[40:43], s57 offen offset:3072 lds\n\t"
    "v_max_f32 v248, v232, v252\n\t"
    "v_mfma_f32_16x16x32_bf16 v[72:75], a[176:179], v[184:187], v[72:75]\n\t"
    "v_exp_f32 v230, v146\n\t"
    "v_mfma_f32_16x16x32_bf16 v[76:79], a[176:179], v[188:191], v[76:79]\n\t"
    "ds_read_b64_tr_b16 a[152:153], v214 offset:32768\n\t"
    "s_add_u32 s57, s57, 0x4000\n\t"
    "s_and_b32 s57, s57, 0x7ffff\n\t"
    "s_cselect_b64 s[40:41], s[40:41], s[78:79]\n\t"
    "v_cmp_lt_f32 vcc, s68, v248\n\t"
    "v_mfma_f32_16x16x32_bf16 v[88:91], a[180:183], v[184:187], v[88:91]\n\t"
    "v_exp_f32 v231, v147\n\t"
    "v_mfma_f32_16x16x32_bf16 v[92:95], a[180:183], v[188:191], v[92:95]\n\t"
    "ds_read_b64_tr_b16 a[154:155], v214 offset:36864\n\t"
    "v_cvt_pk_bf16_f32 v168, v228, v229\n\t"
    "v_mfma_f32_16x16x32_bf16 v[104:107], a[184:187], v[184:187], v[104:107]\n\t"
    "v_exp_f32 v228, v148\n\t"
    "v_mfma_f32_16x16x32_bf16 v[108:111], a[184:187], v[188:191], v[108:111]\n\t"
    "s_cbranch_vccnz L_rs8_%=\n\t"
    "L_ret8_%=:\n\t"
    "ds_read_b64_tr_b16 a[156:157], v215 offset:32768\n\t"
    "v_cvt_pk_bf16_f32 v169, v230, v231\n\t"
    "v_mfma_f32_16x16x32_bf16 v[120:123], a[188:191], v[184:187], v[120:123]\n\t"
    "v_exp_f32 v229, v149\n\t"
    "v_mfma_f32_16x16x32_bf16 v[124:127], a[188:191], v[188:191], v[124:127]\n\t"
    "ds_read_b64_tr_b16 a[158:159], v215 offset:36864\n\t"
    "v_add_f32 v236, v236, v252\n\t"
    "s_waitcnt lgkmcnt(15)\n\t"
    "v_mfma_f32_16x16x32_bf16 v[128:131], a[96:99], a[0:3], v[192:195]\n\t"
    "v_exp_f32 v230, v150\n\t"
    "v_mfma_f32_16x16x32_bf16 v[136:139], a[96:99], a[16:19], v[196:199]\n\t"
    "ds_read_b64_tr_b16 a[160:161], v208 offset:40960\n\t"
    "v_cvt_pk_bf16_f32 v170, v228, v229\n\t"
    "s_waitcnt vmcnt(13)\n\t"
    "s_barrier\n\t"
    "v_mfma_f32_16x16x32_bf16 v[132:135], a[112:115], a[0:3], v[192:195]\n\t"
    "v_exp_f32 v231, v151\n\t"
    "v_mfma_f32_16x16x32_bf16 v[140:143], a[112:115], a[16:19], v[196:199]\n\t"
    "ds_read_b128 a[64:67], v216 offset:49152\n\t"
    "v_add_f32 v237, v237, v232\n\t"
    "v_mfma_f32_16x16x32_bf16 v[128:131], a[100:103], a[4:7], v[128:131]\n\t"
    "v_exp_f32 v228, v152\n\t"
    "v_mfma_f32_16x16x32_bf16 v[136:139], a[100:103], a[20:23], v[136:139]\n\t"
    "ds_read_b128 a[80:83], v216 offset:53248\n\t"
    "s_mov_b32 m0, s59\n\t"
    "v_cvt_pk_bf16_f32 v171, v230, v231\n\t"
    "v_mfma_f32_16x16x32_bf16 v[132:135], a[116:119], a[4:7], v[132:135]\n\t"
    "v_exp_f32 v229, v153\n\t"
    "v_mfma_f32_16x16x32_bf16 v[140:143], a[116:119], a[20:23], v[140:143]\n\t"
    "ds_read_b128 a[68:71], v217 offset:49152\n\t"
    "v_mfma_f32_16x16x32_bf16 v[252:255], a[252:255], v[168:171], 0\n\t"
    "buffer_load_dwordx4 v220, s[36:39], s56 offen offset:0 lds\n\t"
    "v_mfma_f32_16x16x32_bf16 v[128:131], a[104:107], a[8:11], v[128:131]\n\t"
    "v_exp_f32 v230, v154\n\t"
    "v_mfma_f32_16x16x32_bf16 v[136:139], a[104:107], a[24:27], v[136:139]\n\t"
    "v_add_f32 v233, v228, v229\n\t"
    "v_cvt_pk_bf16_f32 v172, v228, v229\n\t"
    "v_mfma_f32_16x16x32_bf16 v[132:135], a[120:123], a[8:11], v[132:135]\n\t"
    "v_exp_f32 v231, v155\n\t"
    "v_mfma_f32_16x16x32_bf16 v[140:143], a[120:123], a[24:27], v[140:143]\n\t"
    "ds_read_b128 a[84:87], v217 offset:53248\n\t"
    "v_add_f32 v233, v233, v230\n\t"
    "v_mfma_f32_16x16x32_bf16 v[128:131], a[108:111], a[12:15], v[128:131]\n\t"
    "v_exp_f32 v228, v156\n\t"
    "v_mfma_f32_16x16x32_bf16 v[136:139], a[108:111], a[28:31], v[136:139]\n\t"
    "ds_read_b128 a[72:75], v218 offset:49152\n\t"
    "v_add_f32 v233, v233, v231\n\t"
    "v_cvt_pk_bf16_f32 v173, v230, v231\n\t"
    "v_mfma_f32_16x16x32_bf16 v[132:135], a[124:127], a[12:15], v[132:135]\n\t"
    "v_exp_f32 v229, v157\n\t"
    "v_mfma_f32_16x16x32_bf16 v[140:143], a[124:127], a[28:31], v[140:143]\n\t"
    "ds_read_b128 a[88:91], v218 offset:53248\n\t"
    "v_add_f32 v233, v233, v228\n\t"
    "s_waitcnt lgkmcnt(7)\n\t"
    "v_mfma_f32_16x16x32_bf16 v[0:3], a[128:131], v[160:163], v[0:3]\n\t"
    "v_exp_f32 v230, v158\n\t"
    "v_mfma_f32_16x16x32_bf16 v[4:7], a[128:131], v[164:167], v[4:7]\n\t"
    "v_add_f32 v233, v233, v229\n\t"
    "v_cvt_pk_bf16_f32 v174, v228, v229\n\t"
    "v_mfma_f32_16x16x32_bf16 v[16:19], a[132:135], v[160:163], v[16:19]\n\t"
    "v_exp_f32 v231, v159\n\t"
    "v_mfma_f32_16x16x32_bf16 v[20:23], a[132:135], v[164:167], v[20:23]\n\t"
    "ds_read_b128 a[76:79], v219 offset:49152\n\t"
    "v_add_f32 v233, v233, v230\n\t"
    "v_mfma_f32_16x16x32_bf16 v[32:35], a[136:139], v[160:163], v[32:35]\n\t"
    "v_exp_f32 v228, v128\n\t"
    "v_mfma_f32_16x16x32_bf16 v[36:39], a[136:139], v[164:167], v[36:39]\n\t"
    "v_add_f32 v233, v233, v231\n\t"
    "v_cvt_pk_bf16_f32 v175, v230, v231\n\t"
    "v_mfma_f32_16x16x32_bf16 v[48:51], a[140:143], v[160:163], v[48:51]\n\t"
    "v_exp_f32 v229, v129\n\t"
    "v_mfma_f32_16x16x32_bf16 v[52:55], a[140:143], v[164:167], v[52:55]\n\t"
    "buffer_load_dwordx4 v221, s[36:39], s56 offen offset:1024 lds\n\t"
    "v_max_f32 v248, v233, v252\n\t"
    "v_mfma_f32_16x16x32_bf16 v[64:67], a[144:147], v[160:163], v[64:67]\n\t"
    "v_exp_f32 v230, v130\n\t"
    "v_mfma_f32_16x16x32_bf16 v[68:71], a[144:147], v[164:167], v[68:71]\n\t"
    "ds_read_b128 a[92:95], v219 offset:53248\n\t"
    "v_cmp_lt_f32 vcc, s68, v248\n\t"
    "v_mfma_f32_16x16x32_bf16 v[80:83], a[148:151], v[160:163], v[80:83]\n\t"
    "v_exp_f32 v231, v131\n\t"
    "v_mfma_f32_16x16x32_bf16 v[84:87], a[148:151], v[164:167], v[84:87]\n\t"
    "ds_read_b64_tr_b16 a[162:163], v208 offset:45056\n\t"
    "v_cvt_pk_bf16_f32 v176, v228, v229\n\t"
    "v_mfma_f32_16x16x32_bf16 v[96:99], a[152:155], v[160:163], v[96:99]\n\t"
    "v_exp_f32 v228, v132\n\t"
    "v_mfma_f32_16x16x32_bf16 v[100:103], a[152:155], v[164:167], v[100:103]\n\t"
    "s_cbranch_vccnz L_rs9_%=\n\t"
    "L_ret9_%=:\n\t"
    "ds_read_b64_tr_b16 a[164:165], v209 offset:40960\n\t"
    "v_cvt_pk_bf16_f32 v177, v230, v231\n\t"
    "v_mfma_f32_16x16x32_bf16 v[112:115], a[156:159], v[160:163], v[112:115]\n\t"
    "v_exp_f32 v229, v133\n\t"
    "v_mfma_f32_16x16x32_bf16 v[116:119], a[156:159], v[164:167], v[116:119]\n\t"
    "ds_read_b64_tr_b16 a[166:167], v209 offset:45056\n\t"
    "v_add_f32 v238, v238, v252\n\t"
    "v_mfma_f32_16x16x32_bf16 v[144:147], a[96:99], a[32:35], v[200:203]\n\t"
    "v_exp_f32 v230, v134\n\t"
    "v_mfma_f32_16x16x32_bf16 v[152:155], a[96:99], a[48:51], v[204:207]\n\t"
    "ds_read_b64_tr_b16 a[168:169], v210 offset:40960\n\t"
    "v_cvt_pk_bf16_f32 v178, v228, v229\n\t"
    "v_mfma_f32_16x16x32_bf16 v[148:151], a[112:115], a[32:35], v[200:203]\n\t"
    "v_exp_f32 v231, v135\n\t"
    "v_mfma_f32_16x16x32_bf16 v[156:159], a[112:115], a[48:51], v[204:207]\n\t"
    "ds_read_b64_tr_b16 a[170:171], v210 offset:45056\n\t"
    "v_add_f32 v239, v239, v233\n\t"
    "v_mfma_f32_16x16x32_bf16 v[144:147], a[100:103], a[36:39], v[144:147]\n\t"
    "v_exp_f32 v228, v136\n\t"
    "v_mfma_f32_16x16x32_bf16 v[152:155], a[100:103], a[52:55], v[152:155]\n\t"
    "ds_read_b64_tr_b16 a[172:173], v211 offset:40960\n\t"
    "v_cvt_pk_bf16_f32 v179, v230, v231\n\t"
    "v_mfma_f32_16x16x32_bf16 v[148:151], a[116:119], a[36:39], v[148:151]\n\t"
    "v_exp_f32 v229, v137\n\t"
    "v_mfma_f32_16x16x32_bf16 v[156:159], a[116:119], a[52:55], v[156:159]\n\t"
    "ds_read_b64_tr_b16 a[174:175], v211 offset:45056\n\t"
    "v_mfma_f32_16x16x32_bf16 v[252:255], a[252:255], v[176:179], 0\n\t"
    "buffer_load_dwordx4 v222, s[36:39], s56 offen offset:2048 lds\n\t"
    "v_mfma_f32_16x16x32_bf16 v[144:147], a[104:107], a[40:43], v[144:147]\n\t"
    "v_exp_f32 v230, v138\n\t"
    "v_mfma_f32_16x16x32_bf16 v[152:155], a[104:107], a[56:59], v[152:155]\n\t"
    "v_add_f32 v232, v228, v229\n\t"
    "v_cvt_pk_bf16_f32 v180, v228, v229\n\t"
    "v_mfma_f32_16x16x32_bf16 v[148:151], a[120:123], a[40:43], v[148:151]\n\t"
    "v_exp_f32 v231, v139\n\t"
    "v_mfma_f32_16x16x32_bf16 v[156:159], a[120:123], a[56:59], v[156:159]\n\t"
    "ds_read_b64_tr_b16 a[176:177], v212 offset:40960\n\t"
    "v_add_f32 v232, v232, v230\n\t"
    "v_mfma_f32_16x16x32_bf16 v[144:147], a[108:111], a[44:47], v[144:147]\n\t"
    "v_exp_f32 v228, v140\n\t"
    "v_mfma_f32_16x16x32_bf16 v[152:155], a[108:111], a[60:63], v[152:155]\n\t"
    "ds_read_b64_tr_b16 a[178:179], v212 offset:45056\n\t"
    "v_add_f32 v232, v232, v231\n\t"
    "v_cvt_pk_bf16_f32 v181, v230, v231\n\t"
    "v_mfma_f32_16x16x32_bf16 v[148:151], a[124:127], a[44:47], v[148:151]\n\t"
    "v_exp_f32 v229, v141\n\t"
    "v_mfma_f32_16x16x32_bf16 v[156:159], a[124:127], a[60:63], v[156:159]\n\t"
    "ds_read_b64_tr_b16 a[180:181], v213 offset:40960\n\t"
    "v_add_f32 v232, v232, v228\n\t"
    "v_mfma_f32_16x16x32_bf16 v[8:11], a[128:131], v[168:171], v[8:11]\n\t"
    "v_exp_f32 v230, v142\n\t"
    "v_mfma_f32_16x16x32_bf16 v[12:15], a[128:131], v[172:175], v[12:15]\n\t"
    "v_add_f32 v232, v232, v229\n\t"
    "v_cvt_pk_bf16_f32 v182, v228, v229\n\t"
    "v_mfma_f32_16x16x32_bf16 v[24:27], a[132:135], v[168:171], v[24:27]\n\t"
    "v_exp_f32 v231, v143\n\t"
    "v_mfma_f32_16x16x32_bf16 v[28:31], a[132:135], v[172:175], v[28:31]\n\t"
    "ds_read_b64_tr_b16 a[182:183], v213 offset:45056\n\t"
    "v_add_f32 v232, v232, v230\n\t"
    "v_mfma_f32_16x16x32_bf16 v[40:43], a[136:139], v[168:171], v[40:43]\n\t"
    "v_exp_f32 v228, v144\n\t"
    "v_mfma_f32_16x16x32_bf16 v[44:47], a[136:139], v[172:175], v[44:47]\n\t"
    "v_add_f32 v232, v232, v231\n\t"
    "v_cvt_pk_bf16_f32 v183, v230, v231\n\t"
    "v_mfma_f32_16x16x32_bf16 v[56:59], a[140:143], v[168:171], v[56:59]\n\t"
    "v_exp_f32 v229, v145\n\t"
    "v_mfma_f32_16x16x32_bf16 v[60:63], a[140:143], v[172:175], v[60:63]\n\t"
    "buffer_load_dwordx4 v223, s[36:39], s56 offen offset:3072 lds\n\t"
    "v_max_f32 v248, v232, v252\n\t"
    "v_mfma_f32_16x16x32_bf16 v[72:75], a[144:147], v[168:171], v[72:75]\n\t"
    "v_exp_f32 v230, v146\n\t"
    "v_mfma_f32_16x16x32_bf16 v[76:79], a[144:147], v[172:175], v[76:79]\n\t"
    "ds_read_b64_tr_b16 a[184:185], v214 offset:40960\n\t"
    "s_add_u32 s56, s56, 0x4000\n\t"
    "s_and_b32 s56, s56, 0x7ffff\n\t"
    "s_cselect_b64 s[36:37], s[36:37], s[76:77]\n\t"
    "v_cmp_lt_f32 vcc, s68, v248\n\t"
    "v_mfma_f32_16x16x32_bf16 v[88:91], a[148:151], v[168:171], v[88:91]\n\t"
    "v_exp_f32 v231, v147\n\t"
    "v_mfma_f32_16x16x32_bf16 v[92:95], a[148:151], v[172:175], v[92:95]\n\t"
    "ds_read_b64_tr_b16 a[186:187], v214 offset:45056\n\t"
    "v_cvt_pk_bf16_f32 v184, v228, v229\n\t"
    "v_mfma_f32_16x16x32_bf16 v[104:107], a[152:155], v[168:171], v[104:107]\n\t"
    "v_exp_f32 v228, v148\n\t"
    "v_mfma_f32_16x16x32_bf16 v[108:111], a[152:155], v[172:175], v[108:111]\n\t"
    "s_cbranch_vccnz L_rs10_%=\n\t"
    "L_ret10_%=:\n\t"
    "ds_read_b64_tr_b16 a[188:189], v215 offset:40960\n\t"
    "v_cvt_pk_bf16_f32 v185, v230, v231\n\t"
    "v_mfma_f32_16x16x32_bf16 v[120:123], a[156:159], v[168:171], v[120:123]\n\t"
    "v_exp_f32 v229, v149\n\t"
    "v_mfma_f32_16x16x32_bf16 v[124:127], a[156:159], v[172:175], v[124:127]\n\t"
    "ds_read_b64_tr_b16 a[190:191], v215 offset:45056\n\t"
    "v_add_f32 v236, v236, v252\n\t"
    "s_waitcnt lgkmcnt(15)\n\t"
    "v_mfma_f32_16x16x32_bf16 v[128:131], a[64:67], a[0:3], v[192:195]\n\t"
    "v_exp_f32 v230, v150\n\t"
    "v_mfma_f32_16x16x32_bf16 v[136:139], a[64:67], a[16:19], v[196:199]\n\t"
    "ds_read_b128 a[96:99], v216 offset:57344\n\t"
    "v_cvt_pk_bf16_f32 v186, v228, v229\n\t"
    "v_mfma_f32_16x16x32_bf16 v[132:135], a[80:83], a[0:3], v[192:195]\n\t"
    "v_exp_f32 v231, v151\n\t"
    "v_mfma_f32_16x16x32_bf16 v[140:143], a[80:83], a[16:19], v[196:199]\n\t"
    "ds_read_b128 a[112:115], v216 offset:61440\n\t"
    "v_add_f32 v237, v237, v232\n\t"
    "v_mfma_f32_16x16x32_bf16 v[128:131], a[68:71], a[4:7], v[128:131]\n\t"
    "v_exp_f32 v228, v152\n\t"
    "v_mfma_f32_16x16x32_bf16 v[136:139], a[68:71], a[20:23], v[136:139]\n\t"
    "ds_read_b128 a[100:103], v217 offset:57344\n\t"
    "s_mov_b32 m0, s63\n\t"
    "v_cvt_pk_bf16_f32 v187, v230, v231\n\t"
    "v_mfma_f32_16x16x32_bf16 v[132:135], a[84:87], a[4:7], v[132:135]\n\t"
    "v_exp_f32 v229, v153\n\t"
    "v_mfma_f32_16x16x32_bf16 v[140:143], a[84:87], a[20:23], v[140:143]\n\t"
    "ds_read_b128 a[116:119], v217 offset:61440\n\t"
    "v_mfma_f32_16x16x32_bf16 v[252:255], a[252:255], v[184:187], 0\n\t"
    "buffer_load_dwordx4 v224, s[40:43], s57 offen offset:0 lds\n\t"
    "v_mfma_f32_16x16x32_bf16 v[128:131], a[72:75], a[8:11], v[128:131]\n\t"
    "v_exp_f32 v230, v154\n\t"
    "v_mfma_f32_16x16x32_bf16 v[136:139], a[72:75], a[24:27], v[136:139]\n\t"
    "v_add_f32 v233, v228, v229\n\t"
    "v_cvt_pk_bf16_f32 v188, v228, v229\n\t"
    "v_mfma_f32_16x16x32_bf16 v[132:135], a[88:91], a[8:11], v[132:135]\n\t"
    "v_exp_f32 v231, v155\n\t"
    "v_mfma_f32_16x16x32_bf16 v[140:143], a[88:91], a[24:27], v[140:143]\n\t"
    "ds_read_b128 a[104:107], v218 offset:57344\n\t"
    "v_add_f32 v233, v233, v230\n\t"
    "v_mfma_f32_16x16x32_bf16 v[128:131], a[76:79], a[12:15], v[128:131]\n\t"
    "v_exp_f32 v228, v156\n\t"
    "v_mfma_f32_16x16x32_bf16 v[136:139], a[76:79], a[28:31], v[136:139]\n\t"
    "ds_read_b128 a[120:123], v218 offset:61440\n\t"
    "v_add_f32 v233, v233, v231\n\t"
    "v_cvt_pk_bf16_f32 v189, v230, v231\n\t"
    "v_mfma_f32_16x16x32_bf16 v[132:135], a[92:95], a[12:15], v[132:135]\n\t"
    "v_exp_f32 v229, v157\n\t"
    "v_mfma_f32_16x16x32_bf16 v[140:143], a[92:95], a[28:31], v[140:143]\n\t"
    "ds_read_b128 a[108:111], v219 offset:57344\n\t"
    "v_add_f32 v233, v233, v228\n\t"
    "s_waitcnt lgkmcnt(7)\n\t"
    "v_mfma_f32_16x16x32_bf16 v[0:3], a[160:163], v[176:179], v[0:3]\n\t"
    "v_exp_f32 v230, v158\n\t"
    "v_mfma_f32_16x16x32_bf16 v[4:7], a[160:163], v[180:183], v[4:7]\n\t"
    "v_add_f32 v233, v233, v229\n\t"
    "v_cvt_pk_bf16_f32 v190, v228, v229\n\t"
    "v_mfma_f32_16x16x32_bf16 v[16:19], a[164:167], v[176:179], v[16:19]\n\t"
    "v_exp_f32 v231, v159\n\t"
    "v_mfma_f32_16x16x32_bf16 v[20:23], a[164:167], v[180:183], v[20:23]\n\t"
    "ds_read_b128 a[124:127], v219 offset:61440\n\t"
    "v_add_f32 v233, v233, v230\n\t"
    "v_mfma_f32_16x16x32_bf16 v[32:35], a[168:171], v[176:179], v[32:35]\n\t"
    "v_exp_f32 v228, v128\n\t"
    "v_mfma_f32_16x16x32_bf16 v[36:39], a[168:171], v[180:183], v[36:39]\n\t"
    "v_add_f32 v233, v233, v231\n\t"
    "v_cvt_pk_bf16_f32 v191, v230, v231\n\t"
    "v_mfma_f32_16x16x32_bf16 v[48:51], a[172:175], v[176:179], v[48:51]\n\t"
    "v_exp_f32 v229, v129\n\t"
    "v_mfma_f32_16x16x32_bf16 v[52:55], a[172:175], v[180:183], v[52:55]\n\t"
    "buffer_load_dwordx4 v225, s[40:43], s57 offen offset:1024 lds\n\t"
    "v_max_f32 v248, v233, v252\n\t"
    "v_mfma_f32_16x16x32_bf16 v[64:67], a[176:179], v[176:179], v[64:67]\n\t"
    "v_exp_f32 v230, v130\n\t"
    "v_mfma_f32_16x16x32_bf16 v[68:71], a[176:179], v[180:183], v[68:71]\n\t"
    "ds_read_b64_tr_b16 a[128:129], v208 offset:49152\n\t"
    "v_cmp_lt_f32 vcc, s68, v248\n\t"
    "v_mfma_f32_16x16x32_bf16 v[80:83], a[180:183], v[176:179], v[80:83]\n\t"
    "v_exp_f32 v231, v131\n\t"
    "v_mfma_f32_16x16x32_bf16 v[84:87], a[180:183], v[180:183], v[84:87]\n\t"
    "ds_read_b64_tr_b16 a[130:131], v208 offset:53248\n\t"
    "v_cvt_pk_bf16_f32 v160, v228, v229\n\t"
    "v_mfma_f32_16x16x32_bf16 v[96:99], a[184:187], v[176:179], v[96:99]\n\t"
    "v_exp_f32 v228, v132\n\t"
    "v_mfma_f32_16x16x32_bf16 v[100:103], a[184:187], v[180:183], v[100:103]\n\t"
    "s_cbranch_vccnz L_rs11_%=\n\t"
    "L_ret11_%=:\n\t"
    "ds_read_b64_tr_b16 a[132:133], v209 offset:49152\n\t"
    "v_cvt_pk_bf16_f32 v161, v230, v231\n\t"
    "v_mfma_f32_16x16x32_bf16 v[112:115], a[188:191], v[176:179], v[112:115]\n\t"
    "v_exp_f32 v229, v133\n\t"
    "v_mfma_f32_16x16x32_bf16 v[116:119], a[188:191], v[180:183], v[116:119]\n\t"
    "ds_read_b64_tr_b16 a[134:135], v209 offset:53248\n\t"
    "v_add_f32 v238, v238, v252\n\t"
    "v_mfma_f32_16x16x32_bf16 v[144:147], a[64:67], a[32:35], v[200:203]\n\t"
    "v_exp_f32 v230, v134\n\t"
    "v_mfma_f32_16x16x32_bf16 v[152:155], a[64:67], a[48:51], v[204:207]\n\t"
    "ds_read_b64_tr_b16 a[136:137], v210 offset:49152\n\t"
    "v_cvt_pk_bf16_f32 v162, v228, v229\n\t"
    "v_mfma_f32_16x16x32_bf16 v[148:151], a[80:83], a[32:35], v[200:203]\n\t"
    "v_exp_f32 v231, v135\n\t"
    "v_mfma_f32_16x16x32_bf16 v[156:159], a[80:83], a[48:51], v[204:207]\n\t"
    "ds_read_b64_tr_b16 a[138:139], v210 offset:53248\n\t"
    "v_add_f32 v239, v239, v233\n\t"
    "v_mfma_f32_16x16x32_bf16 v[144:147], a[68:71], a[36:39], v[144:147]\n\t"
    "v_exp_f32 v228, v136\n\t"
    "v_mfma_f32_16x16x32_bf16 v[152:155], a[68:71], a[52:55], v[152:155]\n\t"
    "ds_read_b64_tr_b16 a[140:141], v211 offset:49152\n\t"
    "v_cvt_pk_bf16_f32 v163, v230, v231\n\t"
    "v_mfma_f32_16x16x32_bf16 v[148:151], a[84:87], a[36:39], v[148:151]\n\t"
    "v_exp_f32 v229, v137\n\t"
    "v_mfma_f32_16x16x32_bf16 v[156:159], a[84:87], a[52:55], v[156:159]\n\t"
    "ds_read_b64_tr_b16 a[142:143], v211 offset:53248\n\t"
    "v_mfma_f32_16x16x32_bf16 v[252:255], a[252:255], v[160:163], 0\n\t"
    "buffer_load_dwordx4 v224, s[40:43], s57 offen offset:2048 lds\n\t"
    "v_mfma_f32_16x16x32_bf16 v[144:147], a[72:75], a[40:43], v[144:147]\n\t"
    "v_exp_f32 v230, v138\n\t"
    "v_mfma_f32_16x16x32_bf16 v[152:155], a[72:75], a[56:59], v[152:155]\n\t"
    "v_add_f32 v232, v228, v229\n\t"
    "v_cvt_pk_bf16_f32 v164, v228, v229\n\t"
    "v_mfma_f32_16x16x32_bf16 v[148:151], a[88:91], a[40:43], v[148:151]\n\t"
    "v_exp_f32 v231, v139\n\t"
    "v_mfma_f32_16x16x32_bf16 v[156:159], a[88:91], a[56:59], v[156:159]\n\t"
    "ds_read_b64_tr_b16 a[144:145], v212 offset:49152\n\t"
    "v_add_f32 v232, v232, v230\n\t"
    "v_mfma_f32_16x16x32_bf16 v[144:147], a[76:79], a[44:47], v[144:147]\n\t"
    "v_exp_f32 v228, v140\n\t"
    "v_mfma_f32_16x16x32_bf16 v[152:155], a[76:79], a[60:63], v[152:155]\n\t"
    "ds_read_b64_tr_b16 a[146:147], v212 offset:53248\n\t"
    "v_add_f32 v232, v232, v231\n\t"
    "v_cvt_pk_bf16_f32 v165, v230, v231\n\t"
    "v_mfma_f32_16x16x32_bf16 v[148:151], a[92:95], a[44:47], v[148:151]\n\t"
    "v_exp_f32 v229, v141\n\t"
    "v_mfma_f32_16x16x32_bf16 v[156:159], a[92:95], a[60:63], v[156:159]\n\t"
    "ds_read_b64_tr_b16 a[148:149], v213 offset:49152\n\t"
    "v_add_f32 v232, v232, v228\n\t"
    "v_mfma_f32_16x16x32_bf16 v[8:11], a[160:163], v[184:187], v[8:11]\n\t"
    "v_exp_f32 v230, v142\n\t"
    "v_mfma_f32_16x16x32_bf16 v[12:15], a[160:163], v[188:191], v[12:15]\n\t"
    "v_add_f32 v232, v232, v229\n\t"
    "v_cvt_pk_bf16_f32 v166, v228, v229\n\t"
    "v_mfma_f32_16x16x32_bf16 v[24:27], a[164:167], v[184:187], v[24:27]\n\t"
    "v_exp_f32 v231, v143\n\t"
    "v_mfma_f32_16x16x32_bf16 v[28:31], a[164:167], v[188:191], v[28:31]\n\t"
    "ds_read_b64_tr_b16 a[150:151], v213 offset:53248\n\t"
    "v_add_f32 v232, v232, v230\n\t"
    "v_mfma_f32_16x16x32_bf16 v[40:43], a[168:171], v[184:187], v[40:43]\n\t"
    "v_exp_f32 v228, v144\n\t"
    "v_mfma_f32_16x16x32_bf16 v[44:47], a[168:171], v[188:191], v[44:47]\n\t"
    "v_add_f32 v232, v232, v231\n\t"
    "v_cvt_pk_bf16_f32 v167, v230, v231\n\t"
    "v_mfma_f32_16x16x32_bf16 v[56:59], a[172:175], v[184:187], v[56:59]\n\t"
    "v_exp_f32 v229, v145\n\t"
    "v_mfma_f32_16x16x32_bf16 v[60:63], a[172:175], v[188:191], v[60:63]\n\t"
    "buffer_load_dwordx4 v225, s[40:43], s57 offen offset:3072 lds\n\t"
    "v_max_f32 v248, v232, v252\n\t"
    "v_mfma_f32_16x16x32_bf16 v[72:75], a[176:179], v[184:187], v[72:75]\n\t"
    "v_exp_f32 v230, v146\n\t"
    "v_mfma_f32_16x16x32_bf16 v[76:79], a[176:179], v[188:191], v[76:79]\n\t"
    "ds_read_b64_tr_b16 a[152:153], v214 offset:49152\n\t"
    "s_add_u32 s57, s57, 0x4000\n\t"
    "s_and_b32 s57, s57, 0x7ffff\n\t"
    "s_cselect_b64 s[40:41], s[40:41], s[78:79]\n\t"
    "v_cmp_lt_f32 vcc, s68, v248\n\t"
    "v_mfma_f32_16x16x32_bf16 v[88:91], a[180:183], v[184:187], v[88:91]\n\t"
    "v_exp_f32 v231, v147\n\t"
    "v_mfma_f32_16x16x32_bf16 v[92:95], a[180:183], v[188:191], v[92:95]\n\t"
    "ds_read_b64_tr_b16 a[154:155], v214 offset:53248\n\t"
    "v_cvt_pk_bf16_f32 v168, v228, v229\n\t"
    "v_mfma_f32_16x16x32_bf16 v[104:107], a[184:187], v[184:187], v[104:107]\n\t"
    "v_exp_f32 v228, v148\n\t"
    "v_mfma_f32_16x16x32_bf16 v[108:111], a[184:187], v[188:191], v[108:111]\n\t"
    "s_cbranch_vccnz L_rs12_%=\n\t"
    "L_ret12_%=:\n\t"
    "ds_read_b64_tr_b16 a[156:157], v215 offset:49152\n\t"
    "v_cvt_pk_bf16_f32 v169, v230, v231\n\t"
    "v_mfma_f32_16x16x32_bf16 v[120:123], a[188:191], v[184:187], v[120:123]\n\t"
    "v_exp_f32 v229, v149\n\t"
    "v_mfma_f32_16x16x32_bf16 v[124:127], a[188:191], v[188:191], v[124:127]\n\t"
    "ds_read_b64_tr_b16 a[158:159], v215 offset:53248\n\t"
    "v_add_f32 v236, v236, v252\n\t"
    "s_waitcnt lgkmcnt(15)\n\t"
    "v_mfma_f32_16x16x32_bf16 v[128:131], a[96:99], a[0:3], v[192:195]\n\t"
    "v_exp_f32 v230, v150\n\t"
    "v_mfma_f32_16x16x32_bf16 v[136:139], a[96:99], a[16:19], v[196:199]\n\t"
    "ds_read_b64_tr_b16 a[160:161], v208 offset:57344\n\t"
    "v_cvt_pk_bf16_f32 v170, v228, v229\n\t"
    "s_waitcnt vmcnt(8)\n\t"
    "s_barrier\n\t"
    "v_mfma_f32_16x16x32_bf16 v[132:135], a[112:115], a[0:3], v[192:195]\n\t"
    "v_exp_f32 v231, v151\n\t"
    "v_mfma_f32_16x16x32_bf16 v[140:143], a[112:115], a[16:19], v[196:199]\n\t"
    "ds_read_b128 a[64:67], v216 offset:0\n\t"
    "v_add_f32 v237, v237, v232\n\t"
    "v_mfma_f32_16x16x32_bf16 v[128:131], a[100:103], a[4:7], v[128:131]\n\t"
    "v_exp_f32 v228, v152\n\t"
    "v_mfma_f32_16x16x32_bf16 v[136:139], a[100:103], a[20:23], v[136:139]\n\t"
    "ds_read_b128 a[80:83], v216 offset:4096\n\t"
    "s_mov_b32 m0, s60\n\t"
    "v_cvt_pk_bf16_f32 v171, v230, v231\n\t"
    "v_mfma_f32_16x16x32_bf16 v[132:135], a[116:119], a[4:7], v[132:135]\n\t"
    "v_exp_f32 v229, v153\n\t"
    "v_mfma_f32_16x16x32_bf16 v[140:143], a[116:119], a[20:23], v[140:143]\n\t"
    "ds_read_b128 a[68:71], v217 offset:0\n\t"
    "v_mfma_f32_16x16x32_bf16 v[252:255], a[252:255], v[168:171], 0\n\t"
    "buffer_load_dwordx4 v220, s[36:39], s56 offen offset:0 lds\n\t"
    "v_mfma_f32_16x16x32_bf16 v[128:131], a[104:107], a[8:11], v[128:131]\n\t"
    "v_exp_f32 v230, v154\n\t"
    "v_mfma_f32_16x16x32_bf16 v[136:139], a[104:107], a[24:27], v[136:139]\n\t"
    "v_add_f32 v233, v228, v229\n\t"
    "v_cvt_pk_bf16_f32 v172, v228, v229\n\t"
    "v_mfma_f32_16x16x32_bf16 v[132:135], a[120:123], a[8:11], v[132:135]\n\t"
    "v_exp_f32 v231, v155\n\t"
    "v_mfma_f32_16x16x32_bf16 v[140:143], a[120:123], a[24:27], v[140:143]\n\t"
    "ds_read_b128 a[84:87], v217 offset:4096\n\t"
    "v_add_f32 v233, v233, v230\n\t"
    "v_mfma_f32_16x16x32_bf16 v[128:131], a[108:111], a[12:15], v[128:131]\n\t"
    "v_exp_f32 v228, v156\n\t"
    "v_mfma_f32_16x16x32_bf16 v[136:139], a[108:111], a[28:31], v[136:139]\n\t"
    "ds_read_b128 a[72:75], v218 offset:0\n\t"
    "v_add_f32 v233, v233, v231\n\t"
    "v_cvt_pk_bf16_f32 v173, v230, v231\n\t"
    "v_mfma_f32_16x16x32_bf16 v[132:135], a[124:127], a[12:15], v[132:135]\n\t"
    "v_exp_f32 v229, v157\n\t"
    "v_mfma_f32_16x16x32_bf16 v[140:143], a[124:127], a[28:31], v[140:143]\n\t"
    "ds_read_b128 a[88:91], v218 offset:4096\n\t"
    "v_add_f32 v233, v233, v228\n\t"
    "s_waitcnt lgkmcnt(7)\n\t"
    "v_mfma_f32_16x16x32_bf16 v[0:3], a[128:131], v[160:163], v[0:3]\n\t"
    "v_exp_f32 v230, v158\n\t"
    "v_mfma_f32_16x16x32_bf16 v[4:7], a[128:131], v[164:167], v[4:7]\n\t"
    "v_add_f32 v233, v233, v229\n\t"
    "v_cvt_pk_bf16_f32 v174, v228, v229\n\t"
    "v_mfma_f32_16x16x32_bf16 v[16:19], a[132:135], v[160:163], v[16:19]\n\t"
    "v_exp_f32 v231, v159\n\t"
    "v_mfma_f32_16x16x32_bf16 v[20:23], a[132:135], v[164:167], v[20:23]\n\t"
    "ds_read_b128 a[76:79], v219 offset:0\n\t"
    "v_add_f32 v233, v233, v230\n\t"
    "v_mfma_f32_16x16x32_bf16 v[32:35], a[136:139], v[160:163], v[32:35]\n\t"
    "v_exp_f32 v228, v128\n\t"
    "v_mfma_f32_16x16x32_bf16 v[36:39], a[136:139], v[164:167], v[36:39]\n\t"
    "v_add_f32 v233, v233, v231\n\t"
    "v_cvt_pk_bf16_f32 v175, v230, v231\n\t"
    "v_mfma_f32_16x16x32_bf16 v[48:51], a[140:143], v[160:163], v[48:51]\n\t"
    "v_exp_f32 v229, v129\n\t"
    "v_mfma_f32_16x16x32_bf16 v[52:55], a[140:143], v[164:167], v[52:55]\n\t"
    "buffer_load_dwordx4 v221, s[36:39], s56 offen offset:1024 lds\n\t"
    "v_max_f32 v248, v233, v252\n\t"
    "v_mfma_f32_16x16x32_bf16 v[64:67], a[144:147], v[160:163], v[64:67]\n\t"
    "v_exp_f32 v230, v130\n\t"
    "v_mfma_f32_16x16x32_bf16 v[68:71], a[144:147], v[164:167], v[68:71]\n\t"
    "ds_read_b128 a[92:95], v219 offset:4096\n\t"
    "v_cmp_lt_f32 vcc, s68, v248\n\t"
    "v_mfma_f32_16x16x32_bf16 v[80:83], a[148:151], v[160:163], v[80:83]\n\t"
    "v_exp_f32 v231, v131\n\t"
    "v_mfma_f32_16x16x32_bf16 v[84:87], a[148:151], v[164:167], v[84:87]\n\t"
    "ds_read_b64_tr_b16 a[162:163], v208 offset:61440\n\t"
    "v_cvt_pk_bf16_f32 v176, v228, v229\n\t"
    "v_mfma_f32_16x16x32_bf16 v[96:99], a[152:155], v[160:163], v[96:99]\n\t"
    "v_exp_f32 v228, v132\n\t"
    "v_mfma_f32_16x16x32_bf16 v[100:103], a[152:155], v[164:167], v[100:103]\n\t"
    "s_cbranch_vccnz L_rs13_%=\n\t"
    "L_ret13_%=:\n\t"
    "ds_read_b64_tr_b16 a[164:165], v209 offset:57344\n\t"
    "v_cvt_pk_bf16_f32 v177, v230, v231\n\t"
    "v_mfma_f32_16x16x32_bf16 v[112:115], a[156:159], v[160:163], v[112:115]\n\t"
    "v_exp_f32 v229, v133\n\t"
    "v_mfma_f32_16x16x32_bf16 v[116:119], a[156:159], v[164:167], v[116:119]\n\t"
    "ds_read_b64_tr_b16 a[166:167], v209 offset:61440\n\t"
    "v_add_f32 v238, v238, v252\n\t"
    "v_mfma_f32_16x16x32_bf16 v[144:147], a[96:99], a[32:35], v[200:203]\n\t"
    "v_exp_f32 v230, v134\n\t"
    "v_mfma_f32_16x16x32_bf16 v[152:155], a[96:99], a[48:51], v[204:207]\n\t"
    "ds_read_b64_tr_b16 a[168:169], v210 offset:57344\n\t"
    "v_cvt_pk_bf16_f32 v178, v228, v229\n\t"
    "v_mfma_f32_16x16x32_bf16 v[148:151], a[112:115], a[32:35], v[200:203]\n\t"
    "v_exp_f32 v231, v135\n\t"
    "v_mfma_f32_16x16x32_bf16 v[156:159], a[112:115], a[48:51], v[204:207]\n\t"
    "ds_read_b64_tr_b16 a[170:171], v210 offset:61440\n\t"
    "v_add_f32 v239, v239, v233\n\t"
    "v_mfma_f32_16x16x32_bf16 v[144:147], a[100:103], a[36:39], v[144:147]\n\t"
    "v_exp_f32 v228, v136\n\t"
    "v_mfma_f32_16x16x32_bf16 v[152:155], a[100:103], a[52:55], v[152:155]\n\t"
    "ds_read_b64_tr_b16 a[172:173], v211 offset:57344\n\t"
    "v_cvt_pk_bf16_f32 v179, v230, v231\n\t"
    "v_mfma_f32_16x16x32_bf16 v[148:151], a[116:119], a[36:39], v[148:151]\n\t"
    "v_exp_f32 v229, v137\n\t"
    "v_mfma_f32_16x16x32_bf16 v[156:159], a[116:119], a[52:55], v[156:159]\n\t"
    "ds_read_b64_tr_b16 a[174:175], v211 offset:61440\n\t"
    "v_mfma_f32_16x16x32_bf16 v[252:255], a[252:255], v[176:179], 0\n\t"
    "buffer_load_dwordx4 v222, s[36:39], s56 offen offset:2048 lds\n\t"
    "v_mfma_f32_16x16x32_bf16 v[144:147], a[104:107], a[40:43], v[144:147]\n\t"
    "v_exp_f32 v230, v138\n\t"
    "v_mfma_f32_16x16x32_bf16 v[152:155], a[104:107], a[56:59], v[152:155]\n\t"
    "v_add_f32 v232, v228, v229\n\t"
    "v_cvt_pk_bf16_f32 v180, v228, v229\n\t"
    "v_mfma_f32_16x16x32_bf16 v[148:151], a[120:123], a[40:43], v[148:151]\n\t"
    "v_exp_f32 v231, v139\n\t"
    "v_mfma_f32_16x16x32_bf16 v[156:159], a[120:123], a[56:59], v[156:159]\n\t"
    "ds_read_b64_tr_b16 a[176:177], v212 offset:57344\n\t"
    "v_add_f32 v232, v232, v230\n\t"
    "v_mfma_f32_16x16x32_bf16 v[144:147], a[108:111], a[44:47], v[144:147]\n\t"
    "v_exp_f32 v228, v140\n\t"
    "v_mfma_f32_16x16x32_bf16 v[152:155], a[108:111], a[60:63], v[152:155]\n\t"
    "ds_read_b64_tr_b16 a[178:179], v212 offset:61440\n\t"
    "v_add_f32 v232, v232, v231\n\t"
    "v_cvt_pk_bf16_f32 v181, v230, v231\n\t"
    "v_mfma_f32_16x16x32_bf16 v[148:151], a[124:127], a[44:47], v[148:151]\n\t"
    "v_exp_f32 v229, v141\n\t"
    "v_mfma_f32_16x16x32_bf16 v[156:159], a[124:127], a[60:63], v[156:159]\n\t"
    "ds_read_b64_tr_b16 a[180:181], v213 offset:57344\n\t"
    "v_add_f32 v232, v232, v228\n\t"
    "v_mfma_f32_16x16x32_bf16 v[8:11], a[128:131], v[168:171], v[8:11]\n\t"
    "v_exp_f32 v230, v142\n\t"
    "v_mfma_f32_16x16x32_bf16 v[12:15], a[128:131], v[172:175], v[12:15]\n\t"
    "v_add_f32 v232, v232, v229\n\t"
    "v_cvt_pk_bf16_f32 v182, v228, v229\n\t"
    "v_mfma_f32_16x16x32_bf16 v[24:27], a[132:135], v[168:171], v[24:27]\n\t"
    "v_exp_f32 v231, v143\n\t"
    "v_mfma_f32_16x16x32_bf16 v[28:31], a[132:135], v[172:175], v[28:31]\n\t"
    "ds_read_b64_tr_b16 a[182:183], v213 offset:61440\n\t"
    "v_add_f32 v232, v232, v230\n\t"
    "v_mfma_f32_16x16x32_bf16 v[40:43], a[136:139], v[168:171], v[40:43]\n\t"
    "v_exp_f32 v228, v144\n\t"
    "v_mfma_f32_16x16x32_bf16 v[44:47], a[136:139], v[172:175], v[44:47]\n\t"
    "v_add_f32 v232, v232, v231\n\t"
    "v_cvt_pk_bf16_f32 v183, v230, v231\n\t"
    "v_mfma_f32_16x16x32_bf16 v[56:59], a[140:143], v[168:171], v[56:59]\n\t"
    "v_exp_f32 v229, v145\n\t"
    "v_mfma_f32_16x16x32_bf16 v[60:63], a[140:143], v[172:175], v[60:63]\n\t"
    "buffer_load_dwordx4 v223, s[36:39], s56 offen offset:3072 lds\n\t"
    "v_max_f32 v248, v232, v252\n\t"
    "v_mfma_f32_16x16x32_bf16 v[72:75], a[144:147], v[168:171], v[72:75]\n\t"
    "v_exp_f32 v230, v146\n\t"
    "v_mfma_f32_16x16x32_bf16 v[76:79], a[144:147], v[172:175], v[76:79]\n\t"
    "ds_read_b64_tr_b16 a[184:185], v214 offset:57344\n\t"
    "s_add_u32 s56, s56, 0x4000\n\t"
    "s_and_b32 s56, s56, 0x7ffff\n\t"
    "s_cselect_b64 s[36:37], s[36:37], s[76:77]\n\t"
    "v_cmp_lt_f32 vcc, s68, v248\n\t"
    "v_mfma_f32_16x16x32_bf16 v[88:91], a[148:151], v[168:171], v[88:91]\n\t"
    "v_exp_f32 v231, v147\n\t"
    "v_mfma_f32_16x16x32_bf16 v[92:95], a[148:151], v[172:175], v[92:95]\n\t"
    "ds_read_b64_tr_b16 a[186:187], v214 offset:61440\n\t"
    "v_cvt_pk_bf16_f32 v184, v228, v229\n\t"
    "v_mfma_f32_16x16x32_bf16 v[104:107], a[152:155], v[168:171], v[104:107]\n\t"
    "v_exp_f32 v228, v148\n\t"
    "v_mfma_f32_16x16x32_bf16 v[108:111], a[152:155], v[172:175], v[108:111]\n\t"
    "s_cbranch_vccnz L_rs14_%=\n\t"
    "L_ret14_%=:\n\t"
    "ds_read_b64_tr_b16 a[188:189], v215 offset:57344\n\t"
    "v_cvt_pk_bf16_f32 v185, v230, v231\n\t"
    "v_mfma_f32_16x16x32_bf16 v[120:123], a[156:159], v[168:171], v[120:123]\n\t"
    "v_exp_f32 v229, v149\n\t"
    "v_mfma_f32_16x16x32_bf16 v[124:127], a[156:159], v[172:175], v[124:127]\n\t"
    "ds_read_b64_tr_b16 a[190:191], v215 offset:61440\n\t"
    "v_add_f32 v236, v236, v252\n\t"
    "s_mov_b32 s66, 7\n\t"
    "L_loop_%=:\n\t"
    "s_waitcnt lgkmcnt(15)\n\t"
    "v_mfma_f32_16x16x32_bf16 v[128:131], a[64:67], a[0:3], v[192:195]\n\t"
    "v_exp_f32 v230, v150\n\t"
    "v_mfma_f32_16x16x32_bf16 v[136:139], a[64:67], a[16:19], v[196:199]\n\t"
    "ds_read_b128 a[96:99], v216 offset:8192\n\t"
    "v_cvt_pk_bf16_f32 v186, v228, v229\n\t"
    "v_mfma_f32_16x16x32_bf16 v[132:135], a[80:83], a[0:3], v[192:195]\n\t"
    "v_exp_f32 v231, v151\n\t"
    "v_mfma_f32_16x16x32_bf16 v[140:143], a[80:83], a[16:19], v[196:199]\n\t"
    "ds_read_b128 a[112:115], v216 offset:12288\n\t"
    "v_add_f32 v237, v237, v232\n\t"
    "v_mfma_f32_16x16x32_bf16 v[128:131], a[68:71], a[4:7], v[128:131]\n\t"
    "v_exp_f32 v228, v152\n\t"
    "v_mfma_f32_16x16x32_bf16 v[136:139], a[68:71], a[20:23], v[136:139]\n\t"
    "ds_read_b128 a[100:103], v217 offset:8192\n\t"
    "s_mov_b32 m0, s64\n\t"
    "v_cvt_pk_bf16_f32 v187, v230, v231\n\t"
    "v_mfma_f32_16x16x32_bf16 v[132:135], a[84:87], a[4:7], v[132:135]\n\t"
    "v_exp_f32 v229, v153\n\t"
    "v_mfma_f32_16x16x32_bf16 v[140:143], a[84:87], a[20:23], v[140:143]\n\t"
    "ds_read_b128 a[116:119], v217 offset:12288\n\t"
    "v_mfma_f32_16x16x32_bf16 v[252:255], a[252:255], v[184:187], 0\n\t"
    "buffer_load_dwordx4 v224, s[40:43], s57 offen offset:0 lds\n\t"
    "v_mfma_f32_16x16x32_bf16 v[128:131], a[72:75], a[8:11], v[128:131]\n\t"
    "v_exp_f32 v230, v154\n\t"
    "v_mfma_f32_16x16x32_bf16 v[136:139], a[72:75], a[24:27], v[136:139]\n\t"
    "v_add_f32 v233, v228, v229\n\t"
    "v_cvt_pk_bf16_f32 v188, v228, v229\n\t"
    "v_mfma_f32_16x16x32_bf16 v[132:135], a[88:91], a[8:11], v[132:135]\n\t"
    "v_exp_f32 v231, v155\n\t"
    "v_mfma_f32_16x16x32_bf16 v[140:143], a[88:91], a[24:27], v[140:143]\n\t"
    "ds_read_b128 a[104:107], v218 offset:8192\n\t"
    "v_add_f32 v233, v233, v230\n\t"
    "v_mfma_f32_16x16x32_bf16 v[128:131], a[76:79], a[12:15], v[128:131]\n\t"
    "v_exp_f32 v228, v156\n\t"
    "v_mfma_f32_16x16x32_bf16 v[136:139], a[76:79], a[28:31], v[136:139]\n\t"
    "ds_read_b128 a[120:123], v218 offset:12288\n\t"
    "v_add_f32 v233, v233, v231\n\t"
    "v_cvt_pk_bf16_f32 v189, v230, v231\n\t"
    "v_mfma_f32_16x16x32_bf16 v[132:135], a[92:95], a[12:15], v[132:135]\n\t"
    "v_exp_f32 v229, v157\n\t"
    "v_mfma_f32_16x16x32_bf16 v[140:143], a[92:95], a[28:31], v[140:143]\n\t"
    "ds_read_b128 a[108:111], v219 offset:8192\n\t"
    "v_add_f32 v233, v233, v228\n\t"
    "s_waitcnt lgkmcnt(7)\n\t"
    "v_mfma_f32_16x16x32_bf16 v[0:3], a[160:163], v[176:179], v[0:3]\n\t"
    "v_exp_f32 v230, v158\n\t"
    "v_mfma_f32_16x16x32_bf16 v[4:7], a[160:163], v[180:183], v[4:7]\n\t"
    "v_add_f32 v233, v233, v229\n\t"
    "v_cvt_pk_bf16_f32 v190, v228, v229\n\t"
    "v_mfma_f32_16x16x32_bf16 v[16:19], a[164:167], v[176:179], v[16:19]\n\t"
    "v_exp_f32 v231, v159\n\t"
    "v_mfma_f32_16x16x32_bf16 v[20:23], a[164:167], v[180:183], v[20:23]\n\t"
    "ds_read_b128 a[124:127], v219 offset:12288\n\t"
    "v_add_f32 v233, v233, v230\n\t"
    "v_mfma_f32_16x16x32_bf16 v[32:35], a[168:171], v[176:179], v[32:35]\n\t"
    "v_exp_f32 v228, v128\n\t"
    "v_mfma_f32_16x16x32_bf16 v[36:39], a[168:171], v[180:183], v[36:39]\n\t"
    "v_add_f32 v233, v233, v231\n\t"
    "v_cvt_pk_bf16_f32 v191, v230, v231\n\t"
    "v_mfma_f32_16x16x32_bf16 v[48:51], a[172:175], v[176:179], v[48:51]\n\t"
    "v_exp_f32 v229, v129\n\t"
    "v_mfma_f32_16x16x32_bf16 v[52:55], a[172:175], v[180:183], v[52:55]\n\t"
    "buffer_load_dwordx4 v225, s[40:43], s57 offen offset:1024 lds\n\t"
    "v_max_f32 v248, v233, v252\n\t"
    "v_mfma_f32_16x16x32_bf16 v[64:67], a[176:179], v[176:179], v[64:67]\n\t"
    "v_exp_f32 v230, v130\n\t"
    "v_mfma_f32_16x16x32_bf16 v[68:71], a[176:179], v[180:183], v[68:71]\n\t"
    "ds_read_b64_tr_b16 a[128:129], v208 offset:0\n\t"
    "v_cmp_lt_f32 vcc, s68, v248\n\t"
    "v_mfma_f32_16x16x32_bf16 v[80:83], a[180:183], v[176:179], v[80:83]\n\t"
    "v_exp_f32 v231, v131\n\t"
    "v_mfma_f32_16x16x32_bf16 v[84:87], a[180:183], v[180:183], v[84:87]\n\t"
    "ds_read_b64_tr_b16 a[130:131], v208 offset:4096\n\t"
    "v_cvt_pk_bf16_f32 v160, v228, v229\n\t"
    "v_mfma_f32_16x16x32_bf16 v[96:99], a[184:187], v[176:179], v[96:99]\n\t"
    "v_exp_f32 v228, v132\n\t"
    "v_mfma_f32_16x16x32_bf16 v[100:103], a[184:187], v[180:183], v[100:103]\n\t"
    "s_cbranch_vccnz L_rs15_%=\n\t"
    "L_ret15_%=:\n\t"
    "ds_read_b64_tr_b16 a[132:133], v209 offset:0\n\t"
    "v_cvt_pk_bf16_f32 v161, v230, v231\n\t"
    "v_mfma_f32_16x16x32_bf16 v[112:115], a[188:191], v[176:179], v[112:115]\n\t"
    "v_exp_f32 v229, v133\n\t"
    "v_mfma_f32_16x16x32_bf16 v[116:119], a[188:191], v[180:183], v[116:119]\n\t"
    "ds_read_b64_tr_b16 a[134:135], v209 offset:4096\n\t"
    "v_add_f32 v238, v238, v252\n\t"
    "v_mfma_f32_16x16x32_bf16 v[144:147], a[64:67], a[32:35], v[200:203]\n\t"
    "v_exp_f32 v230, v134\n\t"
    "v_mfma_f32_16x16x32_bf16 v[152:155], a[64:67], a[48:51], v[204:207]\n\t"
    "ds_read_b64_tr_b16 a[136:137], v210 offset:0\n\t"
    "v_cvt_pk_bf16_f32 v162, v228, v229\n\t"
    "v_mfma_f32_16x16x32_bf16 v[148:151], a[80:83], a[32:35], v[200:203]\n\t"
    "v_exp_f32 v231, v135\n\t"
    "v_mfma_f32_16x16x32_bf16 v[156:159], a[80:83], a[48:51], v[204:207]\n\t"
    "ds_read_b64_tr_b16 a[138:139], v210 offset:4096\n\t"
    "v_add_f32 v239, v239, v233\n\t"
    "v_mfma_f32_16x16x32_bf16 v[144:147], a[68:71], a[36:39], v[144:147]\n\t"
    "v_exp_f32 v228, v136\n\t"
    "v_mfma_f32_16x16x32_bf16 v[152:155], a[68:71], a[52:55], v[152:155]\n\t"
    "ds_read_b64_tr_b16 a[140:141], v211 offset:0\n\t"
    "v_cvt_pk_bf16_f32 v163, v230, v231\n\t"
    "v_mfma_f32_16x16x32_bf16 v[148:151], a[84:87], a[36:39], v[148:151]\n\t"
    "v_exp_f32 v229, v137\n\t"
    "v_mfma_f32_16x16x32_bf16 v[156:159], a[84:87], a[52:55], v[156:159]\n\t"
    "ds_read_b64_tr_b16 a[142:143], v211 offset:4096\n\t"
    "v_mfma_f32_16x16x32_bf16 v[252:255], a[252:255], v[160:163], 0\n\t"
    "buffer_load_dwordx4 v224, s[40:43], s57 offen offset:2048 lds\n\t"
    "v_mfma_f32_16x16x32_bf16 v[144:147], a[72:75], a[40:43], v[144:147]\n\t"
    "v_exp_f32 v230, v138\n\t"
    "v_mfma_f32_16x16x32_bf16 v[152:155], a[72:75], a[56:59], v[152:155]\n\t"
    "v_add_f32 v232, v228, v229\n\t"
    "v_cvt_pk_bf16_f32 v164, v228, v229\n\t"
    "v_mfma_f32_16x16x32_bf16 v[148:151], a[88:91], a[40:43], v[148:151]\n\t"
    "v_exp_f32 v231, v139\n\t"
    "v_mfma_f32_16x16x32_bf16 v[156:159], a[88:91], a[56:59], v[156:159]\n\t"
    "ds_read_b64_tr_b16 a[144:145], v212 offset:0\n\t"
    "v_add_f32 v232, v232, v230\n\t"
    "v_mfma_f32_16x16x32_bf16 v[144:147], a[76:79], a[44:47], v[144:147]\n\t"
    "v_exp_f32 v228, v140\n\t"
    "v_mfma_f32_16x16x32_bf16 v[152:155], a[76:79], a[60:63], v[152:155]\n\t"
    "ds_read_b64_tr_b16 a[146:147], v212 offset:4096\n\t"
    "v_add_f32 v232, v232, v231\n\t"
    "v_cvt_pk_bf16_f32 v165, v230, v231\n\t"
    "v_mfma_f32_16x16x32_bf16 v[148:151], a[92:95], a[44:47], v[148:151]\n\t"
    "v_exp_f32 v229, v141\n\t"
    "v_mfma_f32_16x16x32_bf16 v[156:159], a[92:95], a[60:63], v[156:159]\n\t"
    "ds_read_b64_tr_b16 a[148:149], v213 offset:0\n\t"
    "v_add_f32 v232, v232, v228\n\t"
    "v_mfma_f32_16x16x32_bf16 v[8:11], a[160:163], v[184:187], v[8:11]\n\t"
    "v_exp_f32 v230, v142\n\t"
    "v_mfma_f32_16x16x32_bf16 v[12:15], a[160:163], v[188:191], v[12:15]\n\t"
    "v_add_f32 v232, v232, v229\n\t"
    "v_cvt_pk_bf16_f32 v166, v228, v229\n\t"
    "v_mfma_f32_16x16x32_bf16 v[24:27], a[164:167], v[184:187], v[24:27]\n\t"
    "v_exp_f32 v231, v143\n\t"
    "v_mfma_f32_16x16x32_bf16 v[28:31], a[164:167], v[188:191], v[28:31]\n\t"
    "ds_read_b64_tr_b16 a[150:151], v213 offset:4096\n\t"
    "v_add_f32 v232, v232, v230\n\t"
    "v_mfma_f32_16x16x32_bf16 v[40:43], a[168:171], v[184:187], v[40:43]\n\t"
    "v_exp_f32 v228, v144\n\t"
    "v_mfma_f32_16x16x32_bf16 v[44:47], a[168:171], v[188:191], v[44:47]\n\t"
    "v_add_f32 v232, v232, v231\n\t"
    "v_cvt_pk_bf16_f32 v167, v230, v231\n\t"
    "v_mfma_f32_16x16x32_bf16 v[56:59], a[172:175], v[184:187], v[56:59]\n\t"
    "v_exp_f32 v229, v145\n\t"
    "v_mfma_f32_16x16x32_bf16 v[60:63], a[172:175], v[188:191], v[60:63]\n\t"
    "buffer_load_dwordx4 v225, s[40:43], s57 offen offset:3072 lds\n\t"
    "v_max_f32 v248, v232, v252\n\t"
    "v_mfma_f32_16x16x32_bf16 v[72:75], a[176:179], v[184:187], v[72:75]\n\t"
    "v_exp_f32 v230, v146\n\t"
    "v_mfma_f32_16x16x32_bf16 v[76:79], a[176:179], v[188:191], v[76:79]\n\t"
    "ds_read_b64_tr_b16 a[152:153], v214 offset:0\n\t"
    "s_add_u32 s57, s57, 0x4000\n\t"
    "s_and_b32 s57, s57, 0x7ffff\n\t"
    "s_cselect_b64 s[40:41], s[40:41], s[78:79]\n\t"
    "v_cmp_lt_f32 vcc, s68, v248\n\t"
    "v_mfma_f32_16x16x32_bf16 v[88:91], a[180:183], v[184:187], v[88:91]\n\t"
    "v_exp_f32 v231, v147\n\t"
    "v_mfma_f32_16x16x32_bf16 v[92:95], a[180:183], v[188:191], v[92:95]\n\t"
    "ds_read_b64_tr_b16 a[154:155], v214 offset:4096\n\t"
    "v_cvt_pk_bf16_f32 v168, v228, v229\n\t"
    "v_mfma_f32_16x16x32_bf16 v[104:107], a[184:187], v[184:187], v[104:107]\n\t"
    "v_exp_f32 v228, v148\n\t"
    "v_mfma_f32_16x16x32_bf16 v[108:111], a[184:187], v[188:191], v[108:111]\n\t"
    "s_cbranch_vccnz L_rs16_%=\n\t"
    "L_ret16_%=:\n\t"
    "ds_read_b64_tr_b16 a[156:157], v215 offset:0\n\t"
    "v_cvt_pk_bf16_f32 v169, v230, v231\n\t"
    "v_mfma_f32_16x16x32_bf16 v[120:123], a[188:191], v[184:187], v[120:123]\n\t"
    "v_exp_f32 v229, v149\n\t"
    "v_mfma_f32_16x16x32_bf16 v[124:127], a[188:191], v[188:191], v[124:127]\n\t"
    "ds_read_b64_tr_b16 a[158:159], v215 offset:4096\n\t"
    "v_add_f32 v236, v236, v252\n\t"
    "s_waitcnt lgkmcnt(15)\n\t"
    "v_mfma_f32_16x16x32_bf16 v[128:131], a[96:99], a[0:3], v[192:195]\n\t"
    "v_exp_f32 v230, v150\n\t"
    "v_mfma_f32_16x16x32_bf16 v[136:139], a[96:99], a[16:19], v[196:199]\n\t"
    "ds_read_b64_tr_b16 a[160:161], v208 offset:8192\n\t"
    "v_cvt_pk_bf16_f32 v170, v228, v229\n\t"
    "s_waitcnt vmcnt(8)\n\t"
    "s_barrier\n\t"
    "v_mfma_f32_16x16x32_bf16 v[132:135], a[112:115], a[0:3], v[192:195]\n\t"
    "v_exp_f32 v231, v151\n\t"
    "v_mfma_f32_16x16x32_bf16 v[140:143], a[112:115], a[16:19], v[196:199]\n\t"
    "ds_read_b128 a[64:67], v216 offset:16384\n\t"
    "v_add_f32 v237, v237, v232\n\t"
    "v_mfma_f32_16x16x32_bf16 v[128:131], a[100:103], a[4:7], v[128:131]\n\t"
    "v_exp_f32 v228, v152\n\t"
    "v_mfma_f32_16x16x32_bf16 v[136:139], a[100:103], a[20:23], v[136:139]\n\t"
    "ds_read_b128 a[80:83], v216 offset:20480\n\t"
    "s_mov_b32 m0, s61\n\t"
    "v_cvt_pk_bf16_f32 v171, v230, v231\n\t"
    "v_mfma_f32_16x16x32_bf16 v[132:135], a[116:119], a[4:7], v[132:135]\n\t"
    "v_exp_f32 v229, v153\n\t"
    "v_mfma_f32_16x16x32_bf16 v[140:143], a[116:119], a[20:23], v[140:143]\n\t"
    "ds_read_b128 a[68:71], v217 offset:16384\n\t"
    "v_mfma_f32_16x16x32_bf16 v[252:255], a[252:255], v[168:171], 0\n\t"
    "buffer_load_dwordx4 v220, s[36:39], s56 offen offset:0 lds\n\t"
    "v_mfma_f32_16x16x32_bf16 v[128:131], a[104:107], a[8:11], v[128:131]\n\t"
    "v_exp_f32 v230, v154\n\t"
    "v_mfma_f32_16x16x32_bf16 v[136:139], a[104:107], a[24:27], v[136:139]\n\t"
    "v_add_f32 v233, v228, v229\n\t"
    "v_cvt_pk_bf16_f32 v172, v228, v229\n\t"
    "v_mfma_f32_16x16x32_bf16 v[132:135], a[120:123], a[8:11], v[132:135]\n\t"
    "v_exp_f32 v231, v155\n\t"
    "v_mfma_f32_16x16x32_bf16 v[140:143], a[120:123], a[24:27], v[140:143]\n\t"
    "ds_read_b128 a[84:87], v217 offset:20480\n\t"
    "v_add_f32 v233, v233, v230\n\t"
    "v_mfma_f32_16x16x32_bf16 v[128:131], a[108:111], a[12:15], v[128:131]\n\t"
    "v_exp_f32 v228, v156\n\t"
    "v_mfma_f32_16x16x32_bf16 v[136:139], a[108:111], a[28:31], v[136:139]\n\t"
    "ds_read_b128 a[72:75], v218 offset:16384\n\t"
    "v_add_f32 v233, v233, v231\n\t"
    "v_cvt_pk_bf16_f32 v173, v230, v231\n\t"
    "v_mfma_f32_16x16x32_bf16 v[132:135], a[124:127], a[12:15], v[132:135]\n\t"
    "v_exp_f32 v229, v157\n\t"
    "v_mfma_f32_16x16x32_bf16 v[140:143], a[124:127], a[28:31], v[140:143]\n\t"
    "ds_read_b128 a[88:91], v218 offset:20480\n\t"
    "v_add_f32 v233, v233, v228\n\t"
    "s_waitcnt lgkmcnt(7)\n\t"
    "v_mfma_f32_16x16x32_bf16 v[0:3], a[128:131], v[160:163], v[0:3]\n\t"
    "v_exp_f32 v230, v158\n\t"
    "v_mfma_f32_16x16x32_bf16 v[4:7], a[128:131], v[164:167], v[4:7]\n\t"
    "v_add_f32 v233, v233, v229\n\t"
    "v_cvt_pk_bf16_f32 v174, v228, v229\n\t"
    "v_mfma_f32_16x16x32_bf16 v[16:19], a[132:135], v[160:163], v[16:19]\n\t"
    "v_exp_f32 v231, v159\n\t"
    "v_mfma_f32_16x16x32_bf16 v[20:23], a[132:135], v[164:167], v[20:23]\n\t"
    "ds_read_b128 a[76:79], v219 offset:16384\n\t"
    "v_add_f32 v233, v233, v230\n\t"
    "v_mfma_f32_16x16x32_bf16 v[32:35], a[136:139], v[160:163], v[32:35]\n\t"
    "v_exp_f32 v228, v128\n\t"
    "v_mfma_f32_16x16x32_bf16 v[36:39], a[136:139], v[164:167], v[36:39]\n\t"
    "v_add_f32 v233, v233, v231\n\t"
    "v_cvt_pk_bf16_f32 v175, v230, v231\n\t"
    "v_mfma_f32_16x16x32_bf16 v[48:51], a[140:143], v[160:163], v[48:51]\n\t"
    "v_exp_f32 v229, v129\n\t"
    "v_mfma_f32_16x16x32_bf16 v[52:55], a[140:143], v[164:167], v[52:55]\n\t"
    "buffer_load_dwordx4 v221, s[36:39], s56 offen offset:1024 lds\n\t"
    "v_max_f32 v248, v233, v252\n\t"
    "v_mfma_f32_16x16x32_bf16 v[64:67], a[144:147], v[160:163], v[64:67]\n\t"
    "v_exp_f32 v230, v130\n\t"
    "v_mfma_f32_16x16x32_bf16 v[68:71], a[144:147], v[164:167], v[68:71]\n\t"
    "ds_read_b128 a[92:95], v219 offset:20480\n\t"
    "v_cmp_lt_f32 vcc, s68, v248\n\t"
    "v_mfma_f32_16x16x32_bf16 v[80:83], a[148:151], v[160:163], v[80:83]\n\t"
    "v_exp_f32 v231, v131\n\t"
    "v_mfma_f32_16x16x32_bf16 v[84:87], a[148:151], v[164:167], v[84:87]\n\t"
    "ds_read_b64_tr_b16 a[162:163], v208 offset:12288\n\t"
    "v_cvt_pk_bf16_f32 v176, v228, v229\n\t"
    "v_mfma_f32_16x16x32_bf16 v[96:99], a[152:155], v[160:163], v[96:99]\n\t"
    "v_exp_f32 v228, v132\n\t"
    "v_mfma_f32_16x16x32_bf16 v[100:103], a[152:155], v[164:167], v[100:103]\n\t"
    "s_cbranch_vccnz L_rs17_%=\n\t"
    "L_ret17_%=:\n\t"
    "ds_read_b64_tr_b16 a[164:165], v209 offset:8192\n\t"
    "v_cvt_pk_bf16_f32 v177, v230, v231\n\t"
    "v_mfma_f32_16x16x32_bf16 v[112:115], a[156:159], v[160:163], v[112:115]\n\t"
    "v_exp_f32 v229, v133\n\t"
    "v_mfma_f32_16x16x32_bf16 v[116:119], a[156:159], v[164:167], v[116:119]\n\t"
    "ds_read_b64_tr_b16 a[166:167], v209 offset:12288\n\t"
    "v_add_f32 v238, v238, v252\n\t"
    "v_mfma_f32_16x16x32_bf16 v[144:147], a[96:99], a[32:35], v[200:203]\n\t"
    "v_exp_f32 v230, v134\n\t"
    "v_mfma_f32_16x16x32_bf16 v[152:155], a[96:99], a[48:51], v[204:207]\n\t"
    "ds_read_b64_tr_b16 a[168:169], v210 offset:8192\n\t"
    "v_cvt_pk_bf16_f32 v178, v228, v229\n\t"
    "v_mfma_f32_16x16x32_bf16 v[148:151], a[112:115], a[32:35], v[200:203]\n\t"
    "v_exp_f32 v231, v135\n\t"
    "v_mfma_f32_16x16x32_bf16 v[156:159], a[112:115], a[48:51], v[204:207]\n\t"
    "ds_read_b64_tr_b16 a[170:171], v210 offset:12288\n\t"
    "v_add_f32 v239, v239, v233\n\t"
    "v_mfma_f32_16x16x32_bf16 v[144:147], a[100:103], a[36:39], v[144:147]\n\t"
    "v_exp_f32 v228, v136\n\t"
    "v_mfma_f32_16x16x32_bf16 v[152:155], a[100:103], a[52:55], v[152:155]\n\t"
    "ds_read_b64_tr_b16 a[172:173], v211 offset:8192\n\t"
    "v_cvt_pk_bf16_f32 v179, v230, v231\n\t"
    "v_mfma_f32_16x16x32_bf16 v[148:151], a[116:119], a[36:39], v[148:151]\n\t"
    "v_exp_f32 v229, v137\n\t"
    "v_mfma_f32_16x16x32_bf16 v[156:159], a[116:119], a[52:55], v[156:159]\n\t"
    "ds_read_b64_tr_b16 a[174:175], v211 offset:12288\n\t"
    "v_mfma_f32_16x16x32_bf16 v[252:255], a[252:255], v[176:179], 0\n\t"
    "buffer_load_dwordx4 v222, s[36:39], s56 offen offset:2048 lds\n\t"
    "v_mfma_f32_16x16x32_bf16 v[144:147], a[104:107], a[40:43], v[144:147]\n\t"
    "v_exp_f32 v230, v138\n\t"
    "v_mfma_f32_16x16x32_bf16 v[152:155], a[104:107], a[56:59], v[152:155]\n\t"
    "v_add_f32 v232, v228, v229\n\t"
    "v_cvt_pk_bf16_f32 v180, v228, v229\n\t"
    "v_mfma_f32_16x16x32_bf16 v[148:151], a[120:123], a[40:43], v[148:151]\n\t"
    "v_exp_f32 v231, v139\n\t"
    "v_mfma_f32_16x16x32_bf16 v[156:159], a[120:123], a[56:59], v[156:159]\n\t"
    "ds_read_b64_tr_b16 a[176:177], v212 offset:8192\n\t"
    "v_add_f32 v232, v232, v230\n\t"
    "v_mfma_f32_16x16x32_bf16 v[144:147], a[108:111], a[44:47], v[144:147]\n\t"
    "v_exp_f32 v228, v140\n\t"
    "v_mfma_f32_16x16x32_bf16 v[152:155], a[108:111], a[60:63], v[152:155]\n\t"
    "ds_read_b64_tr_b16 a[178:179], v212 offset:12288\n\t"
    "v_add_f32 v232, v232, v231\n\t"
    "v_cvt_pk_bf16_f32 v181, v230, v231\n\t"
    "v_mfma_f32_16x16x32_bf16 v[148:151], a[124:127], a[44:47], v[148:151]\n\t"
    "v_exp_f32 v229, v141\n\t"
    "v_mfma_f32_16x16x32_bf16 v[156:159], a[124:127], a[60:63], v[156:159]\n\t"
    "ds_read_b64_tr_b16 a[180:181], v213 offset:8192\n\t"
    "v_add_f32 v232, v232, v228\n\t"
    "v_mfma_f32_16x16x32_bf16 v[8:11], a[128:131], v[168:171], v[8:11]\n\t"
    "v_exp_f32 v230, v142\n\t"
    "v_mfma_f32_16x16x32_bf16 v[12:15], a[128:131], v[172:175], v[12:15]\n\t"
    "v_add_f32 v232, v232, v229\n\t"
    "v_cvt_pk_bf16_f32 v182, v228, v229\n\t"
    "v_mfma_f32_16x16x32_bf16 v[24:27], a[132:135], v[168:171], v[24:27]\n\t"
    "v_exp_f32 v231, v143\n\t"
    "v_mfma_f32_16x16x32_bf16 v[28:31], a[132:135], v[172:175], v[28:31]\n\t"
    "ds_read_b64_tr_b16 a[182:183], v213 offset:12288\n\t"
    "v_add_f32 v232, v232, v230\n\t"
    "v_mfma_f32_16x16x32_bf16 v[40:43], a[136:139], v[168:171], v[40:43]\n\t"
    "v_exp_f32 v228, v144\n\t"
    "v_mfma_f32_16x16x32_bf16 v[44:47], a[136:139], v[172:175], v[44:47]\n\t"
    "v_add_f32 v232, v232, v231\n\t"
    "v_cvt_pk_bf16_f32 v183, v230, v231\n\t"
    "v_mfma_f32_16x16x32_bf16 v[56:59], a[140:143], v[168:171], v[56:59]\n\t"
    "v_exp_f32 v229, v145\n\t"
    "v_mfma_f32_16x16x32_bf16 v[60:63], a[140:143], v[172:175], v[60:63]\n\t"
    "buffer_load_dwordx4 v223, s[36:39], s56 offen offset:3072 lds\n\t"
    "v_max_f32 v248, v232, v252\n\t"
    "v_mfma_f32_16x16x32_bf16 v[72:75], a[144:147], v[168:171], v[72:75]\n\t"
    "v_exp_f32 v230, v146\n\t"
    "v_mfma_f32_16x16x32_bf16 v[76:79], a[144:147], v[172:175], v[76:79]\n\t"
    "ds_read_b64_tr_b16 a[184:185], v214 offset:8192\n\t"
    "s_add_u32 s56, s56, 0x4000\n\t"
    "s_and_b32 s56, s56, 0x7ffff\n\t"
    "s_cselect_b64 s[36:37], s[36:37], s[76:77]\n\t"
    "v_cmp_lt_f32 vcc, s68, v248\n\t"
    "v_mfma_f32_16x16x32_bf16 v[88:91], a[148:151], v[168:171], v[88:91]\n\t"
    "v_exp_f32 v231, v147\n\t"
    "v_mfma_f32_16x16x32_bf16 v[92:95], a[148:151], v[172:175], v[92:95]\n\t"
    "ds_read_b64_tr_b16 a[186:187], v214 offset:12288\n\t"
    "v_cvt_pk_bf16_f32 v184, v228, v229\n\t"
    "v_mfma_f32_16x16x32_bf16 v[104:107], a[152:155], v[168:171], v[104:107]\n\t"
    "v_exp_f32 v228, v148\n\t"
    "v_mfma_f32_16x16x32_bf16 v[108:111], a[152:155], v[172:175], v[108:111]\n\t"
    "s_cbranch_vccnz L_rs18_%=\n\t"
    "L_ret18_%=:\n\t"
    "ds_read_b64_tr_b16 a[188:189], v215 offset:8192\n\t"
    "v_cvt_pk_bf16_f32 v185, v230, v231\n\t"
    "v_mfma_f32_16x16x32_bf16 v[120:123], a[156:159], v[168:171], v[120:123]\n\t"
    "v_exp_f32 v229, v149\n\t"
    "v_mfma_f32_16x16x32_bf16 v[124:127], a[156:159], v[172:175], v[124:127]\n\t"
    "ds_read_b64_tr_b16 a[190:191], v215 offset:12288\n\t"
    "v_add_f32 v236, v236, v252\n\t"
    "s_waitcnt lgkmcnt(15)\n\t"
    "v_mfma_f32_16x16x32_bf16 v[128:131], a[64:67], a[0:3], v[192:195]\n\t"
    "v_exp_f32 v230, v150\n\t"
    "v_mfma_f32_16x16x32_bf16 v[136:139], a[64:67], a[16:19], v[196:199]\n\t"
    "ds_read_b128 a[96:99], v216 offset:24576\n\t"
    "v_cvt_pk_bf16_f32 v186, v228, v229\n\t"
    "v_mfma_f32_16x16x32_bf16 v[132:135], a[80:83], a[0:3], v[192:195]\n\t"
    "v_exp_f32 v231, v151\n\t"
    "v_mfma_f32_16x16x32_bf16 v[140:143], a[80:83], a[16:19], v[196:199]\n\t"
    "ds_read_b128 a[112:115], v216 offset:28672\n\t"
    "v_add_f32 v237, v237, v232\n\t"
    "v_mfma_f32_16x16x32_bf16 v[128:131], a[68:71], a[4:7], v[128:131]\n\t"
    "v_exp_f32 v228, v152\n\t"
    "v_mfma_f32_16x16x32_bf16 v[136:139], a[68:71], a[20:23], v[136:139]\n\t"
    "ds_read_b128 a[100:103], v217 offset:24576\n\t"
    "s_mov_b32 m0, s65\n\t"
    "v_cvt_pk_bf16_f32 v187, v230, v231\n\t"
    "v_mfma_f32_16x16x32_bf16 v[132:135], a[84:87], a[4:7], v[132:135]\n\t"
    "v_exp_f32 v229, v153\n\t"
    "v_mfma_f32_16x16x32_bf16 v[140:143], a[84:87], a[20:23], v[140:143]\n\t"
    "ds_read_b128 a[116:119], v217 offset:28672\n\t"
    "v_mfma_f32_16x16x32_bf16 v[252:255], a[252:255], v[184:187], 0\n\t"
    "buffer_load_dwordx4 v224, s[40:43], s57 offen offset:0 lds\n\t"
    "v_mfma_f32_16x16x32_bf16 v[128:131], a[72:75], a[8:11], v[128:131]\n\t"
    "v_exp_f32 v230, v154\n\t"
    "v_mfma_f32_16x16x32_bf16 v[136:139], a[72:75], a[24:27], v[136:139]\n\t"
    "v_add_f32 v233, v228, v229\n\t"
    "v_cvt_pk_bf16_f32 v188, v228, v229\n\t"
    "v_mfma_f32_16x16x32_bf16 v[132:135], a[88:91], a[8:11], v[132:135]\n\t"
    "v_exp_f32 v231, v155\n\t"
    "v_mfma_f32_16x16x32_bf16 v[140:143], a[88:91], a[24:27], v[140:143]\n\t"
    "ds_read_b128 a[104:107], v218 offset:24576\n\t"
    "v_add_f32 v233, v233, v230\n\t"
    "v_mfma_f32_16x16x32_bf16 v[128:131], a[76:79], a[12:15], v[128:131]\n\t"
    "v_exp_f32 v228, v156\n\t"
    "v_mfma_f32_16x16x32_bf16 v[136:139], a[76:79], a[28:31], v[136:139]\n\t"
    "ds_read_b128 a[120:123], v218 offset:28672\n\t"
    "v_add_f32 v233, v233, v231\n\t"
    "v_cvt_pk_bf16_f32 v189, v230, v231\n\t"
    "v_mfma_f32_16x16x32_bf16 v[132:135], a[92:95], a[12:15], v[132:135]\n\t"
    "v_exp_f32 v229, v157\n\t"
    "v_mfma_f32_16x16x32_bf16 v[140:143], a[92:95], a[28:31], v[140:143]\n\t"
    "ds_read_b128 a[108:111], v219 offset:24576\n\t"
    "v_add_f32 v233, v233, v228\n\t"
    "s_waitcnt lgkmcnt(7)\n\t"
    "v_mfma_f32_16x16x32_bf16 v[0:3], a[160:163], v[176:179], v[0:3]\n\t"
    "v_exp_f32 v230, v158\n\t"
    "v_mfma_f32_16x16x32_bf16 v[4:7], a[160:163], v[180:183], v[4:7]\n\t"
    "v_add_f32 v233, v233, v229\n\t"
    "v_cvt_pk_bf16_f32 v190, v228, v229\n\t"
    "v_mfma_f32_16x16x32_bf16 v[16:19], a[164:167], v[176:179], v[16:19]\n\t"
    "v_exp_f32 v231, v159\n\t"
    "v_mfma_f32_16x16x32_bf16 v[20:23], a[164:167], v[180:183], v[20:23]\n\t"
    "ds_read_b128 a[124:127], v219 offset:28672\n\t"
    "v_add_f32 v233, v233, v230\n\t"
    "v_mfma_f32_16x16x32_bf16 v[32:35], a[168:171], v[176:179], v[32:35]\n\t"
    "v_exp_f32 v228, v128\n\t"
    "v_mfma_f32_16x16x32_bf16 v[36:39], a[168:171], v[180:183], v[36:39]\n\t"
    "v_add_f32 v233, v233, v231\n\t"
    "v_cvt_pk_bf16_f32 v191, v230, v231\n\t"
    "v_mfma_f32_16x16x32_bf16 v[48:51], a[172:175], v[176:179], v[48:51]\n\t"
    "v_exp_f32 v229, v129\n\t"
    "v_mfma_f32_16x16x32_bf16 v[52:55], a[172:175], v[180:183], v[52:55]\n\t"
    "buffer_load_dwordx4 v225, s[40:43], s57 offen offset:1024 lds\n\t"
    "v_max_f32 v248, v233, v252\n\t"
    "v_mfma_f32_16x16x32_bf16 v[64:67], a[176:179], v[176:179], v[64:67]\n\t"
    "v_exp_f32 v230, v130\n\t"
    "v_mfma_f32_16x16x32_bf16 v[68:71], a[176:179], v[180:183], v[68:71]\n\t"
    "ds_read_b64_tr_b16 a[128:129], v208 offset:16384\n\t"
    "v_cmp_lt_f32 vcc, s68, v248\n\t"
    "v_mfma_f32_16x16x32_bf16 v[80:83], a[180:183], v[176:179], v[80:83]\n\t"
    "v_exp_f32 v231, v131\n\t"
    "v_mfma_f32_16x16x32_bf16 v[84:87], a[180:183], v[180:183], v[84:87]\n\t"
    "ds_read_b64_tr_b16 a[130:131], v208 offset:20480\n\t"
    "v_cvt_pk_bf16_f32 v160, v228, v229\n\t"
    "v_mfma_f32_16x16x32_bf16 v[96:99], a[184:187], v[176:179], v[96:99]\n\t"
    "v_exp_f32 v228, v132\n\t"
    "v_mfma_f32_16x16x32_bf16 v[100:103], a[184:187], v[180:183], v[100:103]\n\t"
    "s_cbranch_vccnz L_rs19_%=\n\t"
    "L_ret19_%=:\n\t"
    "ds_read_b64_tr_b16 a[132:133], v209 offset:16384\n\t"
    "v_cvt_pk_bf16_f32 v161, v230, v231\n\t"
    "v_mfma_f32_16x16x32_bf16 v[112:115], a[188:191], v[176:179], v[112:115]\n\t"
    "v_exp_f32 v229, v133\n\t"
    "v_mfma_f32_16x16x32_bf16 v[116:119], a[188:191], v[180:183], v[116:119]\n\t"
    "ds_read_b64_tr_b16 a[134:135], v209 offset:20480\n\t"
    "v_add_f32 v238, v238, v252\n\t"
    "v_mfma_f32_16x16x32_bf16 v[144:147], a[64:67], a[32:35], v[200:203]\n\t"
    "v_exp_f32 v230, v134\n\t"
    "v_mfma_f32_16x16x32_bf16 v[152:155], a[64:67], a[48:51], v[204:207]\n\t"
    "ds_read_b64_tr_b16 a[136:137], v210 offset:16384\n\t"
    "v_cvt_pk_bf16_f32 v162, v228, v229\n\t"
    "v_mfma_f32_16x16x32_bf16 v[148:151], a[80:83], a[32:35], v[200:203]\n\t"
    "v_exp_f32 v231, v135\n\t"
    "v_mfma_f32_16x16x32_bf16 v[156:159], a[80:83], a[48:51], v[204:207]\n\t"
    "ds_read_b64_tr_b16 a[138:139], v210 offset:20480\n\t"
    "v_add_f32 v239, v239, v233\n\t"
    "v_mfma_f32_16x16x32_bf16 v[144:147], a[68:71], a[36:39], v[144:147]\n\t"
    "v_exp_f32 v228, v136\n\t"
    "v_mfma_f32_16x16x32_bf16 v[152:155], a[68:71], a[52:55], v[152:155]\n\t"
    "ds_read_b64_tr_b16 a[140:141], v211 offset:16384\n\t"
    "v_cvt_pk_bf16_f32 v163, v230, v231\n\t"
    "v_mfma_f32_16x16x32_bf16 v[148:151], a[84:87], a[36:39], v[148:151]\n\t"
    "v_exp_f32 v229, v137\n\t"
    "v_mfma_f32_16x16x32_bf16 v[156:159], a[84:87], a[52:55], v[156:159]\n\t"
    "ds_read_b64_tr_b16 a[142:143], v211 offset:20480\n\t"
    "v_mfma_f32_16x16x32_bf16 v[252:255], a[252:255], v[160:163], 0\n\t"
    "buffer_load_dwordx4 v224, s[40:43], s57 offen offset:2048 lds\n\t"
    "v_mfma_f32_16x16x32_bf16 v[144:147], a[72:75], a[40:43], v[144:147]\n\t"
    "v_exp_f32 v230, v138\n\t"
    "v_mfma_f32_16x16x32_bf16 v[152:155], a[72:75], a[56:59], v[152:155]\n\t"
    "v_add_f32 v232, v228, v229\n\t"
    "v_cvt_pk_bf16_f32 v164, v228, v229\n\t"
    "v_mfma_f32_16x16x32_bf16 v[148:151], a[88:91], a[40:43], v[148:151]\n\t"
    "v_exp_f32 v231, v139\n\t"
    "v_mfma_f32_16x16x32_bf16 v[156:159], a[88:91], a[56:59], v[156:159]\n\t"
    "ds_read_b64_tr_b16 a[144:145], v212 offset:16384\n\t"
    "v_add_f32 v232, v232, v230\n\t"
    "v_mfma_f32_16x16x32_bf16 v[144:147], a[76:79], a[44:47], v[144:147]\n\t"
    "v_exp_f32 v228, v140\n\t"
    "v_mfma_f32_16x16x32_bf16 v[152:155], a[76:79], a[60:63], v[152:155]\n\t"
    "ds_read_b64_tr_b16 a[146:147], v212 offset:20480\n\t"
    "v_add_f32 v232, v232, v231\n\t"
    "v_cvt_pk_bf16_f32 v165, v230, v231\n\t"
    "v_mfma_f32_16x16x32_bf16 v[148:151], a[92:95], a[44:47], v[148:151]\n\t"
    "v_exp_f32 v229, v141\n\t"
    "v_mfma_f32_16x16x32_bf16 v[156:159], a[92:95], a[60:63], v[156:159]\n\t"
    "ds_read_b64_tr_b16 a[148:149], v213 offset:16384\n\t"
    "v_add_f32 v232, v232, v228\n\t"
    "v_mfma_f32_16x16x32_bf16 v[8:11], a[160:163], v[184:187], v[8:11]\n\t"
    "v_exp_f32 v230, v142\n\t"
    "v_mfma_f32_16x16x32_bf16 v[12:15], a[160:163], v[188:191], v[12:15]\n\t"
    "v_add_f32 v232, v232, v229\n\t"
    "v_cvt_pk_bf16_f32 v166, v228, v229\n\t"
    "v_mfma_f32_16x16x32_bf16 v[24:27], a[164:167], v[184:187], v[24:27]\n\t"
    "v_exp_f32 v231, v143\n\t"
    "v_mfma_f32_16x16x32_bf16 v[28:31], a[164:167], v[188:191], v[28:31]\n\t"
    "ds_read_b64_tr_b16 a[150:151], v213 offset:20480\n\t"
    "v_add_f32 v232, v232, v230\n\t"
    "v_mfma_f32_16x16x32_bf16 v[40:43], a[168:171], v[184:187], v[40:43]\n\t"
    "v_exp_f32 v228, v144\n\t"
    "v_mfma_f32_16x16x32_bf16 v[44:47], a[168:171], v[188:191], v[44:47]\n\t"
    "v_add_f32 v232, v232, v231\n\t"
    "v_cvt_pk_bf16_f32 v167, v230, v231\n\t"
    "v_mfma_f32_16x16x32_bf16 v[56:59], a[172:175], v[184:187], v[56:59]\n\t"
    "v_exp_f32 v229, v145\n\t"
    "v_mfma_f32_16x16x32_bf16 v[60:63], a[172:175], v[188:191], v[60:63]\n\t"
    "buffer_load_dwordx4 v225, s[40:43], s57 offen offset:3072 lds\n\t"
    "v_max_f32 v248, v232, v252\n\t"
    "v_mfma_f32_16x16x32_bf16 v[72:75], a[176:179], v[184:187], v[72:75]\n\t"
    "v_exp_f32 v230, v146\n\t"
    "v_mfma_f32_16x16x32_bf16 v[76:79], a[176:179], v[188:191], v[76:79]\n\t"
    "ds_read_b64_tr_b16 a[152:153], v214 offset:16384\n\t"
    "s_add_u32 s57, s57, 0x4000\n\t"
    "s_and_b32 s57, s57, 0x7ffff\n\t"
    "s_cselect_b64 s[40:41], s[40:41], s[78:79]\n\t"
    "v_cmp_lt_f32 vcc, s68, v248\n\t"
    "v_mfma_f32_16x16x32_bf16 v[88:91], a[180:183], v[184:187], v[88:91]\n\t"
    "v_exp_f32 v231, v147\n\t"
    "v_mfma_f32_16x16x32_bf16 v[92:95], a[180:183], v[188:191], v[92:95]\n\t"
    "ds_read_b64_tr_b16 a[154:155], v214 offset:20480\n\t"
    "v_cvt_pk_bf16_f32 v168, v228, v229\n\t"
    "v_mfma_f32_16x16x32_bf16 v[104:107], a[184:187], v[184:187], v[104:107]\n\t"
    "v_exp_f32 v228, v148\n\t"
    "v_mfma_f32_16x16x32_bf16 v[108:111], a[184:187], v[188:191], v[108:111]\n\t"
    "s_cbranch_vccnz L_rs20_%=\n\t"
    "L_ret20_%=:\n\t"
    "ds_read_b64_tr_b16 a[156:157], v215 offset:16384\n\t"
    "v_cvt_pk_bf16_f32 v169, v230, v231\n\t"
    "v_mfma_f32_16x16x32_bf16 v[120:123], a[188:191], v[184:187], v[120:123]\n\t"
    "v_exp_f32 v229, v149\n\t"
    "v_mfma_f32_16x16x32_bf16 v[124:127], a[188:191], v[188:191], v[124:127]\n\t"
    "ds_read_b64_tr_b16 a[158:159], v215 offset:20480\n\t"
    "v_add_f32 v236, v236, v252\n\t"
    "s_waitcnt lgkmcnt(15)\n\t"
    "v_mfma_f32_16x16x32_bf16 v[128:131], a[96:99], a[0:3], v[192:195]\n\t"
    "v_exp_f32 v230, v150\n\t"
    "v_mfma_f32_16x16x32_bf16 v[136:139], a[96:99], a[16:19], v[196:199]\n\t"
    "ds_read_b64_tr_b16 a[160:161], v208 offset:24576\n\t"
    "v_cvt_pk_bf16_f32 v170, v228, v229\n\t"
    "s_waitcnt vmcnt(8)\n\t"
    "s_barrier\n\t"
    "v_mfma_f32_16x16x32_bf16 v[132:135], a[112:115], a[0:3], v[192:195]\n\t"
    "v_exp_f32 v231, v151\n\t"
    "v_mfma_f32_16x16x32_bf16 v[140:143], a[112:115], a[16:19], v[196:199]\n\t"
    "ds_read_b128 a[64:67], v216 offset:32768\n\t"
    "v_add_f32 v237, v237, v232\n\t"
    "v_mfma_f32_16x16x32_bf16 v[128:131], a[100:103], a[4:7], v[128:131]\n\t"
    "v_exp_f32 v228, v152\n\t"
    "v_mfma_f32_16x16x32_bf16 v[136:139], a[100:103], a[20:23], v[136:139]\n\t"
    "ds_read_b128 a[80:83], v216 offset:36864\n\t"
    "s_mov_b32 m0, s58\n\t"
    "v_cvt_pk_bf16_f32 v171, v230, v231\n\t"
    "v_mfma_f32_16x16x32_bf16 v[132:135], a[116:119], a[4:7], v[132:135]\n\t"
    "v_exp_f32 v229, v153\n\t"
    "v_mfma_f32_16x16x32_bf16 v[140:143], a[116:119], a[20:23], v[140:143]\n\t"
    "ds_read_b128 a[68:71], v217 offset:32768\n\t"
    "v_mfma_f32_16x16x32_bf16 v[252:255], a[252:255], v[168:171], 0\n\t"
    "buffer_load_dwordx4 v220, s[36:39], s56 offen offset:0 lds\n\t"
    "v_mfma_f32_16x16x32_bf16 v[128:131], a[104:107], a[8:11], v[128:131]\n\t"
    "v_exp_f32 v230, v154\n\t"
    "v_mfma_f32_16x16x32_bf16 v[136:139], a[104:107], a[24:27], v[136:139]\n\t"
    "v_add_f32 v233, v228, v229\n\t"
    "v_cvt_pk_bf16_f32 v172, v228, v229\n\t"
    "v_mfma_f32_16x16x32_bf16 v[132:135], a[120:123], a[8:11], v[132:135]\n\t"
    "v_exp_f32 v231, v155\n\t"
    "v_mfma_f32_16x16x32_bf16 v[140:143], a[120:123], a[24:27], v[140:143]\n\t"
    "ds_read_b128 a[84:87], v217 offset:36864\n\t"
    "v_add_f32 v233, v233, v230\n\t"
    "v_mfma_f32_16x16x32_bf16 v[128:131], a[108:111], a[12:15], v[128:131]\n\t"
    "v_exp_f32 v228, v156\n\t"
    "v_mfma_f32_16x16x32_bf16 v[136:139], a[108:111], a[28:31], v[136:139]\n\t"
    "ds_read_b128 a[72:75], v218 offset:32768\n\t"
    "v_add_f32 v233, v233, v231\n\t"
    "v_cvt_pk_bf16_f32 v173, v230, v231\n\t"
    "v_mfma_f32_16x16x32_bf16 v[132:135], a[124:127], a[12:15], v[132:135]\n\t"
    "v_exp_f32 v229, v157\n\t"
    "v_mfma_f32_16x16x32_bf16 v[140:143], a[124:127], a[28:31], v[140:143]\n\t"
    "ds_read_b128 a[88:91], v218 offset:36864\n\t"
    "v_add_f32 v233, v233, v228\n\t"
    "s_waitcnt lgkmcnt(7)\n\t"
    "v_mfma_f32_16x16x32_bf16 v[0:3], a[128:131], v[160:163], v[0:3]\n\t"
    "v_exp_f32 v230, v158\n\t"
    "v_mfma_f32_16x16x32_bf16 v[4:7], a[128:131], v[164:167], v[4:7]\n\t"
    "v_add_f32 v233, v233, v229\n\t"
    "v_cvt_pk_bf16_f32 v174, v228, v229\n\t"
    "v_mfma_f32_16x16x32_bf16 v[16:19], a[132:135], v[160:163], v[16:19]\n\t"
    "v_exp_f32 v231, v159\n\t"
    "v_mfma_f32_16x16x32_bf16 v[20:23], a[132:135], v[164:167], v[20:23]\n\t"
    "ds_read_b128 a[76:79], v219 offset:32768\n\t"
    "v_add_f32 v233, v233, v230\n\t"
    "v_mfma_f32_16x16x32_bf16 v[32:35], a[136:139], v[160:163], v[32:35]\n\t"
    "v_exp_f32 v228, v128\n\t"
    "v_mfma_f32_16x16x32_bf16 v[36:39], a[136:139], v[164:167], v[36:39]\n\t"
    "v_add_f32 v233, v233, v231\n\t"
    "v_cvt_pk_bf16_f32 v175, v230, v231\n\t"
    "v_mfma_f32_16x16x32_bf16 v[48:51], a[140:143], v[160:163], v[48:51]\n\t"
    "v_exp_f32 v229, v129\n\t"
    "v_mfma_f32_16x16x32_bf16 v[52:55], a[140:143], v[164:167], v[52:55]\n\t"
    "buffer_load_dwordx4 v221, s[36:39], s56 offen offset:1024 lds\n\t"
    "v_max_f32 v248, v233, v252\n\t"
    "v_mfma_f32_16x16x32_bf16 v[64:67], a[144:147], v[160:163], v[64:67]\n\t"
    "v_exp_f32 v230, v130\n\t"
    "v_mfma_f32_16x16x32_bf16 v[68:71], a[144:147], v[164:167], v[68:71]\n\t"
    "ds_read_b128 a[92:95], v219 offset:36864\n\t"
    "v_cmp_lt_f32 vcc, s68, v248\n\t"
    "v_mfma_f32_16x16x32_bf16 v[80:83], a[148:151], v[160:163], v[80:83]\n\t"
    "v_exp_f32 v231, v131\n\t"
    "v_mfma_f32_16x16x32_bf16 v[84:87], a[148:151], v[164:167], v[84:87]\n\t"
    "ds_read_b64_tr_b16 a[162:163], v208 offset:28672\n\t"
    "v_cvt_pk_bf16_f32 v176, v228, v229\n\t"
    "v_mfma_f32_16x16x32_bf16 v[96:99], a[152:155], v[160:163], v[96:99]\n\t"
    "v_exp_f32 v228, v132\n\t"
    "v_mfma_f32_16x16x32_bf16 v[100:103], a[152:155], v[164:167], v[100:103]\n\t"
    "s_cbranch_vccnz L_rs21_%=\n\t"
    "L_ret21_%=:\n\t"
    "ds_read_b64_tr_b16 a[164:165], v209 offset:24576\n\t"
    "v_cvt_pk_bf16_f32 v177, v230, v231\n\t"
    "v_mfma_f32_16x16x32_bf16 v[112:115], a[156:159], v[160:163], v[112:115]\n\t"
    "v_exp_f32 v229, v133\n\t"
    "v_mfma_f32_16x16x32_bf16 v[116:119], a[156:159], v[164:167], v[116:119]\n\t"
    "ds_read_b64_tr_b16 a[166:167], v209 offset:28672\n\t"
    "v_add_f32 v238, v238, v252\n\t"
    "v_mfma_f32_16x16x32_bf16 v[144:147], a[96:99], a[32:35], v[200:203]\n\t"
    "v_exp_f32 v230, v134\n\t"
    "v_mfma_f32_16x16x32_bf16 v[152:155], a[96:99], a[48:51], v[204:207]\n\t"
    "ds_read_b64_tr_b16 a[168:169], v210 offset:24576\n\t"
    "v_cvt_pk_bf16_f32 v178, v228, v229\n\t"
    "v_mfma_f32_16x16x32_bf16 v[148:151], a[112:115], a[32:35], v[200:203]\n\t"
    "v_exp_f32 v231, v135\n\t"
    "v_mfma_f32_16x16x32_bf16 v[156:159], a[112:115], a[48:51], v[204:207]\n\t"
    "ds_read_b64_tr_b16 a[170:171], v210 offset:28672\n\t"
    "v_add_f32 v239, v239, v233\n\t"
    "v_mfma_f32_16x16x32_bf16 v[144:147], a[100:103], a[36:39], v[144:147]\n\t"
    "v_exp_f32 v228, v136\n\t"
    "v_mfma_f32_16x16x32_bf16 v[152:155], a[100:103], a[52:55], v[152:155]\n\t"
    "ds_read_b64_tr_b16 a[172:173], v211 offset:24576\n\t"
    "v_cvt_pk_bf16_f32 v179, v230, v231\n\t"
    "v_mfma_f32_16x16x32_bf16 v[148:151], a[116:119], a[36:39], v[148:151]\n\t"
    "v_exp_f32 v229, v137\n\t"
    "v_mfma_f32_16x16x32_bf16 v[156:159], a[116:119], a[52:55], v[156:159]\n\t"
    "ds_read_b64_tr_b16 a[174:175], v211 offset:28672\n\t"
    "v_mfma_f32_16x16x32_bf16 v[252:255], a[252:255], v[176:179], 0\n\t"
    "buffer_load_dwordx4 v222, s[36:39], s56 offen offset:2048 lds\n\t"
    "v_mfma_f32_16x16x32_bf16 v[144:147], a[104:107], a[40:43], v[144:147]\n\t"
    "v_exp_f32 v230, v138\n\t"
    "v_mfma_f32_16x16x32_bf16 v[152:155], a[104:107], a[56:59], v[152:155]\n\t"
    "v_add_f32 v232, v228, v229\n\t"
    "v_cvt_pk_bf16_f32 v180, v228, v229\n\t"
    "v_mfma_f32_16x16x32_bf16 v[148:151], a[120:123], a[40:43], v[148:151]\n\t"
    "v_exp_f32 v231, v139\n\t"
    "v_mfma_f32_16x16x32_bf16 v[156:159], a[120:123], a[56:59], v[156:159]\n\t"
    "ds_read_b64_tr_b16 a[176:177], v212 offset:24576\n\t"
    "v_add_f32 v232, v232, v230\n\t"
    "v_mfma_f32_16x16x32_bf16 v[144:147], a[108:111], a[44:47], v[144:147]\n\t"
    "v_exp_f32 v228, v140\n\t"
    "v_mfma_f32_16x16x32_bf16 v[152:155], a[108:111], a[60:63], v[152:155]\n\t"
    "ds_read_b64_tr_b16 a[178:179], v212 offset:28672\n\t"
    "v_add_f32 v232, v232, v231\n\t"
    "v_cvt_pk_bf16_f32 v181, v230, v231\n\t"
    "v_mfma_f32_16x16x32_bf16 v[148:151], a[124:127], a[44:47], v[148:151]\n\t"
    "v_exp_f32 v229, v141\n\t"
    "v_mfma_f32_16x16x32_bf16 v[156:159], a[124:127], a[60:63], v[156:159]\n\t"
    "ds_read_b64_tr_b16 a[180:181], v213 offset:24576\n\t"
    "v_add_f32 v232, v232, v228\n\t"
    "v_mfma_f32_16x16x32_bf16 v[8:11], a[128:131], v[168:171], v[8:11]\n\t"
    "v_exp_f32 v230, v142\n\t"
    "v_mfma_f32_16x16x32_bf16 v[12:15], a[128:131], v[172:175], v[12:15]\n\t"
    "v_add_f32 v232, v232, v229\n\t"
    "v_cvt_pk_bf16_f32 v182, v228, v229\n\t"
    "v_mfma_f32_16x16x32_bf16 v[24:27], a[132:135], v[168:171], v[24:27]\n\t"
    "v_exp_f32 v231, v143\n\t"
    "v_mfma_f32_16x16x32_bf16 v[28:31], a[132:135], v[172:175], v[28:31]\n\t"
    "ds_read_b64_tr_b16 a[182:183], v213 offset:28672\n\t"
    "v_add_f32 v232, v232, v230\n\t"
    "v_mfma_f32_16x16x32_bf16 v[40:43], a[136:139], v[168:171], v[40:43]\n\t"
    "v_exp_f32 v228, v144\n\t"
    "v_mfma_f32_16x16x32_bf16 v[44:47], a[136:139], v[172:175], v[44:47]\n\t"
    "v_add_f32 v232, v232, v231\n\t"
    "v_cvt_pk_bf16_f32 v183, v230, v231\n\t"
    "v_mfma_f32_16x16x32_bf16 v[56:59], a[140:143], v[168:171], v[56:59]\n\t"
    "v_exp_f32 v229, v145\n\t"
    "v_mfma_f32_16x16x32_bf16 v[60:63], a[140:143], v[172:175], v[60:63]\n\t"
    "buffer_load_dwordx4 v223, s[36:39], s56 offen offset:3072 lds\n\t"
    "v_max_f32 v248, v232, v252\n\t"
    "v_mfma_f32_16x16x32_bf16 v[72:75], a[144:147], v[168:171], v[72:75]\n\t"
    "v_exp_f32 v230, v146\n\t"
    "v_mfma_f32_16x16x32_bf16 v[76:79], a[144:147], v[172:175], v[76:79]\n\t"
    "ds_read_b64_tr_b16 a[184:185], v214 offset:24576\n\t"
    "s_add_u32 s56, s56, 0x4000\n\t"
    "s_and_b32 s56, s56, 0x7ffff\n\t"
    "s_cselect_b64 s[36:37], s[36:37], s[76:77]\n\t"
    "v_cmp_lt_f32 vcc, s68, v248\n\t"
    "v_mfma_f32_16x16x32_bf16 v[88:91], a[148:151], v[168:171], v[88:91]\n\t"
    "v_exp_f32 v231, v147\n\t"
    "v_mfma_f32_16x16x32_bf16 v[92:95], a[148:151], v[172:175], v[92:95]\n\t"
    "ds_read_b64_tr_b16 a[186:187], v214 offset:28672\n\t"
    "v_cvt_pk_bf16_f32 v184, v228, v229\n\t"
    "v_mfma_f32_16x16x32_bf16 v[104:107], a[152:155], v[168:171], v[104:107]\n\t"
    "v_exp_f32 v228, v148\n\t"
    "v_mfma_f32_16x16x32_bf16 v[108:111], a[152:155], v[172:175], v[108:111]\n\t"
    "s_cbranch_vccnz L_rs22_%=\n\t"
    "L_ret22_%=:\n\t"
    "ds_read_b64_tr_b16 a[188:189], v215 offset:24576\n\t"
    "v_cvt_pk_bf16_f32 v185, v230, v231\n\t"
    "v_mfma_f32_16x16x32_bf16 v[120:123], a[156:159], v[168:171], v[120:123]\n\t"
    "v_exp_f32 v229, v149\n\t"
    "v_mfma_f32_16x16x32_bf16 v[124:127], a[156:159], v[172:175], v[124:127]\n\t"
    "ds_read_b64_tr_b16 a[190:191], v215 offset:28672\n\t"
    "v_add_f32 v236, v236, v252\n\t"
    "s_waitcnt lgkmcnt(15)\n\t"
    "v_mfma_f32_16x16x32_bf16 v[128:131], a[64:67], a[0:3], v[192:195]\n\t"
    "v_exp_f32 v230, v150\n\t"
    "v_mfma_f32_16x16x32_bf16 v[136:139], a[64:67], a[16:19], v[196:199]\n\t"
    "ds_read_b128 a[96:99], v216 offset:40960\n\t"
    "v_cvt_pk_bf16_f32 v186, v228, v229\n\t"
    "v_mfma_f32_16x16x32_bf16 v[132:135], a[80:83], a[0:3], v[192:195]\n\t"
    "v_exp_f32 v231, v151\n\t"
    "v_mfma_f32_16x16x32_bf16 v[140:143], a[80:83], a[16:19], v[196:199]\n\t"
    "ds_read_b128 a[112:115], v216 offset:45056\n\t"
    "v_add_f32 v237, v237, v232\n\t"
    "v_mfma_f32_16x16x32_bf16 v[128:131], a[68:71], a[4:7], v[128:131]\n\t"
    "v_exp_f32 v228, v152\n\t"
    "v_mfma_f32_16x16x32_bf16 v[136:139], a[68:71], a[20:23], v[136:139]\n\t"
    "ds_read_b128 a[100:103], v217 offset:40960\n\t"
    "s_mov_b32 m0, s62\n\t"
    "v_cvt_pk_bf16_f32 v187, v230, v231\n\t"
    "v_mfma_f32_16x16x32_bf16 v[132:135], a[84:87], a[4:7], v[132:135]\n\t"
    "v_exp_f32 v229, v153\n\t"
    "v_mfma_f32_16x16x32_bf16 v[140:143], a[84:87], a[20:23], v[140:143]\n\t"
    "ds_read_b128 a[116:119], v217 offset:45056\n\t"
    "v_mfma_f32_16x16x32_bf16 v[252:255], a[252:255], v[184:187], 0\n\t"
    "buffer_load_dwordx4 v224, s[40:43], s57 offen offset:0 lds\n\t"
    "v_mfma_f32_16x16x32_bf16 v[128:131], a[72:75], a[8:11], v[128:131]\n\t"
    "v_exp_f32 v230, v154\n\t"
    "v_mfma_f32_16x16x32_bf16 v[136:139], a[72:75], a[24:27], v[136:139]\n\t"
    "v_add_f32 v233, v228, v229\n\t"
    "v_cvt_pk_bf16_f32 v188, v228, v229\n\t"
    "v_mfma_f32_16x16x32_bf16 v[132:135], a[88:91], a[8:11], v[132:135]\n\t"
    "v_exp_f32 v231, v155\n\t"
    "v_mfma_f32_16x16x32_bf16 v[140:143], a[88:91], a[24:27], v[140:143]\n\t"
    "ds_read_b128 a[104:107], v218 offset:40960\n\t"
    "v_add_f32 v233, v233, v230\n\t"
    "v_mfma_f32_16x16x32_bf16 v[128:131], a[76:79], a[12:15], v[128:131]\n\t"
    "v_exp_f32 v228, v156\n\t"
    "v_mfma_f32_16x16x32_bf16 v[136:139], a[76:79], a[28:31], v[136:139]\n\t"
    "ds_read_b128 a[120:123], v218 offset:45056\n\t"
    "v_add_f32 v233, v233, v231\n\t"
    "v_cvt_pk_bf16_f32 v189, v230, v231\n\t"
    "v_mfma_f32_16x16x32_bf16 v[132:135], a[92:95], a[12:15], v[132:135]\n\t"
    "v_exp_f32 v229, v157\n\t"
    "v_mfma_f32_16x16x32_bf16 v[140:143], a[92:95], a[28:31], v[140:143]\n\t"
    "ds_read_b128 a[108:111], v219 offset:40960\n\t"
    "v_add_f32 v233, v233, v228\n\t"
    "s_waitcnt lgkmcnt(7)\n\t"
    "v_mfma_f32_16x16x32_bf16 v[0:3], a[160:163], v[176:179], v[0:3]\n\t"
    "v_exp_f32 v230, v158\n\t"
    "v_mfma_f32_16x16x32_bf16 v[4:7], a[160:163], v[180:183], v[4:7]\n\t"
    "v_add_f32 v233, v233, v229\n\t"
    "v_cvt_pk_bf16_f32 v190, v228, v229\n\t"
    "v_mfma_f32_16x16x32_bf16 v[16:19], a[164:167], v[176:179], v[16:19]\n\t"
    "v_exp_f32 v231, v159\n\t"
    "v_mfma_f32_16x16x32_bf16 v[20:23], a[164:167], v[180:183], v[20:23]\n\t"
    "ds_read_b128 a[124:127], v219 offset:45056\n\t"
    "v_add_f32 v233, v233, v230\n\t"
    "v_mfma_f32_16x16x32_bf16 v[32:35], a[168:171], v[176:179], v[32:35]\n\t"
    "v_exp_f32 v228, v128\n\t"
    "v_mfma_f32_16x16x32_bf16 v[36:39], a[168:171], v[180:183], v[36:39]\n\t"
    "v_add_f32 v233, v233, v231\n\t"
    "v_cvt_pk_bf16_f32 v191, v230, v231\n\t"
    "v_mfma_f32_16x16x32_bf16 v[48:51], a[172:175], v[176:179], v[48:51]\n\t"
    "v_exp_f32 v229, v129\n\t"
    "v_mfma_f32_16x16x32_bf16 v[52:55], a[172:175], v[180:183], v[52:55]\n\t"
    "buffer_load_dwordx4 v225, s[40:43], s57 offen offset:1024 lds\n\t"
    "v_max_f32 v248, v233, v252\n\t"
    "v_mfma_f32_16x16x32_bf16 v[64:67], a[176:179], v[176:179], v[64:67]\n\t"
    "v_exp_f32 v230, v130\n\t"
    "v_mfma_f32_16x16x32_bf16 v[68:71], a[176:179], v[180:183], v[68:71]\n\t"
    "ds_read_b64_tr_b16 a[128:129], v208 offset:32768\n\t"
    "v_cmp_lt_f32 vcc, s68, v248\n\t"
    "v_mfma_f32_16x16x32_bf16 v[80:83], a[180:183], v[176:179], v[80:83]\n\t"
    "v_exp_f32 v231, v131\n\t"
    "v_mfma_f32_16x16x32_bf16 v[84:87], a[180:183], v[180:183], v[84:87]\n\t"
    "ds_read_b64_tr_b16 a[130:131], v208 offset:36864\n\t"
    "v_cvt_pk_bf16_f32 v160, v228, v229\n\t"
    "v_mfma_f32_16x16x32_bf16 v[96:99], a[184:187], v[176:179], v[96:99]\n\t"
    "v_exp_f32 v228, v132\n\t"
    "v_mfma_f32_16x16x32_bf16 v[100:103], a[184:187], v[180:183], v[100:103]\n\t"
    "s_cbranch_vccnz L_rs23_%=\n\t"
    "L_ret23_%=:\n\t"
    "ds_read_b64_tr_b16 a[132:133], v209 offset:32768\n\t"
    "v_cvt_pk_bf16_f32 v161, v230, v231\n\t"
    "v_mfma_f32_16x16x32_bf16 v[112:115], a[188:191], v[176:179], v[112:115]\n\t"
    "v_exp_f32 v229, v133\n\t"
    "v_mfma_f32_16x16x32_bf16 v[116:119], a[188:191], v[180:183], v[116:119]\n\t"
    "ds_read_b64_tr_b16 a[134:135], v209 offset:36864\n\t"
    "v_add_f32 v238, v238, v252\n\t"
    "v_mfma_f32_16x16x32_bf16 v[144:147], a[64:67], a[32:35], v[200:203]\n\t"
    "v_exp_f32 v230, v134\n\t"
    "v_mfma_f32_16x16x32_bf16 v[152:155], a[64:67], a[48:51], v[204:207]\n\t"
    "ds_read_b64_tr_b16 a[136:137], v210 offset:32768\n\t"
    "v_cvt_pk_bf16_f32 v162, v228, v229\n\t"
    "v_mfma_f32_16x16x32_bf16 v[148:151], a[80:83], a[32:35], v[200:203]\n\t"
    "v_exp_f32 v231, v135\n\t"
    "v_mfma_f32_16x16x32_bf16 v[156:159], a[80:83], a[48:51], v[204:207]\n\t"
    "ds_read_b64_tr_b16 a[138:139], v210 offset:36864\n\t"
    "v_add_f32 v239, v239, v233\n\t"
    "v_mfma_f32_16x16x32_bf16 v[144:147], a[68:71], a[36:39], v[144:147]\n\t"
    "v_exp_f32 v228, v136\n\t"
    "v_mfma_f32_16x16x32_bf16 v[152:155], a[68:71], a[52:55], v[152:155]\n\t"
    "ds_read_b64_tr_b16 a[140:141], v211 offset:32768\n\t"
    "v_cvt_pk_bf16_f32 v163, v230, v231\n\t"
    "v_mfma_f32_16x16x32_bf16 v[148:151], a[84:87], a[36:39], v[148:151]\n\t"
    "v_exp_f32 v229, v137\n\t"
    "v_mfma_f32_16x16x32_bf16 v[156:159], a[84:87], a[52:55], v[156:159]\n\t"
    "ds_read_b64_tr_b16 a[142:143], v211 offset:36864\n\t"
    "v_mfma_f32_16x16x32_bf16 v[252:255], a[252:255], v[160:163], 0\n\t"
    "buffer_load_dwordx4 v224, s[40:43], s57 offen offset:2048 lds\n\t"
    "v_mfma_f32_16x16x32_bf16 v[144:147], a[72:75], a[40:43], v[144:147]\n\t"
    "v_exp_f32 v230, v138\n\t"
    "v_mfma_f32_16x16x32_bf16 v[152:155], a[72:75], a[56:59], v[152:155]\n\t"
    "v_add_f32 v232, v228, v229\n\t"
    "v_cvt_pk_bf16_f32 v164, v228, v229\n\t"
    "v_mfma_f32_16x16x32_bf16 v[148:151], a[88:91], a[40:43], v[148:151]\n\t"
    "v_exp_f32 v231, v139\n\t"
    "v_mfma_f32_16x16x32_bf16 v[156:159], a[88:91], a[56:59], v[156:159]\n\t"
    "ds_read_b64_tr_b16 a[144:145], v212 offset:32768\n\t"
    "v_add_f32 v232, v232, v230\n\t"
    "v_mfma_f32_16x16x32_bf16 v[144:147], a[76:79], a[44:47], v[144:147]\n\t"
    "v_exp_f32 v228, v140\n\t"
    "v_mfma_f32_16x16x32_bf16 v[152:155], a[76:79], a[60:63], v[152:155]\n\t"
    "ds_read_b64_tr_b16 a[146:147], v212 offset:36864\n\t"
    "v_add_f32 v232, v232, v231\n\t"
    "v_cvt_pk_bf16_f32 v165, v230, v231\n\t"
    "v_mfma_f32_16x16x32_bf16 v[148:151], a[92:95], a[44:47], v[148:151]\n\t"
    "v_exp_f32 v229, v141\n\t"
    "v_mfma_f32_16x16x32_bf16 v[156:159], a[92:95], a[60:63], v[156:159]\n\t"
    "ds_read_b64_tr_b16 a[148:149], v213 offset:32768\n\t"
    "v_add_f32 v232, v232, v228\n\t"
    "v_mfma_f32_16x16x32_bf16 v[8:11], a[160:163], v[184:187], v[8:11]\n\t"
    "v_exp_f32 v230, v142\n\t"
    "v_mfma_f32_16x16x32_bf16 v[12:15], a[160:163], v[188:191], v[12:15]\n\t"
    "v_add_f32 v232, v232, v229\n\t"
    "v_cvt_pk_bf16_f32 v166, v228, v229\n\t"
    "v_mfma_f32_16x16x32_bf16 v[24:27], a[164:167], v[184:187], v[24:27]\n\t"
    "v_exp_f32 v231, v143\n\t"
    "v_mfma_f32_16x16x32_bf16 v[28:31], a[164:167], v[188:191], v[28:31]\n\t"
    "ds_read_b64_tr_b16 a[150:151], v213 offset:36864\n\t"
    "v_add_f32 v232, v232, v230\n\t"
    "v_mfma_f32_16x16x32_bf16 v[40:43], a[168:171], v[184:187], v[40:43]\n\t"
    "v_exp_f32 v228, v144\n\t"
    "v_mfma_f32_16x16x32_bf16 v[44:47], a[168:171], v[188:191], v[44:47]\n\t"
    "v_add_f32 v232, v232, v231\n\t"
    "v_cvt_pk_bf16_f32 v167, v230, v231\n\t"
    "v_mfma_f32_16x16x32_bf16 v[56:59], a[172:175], v[184:187], v[56:59]\n\t"
    "v_exp_f32 v229, v145\n\t"
    "v_mfma_f32_16x16x32_bf16 v[60:63], a[172:175], v[188:191], v[60:63]\n\t"
    "buffer_load_dwordx4 v225, s[40:43], s57 offen offset:3072 lds\n\t"
    "v_max_f32 v248, v232, v252\n\t"
    "v_mfma_f32_16x16x32_bf16 v[72:75], a[176:179], v[184:187], v[72:75]\n\t"
    "v_exp_f32 v230, v146\n\t"
    "v_mfma_f32_16x16x32_bf16 v[76:79], a[176:179], v[188:191], v[76:79]\n\t"
    "ds_read_b64_tr_b16 a[152:153], v214 offset:32768\n\t"
    "s_add_u32 s57, s57, 0x4000\n\t"
    "s_and_b32 s57, s57, 0x7ffff\n\t"
    "s_cselect_b64 s[40:41], s[40:41], s[78:79]\n\t"
    "v_cmp_lt_f32 vcc, s68, v248\n\t"
    "v_mfma_f32_16x16x32_bf16 v[88:91], a[180:183], v[184:187], v[88:91]\n\t"
    "v_exp_f32 v231, v147\n\t"
    "v_mfma_f32_16x16x32_bf16 v[92:95], a[180:183], v[188:191], v[92:95]\n\t"
    "ds_read_b64_tr_b16 a[154:155], v214 offset:36864\n\t"
    "v_cvt_pk_bf16_f32 v168, v228, v229\n\t"
    "v_mfma_f32_16x16x32_bf16 v[104:107], a[184:187], v[184:187], v[104:107]\n\t"
    "v_exp_f32 v228, v148\n\t"
    "v_mfma_f32_16x16x32_bf16 v[108:111], a[184:187], v[188:191], v[108:111]\n\t"
    "s_cbranch_vccnz L_rs24_%=\n\t"
    "L_ret24_%=:\n\t"
    "ds_read_b64_tr_b16 a[156:157], v215 offset:32768\n\t"
    "v_cvt_pk_bf16_f32 v169, v230, v231\n\t"
    "v_mfma_f32_16x16x32_bf16 v[120:123], a[188:191], v[184:187], v[120:123]\n\t"
    "v_exp_f32 v229, v149\n\t"
    "v_mfma_f32_16x16x32_bf16 v[124:127], a[188:191], v[188:191], v[124:127]\n\t"
    "ds_read_b64_tr_b16 a[158:159], v215 offset:36864\n\t"
    "v_add_f32 v236, v236, v252\n\t"
    "s_waitcnt lgkmcnt(15)\n\t"
    "v_mfma_f32_16x16x32_bf16 v[128:131], a[96:99], a[0:3], v[192:195]\n\t"
    "v_exp_f32 v230, v150\n\t"
    "v_mfma_f32_16x16x32_bf16 v[136:139], a[96:99], a[16:19], v[196:199]\n\t"
    "ds_read_b64_tr_b16 a[160:161], v208 offset:40960\n\t"
    "v_cvt_pk_bf16_f32 v170, v228, v229\n\t"
    "s_waitcnt vmcnt(8)\n\t"
    "s_barrier\n\t"
    "v_mfma_f32_16x16x32_bf16 v[132:135], a[112:115], a[0:3], v[192:195]\n\t"
    "v_exp_f32 v231, v151\n\t"
    "v_mfma_f32_16x16x32_bf16 v[140:143], a[112:115], a[16:19], v[196:199]\n\t"
    "ds_read_b128 a[64:67], v216 offset:49152\n\t"
    "v_add_f32 v237, v237, v232\n\t"
    "v_mfma_f32_16x16x32_bf16 v[128:131], a[100:103], a[4:7], v[128:131]\n\t"
    "v_exp_f32 v228, v152\n\t"
    "v_mfma_f32_16x16x32_bf16 v[136:139], a[100:103], a[20:23], v[136:139]\n\t"
    "ds_read_b128 a[80:83], v216 offset:53248\n\t"
    "s_mov_b32 m0, s59\n\t"
    "v_cvt_pk_bf16_f32 v171, v230, v231\n\t"
    "v_mfma_f32_16x16x32_bf16 v[132:135], a[116:119], a[4:7], v[132:135]\n\t"
    "v_exp_f32 v229, v153\n\t"
    "v_mfma_f32_16x16x32_bf16 v[140:143], a[116:119], a[20:23], v[140:143]\n\t"
    "ds_read_b128 a[68:71], v217 offset:49152\n\t"
    "v_mfma_f32_16x16x32_bf16 v[252:255], a[252:255], v[168:171], 0\n\t"
    "buffer_load_dwordx4 v220, s[36:39], s56 offen offset:0 lds\n\t"
    "v_mfma_f32_16x16x32_bf16 v[128:131], a[104:107], a[8:11], v[128:131]\n\t"
    "v_exp_f32 v230, v154\n\t"
    "v_mfma_f32_16x16x32_bf16 v[136:139], a[104:107], a[24:27], v[136:139]\n\t"
    "v_add_f32 v233, v228, v229\n\t"
    "v_cvt_pk_bf16_f32 v172, v228, v229\n\t"
    "v_mfma_f32_16x16x32_bf16 v[132:135], a[120:123], a[8:11], v[132:135]\n\t"
    "v_exp_f32 v231, v155\n\t"
    "v_mfma_f32_16x16x32_bf16 v[140:143], a[120:123], a[24:27], v[140:143]\n\t"
    "ds_read_b128 a[84:87], v217 offset:53248\n\t"
    "v_add_f32 v233, v233, v230\n\t"
    "v_mfma_f32_16x16x32_bf16 v[128:131], a[108:111], a[12:15], v[128:131]\n\t"
    "v_exp_f32 v228, v156\n\t"
    "v_mfma_f32_16x16x32_bf16 v[136:139], a[108:111], a[28:31], v[136:139]\n\t"
    "ds_read_b128 a[72:75], v218 offset:49152\n\t"
    "v_add_f32 v233, v233, v231\n\t"
    "v_cvt_pk_bf16_f32 v173, v230, v231\n\t"
    "v_mfma_f32_16x16x32_bf16 v[132:135], a[124:127], a[12:15], v[132:135]\n\t"
    "v_exp_f32 v229, v157\n\t"
    "v_mfma_f32_16x16x32_bf16 v[140:143], a[124:127], a[28:31], v[140:143]\n\t"
    "ds_read_b128 a[88:91], v218 offset:53248\n\t"
    "v_add_f32 v233, v233, v228\n\t"
    "s_waitcnt lgkmcnt(7)\n\t"
    "v_mfma_f32_16x16x32_bf16 v[0:3], a[128:131], v[160:163], v[0:3]\n\t"
    "v_exp_f32 v230, v158\n\t"
    "v_mfma_f32_16x16x32_bf16 v[4:7], a[128:131], v[164:167], v[4:7]\n\t"
    "v_add_f32 v233, v233, v229\n\t"
    "v_cvt_pk_bf16_f32 v174, v228, v229\n\t"
    "v_mfma_f32_16x16x32_bf16 v[16:19], a[132:135], v[160:163], v[16:19]\n\t"
    "v_exp_f32 v231, v159\n\t"
    "v_mfma_f32_16x16x32_bf16 v[20:23], a[132:135], v[164:167], v[20:23]\n\t"
    "ds_read_b128 a[76:79], v219 offset:49152\n\t"
    "v_add_f32 v233, v233, v230\n\t"
    "v_mfma_f32_16x16x32_bf16 v[32:35], a[136:139], v[160:163], v[32:35]\n\t"
    "v_exp_f32 v228, v128\n\t"
    "v_mfma_f32_16x16x32_bf16 v[36:39], a[136:139], v[164:167], v[36:39]\n\t"
    "v_add_f32 v233, v233, v231\n\t"
    "v_cvt_pk_bf16_f32 v175, v230, v231\n\t"
    "v_mfma_f32_16x16x32_bf16 v[48:51], a[140:143], v[160:163], v[48:51]\n\t"
    "v_exp_f32 v229, v129\n\t"
    "v_mfma_f32_16x16x32_bf16 v[52:55], a[140:143], v[164:167], v[52:55]\n\t"
    "buffer_load_dwordx4 v221, s[36:39], s56 offen offset:1024 lds\n\t"
    "v_max_f32 v248, v233, v252\n\t"
    "v_mfma_f32_16x16x32_bf16 v[64:67], a[144:147], v[160:163], v[64:67]\n\t"
    "v_exp_f32 v230, v130\n\t"
    "v_mfma_f32_16x16x32_bf16 v[68:71], a[144:147], v[164:167], v[68:71]\n\t"
    "ds_read_b128 a[92:95], v219 offset:53248\n\t"
    "v_cmp_lt_f32 vcc, s68, v248\n\t"
    "v_mfma_f32_16x16x32_bf16 v[80:83], a[148:151], v[160:163], v[80:83]\n\t"
    "v_exp_f32 v231, v131\n\t"
    "v_mfma_f32_16x16x32_bf16 v[84:87], a[148:151], v[164:167], v[84:87]\n\t"
    "ds_read_b64_tr_b16 a[162:163], v208 offset:45056\n\t"
    "v_cvt_pk_bf16_f32 v176, v228, v229\n\t"
    "v_mfma_f32_16x16x32_bf16 v[96:99], a[152:155], v[160:163], v[96:99]\n\t"
    "v_exp_f32 v228, v132\n\t"
    "v_mfma_f32_16x16x32_bf16 v[100:103], a[152:155], v[164:167], v[100:103]\n\t"
    "s_cbranch_vccnz L_rs25_%=\n\t"
    "L_ret25_%=:\n\t"
    "ds_read_b64_tr_b16 a[164:165], v209 offset:40960\n\t"
    "v_cvt_pk_bf16_f32 v177, v230, v231\n\t"
    "v_mfma_f32_16x16x32_bf16 v[112:115], a[156:159], v[160:163], v[112:115]\n\t"
    "v_exp_f32 v229, v133\n\t"
    "v_mfma_f32_16x16x32_bf16 v[116:119], a[156:159], v[164:167], v[116:119]\n\t"
    "ds_read_b64_tr_b16 a[166:167], v209 offset:45056\n\t"
    "v_add_f32 v238, v238, v252\n\t"
    "v_mfma_f32_16x16x32_bf16 v[144:147], a[96:99], a[32:35], v[200:203]\n\t"
    "v_exp_f32 v230, v134\n\t"
    "v_mfma_f32_16x16x32_bf16 v[152:155], a[96:99], a[48:51], v[204:207]\n\t"
    "ds_read_b64_tr_b16 a[168:169], v210 offset:40960\n\t"
    "v_cvt_pk_bf16_f32 v178, v228, v229\n\t"
    "v_mfma_f32_16x16x32_bf16 v[148:151], a[112:115], a[32:35], v[200:203]\n\t"
    "v_exp_f32 v231, v135\n\t"
    "v_mfma_f32_16x16x32_bf16 v[156:159], a[112:115], a[48:51], v[204:207]\n\t"
    "ds_read_b64_tr_b16 a[170:171], v210 offset:45056\n\t"
    "v_add_f32 v239, v239, v233\n\t"
    "v_mfma_f32_16x16x32_bf16 v[144:147], a[100:103], a[36:39], v[144:147]\n\t"
    "v_exp_f32 v228, v136\n\t"
    "v_mfma_f32_16x16x32_bf16 v[152:155], a[100:103], a[52:55], v[152:155]\n\t"
    "ds_read_b64_tr_b16 a[172:173], v211 offset:40960\n\t"
    "v_cvt_pk_bf16_f32 v179, v230, v231\n\t"
    "v_mfma_f32_16x16x32_bf16 v[148:151], a[116:119], a[36:39], v[148:151]\n\t"
    "v_exp_f32 v229, v137\n\t"
    "v_mfma_f32_16x16x32_bf16 v[156:159], a[116:119], a[52:55], v[156:159]\n\t"
    "ds_read_b64_tr_b16 a[174:175], v211 offset:45056\n\t"
    "v_mfma_f32_16x16x32_bf16 v[252:255], a[252:255], v[176:179], 0\n\t"
    "buffer_load_dwordx4 v222, s[36:39], s56 offen offset:2048 lds\n\t"
    "v_mfma_f32_16x16x32_bf16 v[144:147], a[104:107], a[40:43], v[144:147]\n\t"
    "v_exp_f32 v230, v138\n\t"
    "v_mfma_f32_16x16x32_bf16 v[152:155], a[104:107], a[56:59], v[152:155]\n\t"
    "v_add_f32 v232, v228, v229\n\t"
    "v_cvt_pk_bf16_f32 v180, v228, v229\n\t"
    "v_mfma_f32_16x16x32_bf16 v[148:151], a[120:123], a[40:43], v[148:151]\n\t"
    "v_exp_f32 v231, v139\n\t"
    "v_mfma_f32_16x16x32_bf16 v[156:159], a[120:123], a[56:59], v[156:159]\n\t"
    "ds_read_b64_tr_b16 a[176:177], v212 offset:40960\n\t"
    "v_add_f32 v232, v232, v230\n\t"
    "v_mfma_f32_16x16x32_bf16 v[144:147], a[108:111], a[44:47], v[144:147]\n\t"
    "v_exp_f32 v228, v140\n\t"
    "v_mfma_f32_16x16x32_bf16 v[152:155], a[108:111], a[60:63], v[152:155]\n\t"
    "ds_read_b64_tr_b16 a[178:179], v212 offset:45056\n\t"
    "v_add_f32 v232, v232, v231\n\t"
    "v_cvt_pk_bf16_f32 v181, v230, v231\n\t"
    "v_mfma_f32_16x16x32_bf16 v[148:151], a[124:127], a[44:47], v[148:151]\n\t"
    "v_exp_f32 v229, v141\n\t"
    "v_mfma_f32_16x16x32_bf16 v[156:159], a[124:127], a[60:63], v[156:159]\n\t"
    "ds_read_b64_tr_b16 a[180:181], v213 offset:40960\n\t"
    "v_add_f32 v232, v232, v228\n\t"
    "v_mfma_f32_16x16x32_bf16 v[8:11], a[128:131], v[168:171], v[8:11]\n\t"
    "v_exp_f32 v230, v142\n\t"
    "v_mfma_f32_16x16x32_bf16 v[12:15], a[128:131], v[172:175], v[12:15]\n\t"
    "v_add_f32 v232, v232, v229\n\t"
    "v_cvt_pk_bf16_f32 v182, v228, v229\n\t"
    "v_mfma_f32_16x16x32_bf16 v[24:27], a[132:135], v[168:171], v[24:27]\n\t"
    "v_exp_f32 v231, v143\n\t"
    "v_mfma_f32_16x16x32_bf16 v[28:31], a[132:135], v[172:175], v[28:31]\n\t"
    "ds_read_b64_tr_b16 a[182:183], v213 offset:45056\n\t"
    "v_add_f32 v232, v232, v230\n\t"
    "v_mfma_f32_16x16x32_bf16 v[40:43], a[136:139], v[168:171], v[40:43]\n\t"
    "v_exp_f32 v228, v144\n\t"
    "v_mfma_f32_16x16x32_bf16 v[44:47], a[136:139], v[172:175], v[44:47]\n\t"
    "v_add_f32 v232, v232, v231\n\t"
    "v_cvt_pk_bf16_f32 v183, v230, v231\n\t"
    "v_mfma_f32_16x16x32_bf16 v[56:59], a[140:143], v[168:171], v[56:59]\n\t"
    "v_exp_f32 v229, v145\n\t"
    "v_mfma_f32_16x16x32_bf16 v[60:63], a[140:143], v[172:175], v[60:63]\n\t"
    "buffer_load_dwordx4 v223, s[36:39], s56 offen offset:3072 lds\n\t"
    "v_max_f32 v248, v232, v252\n\t"
    "v_mfma_f32_16x16x32_bf16 v[72:75], a[144:147], v[168:171], v[72:75]\n\t"
    "v_exp_f32 v230, v146\n\t"
    "v_mfma_f32_16x16x32_bf16 v[76:79], a[144:147], v[172:175], v[76:79]\n\t"
    "ds_read_b64_tr_b16 a[184:185], v214 offset:40960\n\t"
    "s_add_u32 s56, s56, 0x4000\n\t"
    "s_and_b32 s56, s56, 0x7ffff\n\t"
    "s_cselect_b64 s[36:37], s[36:37], s[76:77]\n\t"
    "v_cmp_lt_f32 vcc, s68, v248\n\t"
    "v_mfma_f32_16x16x32_bf16 v[88:91], a[148:151], v[168:171], v[88:91]\n\t"
    "v_exp_f32 v231, v147\n\t"
    "v_mfma_f32_16x16x32_bf16 v[92:95], a[148:151], v[172:175], v[92:95]\n\t"
    "ds_read_b64_tr_b16 a[186:187], v214 offset:45056\n\t"
    "v_cvt_pk_bf16_f32 v184, v228, v229\n\t"
    "v_mfma_f32_16x16x32_bf16 v[104:107], a[152:155], v[168:171], v[104:107]\n\t"
    "v_exp_f32 v228, v148\n\t"
    "v_mfma_f32_16x16x32_bf16 v[108:111], a[152:155], v[172:175], v[108:111]\n\t"
    "s_cbranch_vccnz L_rs26_%=\n\t"
    "L_ret26_%=:\n\t"
    "ds_read_b64_tr_b16 a[188:189], v215 offset:40960\n\t"
    "v_cvt_pk_bf16_f32 v185, v230, v231\n\t"
    "v_mfma_f32_16x16x32_bf16 v[120:123], a[156:159], v[168:171], v[120:123]\n\t"
    "v_exp_f32 v229, v149\n\t"
    "v_mfma_f32_16x16x32_bf16 v[124:127], a[156:159], v[172:175], v[124:127]\n\t"
    "ds_read_b64_tr_b16 a[190:191], v215 offset:45056\n\t"
    "v_add_f32 v236, v236, v252\n\t"
    "s_waitcnt lgkmcnt(15)\n\t"
    "v_mfma_f32_16x16x32_bf16 v[128:131], a[64:67], a[0:3], v[192:195]\n\t"
    "v_exp_f32 v230, v150\n\t"
    "v_mfma_f32_16x16x32_bf16 v[136:139], a[64:67], a[16:19], v[196:199]\n\t"
    "ds_read_b128 a[96:99], v216 offset:57344\n\t"
    "v_cvt_pk_bf16_f32 v186, v228, v229\n\t"
    "v_mfma_f32_16x16x32_bf16 v[132:135], a[80:83], a[0:3], v[192:195]\n\t"
    "v_exp_f32 v231, v151\n\t"
    "v_mfma_f32_16x16x32_bf16 v[140:143], a[80:83], a[16:19], v[196:199]\n\t"
    "ds_read_b128 a[112:115], v216 offset:61440\n\t"
    "v_add_f32 v237, v237, v232\n\t"
    "v_mfma_f32_16x16x32_bf16 v[128:131], a[68:71], a[4:7], v[128:131]\n\t"
    "v_exp_f32 v228, v152\n\t"
    "v_mfma_f32_16x16x32_bf16 v[136:139], a[68:71], a[20:23], v[136:139]\n\t"
    "ds_read_b128 a[100:103], v217 offset:57344\n\t"
    "s_mov_b32 m0, s63\n\t"
    "v_cvt_pk_bf16_f32 v187, v230, v231\n\t"
    "v_mfma_f32_16x16x32_bf16 v[132:135], a[84:87], a[4:7], v[132:135]\n\t"
    "v_exp_f32 v229, v153\n\t"
    "v_mfma_f32_16x16x32_bf16 v[140:143], a[84:87], a[20:23], v[140:143]\n\t"
    "ds_read_b128 a[116:119], v217 offset:61440\n\t"
    "v_mfma_f32_16x16x32_bf16 v[252:255], a[252:255], v[184:187], 0\n\t"
    "buffer_load_dwordx4 v224, s[40:43], s57 offen offset:0 lds\n\t"
    "v_mfma_f32_16x16x32_bf16 v[128:131], a[72:75], a[8:11], v[128:131]\n\t"
    "v_exp_f32 v230, v154\n\t"
    "v_mfma_f32_16x16x32_bf16 v[136:139], a[72:75], a[24:27], v[136:139]\n\t"
    "v_add_f32 v233, v228, v229\n\t"
    "v_cvt_pk_bf16_f32 v188, v228, v229\n\t"
    "v_mfma_f32_16x16x32_bf16 v[132:135], a[88:91], a[8:11], v[132:135]\n\t"
    "v_exp_f32 v231, v155\n\t"
    "v_mfma_f32_16x16x32_bf16 v[140:143], a[88:91], a[24:27], v[140:143]\n\t"
    "ds_read_b128 a[104:107], v218 offset:57344\n\t"
    "v_add_f32 v233, v233, v230\n\t"
    "v_mfma_f32_16x16x32_bf16 v[128:131], a[76:79], a[12:15], v[128:131]\n\t"
    "v_exp_f32 v228, v156\n\t"
    "v_mfma_f32_16x16x32_bf16 v[136:139], a[76:79], a[28:31], v[136:139]\n\t"
    "ds_read_b128 a[120:123], v218 offset:61440\n\t"
    "v_add_f32 v233, v233, v231\n\t"
    "v_cvt_pk_bf16_f32 v189, v230, v231\n\t"
    "v_mfma_f32_16x16x32_bf16 v[132:135], a[92:95], a[12:15], v[132:135]\n\t"
    "v_exp_f32 v229, v157\n\t"
    "v_mfma_f32_16x16x32_bf16 v[140:143], a[92:95], a[28:31], v[140:143]\n\t"
    "ds_read_b128 a[108:111], v219 offset:57344\n\t"
    "v_add_f32 v233, v233, v228\n\t"
    "s_waitcnt lgkmcnt(7)\n\t"
    "v_mfma_f32_16x16x32_bf16 v[0:3], a[160:163], v[176:179], v[0:3]\n\t"
    "v_exp_f32 v230, v158\n\t"
    "v_mfma_f32_16x16x32_bf16 v[4:7], a[160:163], v[180:183], v[4:7]\n\t"
    "v_add_f32 v233, v233, v229\n\t"
    "v_cvt_pk_bf16_f32 v190, v228, v229\n\t"
    "v_mfma_f32_16x16x32_bf16 v[16:19], a[164:167], v[176:179], v[16:19]\n\t"
    "v_exp_f32 v231, v159\n\t"
    "v_mfma_f32_16x16x32_bf16 v[20:23], a[164:167], v[180:183], v[20:23]\n\t"
    "ds_read_b128 a[124:127], v219 offset:61440\n\t"
    "v_add_f32 v233, v233, v230\n\t"
    "v_mfma_f32_16x16x32_bf16 v[32:35], a[168:171], v[176:179], v[32:35]\n\t"
    "v_exp_f32 v228, v128\n\t"
    "v_mfma_f32_16x16x32_bf16 v[36:39], a[168:171], v[180:183], v[36:39]\n\t"
    "v_add_f32 v233, v233, v231\n\t"
    "v_cvt_pk_bf16_f32 v191, v230, v231\n\t"
    "v_mfma_f32_16x16x32_bf16 v[48:51], a[172:175], v[176:179], v[48:51]\n\t"
    "v_exp_f32 v229, v129\n\t"
    "v_mfma_f32_16x16x32_bf16 v[52:55], a[172:175], v[180:183], v[52:55]\n\t"
    "buffer_load_dwordx4 v225, s[40:43], s57 offen offset:1024 lds\n\t"
    "v_max_f32 v248, v233, v252\n\t"
    "v_mfma_f32_16x16x32_bf16 v[64:67], a[176:179], v[176:179], v[64:67]\n\t"
    "v_exp_f32 v230, v130\n\t"
    "v_mfma_f32_16x16x32_bf16 v[68:71], a[176:179], v[180:183], v[68:71]\n\t"
    "ds_read_b64_tr_b16 a[128:129], v208 offset:49152\n\t"
    "v_cmp_lt_f32 vcc, s68, v248\n\t"
    "v_mfma_f32_16x16x32_bf16 v[80:83], a[180:183], v[176:179], v[80:83]\n\t"
    "v_exp_f32 v231, v131\n\t"
    "v_mfma_f32_16x16x32_bf16 v[84:87], a[180:183], v[180:183], v[84:87]\n\t"
    "ds_read_b64_tr_b16 a[130:131], v208 offset:53248\n\t"
    "v_cvt_pk_bf16_f32 v160, v228, v229\n\t"
    "v_mfma_f32_16x16x32_bf16 v[96:99], a[184:187], v[176:179], v[96:99]\n\t"
    "v_exp_f32 v228, v132\n\t"
    "v_mfma_f32_16x16x32_bf16 v[100:103], a[184:187], v[180:183], v[100:103]\n\t"
    "s_cbranch_vccnz L_rs27_%=\n\t"
    "L_ret27_%=:\n\t"
    "ds_read_b64_tr_b16 a[132:133], v209 offset:49152\n\t"
    "v_cvt_pk_bf16_f32 v161, v230, v231\n\t"
    "v_mfma_f32_16x16x32_bf16 v[112:115], a[188:191], v[176:179], v[112:115]\n\t"
    "v_exp_f32 v229, v133\n\t"
    "v_mfma_f32_16x16x32_bf16 v[116:119], a[188:191], v[180:183], v[116:119]\n\t"
    "ds_read_b64_tr_b16 a[134:135], v209 offset:53248\n\t"
    "v_add_f32 v238, v238, v252\n\t"
    "v_mfma_f32_16x16x32_bf16 v[144:147], a[64:67], a[32:35], v[200:203]\n\t"
    "v_exp_f32 v230, v134\n\t"
    "v_mfma_f32_16x16x32_bf16 v[152:155], a[64:67], a[48:51], v[204:207]\n\t"
    "ds_read_b64_tr_b16 a[136:137], v210 offset:49152\n\t"
    "v_cvt_pk_bf16_f32 v162, v228, v229\n\t"
    "v_mfma_f32_16x16x32_bf16 v[148:151], a[80:83], a[32:35], v[200:203]\n\t"
    "v_exp_f32 v231, v135\n\t"
    "v_mfma_f32_16x16x32_bf16 v[156:159], a[80:83], a[48:51], v[204:207]\n\t"
    "ds_read_b64_tr_b16 a[138:139], v210 offset:53248\n\t"
    "v_add_f32 v239, v239, v233\n\t"
    "v_mfma_f32_16x16x32_bf16 v[144:147], a[68:71], a[36:39], v[144:147]\n\t"
    "v_exp_f32 v228, v136\n\t"
    "v_mfma_f32_16x16x32_bf16 v[152:155], a[68:71], a[52:55], v[152:155]\n\t"
    "ds_read_b64_tr_b16 a[140:141], v211 offset:49152\n\t"
    "v_cvt_pk_bf16_f32 v163, v230, v231\n\t"
    "v_mfma_f32_16x16x32_bf16 v[148:151], a[84:87], a[36:39], v[148:151]\n\t"
    "v_exp_f32 v229, v137\n\t"
    "v_mfma_f32_16x16x32_bf16 v[156:159], a[84:87], a[52:55], v[156:159]\n\t"
    "ds_read_b64_tr_b16 a[142:143], v211 offset:53248\n\t"
    "v_mfma_f32_16x16x32_bf16 v[252:255], a[252:255], v[160:163], 0\n\t"
    "buffer_load_dwordx4 v224, s[40:43], s57 offen offset:2048 lds\n\t"
    "v_mfma_f32_16x16x32_bf16 v[144:147], a[72:75], a[40:43], v[144:147]\n\t"
    "v_exp_f32 v230, v138\n\t"
    "v_mfma_f32_16x16x32_bf16 v[152:155], a[72:75], a[56:59], v[152:155]\n\t"
    "v_add_f32 v232, v228, v229\n\t"
    "v_cvt_pk_bf16_f32 v164, v228, v229\n\t"
    "v_mfma_f32_16x16x32_bf16 v[148:151], a[88:91], a[40:43], v[148:151]\n\t"
    "v_exp_f32 v231, v139\n\t"
    "v_mfma_f32_16x16x32_bf16 v[156:159], a[88:91], a[56:59], v[156:159]\n\t"
    "ds_read_b64_tr_b16 a[144:145], v212 offset:49152\n\t"
    "v_add_f32 v232, v232, v230\n\t"
    "v_mfma_f32_16x16x32_bf16 v[144:147], a[76:79], a[44:47], v[144:147]\n\t"
    "v_exp_f32 v228, v140\n\t"
    "v_mfma_f32_16x16x32_bf16 v[152:155], a[76:79], a[60:63], v[152:155]\n\t"
    "ds_read_b64_tr_b16 a[146:147], v212 offset:53248\n\t"
    "v_add_f32 v232, v232, v231\n\t"
    "v_cvt_pk_bf16_f32 v165, v230, v231\n\t"
    "v_mfma_f32_16x16x32_bf16 v[148:151], a[92:95], a[44:47], v[148:151]\n\t"
    "v_exp_f32 v229, v141\n\t"
    "v_mfma_f32_16x16x32_bf16 v[156:159], a[92:95], a[60:63], v[156:159]\n\t"
    "ds_read_b64_tr_b16 a[148:149], v213 offset:49152\n\t"
    "v_add_f32 v232, v232, v228\n\t"
    "v_mfma_f32_16x16x32_bf16 v[8:11], a[160:163], v[184:187], v[8:11]\n\t"
    "v_exp_f32 v230, v142\n\t"
    "v_mfma_f32_16x16x32_bf16 v[12:15], a[160:163], v[188:191], v[12:15]\n\t"
    "v_add_f32 v232, v232, v229\n\t"
    "v_cvt_pk_bf16_f32 v166, v228, v229\n\t"
    "v_mfma_f32_16x16x32_bf16 v[24:27], a[164:167], v[184:187], v[24:27]\n\t"
    "v_exp_f32 v231, v143\n\t"
    "v_mfma_f32_16x16x32_bf16 v[28:31], a[164:167], v[188:191], v[28:31]\n\t"
    "ds_read_b64_tr_b16 a[150:151], v213 offset:53248\n\t"
    "v_add_f32 v232, v232, v230\n\t"
    "v_mfma_f32_16x16x32_bf16 v[40:43], a[168:171], v[184:187], v[40:43]\n\t"
    "v_exp_f32 v228, v144\n\t"
    "v_mfma_f32_16x16x32_bf16 v[44:47], a[168:171], v[188:191], v[44:47]\n\t"
    "v_add_f32 v232, v232, v231\n\t"
    "v_cvt_pk_bf16_f32 v167, v230, v231\n\t"
    "v_mfma_f32_16x16x32_bf16 v[56:59], a[172:175], v[184:187], v[56:59]\n\t"
    "v_exp_f32 v229, v145\n\t"
    "v_mfma_f32_16x16x32_bf16 v[60:63], a[172:175], v[188:191], v[60:63]\n\t"
    "buffer_load_dwordx4 v225, s[40:43], s57 offen offset:3072 lds\n\t"
    "v_max_f32 v248, v232, v252\n\t"
    "v_mfma_f32_16x16x32_bf16 v[72:75], a[176:179], v[184:187], v[72:75]\n\t"
    "v_exp_f32 v230, v146\n\t"
    "v_mfma_f32_16x16x32_bf16 v[76:79], a[176:179], v[188:191], v[76:79]\n\t"
    "ds_read_b64_tr_b16 a[152:153], v214 offset:49152\n\t"
    "s_add_u32 s57, s57, 0x4000\n\t"
    "s_and_b32 s57, s57, 0x7ffff\n\t"
    "s_cselect_b64 s[40:41], s[40:41], s[78:79]\n\t"
    "v_cmp_lt_f32 vcc, s68, v248\n\t"
    "v_mfma_f32_16x16x32_bf16 v[88:91], a[180:183], v[184:187], v[88:91]\n\t"
    "v_exp_f32 v231, v147\n\t"
    "v_mfma_f32_16x16x32_bf16 v[92:95], a[180:183], v[188:191], v[92:95]\n\t"
    "ds_read_b64_tr_b16 a[154:155], v214 offset:53248\n\t"
    "v_cvt_pk_bf16_f32 v168, v228, v229\n\t"
    "v_mfma_f32_16x16x32_bf16 v[104:107], a[184:187], v[184:187], v[104:107]\n\t"
    "v_exp_f32 v228, v148\n\t"
    "v_mfma_f32_16x16x32_bf16 v[108:111], a[184:187], v[188:191], v[108:111]\n\t"
    "s_cbranch_vccnz L_rs28_%=\n\t"
    "L_ret28_%=:\n\t"
    "ds_read_b64_tr_b16 a[156:157], v215 offset:49152\n\t"
    "v_cvt_pk_bf16_f32 v169, v230, v231\n\t"
    "v_mfma_f32_16x16x32_bf16 v[120:123], a[188:191], v[184:187], v[120:123]\n\t"
    "v_exp_f32 v229, v149\n\t"
    "v_mfma_f32_16x16x32_bf16 v[124:127], a[188:191], v[188:191], v[124:127]\n\t"
    "ds_read_b64_tr_b16 a[158:159], v215 offset:53248\n\t"
    "v_add_f32 v236, v236, v252\n\t"
    "s_waitcnt lgkmcnt(15)\n\t"
    "v_mfma_f32_16x16x32_bf16 v[128:131], a[96:99], a[0:3], v[192:195]\n\t"
    "v_exp_f32 v230, v150\n\t"
    "v_mfma_f32_16x16x32_bf16 v[136:139], a[96:99], a[16:19], v[196:199]\n\t"
    "ds_read_b64_tr_b16 a[160:161], v208 offset:57344\n\t"
    "v_cvt_pk_bf16_f32 v170, v228, v229\n\t"
    "s_waitcnt vmcnt(8)\n\t"
    "s_barrier\n\t"
    "v_mfma_f32_16x16x32_bf16 v[132:135], a[112:115], a[0:3], v[192:195]\n\t"
    "v_exp_f32 v231, v151\n\t"
    "v_mfma_f32_16x16x32_bf16 v[140:143], a[112:115], a[16:19], v[196:199]\n\t"
    "ds_read_b128 a[64:67], v216 offset:0\n\t"
    "v_add_f32 v237, v237, v232\n\t"
    "v_mfma_f32_16x16x32_bf16 v[128:131], a[100:103], a[4:7], v[128:131]\n\t"
    "v_exp_f32 v228, v152\n\t"
    "v_mfma_f32_16x16x32_bf16 v[136:139], a[100:103], a[20:23], v[136:139]\n\t"
    "ds_read_b128 a[80:83], v216 offset:4096\n\t"
    "s_mov_b32 m0, s60\n\t"
    "v_cvt_pk_bf16_f32 v171, v230, v231\n\t"
    "v_mfma_f32_16x16x32_bf16 v[132:135], a[116:119], a[4:7], v[132:135]\n\t"
    "v_exp_f32 v229, v153\n\t"
    "v_mfma_f32_16x16x32_bf16 v[140:143], a[116:119], a[20:23], v[140:143]\n\t"
    "ds_read_b128 a[68:71], v217 offset:0\n\t"
    "v_mfma_f32_16x16x32_bf16 v[252:255], a[252:255], v[168:171], 0\n\t"
    "buffer_load_dwordx4 v220, s[36:39], s56 offen offset:0 lds\n\t"
    "v_mfma_f32_16x16x32_bf16 v[128:131], a[104:107], a[8:11], v[128:131]\n\t"
    "v_exp_f32 v230, v154\n\t"
    "v_mfma_f32_16x16x32_bf16 v[136:139], a[104:107], a[24:27], v[136:139]\n\t"
    "v_add_f32 v233, v228, v229\n\t"
    "v_cvt_pk_bf16_f32 v172, v228, v229\n\t"
    "v_mfma_f32_16x16x32_bf16 v[132:135], a[120:123], a[8:11], v[132:135]\n\t"
    "v_exp_f32 v231, v155\n\t"
    "v_mfma_f32_16x16x32_bf16 v[140:143], a[120:123], a[24:27], v[140:143]\n\t"
    "ds_read_b128 a[84:87], v217 offset:4096\n\t"
    "v_add_f32 v233, v233, v230\n\t"
    "v_mfma_f32_16x16x32_bf16 v[128:131], a[108:111], a[12:15], v[128:131]\n\t"
    "v_exp_f32 v228, v156\n\t"
    "v_mfma_f32_16x16x32_bf16 v[136:139], a[108:111], a[28:31], v[136:139]\n\t"
    "ds_read_b128 a[72:75], v218 offset:0\n\t"
    "v_add_f32 v233, v233, v231\n\t"
    "v_cvt_pk_bf16_f32 v173, v230, v231\n\t"
    "v_mfma_f32_16x16x32_bf16 v[132:135], a[124:127], a[12:15], v[132:135]\n\t"
    "v_exp_f32 v229, v157\n\t"
    "v_mfma_f32_16x16x32_bf16 v[140:143], a[124:127], a[28:31], v[140:143]\n\t"
    "ds_read_b128 a[88:91], v218 offset:4096\n\t"
    "v_add_f32 v233, v233, v228\n\t"
    "s_waitcnt lgkmcnt(7)\n\t"
    "v_mfma_f32_16x16x32_bf16 v[0:3], a[128:131], v[160:163], v[0:3]\n\t"
    "v_exp_f32 v230, v158\n\t"
    "v_mfma_f32_16x16x32_bf16 v[4:7], a[128:131], v[164:167], v[4:7]\n\t"
    "v_add_f32 v233, v233, v229\n\t"
    "v_cvt_pk_bf16_f32 v174, v228, v229\n\t"
    "v_mfma_f32_16x16x32_bf16 v[16:19], a[132:135], v[160:163], v[16:19]\n\t"
    "v_exp_f32 v231, v159\n\t"
    "v_mfma_f32_16x16x32_bf16 v[20:23], a[132:135], v[164:167], v[20:23]\n\t"
    "ds_read_b128 a[76:79], v219 offset:0\n\t"
    "v_add_f32 v233, v233, v230\n\t"
    "v_mfma_f32_16x16x32_bf16 v[32:35], a[136:139], v[160:163], v[32:35]\n\t"
    "v_exp_f32 v228, v128\n\t"
    "v_mfma_f32_16x16x32_bf16 v[36:39], a[136:139], v[164:167], v[36:39]\n\t"
    "v_add_f32 v233, v233, v231\n\t"
    "v_cvt_pk_bf16_f32 v175, v230, v231\n\t"
    "v_mfma_f32_16x16x32_bf16 v[48:51], a[140:143], v[160:163], v[48:51]\n\t"
    "v_exp_f32 v229, v129\n\t"
    "v_mfma_f32_16x16x32_bf16 v[52:55], a[140:143], v[164:167], v[52:55]\n\t"
    "buffer_load_dwordx4 v221, s[36:39], s56 offen offset:1024 lds\n\t"
    "v_max_f32 v248, v233, v252\n\t"
    "v_mfma_f32_16x16x32_bf16 v[64:67], a[144:147], v[160:163], v[64:67]\n\t"
    "v_exp_f32 v230, v130\n\t"
    "v_mfma_f32_16x16x32_bf16 v[68:71], a[144:147], v[164:167], v[68:71]\n\t"
    "ds_read_b128 a[92:95], v219 offset:4096\n\t"
    "v_cmp_lt_f32 vcc, s68, v248\n\t"
    "v_mfma_f32_16x16x32_bf16 v[80:83], a[148:151], v[160:163], v[80:83]\n\t"
    "v_exp_f32 v231, v131\n\t"
    "v_mfma_f32_16x16x32_bf16 v[84:87], a[148:151], v[164:167], v[84:87]\n\t"
    "ds_read_b64_tr_b16 a[162:163], v208 offset:61440\n\t"
    "v_cvt_pk_bf16_f32 v176, v228, v229\n\t"
    "v_mfma_f32_16x16x32_bf16 v[96:99], a[152:155], v[160:163], v[96:99]\n\t"
    "v_exp_f32 v228, v132\n\t"
    "v_mfma_f32_16x16x32_bf16 v[100:103], a[152:155], v[164:167], v[100:103]\n\t"
    "s_cbranch_vccnz L_rs29_%=\n\t"
    "L_ret29_%=:\n\t"
    "ds_read_b64_tr_b16 a[164:165], v209 offset:57344\n\t"
    "v_cvt_pk_bf16_f32 v177, v230, v231\n\t"
    "v_mfma_f32_16x16x32_bf16 v[112:115], a[156:159], v[160:163], v[112:115]\n\t"
    "v_exp_f32 v229, v133\n\t"
    "v_mfma_f32_16x16x32_bf16 v[116:119], a[156:159], v[164:167], v[116:119]\n\t"
    "ds_read_b64_tr_b16 a[166:167], v209 offset:61440\n\t"
    "v_add_f32 v238, v238, v252\n\t"
    "v_mfma_f32_16x16x32_bf16 v[144:147], a[96:99], a[32:35], v[200:203]\n\t"
    "v_exp_f32 v230, v134\n\t"
    "v_mfma_f32_16x16x32_bf16 v[152:155], a[96:99], a[48:51], v[204:207]\n\t"
    "ds_read_b64_tr_b16 a[168:169], v210 offset:57344\n\t"
    "v_cvt_pk_bf16_f32 v178, v228, v229\n\t"
    "v_mfma_f32_16x16x32_bf16 v[148:151], a[112:115], a[32:35], v[200:203]\n\t"
    "v_exp_f32 v231, v135\n\t"
    "v_mfma_f32_16x16x32_bf16 v[156:159], a[112:115], a[48:51], v[204:207]\n\t"
    "ds_read_b64_tr_b16 a[170:171], v210 offset:61440\n\t"
    "v_add_f32 v239, v239, v233\n\t"
    "v_mfma_f32_16x16x32_bf16 v[144:147], a[100:103], a[36:39], v[144:147]\n\t"
    "v_exp_f32 v228, v136\n\t"
    "v_mfma_f32_16x16x32_bf16 v[152:155], a[100:103], a[52:55], v[152:155]\n\t"
    "ds_read_b64_tr_b16 a[172:173], v211 offset:57344\n\t"
    "v_cvt_pk_bf16_f32 v179, v230, v231\n\t"
    "v_mfma_f32_16x16x32_bf16 v[148:151], a[116:119], a[36:39], v[148:151]\n\t"
    "v_exp_f32 v229, v137\n\t"
    "v_mfma_f32_16x16x32_bf16 v[156:159], a[116:119], a[52:55], v[156:159]\n\t"
    "ds_read_b64_tr_b16 a[174:175], v211 offset:61440\n\t"
    "v_mfma_f32_16x16x32_bf16 v[252:255], a[252:255], v[176:179], 0\n\t"
    "buffer_load_dwordx4 v222, s[36:39], s56 offen offset:2048 lds\n\t"
    "v_mfma_f32_16x16x32_bf16 v[144:147], a[104:107], a[40:43], v[144:147]\n\t"
    "v_exp_f32 v230, v138\n\t"
    "v_mfma_f32_16x16x32_bf16 v[152:155], a[104:107], a[56:59], v[152:155]\n\t"
    "v_add_f32 v232, v228, v229\n\t"
    "v_cvt_pk_bf16_f32 v180, v228, v229\n\t"
    "v_mfma_f32_16x16x32_bf16 v[148:151], a[120:123], a[40:43], v[148:151]\n\t"
    "v_exp_f32 v231, v139\n\t"
    "v_mfma_f32_16x16x32_bf16 v[156:159], a[120:123], a[56:59], v[156:159]\n\t"
    "ds_read_b64_tr_b16 a[176:177], v212 offset:57344\n\t"
    "v_add_f32 v232, v232, v230\n\t"
    "v_mfma_f32_16x16x32_bf16 v[144:147], a[108:111], a[44:47], v[144:147]\n\t"
    "v_exp_f32 v228, v140\n\t"
    "v_mfma_f32_16x16x32_bf16 v[152:155], a[108:111], a[60:63], v[152:155]\n\t"
    "ds_read_b64_tr_b16 a[178:179], v212 offset:61440\n\t"
    "v_add_f32 v232, v232, v231\n\t"
    "v_cvt_pk_bf16_f32 v181, v230, v231\n\t"
    "v_mfma_f32_16x16x32_bf16 v[148:151], a[124:127], a[44:47], v[148:151]\n\t"
    "v_exp_f32 v229, v141\n\t"
    "v_mfma_f32_16x16x32_bf16 v[156:159], a[124:127], a[60:63], v[156:159]\n\t"
    "ds_read_b64_tr_b16 a[180:181], v213 offset:57344\n\t"
    "v_add_f32 v232, v232, v228\n\t"
    "v_mfma_f32_16x16x32_bf16 v[8:11], a[128:131], v[168:171], v[8:11]\n\t"
    "v_exp_f32 v230, v142\n\t"
    "v_mfma_f32_16x16x32_bf16 v[12:15], a[128:131], v[172:175], v[12:15]\n\t"
    "v_add_f32 v232, v232, v229\n\t"
    "v_cvt_pk_bf16_f32 v182, v228, v229\n\t"
    "v_mfma_f32_16x16x32_bf16 v[24:27], a[132:135], v[168:171], v[24:27]\n\t"
    "v_exp_f32 v231, v143\n\t"
    "v_mfma_f32_16x16x32_bf16 v[28:31], a[132:135], v[172:175], v[28:31]\n\t"
    "ds_read_b64_tr_b16 a[182:183], v213 offset:61440\n\t"
    "v_add_f32 v232, v232, v230\n\t"
    "v_mfma_f32_16x16x32_bf16 v[40:43], a[136:139], v[168:171], v[40:43]\n\t"
    "v_exp_f32 v228, v144\n\t"
    "v_mfma_f32_16x16x32_bf16 v[44:47], a[136:139], v[172:175], v[44:47]\n\t"
    "v_add_f32 v232, v232, v231\n\t"
    "v_cvt_pk_bf16_f32 v183, v230, v231\n\t"
    "v_mfma_f32_16x16x32_bf16 v[56:59], a[140:143], v[168:171], v[56:59]\n\t"
    "v_exp_f32 v229, v145\n\t"
    "v_mfma_f32_16x16x32_bf16 v[60:63], a[140:143], v[172:175], v[60:63]\n\t"
    "buffer_load_dwordx4 v223, s[36:39], s56 offen offset:3072 lds\n\t"
    "v_max_f32 v248, v232, v252\n\t"
    "v_mfma_f32_16x16x32_bf16 v[72:75], a[144:147], v[168:171], v[72:75]\n\t"
    "v_exp_f32 v230, v146\n\t"
    "v_mfma_f32_16x16x32_bf16 v[76:79], a[144:147], v[172:175], v[76:79]\n\t"
    "ds_read_b64_tr_b16 a[184:185], v214 offset:57344\n\t"
    "s_add_u32 s56, s56, 0x4000\n\t"
    "s_and_b32 s56, s56, 0x7ffff\n\t"
    "s_cselect_b64 s[36:37], s[36:37], s[76:77]\n\t"
    "v_cmp_lt_f32 vcc, s68, v248\n\t"
    "v_mfma_f32_16x16x32_bf16 v[88:91], a[148:151], v[168:171], v[88:91]\n\t"
    "v_exp_f32 v231, v147\n\t"
    "v_mfma_f32_16x16x32_bf16 v[92:95], a[148:151], v[172:175], v[92:95]\n\t"
    "ds_read_b64_tr_b16 a[186:187], v214 offset:61440\n\t"
    "v_cvt_pk_bf16_f32 v184, v228, v229\n\t"
    "v_mfma_f32_16x16x32_bf16 v[104:107], a[152:155], v[168:171], v[104:107]\n\t"
    "v_exp_f32 v228, v148\n\t"
    "v_mfma_f32_16x16x32_bf16 v[108:111], a[152:155], v[172:175], v[108:111]\n\t"
    "s_cbranch_vccnz L_rs30_%=\n\t"
    "L_ret30_%=:\n\t"
    "ds_read_b64_tr_b16 a[188:189], v215 offset:57344\n\t"
    "v_cvt_pk_bf16_f32 v185, v230, v231\n\t"
    "v_mfma_f32_16x16x32_bf16 v[120:123], a[156:159], v[168:171], v[120:123]\n\t"
    "v_exp_f32 v229, v149\n\t"
    "v_mfma_f32_16x16x32_bf16 v[124:127], a[156:159], v[172:175], v[124:127]\n\t"
    "ds_read_b64_tr_b16 a[190:191], v215 offset:61440\n\t"
    "v_add_f32 v236, v236, v252\n\t"
    "s_sub_u32 s66, s66, 1\n\t"
    "s_cmp_lg_u32 s66, 0\n\t"
    "s_cbranch_scc1 L_loop_%=\n\t"
    "v_exp_f32 v230, v150\n\t"
    "v_cvt_pk_bf16_f32 v186, v228, v229\n\t"
    "v_exp_f32 v231, v151\n\t"
    "v_add_f32 v237, v237, v232\n\t"
    "v_exp_f32 v228, v152\n\t"
    "v_cvt_pk_bf16_f32 v187, v230, v231\n\t"
    "v_exp_f32 v229, v153\n\t"
    "s_nop 0\n\t"
    "v_mfma_f32_16x16x32_bf16 v[252:255], a[252:255], v[184:187], 0\n\t"
    "v_exp_f32 v230, v154\n\t"
    "v_add_f32 v233, v228, v229\n\t"
    "v_cvt_pk_bf16_f32 v188, v228, v229\n\t"
    "v_exp_f32 v231, v155\n\t"
    "v_add_f32 v233, v233, v230\n\t"
    "v_exp_f32 v228, v156\n\t"
    "v_add_f32 v233, v233, v231\n\t"
    "v_cvt_pk_bf16_f32 v189, v230, v231\n\t"
    "v_exp_f32 v229, v157\n\t"
    "v_add_f32 v233, v233, v228\n\t"
    "s_waitcnt lgkmcnt(0)\n\t"
    "v_mfma_f32_16x16x32_bf16 v[0:3], a[160:163], v[176:179], v[0:3]\n\t"
    "v_exp_f32 v230, v158\n\t"
    "v_mfma_f32_16x16x32_bf16 v[4:7], a[160:163], v[180:183], v[4:7]\n\t"
    "v_add_f32 v233, v233, v229\n\t"
    "v_cvt_pk_bf16_f32 v190, v228, v229\n\t"
    "v_mfma_f32_16x16x32_bf16 v[16:19], a[164:167], v[176:179], v[16:19]\n\t"
    "v_exp_f32 v231, v159\n\t"
    "v_mfma_f32_16x16x32_bf16 v[20:23], a[164:167], v[180:183], v[20:23]\n\t"
    "v_add_f32 v233, v233, v230\n\t"
    "v_mfma_f32_16x16x32_bf16 v[32:35], a[168:171], v[176:179], v[32:35]\n\t"
    "v_mfma_f32_16x16x32_bf16 v[36:39], a[168:171], v[180:183], v[36:39]\n\t"
    "v_add_f32 v233, v233, v231\n\t"
    "v_cvt_pk_bf16_f32 v191, v230, v231\n\t"
    "v_mfma_f32_16x16x32_bf16 v[48:51], a[172:175], v[176:179], v[48:51]\n\t"
    "v_mfma_f32_16x16x32_bf16 v[52:55], a[172:175], v[180:183], v[52:55]\n\t"
    "v_max_f32 v248, v233, v252\n\t"
    "v_mfma_f32_16x16x32_bf16 v[64:67], a[176:179], v[176:179], v[64:67]\n\t"
    "v_mfma_f32_16x16x32_bf16 v[68:71], a[176:179], v[180:183], v[68:71]\n\t"
    "v_cmp_lt_f32 vcc, s68, v248\n\t"
    "v_mfma_f32_16x16x32_bf16 v[80:83], a[180:183], v[176:179], v[80:83]\n\t"
    "v_mfma_f32_16x16x32_bf16 v[84:87], a[180:183], v[180:183], v[84:87]\n\t"
    "v_mfma_f32_16x16x32_bf16 v[96:99], a[184:187], v[176:179], v[96:99]\n\t"
    "v_mfma_f32_16x16x32_bf16 v[100:103], a[184:187], v[180:183], v[100:103]\n\t"
    "s_cbranch_vccnz L_rs31_%=\n\t"
    "L_ret31_%=:\n\t"
    "v_mfma_f32_16x16x32_bf16 v[112:115], a[188:191], v[176:179], v[112:115]\n\t"
    "v_mfma_f32_16x16x32_bf16 v[116:119], a[188:191], v[180:183], v[116:119]\n\t"
    "v_add_f32 v238, v238, v252\n\t"
    "v_add_f32 v239, v239, v233\n\t"
    "v_rcp_f32 v250, v236\n\t"
    "v_mov_b32 v249, v237\n\t"
    "s_nop 1\n\t"
    "v_permlane32_swap_b32 v237, v249\n\t"
    "v_add_f32 v237, v237, v249\n\t"
    "v_mov_b32 v249, v237\n\t"
    "s_nop 1\n\t"
    "v_permlane16_swap_b32 v237, v249\n\t"
    "v_add_f32 v237, v237, v249\n\t"
    "v_rcp_f32 v251, v237\n\t"
    "v_mul_f32 v0, v0, v250\n\t"
    "v_mul_f32 v1, v1, v250\n\t"
    "v_mul_f32 v2, v2, v250\n\t"
    "v_mul_f32 v3, v3, v250\n\t"
    "v_cvt_pk_bf16_f32 v160, v0, v1\n\t"
    "v_cvt_pk_bf16_f32 v161, v2, v3\n\t"
    "v_mul_f32 v16, v16, v250\n\t"
    "v_mul_f32 v17, v17, v250\n\t"
    "v_mul_f32 v18, v18, v250\n\t"
    "v_mul_f32 v19, v19, v250\n\t"
    "v_cvt_pk_bf16_f32 v162, v16, v17\n\t"
    "v_cvt_pk_bf16_f32 v163, v18, v19\n\t"
    "s_mov_b32 s70, 0\n\t"
    "buffer_store_dwordx4 v[160:163], v226, s[52:55], s70 offen offset:0 sc1\n\t"
    "v_mul_f32 v32, v32, v250\n\t"
    "v_mul_f32 v33, v33, v250\n\t"
    "v_mul_f32 v34, v34, v250\n\t"
    "v_mul_f32 v35, v35, v250\n\t"
    "v_cvt_pk_bf16_f32 v164, v32, v33\n\t"
    "v_cvt_pk_bf16_f32 v165, v34, v35\n\t"
    "v_mul_f32 v48, v48, v250\n\t"
    "v_mul_f32 v49, v49, v250\n\t"
    "v_mul_f32 v50, v50, v250\n\t"
    "v_mul_f32 v51, v51, v250\n\t"
    "v_cvt_pk_bf16_f32 v166, v48, v49\n\t"
    "v_cvt_pk_bf16_f32 v167, v50, v51\n\t"
    "s_mov_b32 s70, 0\n\t"
    "buffer_store_dwordx4 v[164:167], v226, s[52:55], s70 offen offset:1024 sc1\n\t"
    "v_mul_f32 v64, v64, v250\n\t"
    "v_mul_f32 v65, v65, v250\n\t"
    "v_mul_f32 v66, v66, v250\n\t"
    "v_mul_f32 v67, v67, v250\n\t"
    "v_cvt_pk_bf16_f32 v168, v64, v65\n\t"
    "v_cvt_pk_bf16_f32 v169, v66, v67\n\t"
    "v_mul_f32 v80, v80, v250\n\t"
    "v_mul_f32 v81, v81, v250\n\t"
    "v_mul_f32 v82, v82, v250\n\t"
    "v_mul_f32 v83, v83, v250\n\t"
    "v_cvt_pk_bf16_f32 v170, v80, v81\n\t"
    "v_cvt_pk_bf16_f32 v171, v82, v83\n\t"
    "s_mov_b32 s70, 0\n\t"
    "buffer_store_dwordx4 v[168:171], v226, s[52:55], s70 offen offset:2048 sc1\n\t"
    "v_mul_f32 v96, v96, v250\n\t"
    "v_mul_f32 v97, v97, v250\n\t"
    "v_mul_f32 v98, v98, v250\n\t"
    "v_mul_f32 v99, v99, v250\n\t"
    "v_cvt_pk_bf16_f32 v172, v96, v97\n\t"
    "v_cvt_pk_bf16_f32 v173, v98, v99\n\t"
    "v_mul_f32 v112, v112, v250\n\t"
    "v_mul_f32 v113, v113, v250\n\t"
    "v_mul_f32 v114, v114, v250\n\t"
    "v_mul_f32 v115, v115, v250\n\t"
    "v_cvt_pk_bf16_f32 v174, v112, v113\n\t"
    "v_cvt_pk_bf16_f32 v175, v114, v115\n\t"
    "s_mov_b32 s70, 0\n\t"
    "buffer_store_dwordx4 v[172:175], v226, s[52:55], s70 offen offset:3072 sc1\n\t"
    "v_mul_f32 v4, v4, v251\n\t"
    "v_mul_f32 v5, v5, v251\n\t"
    "v_mul_f32 v6, v6, v251\n\t"
    "v_mul_f32 v7, v7, v251\n\t"
    "v_cvt_pk_bf16_f32 v160, v4, v5\n\t"
    "v_cvt_pk_bf16_f32 v161, v6, v7\n\t"
    "v_mul_f32 v20, v20, v251\n\t"
    "v_mul_f32 v21, v21, v251\n\t"
    "v_mul_f32 v22, v22, v251\n\t"
    "v_mul_f32 v23, v23, v251\n\t"
    "v_cvt_pk_bf16_f32 v162, v20, v21\n\t"
    "v_cvt_pk_bf16_f32 v163, v22, v23\n\t"
    "s_mov_b32 s70, 131072\n\t"
    "buffer_store_dwordx4 v[160:163], v226, s[52:55], s70 offen offset:0 sc1\n\t"
    "v_mul_f32 v36, v36, v251\n\t"
    "v_mul_f32 v37, v37, v251\n\t"
    "v_mul_f32 v38, v38, v251\n\t"
    "v_mul_f32 v39, v39, v251\n\t"
    "v_cvt_pk_bf16_f32 v164, v36, v37\n\t"
    "v_cvt_pk_bf16_f32 v165, v38, v39\n\t"
    "v_mul_f32 v52, v52, v251\n\t"
    "v_mul_f32 v53, v53, v251\n\t"
    "v_mul_f32 v54, v54, v251\n\t"
    "v_mfma_f32_16x16x32_bf16 v[8:11], a[160:163], v[184:187], v[8:11]\n\t"
    "v_mfma_f32_16x16x32_bf16 v[12:15], a[160:163], v[188:191], v[12:15]\n\t"
    "v_mul_f32 v55, v55, v251\n\t"
    "v_cvt_pk_bf16_f32 v166, v52, v53\n\t"
    "v_cvt_pk_bf16_f32 v167, v54, v55\n\t"
    "s_mov_b32 s70, 131072\n\t"
    "buffer_store_dwordx4 v[164:167], v226, s[52:55], s70 offen offset:1024 sc1\n\t"
    "v_mfma_f32_16x16x32_bf16 v[24:27], a[164:167], v[184:187], v[24:27]\n\t"
    "v_mfma_f32_16x16x32_bf16 v[28:31], a[164:167], v[188:191], v[28:31]\n\t"
    "v_mul_f32 v68, v68, v251\n\t"
    "v_mul_f32 v69, v69, v251\n\t"
    "v_mul_f32 v70, v70, v251\n\t"
    "v_mul_f32 v71, v71, v251\n\t"
    "v_mfma_f32_16x16x32_bf16 v[40:43], a[168:171], v[184:187], v[40:43]\n\t"
    "v_mfma_f32_16x16x32_bf16 v[44:47], a[168:171], v[188:191], v[44:47]\n\t"
    "v_cvt_pk_bf16_f32 v168, v68, v69\n\t"
    "v_cvt_pk_bf16_f32 v169, v70, v71\n\t"
    "v_mul_f32 v84, v84, v251\n\t"
    "v_mul_f32 v85, v85, v251\n\t"
    "v_mfma_f32_16x16x32_bf16 v[56:59], a[172:175], v[184:187], v[56:59]\n\t"
    "v_mfma_f32_16x16x32_bf16 v[60:63], a[172:175], v[188:191], v[60:63]\n\t"
    "v_mul_f32 v86, v86, v251\n\t"
    "v_mul_f32 v87, v87, v251\n\t"
    "v_cvt_pk_bf16_f32 v170, v84, v85\n\t"
    "v_cvt_pk_bf16_f32 v171, v86, v87\n\t"
    "v_mfma_f32_16x16x32_bf16 v[72:75], a[176:179], v[184:187], v[72:75]\n\t"
    "v_mfma_f32_16x16x32_bf16 v[76:79], a[176:179], v[188:191], v[76:79]\n\t"
    "s_mov_b32 s70, 131072\n\t"
    "buffer_store_dwordx4 v[168:171], v226, s[52:55], s70 offen offset:2048 sc1\n\t"
    "v_mul_f32 v100, v100, v251\n\t"
    "v_mul_f32 v101, v101, v251\n\t"
    "v_mul_f32 v102, v102, v251\n\t"
    "v_mfma_f32_16x16x32_bf16 v[88:91], a[180:183], v[184:187], v[88:91]\n\t"
    "v_mfma_f32_16x16x32_bf16 v[92:95], a[180:183], v[188:191], v[92:95]\n\t"
    "v_mul_f32 v103, v103, v251\n\t"
    "v_cvt_pk_bf16_f32 v172, v100, v101\n\t"
    "v_cvt_pk_bf16_f32 v173, v102, v103\n\t"
    "v_mul_f32 v116, v116, v251\n\t"
    "v_mfma_f32_16x16x32_bf16 v[104:107], a[184:187], v[184:187], v[104:107]\n\t"
    "v_mfma_f32_16x16x32_bf16 v[108:111], a[184:187], v[188:191], v[108:111]\n\t"
    "v_mul_f32 v117, v117, v251\n\t"
    "v_mul_f32 v118, v118, v251\n\t"
    "v_mul_f32 v119, v119, v251\n\t"
    "v_cvt_pk_bf16_f32 v174, v116, v117\n\t"
    "v_mfma_f32_16x16x32_bf16 v[120:123], a[188:191], v[184:187], v[120:123]\n\t"
    "v_mfma_f32_16x16x32_bf16 v[124:127], a[188:191], v[188:191], v[124:127]\n\t"
    "v_cvt_pk_bf16_f32 v175, v118, v119\n\t"
    "s_mov_b32 s70, 131072\n\t"
    "buffer_store_dwordx4 v[172:175], v226, s[52:55], s70 offen offset:3072 sc1\n\t"
    "v_rcp_f32 v250, v238\n\t"
    "v_mov_b32 v249, v239\n\t"
    "s_nop 1\n\t"
    "v_permlane32_swap_b32 v239, v249\n\t"
    "v_add_f32 v239, v239, v249\n\t"
    "v_mov_b32 v249, v239\n\t"
    "s_nop 1\n\t"
    "v_permlane16_swap_b32 v239, v249\n\t"
    "v_add_f32 v239, v239, v249\n\t"
    "v_rcp_f32 v251, v239\n\t"
    "v_mul_f32 v8, v8, v250\n\t"
    "v_mul_f32 v9, v9, v250\n\t"
    "v_mul_f32 v10, v10, v250\n\t"
    "v_mul_f32 v11, v11, v250\n\t"
    "v_cvt_pk_bf16_f32 v160, v8, v9\n\t"
    "v_cvt_pk_bf16_f32 v161, v10, v11\n\t"
    "v_mul_f32 v24, v24, v250\n\t"
    "v_mul_f32 v25, v25, v250\n\t"
    "v_mul_f32 v26, v26, v250\n\t"
    "v_mul_f32 v27, v27, v250\n\t"
    "v_cvt_pk_bf16_f32 v162, v24, v25\n\t"
    "v_cvt_pk_bf16_f32 v163, v26, v27\n\t"
    "s_mov_b32 s70, 262144\n\t"
    "buffer_store_dwordx4 v[160:163], v226, s[52:55], s70 offen offset:0 sc1\n\t"
    "v_mul_f32 v40, v40, v250\n\t"
    "v_mul_f32 v41, v41, v250\n\t"
    "v_mul_f32 v42, v42, v250\n\t"
    "v_mul_f32 v43, v43, v250\n\t"
    "v_cvt_pk_bf16_f32 v164, v40, v41\n\t"
    "v_cvt_pk_bf16_f32 v165, v42, v43\n\t"
    "v_mul_f32 v56, v56, v250\n\t"
    "v_mul_f32 v57, v57, v250\n\t"
    "v_mul_f32 v58, v58, v250\n\t"
    "v_mul_f32 v59, v59, v250\n\t"
    "v_cvt_pk_bf16_f32 v166, v56, v57\n\t"
    "v_cvt_pk_bf16_f32 v167, v58, v59\n\t"
    "s_mov_b32 s70, 262144\n\t"
    "buffer_store_dwordx4 v[164:167], v226, s[52:55], s70 offen offset:1024 sc1\n\t"
    "v_mul_f32 v72, v72, v250\n\t"
    "v_mul_f32 v73, v73, v250\n\t"
    "v_mul_f32 v74, v74, v250\n\t"
    "v_mul_f32 v75, v75, v250\n\t"
    "v_cvt_pk_bf16_f32 v168, v72, v73\n\t"
    "v_cvt_pk_bf16_f32 v169, v74, v75\n\t"
    "v_mul_f32 v88, v88, v250\n\t"
    "v_mul_f32 v89, v89, v250\n\t"
    "v_mul_f32 v90, v90, v250\n\t"
    "v_mul_f32 v91, v91, v250\n\t"
    "v_cvt_pk_bf16_f32 v170, v88, v89\n\t"
    "v_cvt_pk_bf16_f32 v171, v90, v91\n\t"
    "s_mov_b32 s70, 262144\n\t"
    "buffer_store_dwordx4 v[168:171], v226, s[52:55], s70 offen offset:2048 sc1\n\t"
    "v_mul_f32 v104, v104, v250\n\t"
    "v_mul_f32 v105, v105, v250\n\t"
    "v_mul_f32 v106, v106, v250\n\t"
    "v_mul_f32 v107, v107, v250\n\t"
    "v_cvt_pk_bf16_f32 v172, v104, v105\n\t"
    "v_cvt_pk_bf16_f32 v173, v106, v107\n\t"
    "v_mul_f32 v120, v120, v250\n\t"
    "v_mul_f32 v121, v121, v250\n\t"
    "v_mul_f32 v122, v122, v250\n\t"
    "v_mul_f32 v123, v123, v250\n\t"
    "v_cvt_pk_bf16_f32 v174, v120, v121\n\t"
    "v_cvt_pk_bf16_f32 v175, v122, v123\n\t"
    "s_mov_b32 s70, 262144\n\t"
    "buffer_store_dwordx4 v[172:175], v226, s[52:55], s70 offen offset:3072 sc1\n\t"
    "v_mul_f32 v12, v12, v251\n\t"
    "v_mul_f32 v13, v13, v251\n\t"
    "v_mul_f32 v14, v14, v251\n\t"
    "v_mul_f32 v15, v15, v251\n\t"
    "v_cvt_pk_bf16_f32 v160, v12, v13\n\t"
    "v_cvt_pk_bf16_f32 v161, v14, v15\n\t"
    "v_mul_f32 v28, v28, v251\n\t"
    "v_mul_f32 v29, v29, v251\n\t"
    "v_mul_f32 v30, v30, v251\n\t"
    "v_mul_f32 v31, v31, v251\n\t"
    "v_cvt_pk_bf16_f32 v162, v28, v29\n\t"
    "v_cvt_pk_bf16_f32 v163, v30, v31\n\t"
    "s_mov_b32 s70, 393216\n\t"
    "buffer_store_dwordx4 v[160:163], v226, s[52:55], s70 offen offset:0 sc1\n\t"
    "v_mul_f32 v44, v44, v251\n\t"
    "v_mul_f32 v45, v45, v251\n\t"
    "v_mul_f32 v46, v46, v251\n\t"
    "v_mul_f32 v47, v47, v251\n\t"
    "v_cvt_pk_bf16_f32 v164, v44, v45\n\t"
    "v_cvt_pk_bf16_f32 v165, v46, v47\n\t"
    "v_mul_f32 v60, v60, v251\n\t"
    "v_mul_f32 v61, v61, v251\n\t"
    "v_mul_f32 v62, v62, v251\n\t"
    "v_mul_f32 v63, v63, v251\n\t"
    "v_cvt_pk_bf16_f32 v166, v60, v61\n\t"
    "v_cvt_pk_bf16_f32 v167, v62, v63\n\t"
    "s_mov_b32 s70, 393216\n\t"
    "buffer_store_dwordx4 v[164:167], v226, s[52:55], s70 offen offset:1024 sc1\n\t"
    "v_mul_f32 v76, v76, v251\n\t"
    "v_mul_f32 v77, v77, v251\n\t"
    "v_mul_f32 v78, v78, v251\n\t"
    "v_mul_f32 v79, v79, v251\n\t"
    "v_cvt_pk_bf16_f32 v168, v76, v77\n\t"
    "v_cvt_pk_bf16_f32 v169, v78, v79\n\t"
    "v_mul_f32 v92, v92, v251\n\t"
    "v_mul_f32 v93, v93, v251\n\t"
    "v_mul_f32 v94, v94, v251\n\t"
    "v_mul_f32 v95, v95, v251\n\t"
    "v_cvt_pk_bf16_f32 v170, v92, v93\n\t"
    "v_cvt_pk_bf16_f32 v171, v94, v95\n\t"
    "s_mov_b32 s70, 393216\n\t"
    "buffer_store_dwordx4 v[168:171], v226, s[52:55], s70 offen offset:2048 sc1\n\t"
    "v_mul_f32 v108, v108, v251\n\t"
    "v_mul_f32 v109, v109, v251\n\t"
    "v_mul_f32 v110, v110, v251\n\t"
    "v_mul_f32 v111, v111, v251\n\t"
    "v_cvt_pk_bf16_f32 v172, v108, v109\n\t"
    "v_cvt_pk_bf16_f32 v173, v110, v111\n\t"
    "v_mul_f32 v124, v124, v251\n\t"
    "v_mul_f32 v125, v125, v251\n\t"
    "v_mul_f32 v126, v126, v251\n\t"
    "v_mul_f32 v127, v127, v251\n\t"
    "v_cvt_pk_bf16_f32 v174, v124, v125\n\t"
    "v_cvt_pk_bf16_f32 v175, v126, v127\n\t"
    "s_mov_b32 s70, 393216\n\t"
    "buffer_store_dwordx4 v[172:175], v226, s[52:55], s70 offen offset:3072 sc1\n\t"
    "s_waitcnt vmcnt(0) lgkmcnt(0)\n\t"
    "s_barrier\n\t"
    "s_branch L_end_%=\n\t"
    "L_rs0_%=:\n\t"
    "s_mov_b32 s67, 0\n\t"
    "s_branch L_rare0_%=\n\t"
    "L_rs1_%=:\n\t"
    "s_mov_b32 s67, 1\n\t"
    "s_branch L_rare1_%=\n\t"
    "L_rs2_%=:\n\t"
    "s_mov_b32 s67, 2\n\t"
    "s_branch L_rare2_%=\n\t"
    "L_rs3_%=:\n\t"
    "s_mov_b32 s67, 3\n\t"
    "s_branch L_rare3_%=\n\t"
    "L_rs4_%=:\n\t"
    "s_mov_b32 s67, 4\n\t"
    "s_branch L_rare0_%=\n\t"
    "L_rs5_%=:\n\t"
    "s_mov_b32 s67, 5\n\t"
    "s_branch L_rare1_%=\n\t"
    "L_rs6_%=:\n\t"
    "s_mov_b32 s67, 6\n\t"
    "s_branch L_rare2_%=\n\t"
    "L_rs7_%=:\n\t"
    "s_mov_b32 s67, 7\n\t"
    "s_branch L_rare3_%=\n\t"
    "L_rs8_%=:\n\t"
    "s_mov_b32 s67, 8\n\t"
    "s_branch L_rare0_%=\n\t"
    "L_rs9_%=:\n\t"
    "s_mov_b32 s67, 9\n\t"
    "s_branch L_rare1_%=\n\t"
    "L_rs10_%=:\n\t"
    "s_mov_b32 s67, 10\n\t"
    "s_branch L_rare2_%=\n\t"
    "L_rs11_%=:\n\t"
    "s_mov_b32 s67, 11\n\t"
    "s_branch L_rare3_%=\n\t"
    "L_rs12_%=:\n\t"
    "s_mov_b32 s67, 12\n\t"
    "s_branch L_rare0_%=\n\t"
    "L_rs13_%=:\n\t"
    "s_mov_b32 s67, 13\n\t"
    "s_branch L_rare1_%=\n\t"
    "L_rs14_%=:\n\t"
    "s_mov_b32 s67, 14\n\t"
    "s_branch L_rare2_%=\n\t"
    "L_rs15_%=:\n\t"
    "s_mov_b32 s67, 15\n\t"
    "s_branch L_rare3_%=\n\t"
    "L_rs16_%=:\n\t"
    "s_mov_b32 s67, 16\n\t"
    "s_branch L_rare0_%=\n\t"
    "L_rs17_%=:\n\t"
    "s_mov_b32 s67, 17\n\t"
    "s_branch L_rare1_%=\n\t"
    "L_rs18_%=:\n\t"
    "s_mov_b32 s67, 18\n\t"
    "s_branch L_rare2_%=\n\t"
    "L_rs19_%=:\n\t"
    "s_mov_b32 s67, 19\n\t"
    "s_branch L_rare3_%=\n\t"
    "L_rs20_%=:\n\t"
    "s_mov_b32 s67, 20\n\t"
    "s_branch L_rare0_%=\n\t"
    "L_rs21_%=:\n\t"
    "s_mov_b32 s67, 21\n\t"
    "s_branch L_rare1_%=\n\t"
    "L_rs22_%=:\n\t"
    "s_mov_b32 s67, 22\n\t"
    "s_branch L_rare2_%=\n\t"
    "L_rs23_%=:\n\t"
    "s_mov_b32 s67, 23\n\t"
    "s_branch L_rare3_%=\n\t"
    "L_rs24_%=:\n\t"
    "s_mov_b32 s67, 24\n\t"
    "s_branch L_rare0_%=\n\t"
    "L_rs25_%=:\n\t"
    "s_mov_b32 s67, 25\n\t"
    "s_branch L_rare1_%=\n\t"
    "L_rs26_%=:\n\t"
    "s_mov_b32 s67, 26\n\t"
    "s_branch L_rare2_%=\n\t"
    "L_rs27_%=:\n\t"
    "s_mov_b32 s67, 27\n\t"
    "s_branch L_rare3_%=\n\t"
    "L_rs28_%=:\n\t"
    "s_mov_b32 s67, 28\n\t"
    "s_branch L_rare0_%=\n\t"
    "L_rs29_%=:\n\t"
    "s_mov_b32 s67, 29\n\t"
    "s_branch L_rare1_%=\n\t"
    "L_rs30_%=:\n\t"
    "s_mov_b32 s67, 30\n\t"
    "s_branch L_rare2_%=\n\t"
    "L_rs31_%=:\n\t"
    "s_mov_b32 s67, 31\n\t"
    "s_branch L_rare3_%=\n\t"
    "L_rare0_%=:\n\t"
    "v_max3_f32 v240, v128, v129, v130\n\t"
    "v_max3_f32 v240, v240, v131, v132\n\t"
    "v_max3_f32 v240, v240, v133, v134\n\t"
    "v_max_f32 v240, v240, v135\n\t"
    "v_mov_b32 v241, v240\n\t"
    "s_nop 1\n\t"
    "v_permlane32_swap_b32 v240, v241\n\t"
    "v_max_f32 v240, v240, v241\n\t"
    "v_mov_b32 v241, v240\n\t"
    "s_nop 1\n\t"
    "v_permlane16_swap_b32 v240, v241\n\t"
    "v_max_f32 v240, v240, v241\n\t"
    "v_add_f32 v242, s69, v240\n\t"
    "v_max_f32 v242, 0, v242\n\t"
    "v_exp_f32 v243, -v242\n\t"
    "v_sub_f32 v192, v192, v242\n\t"
    "v_sub_f32 v193, v193, v242\n\t"
    "v_sub_f32 v194, v194, v242\n\t"
    "v_sub_f32 v195, v195, v242\n\t"
    "v_sub_f32 v244, v128, v242\n\t"
    "v_sub_f32 v245, v129, v242\n\t"
    "v_exp_f32 v244, v244\n\t"
    "v_exp_f32 v245, v245\n\t"
    "s_nop 0\n\t"
    "v_cvt_pk_bf16_f32 v160, v244, v245\n\t"
    "v_sub_f32 v244, v130, v242\n\t"
    "v_sub_f32 v245, v131, v242\n\t"
    "v_exp_f32 v244, v244\n\t"
    "v_exp_f32 v245, v245\n\t"
    "s_nop 0\n\t"
    "v_cvt_pk_bf16_f32 v161, v244, v245\n\t"
    "v_sub_f32 v244, v132, v242\n\t"
    "v_sub_f32 v245, v133, v242\n\t"
    "v_exp_f32 v244, v244\n\t"
    "v_exp_f32 v245, v245\n\t"
    "s_nop 0\n\t"
    "v_cvt_pk_bf16_f32 v162, v244, v245\n\t"
    "v_sub_f32 v244, v134, v242\n\t"
    "v_sub_f32 v245, v135, v242\n\t"
    "v_exp_f32 v244, v244\n\t"
    "v_exp_f32 v245, v245\n\t"
    "s_nop 0\n\t"
    "v_cvt_pk_bf16_f32 v163, v244, v245\n\t"
    "s_nop 1\n\t"
    "v_mfma_f32_16x16x32_bf16 v[252:255], a[252:255], v[160:163], 0\n\t"
    "v_mul_f32 v0, v0, v243\n\t"
    "v_mul_f32 v1, v1, v243\n\t"
    "v_mul_f32 v2, v2, v243\n\t"
    "v_mul_f32 v3, v3, v243\n\t"
    "v_mul_f32 v16, v16, v243\n\t"
    "v_mul_f32 v17, v17, v243\n\t"
    "v_mul_f32 v18, v18, v243\n\t"
    "v_mul_f32 v19, v19, v243\n\t"
    "v_mul_f32 v32, v32, v243\n\t"
    "v_mul_f32 v33, v33, v243\n\t"
    "v_mul_f32 v34, v34, v243\n\t"
    "v_mul_f32 v35, v35, v243\n\t"
    "v_mul_f32 v48, v48, v243\n\t"
    "v_mul_f32 v49, v49, v243\n\t"
    "v_mul_f32 v50, v50, v243\n\t"
    "v_mul_f32 v51, v51, v243\n\t"
    "v_mul_f32 v64, v64, v243\n\t"
    "v_mul_f32 v65, v65, v243\n\t"
    "v_mul_f32 v66, v66, v243\n\t"
    "v_mul_f32 v67, v67, v243\n\t"
    "v_mul_f32 v80, v80, v243\n\t"
    "v_mul_f32 v81, v81, v243\n\t"
    "v_mul_f32 v82, v82, v243\n\t"
    "v_mul_f32 v83, v83, v243\n\t"
    "v_mul_f32 v96, v96, v243\n\t"
    "v_mul_f32 v97, v97, v243\n\t"
    "v_mul_f32 v98, v98, v243\n\t"
    "v_mul_f32 v99, v99, v243\n\t"
    "v_mul_f32 v112, v112, v243\n\t"
    "v_mul_f32 v113, v113, v243\n\t"
    "v_mul_f32 v114, v114, v243\n\t"
    "v_mul_f32 v115, v115, v243\n\t"
    "v_mul_f32 v236, v236, v243\n\t"
    "v_max3_f32 v240, v136, v137, v138\n\t"
    "v_max3_f32 v240, v240, v139, v140\n\t"
    "v_max3_f32 v240, v240, v141, v142\n\t"
    "v_max_f32 v240, v240, v143\n\t"
    "v_mov_b32 v241, v240\n\t"
    "s_nop 1\n\t"
    "v_permlane32_swap_b32 v240, v241\n\t"
    "v_max_f32 v240, v240, v241\n\t"
    "v_mov_b32 v241, v240\n\t"
    "s_nop 1\n\t"
    "v_permlane16_swap_b32 v240, v241\n\t"
    "v_max_f32 v240, v240, v241\n\t"
    "v_add_f32 v242, s69, v240\n\t"
    "v_max_f32 v242, 0, v242\n\t"
    "v_exp_f32 v243, -v242\n\t"
    "v_sub_f32 v196, v196, v242\n\t"
    "v_sub_f32 v197, v197, v242\n\t"
    "v_sub_f32 v198, v198, v242\n\t"
    "v_sub_f32 v199, v199, v242\n\t"
    "v_sub_f32 v244, v136, v242\n\t"
    "v_sub_f32 v245, v137, v242\n\t"
    "v_exp_f32 v244, v244\n\t"
    "v_exp_f32 v245, v245\n\t"
    "s_nop 0\n\t"
    "v_add_f32 v232, v244, v245\n\t"
    "v_cvt_pk_bf16_f32 v164, v244, v245\n\t"
    "v_sub_f32 v244, v138, v242\n\t"
    "v_sub_f32 v245, v139, v242\n\t"
    "v_exp_f32 v244, v244\n\t"
    "v_exp_f32 v245, v245\n\t"
    "s_nop 0\n\t"
    "v_add_f32 v232, v232, v244\n\t"
    "v_add_f32 v232, v232, v245\n\t"
    "v_cvt_pk_bf16_f32 v165, v244, v245\n\t"
    "v_sub_f32 v244, v140, v242\n\t"
    "v_sub_f32 v245, v141, v242\n\t"
    "v_exp_f32 v244, v244\n\t"
    "v_exp_f32 v245, v245\n\t"
    "s_nop 0\n\t"
    "v_add_f32 v232, v232, v244\n\t"
    "v_add_f32 v232, v232, v245\n\t"
    "v_cvt_pk_bf16_f32 v166, v244, v245\n\t"
    "v_sub_f32 v244, v142, v242\n\t"
    "v_sub_f32 v245, v143, v242\n\t"
    "v_exp_f32 v244, v244\n\t"
    "v_exp_f32 v245, v245\n\t"
    "s_nop 0\n\t"
    "v_add_f32 v232, v232, v244\n\t"
    "v_add_f32 v232, v232, v245\n\t"
    "v_cvt_pk_bf16_f32 v167, v244, v245\n\t"
    "v_mul_f32 v4, v4, v243\n\t"
    "v_mul_f32 v5, v5, v243\n\t"
    "v_mul_f32 v6, v6, v243\n\t"
    "v_mul_f32 v7, v7, v243\n\t"
    "v_mul_f32 v20, v20, v243\n\t"
    "v_mul_f32 v21, v21, v243\n\t"
    "v_mul_f32 v22, v22, v243\n\t"
    "v_mul_f32 v23, v23, v243\n\t"
    "v_mul_f32 v36, v36, v243\n\t"
    "v_mul_f32 v37, v37, v243\n\t"
    "v_mul_f32 v38, v38, v243\n\t"
    "v_mul_f32 v39, v39, v243\n\t"
    "v_mul_f32 v52, v52, v243\n\t"
    "v_mul_f32 v53, v53, v243\n\t"
    "v_mul_f32 v54, v54, v243\n\t"
    "v_mul_f32 v55, v55, v243\n\t"
    "v_mul_f32 v68, v68, v243\n\t"
    "v_mul_f32 v69, v69, v243\n\t"
    "v_mul_f32 v70, v70, v243\n\t"
    "v_mul_f32 v71, v71, v243\n\t"
    "v_mul_f32 v84, v84, v243\n\t"
    "v_mul_f32 v85, v85, v243\n\t"
    "v_mul_f32 v86, v86, v243\n\t"
    "v_mul_f32 v87, v87, v243\n\t"
    "v_mul_f32 v100, v100, v243\n\t"
    "v_mul_f32 v101, v101, v243\n\t"
    "v_mul_f32 v102, v102, v243\n\t"
    "v_mul_f32 v103, v103, v243\n\t"
    "v_mul_f32 v116, v116, v243\n\t"
    "v_mul_f32 v117, v117, v243\n\t"
    "v_mul_f32 v118, v118, v243\n\t"
    "v_mul_f32 v119, v119, v243\n\t"
    "v_mul_f32 v237, v237, v243\n\t"
    "s_nop 1\n\t"
    "s_cmp_eq_u32 s67, 0\n\t"
    "s_cbranch_scc1 L_ret0_%=\n\t"
    "s_cmp_eq_u32 s67, 4\n\t"
    "s_cbranch_scc1 L_ret4_%=\n\t"
    "s_cmp_eq_u32 s67, 8\n\t"
    "s_cbranch_scc1 L_ret8_%=\n\t"
    "s_cmp_eq_u32 s67, 12\n\t"
    "s_cbranch_scc1 L_ret12_%=\n\t"
    "s_cmp_eq_u32 s67, 16\n\t"
    "s_cbranch_scc1 L_ret16_%=\n\t"
    "s_cmp_eq_u32 s67, 20\n\t"
    "s_cbranch_scc1 L_ret20_%=\n\t"
    "s_cmp_eq_u32 s67, 24\n\t"
    "s_cbranch_scc1 L_ret24_%=\n\t"
    "s_branch L_ret28_%=\n\t"
    "L_rare1_%=:\n\t"
    "v_max3_f32 v240, v144, v145, v146\n\t"
    "v_max3_f32 v240, v240, v147, v148\n\t"
    "v_max3_f32 v240, v240, v149, v150\n\t"
    "v_max_f32 v240, v240, v151\n\t"
    "v_mov_b32 v241, v240\n\t"
    "s_nop 1\n\t"
    "v_permlane32_swap_b32 v240, v241\n\t"
    "v_max_f32 v240, v240, v241\n\t"
    "v_mov_b32 v241, v240\n\t"
    "s_nop 1\n\t"
    "v_permlane16_swap_b32 v240, v241\n\t"
    "v_max_f32 v240, v240, v241\n\t"
    "v_add_f32 v242, s69, v240\n\t"
    "v_max_f32 v242, 0, v242\n\t"
    "v_exp_f32 v243, -v242\n\t"
    "v_sub_f32 v200, v200, v242\n\t"
    "v_sub_f32 v201, v201, v242\n\t"
    "v_sub_f32 v202, v202, v242\n\t"
    "v_sub_f32 v203, v203, v242\n\t"
    "v_sub_f32 v244, v144, v242\n\t"
    "v_sub_f32 v245, v145, v242\n\t"
    "v_exp_f32 v244, v244\n\t"
    "v_exp_f32 v245, v245\n\t"
    "s_nop 0\n\t"
    "v_cvt_pk_bf16_f32 v168, v244, v245\n\t"
    "v_sub_f32 v244, v146, v242\n\t"
    "v_sub_f32 v245, v147, v242\n\t"
    "v_exp_f32 v244, v244\n\t"
    "v_exp_f32 v245, v245\n\t"
    "s_nop 0\n\t"
    "v_cvt_pk_bf16_f32 v169, v244, v245\n\t"
    "v_sub_f32 v244, v148, v242\n\t"
    "v_sub_f32 v245, v149, v242\n\t"
    "v_exp_f32 v244, v244\n\t"
    "v_exp_f32 v245, v245\n\t"
    "s_nop 0\n\t"
    "v_cvt_pk_bf16_f32 v170, v244, v245\n\t"
    "v_sub_f32 v244, v150, v242\n\t"
    "v_sub_f32 v245, v151, v242\n\t"
    "v_exp_f32 v244, v244\n\t"
    "v_exp_f32 v245, v245\n\t"
    "s_nop 0\n\t"
    "v_cvt_pk_bf16_f32 v171, v244, v245\n\t"
    "s_nop 1\n\t"
    "v_mfma_f32_16x16x32_bf16 v[252:255], a[252:255], v[168:171], 0\n\t"
    "v_mul_f32 v8, v8, v243\n\t"
    "v_mul_f32 v9, v9, v243\n\t"
    "v_mul_f32 v10, v10, v243\n\t"
    "v_mul_f32 v11, v11, v243\n\t"
    "v_mul_f32 v24, v24, v243\n\t"
    "v_mul_f32 v25, v25, v243\n\t"
    "v_mul_f32 v26, v26, v243\n\t"
    "v_mul_f32 v27, v27, v243\n\t"
    "v_mul_f32 v40, v40, v243\n\t"
    "v_mul_f32 v41, v41, v243\n\t"
    "v_mul_f32 v42, v42, v243\n\t"
    "v_mul_f32 v43, v43, v243\n\t"
    "v_mul_f32 v56, v56, v243\n\t"
    "v_mul_f32 v57, v57, v243\n\t"
    "v_mul_f32 v58, v58, v243\n\t"
    "v_mul_f32 v59, v59, v243\n\t"
    "v_mul_f32 v72, v72, v243\n\t"
    "v_mul_f32 v73, v73, v243\n\t"
    "v_mul_f32 v74, v74, v243\n\t"
    "v_mul_f32 v75, v75, v243\n\t"
    "v_mul_f32 v88, v88, v243\n\t"
    "v_mul_f32 v89, v89, v243\n\t"
    "v_mul_f32 v90, v90, v243\n\t"
    "v_mul_f32 v91, v91, v243\n\t"
    "v_mul_f32 v104, v104, v243\n\t"
    "v_mul_f32 v105, v105, v243\n\t"
    "v_mul_f32 v106, v106, v243\n\t"
    "v_mul_f32 v107, v107, v243\n\t"
    "v_mul_f32 v120, v120, v243\n\t"
    "v_mul_f32 v121, v121, v243\n\t"
    "v_mul_f32 v122, v122, v243\n\t"
    "v_mul_f32 v123, v123, v243\n\t"
    "v_mul_f32 v238, v238, v243\n\t"
    "v_max3_f32 v240, v152, v153, v154\n\t"
    "v_max3_f32 v240, v240, v155, v156\n\t"
    "v_max3_f32 v240, v240, v157, v158\n\t"
    "v_max_f32 v240, v240, v159\n\t"
    "v_mov_b32 v241, v240\n\t"
    "s_nop 1\n\t"
    "v_permlane32_swap_b32 v240, v241\n\t"
    "v_max_f32 v240, v240, v241\n\t"
    "v_mov_b32 v241, v240\n\t"
    "s_nop 1\n\t"
    "v_permlane16_swap_b32 v240, v241\n\t"
    "v_max_f32 v240, v240, v241\n\t"
    "v_add_f32 v242, s69, v240\n\t"
    "v_max_f32 v242, 0, v242\n\t"
    "v_exp_f32 v243, -v242\n\t"
    "v_sub_f32 v204, v204, v242\n\t"
    "v_sub_f32 v205, v205, v242\n\t"
    "v_sub_f32 v206, v206, v242\n\t"
    "v_sub_f32 v207, v207, v242\n\t"
    "v_sub_f32 v244, v152, v242\n\t"
    "v_sub_f32 v245, v153, v242\n\t"
    "v_exp_f32 v244, v244\n\t"
    "v_exp_f32 v245, v245\n\t"
    "s_nop 0\n\t"
    "v_add_f32 v233, v244, v245\n\t"
    "v_cvt_pk_bf16_f32 v172, v244, v245\n\t"
    "v_sub_f32 v244, v154, v242\n\t"
    "v_sub_f32 v245, v155, v242\n\t"
    "v_exp_f32 v244, v244\n\t"
    "v_exp_f32 v245, v245\n\t"
    "s_nop 0\n\t"
    "v_add_f32 v233, v233, v244\n\t"
    "v_add_f32 v233, v233, v245\n\t"
    "v_cvt_pk_bf16_f32 v173, v244, v245\n\t"
    "v_sub_f32 v244, v156, v242\n\t"
    "v_sub_f32 v245, v157, v242\n\t"
    "v_exp_f32 v244, v244\n\t"
    "v_exp_f32 v245, v245\n\t"
    "s_nop 0\n\t"
    "v_add_f32 v233, v233, v244\n\t"
    "v_add_f32 v233, v233, v245\n\t"
    "v_cvt_pk_bf16_f32 v174, v244, v245\n\t"
    "v_sub_f32 v244, v158, v242\n\t"
    "v_sub_f32 v245, v159, v242\n\t"
    "v_exp_f32 v244, v244\n\t"
    "v_exp_f32 v245, v245\n\t"
    "s_nop 0\n\t"
    "v_add_f32 v233, v233, v244\n\t"
    "v_add_f32 v233, v233, v245\n\t"
    "v_cvt_pk_bf16_f32 v175, v244, v245\n\t"
    "v_mul_f32 v12, v12, v243\n\t"
    "v_mul_f32 v13, v13, v243\n\t"
    "v_mul_f32 v14, v14, v243\n\t"
    "v_mul_f32 v15, v15, v243\n\t"
    "v_mul_f32 v28, v28, v243\n\t"
    "v_mul_f32 v29, v29, v243\n\t"
    "v_mul_f32 v30, v30, v243\n\t"
    "v_mul_f32 v31, v31, v243\n\t"
    "v_mul_f32 v44, v44, v243\n\t"
    "v_mul_f32 v45, v45, v243\n\t"
    "v_mul_f32 v46, v46, v243\n\t"
    "v_mul_f32 v47, v47, v243\n\t"
    "v_mul_f32 v60, v60, v243\n\t"
    "v_mul_f32 v61, v61, v243\n\t"
    "v_mul_f32 v62, v62, v243\n\t"
    "v_mul_f32 v63, v63, v243\n\t"
    "v_mul_f32 v76, v76, v243\n\t"
    "v_mul_f32 v77, v77, v243\n\t"
    "v_mul_f32 v78, v78, v243\n\t"
    "v_mul_f32 v79, v79, v243\n\t"
    "v_mul_f32 v92, v92, v243\n\t"
    "v_mul_f32 v93, v93, v243\n\t"
    "v_mul_f32 v94, v94, v243\n\t"
    "v_mul_f32 v95, v95, v243\n\t"
    "v_mul_f32 v108, v108, v243\n\t"
    "v_mul_f32 v109, v109, v243\n\t"
    "v_mul_f32 v110, v110, v243\n\t"
    "v_mul_f32 v111, v111, v243\n\t"
    "v_mul_f32 v124, v124, v243\n\t"
    "v_mul_f32 v125, v125, v243\n\t"
    "v_mul_f32 v126, v126, v243\n\t"
    "v_mul_f32 v127, v127, v243\n\t"
    "v_mul_f32 v239, v239, v243\n\t"
    "s_nop 1\n\t"
    "s_cmp_eq_u32 s67, 1\n\t"
    "s_cbranch_scc1 L_ret1_%=\n\t"
    "s_cmp_eq_u32 s67, 5\n\t"
    "s_cbranch_scc1 L_ret5_%=\n\t"
    "s_cmp_eq_u32 s67, 9\n\t"
    "s_cbranch_scc1 L_ret9_%=\n\t"
    "s_cmp_eq_u32 s67, 13\n\t"
    "s_cbranch_scc1 L_ret13_%=\n\t"
    "s_cmp_eq_u32 s67, 17\n\t"
    "s_cbranch_scc1 L_ret17_%=\n\t"
    "s_cmp_eq_u32 s67, 21\n\t"
    "s_cbranch_scc1 L_ret21_%=\n\t"
    "s_cmp_eq_u32 s67, 25\n\t"
    "s_cbranch_scc1 L_ret25_%=\n\t"
    "s_branch L_ret29_%=\n\t"
    "L_rare2_%=:\n\t"
    "v_max3_f32 v240, v128, v129, v130\n\t"
    "v_max3_f32 v240, v240, v131, v132\n\t"
    "v_max3_f32 v240, v240, v133, v134\n\t"
    "v_max_f32 v240, v240, v135\n\t"
    "v_mov_b32 v241, v240\n\t"
    "s_nop 1\n\t"
    "v_permlane32_swap_b32 v240, v241\n\t"
    "v_max_f32 v240, v240, v241\n\t"
    "v_mov_b32 v241, v240\n\t"
    "s_nop 1\n\t"
    "v_permlane16_swap_b32 v240, v241\n\t"
    "v_max_f32 v240, v240, v241\n\t"
    "v_add_f32 v242, s69, v240\n\t"
    "v_max_f32 v242, 0, v242\n\t"
    "v_exp_f32 v243, -v242\n\t"
    "v_sub_f32 v192, v192, v242\n\t"
    "v_sub_f32 v193, v193, v242\n\t"
    "v_sub_f32 v194, v194, v242\n\t"
    "v_sub_f32 v195, v195, v242\n\t"
    "v_sub_f32 v244, v128, v242\n\t"
    "v_sub_f32 v245, v129, v242\n\t"
    "v_exp_f32 v244, v244\n\t"
    "v_exp_f32 v245, v245\n\t"
    "s_nop 0\n\t"
    "v_cvt_pk_bf16_f32 v176, v244, v245\n\t"
    "v_sub_f32 v244, v130, v242\n\t"
    "v_sub_f32 v245, v131, v242\n\t"
    "v_exp_f32 v244, v244\n\t"
    "v_exp_f32 v245, v245\n\t"
    "s_nop 0\n\t"
    "v_cvt_pk_bf16_f32 v177, v244, v245\n\t"
    "v_sub_f32 v244, v132, v242\n\t"
    "v_sub_f32 v245, v133, v242\n\t"
    "v_exp_f32 v244, v244\n\t"
    "v_exp_f32 v245, v245\n\t"
    "s_nop 0\n\t"
    "v_cvt_pk_bf16_f32 v178, v244, v245\n\t"
    "v_sub_f32 v244, v134, v242\n\t"
    "v_sub_f32 v245, v135, v242\n\t"
    "v_exp_f32 v244, v244\n\t"
    "v_exp_f32 v245, v245\n\t"
    "s_nop 0\n\t"
    "v_cvt_pk_bf16_f32 v179, v244, v245\n\t"
    "s_nop 1\n\t"
    "v_mfma_f32_16x16x32_bf16 v[252:255], a[252:255], v[176:179], 0\n\t"
    "v_mul_f32 v0, v0, v243\n\t"
    "v_mul_f32 v1, v1, v243\n\t"
    "v_mul_f32 v2, v2, v243\n\t"
    "v_mul_f32 v3, v3, v243\n\t"
    "v_mul_f32 v16, v16, v243\n\t"
    "v_mul_f32 v17, v17, v243\n\t"
    "v_mul_f32 v18, v18, v243\n\t"
    "v_mul_f32 v19, v19, v243\n\t"
    "v_mul_f32 v32, v32, v243\n\t"
    "v_mul_f32 v33, v33, v243\n\t"
    "v_mul_f32 v34, v34, v243\n\t"
    "v_mul_f32 v35, v35, v243\n\t"
    "v_mul_f32 v48, v48, v243\n\t"
    "v_mul_f32 v49, v49, v243\n\t"
    "v_mul_f32 v50, v50, v243\n\t"
    "v_mul_f32 v51, v51, v243\n\t"
    "v_mul_f32 v64, v64, v243\n\t"
    "v_mul_f32 v65, v65, v243\n\t"
    "v_mul_f32 v66, v66, v243\n\t"
    "v_mul_f32 v67, v67, v243\n\t"
    "v_mul_f32 v80, v80, v243\n\t"
    "v_mul_f32 v81, v81, v243\n\t"
    "v_mul_f32 v82, v82, v243\n\t"
    "v_mul_f32 v83, v83, v243\n\t"
    "v_mul_f32 v96, v96, v243\n\t"
    "v_mul_f32 v97, v97, v243\n\t"
    "v_mul_f32 v98, v98, v243\n\t"
    "v_mul_f32 v99, v99, v243\n\t"
    "v_mul_f32 v112, v112, v243\n\t"
    "v_mul_f32 v113, v113, v243\n\t"
    "v_mul_f32 v114, v114, v243\n\t"
    "v_mul_f32 v115, v115, v243\n\t"
    "v_mul_f32 v236, v236, v243\n\t"
    "v_max3_f32 v240, v136, v137, v138\n\t"
    "v_max3_f32 v240, v240, v139, v140\n\t"
    "v_max3_f32 v240, v240, v141, v142\n\t"
    "v_max_f32 v240, v240, v143\n\t"
    "v_mov_b32 v241, v240\n\t"
    "s_nop 1\n\t"
    "v_permlane32_swap_b32 v240, v241\n\t"
    "v_max_f32 v240, v240, v241\n\t"
    "v_mov_b32 v241, v240\n\t"
    "s_nop 1\n\t"
    "v_permlane16_swap_b32 v240, v241\n\t"
    "v_max_f32 v240, v240, v241\n\t"
    "v_add_f32 v242, s69, v240\n\t"
    "v_max_f32 v242, 0, v242\n\t"
    "v_exp_f32 v243, -v242\n\t"
    "v_sub_f32 v196, v196, v242\n\t"
    "v_sub_f32 v197, v197, v242\n\t"
    "v_sub_f32 v198, v198, v242\n\t"
    "v_sub_f32 v199, v199, v242\n\t"
    "v_sub_f32 v244, v136, v242\n\t"
    "v_sub_f32 v245, v137, v242\n\t"
    "v_exp_f32 v244, v244\n\t"
    "v_exp_f32 v245, v245\n\t"
    "s_nop 0\n\t"
    "v_add_f32 v232, v244, v245\n\t"
    "v_cvt_pk_bf16_f32 v180, v244, v245\n\t"
    "v_sub_f32 v244, v138, v242\n\t"
    "v_sub_f32 v245, v139, v242\n\t"
    "v_exp_f32 v244, v244\n\t"
    "v_exp_f32 v245, v245\n\t"
    "s_nop 0\n\t"
    "v_add_f32 v232, v232, v244\n\t"
    "v_add_f32 v232, v232, v245\n\t"
    "v_cvt_pk_bf16_f32 v181, v244, v245\n\t"
    "v_sub_f32 v244, v140, v242\n\t"
    "v_sub_f32 v245, v141, v242\n\t"
    "v_exp_f32 v244, v244\n\t"
    "v_exp_f32 v245, v245\n\t"
    "s_nop 0\n\t"
    "v_add_f32 v232, v232, v244\n\t"
    "v_add_f32 v232, v232, v245\n\t"
    "v_cvt_pk_bf16_f32 v182, v244, v245\n\t"
    "v_sub_f32 v244, v142, v242\n\t"
    "v_sub_f32 v245, v143, v242\n\t"
    "v_exp_f32 v244, v244\n\t"
    "v_exp_f32 v245, v245\n\t"
    "s_nop 0\n\t"
    "v_add_f32 v232, v232, v244\n\t"
    "v_add_f32 v232, v232, v245\n\t"
    "v_cvt_pk_bf16_f32 v183, v244, v245\n\t"
    "v_mul_f32 v4, v4, v243\n\t"
    "v_mul_f32 v5, v5, v243\n\t"
    "v_mul_f32 v6, v6, v243\n\t"
    "v_mul_f32 v7, v7, v243\n\t"
    "v_mul_f32 v20, v20, v243\n\t"
    "v_mul_f32 v21, v21, v243\n\t"
    "v_mul_f32 v22, v22, v243\n\t"
    "v_mul_f32 v23, v23, v243\n\t"
    "v_mul_f32 v36, v36, v243\n\t"
    "v_mul_f32 v37, v37, v243\n\t"
    "v_mul_f32 v38, v38, v243\n\t"
    "v_mul_f32 v39, v39, v243\n\t"
    "v_mul_f32 v52, v52, v243\n\t"
    "v_mul_f32 v53, v53, v243\n\t"
    "v_mul_f32 v54, v54, v243\n\t"
    "v_mul_f32 v55, v55, v243\n\t"
    "v_mul_f32 v68, v68, v243\n\t"
    "v_mul_f32 v69, v69, v243\n\t"
    "v_mul_f32 v70, v70, v243\n\t"
    "v_mul_f32 v71, v71, v243\n\t"
    "v_mul_f32 v84, v84, v243\n\t"
    "v_mul_f32 v85, v85, v243\n\t"
    "v_mul_f32 v86, v86, v243\n\t"
    "v_mul_f32 v87, v87, v243\n\t"
    "v_mul_f32 v100, v100, v243\n\t"
    "v_mul_f32 v101, v101, v243\n\t"
    "v_mul_f32 v102, v102, v243\n\t"
    "v_mul_f32 v103, v103, v243\n\t"
    "v_mul_f32 v116, v116, v243\n\t"
    "v_mul_f32 v117, v117, v243\n\t"
    "v_mul_f32 v118, v118, v243\n\t"
    "v_mul_f32 v119, v119, v243\n\t"
    "v_mul_f32 v237, v237, v243\n\t"
    "s_nop 1\n\t"
    "s_cmp_eq_u32 s67, 2\n\t"
    "s_cbranch_scc1 L_ret2_%=\n\t"
    "s_cmp_eq_u32 s67, 6\n\t"
    "s_cbranch_scc1 L_ret6_%=\n\t"
    "s_cmp_eq_u32 s67, 10\n\t"
    "s_cbranch_scc1 L_ret10_%=\n\t"
    "s_cmp_eq_u32 s67, 14\n\t"
    "s_cbranch_scc1 L_ret14_%=\n\t"
    "s_cmp_eq_u32 s67, 18\n\t"
    "s_cbranch_scc1 L_ret18_%=\n\t"
    "s_cmp_eq_u32 s67, 22\n\t"
    "s_cbranch_scc1 L_ret22_%=\n\t"
    "s_cmp_eq_u32 s67, 26\n\t"
    "s_cbranch_scc1 L_ret26_%=\n\t"
    "s_branch L_ret30_%=\n\t"
    "L_rare3_%=:\n\t"
    "v_max3_f32 v240, v144, v145, v146\n\t"
    "v_max3_f32 v240, v240, v147, v148\n\t"
    "v_max3_f32 v240, v240, v149, v150\n\t"
    "v_max_f32 v240, v240, v151\n\t"
    "v_mov_b32 v241, v240\n\t"
    "s_nop 1\n\t"
    "v_permlane32_swap_b32 v240, v241\n\t"
    "v_max_f32 v240, v240, v241\n\t"
    "v_mov_b32 v241, v240\n\t"
    "s_nop 1\n\t"
    "v_permlane16_swap_b32 v240, v241\n\t"
    "v_max_f32 v240, v240, v241\n\t"
    "v_add_f32 v242, s69, v240\n\t"
    "v_max_f32 v242, 0, v242\n\t"
    "v_exp_f32 v243, -v242\n\t"
    "v_sub_f32 v200, v200, v242\n\t"
    "v_sub_f32 v201, v201, v242\n\t"
    "v_sub_f32 v202, v202, v242\n\t"
    "v_sub_f32 v203, v203, v242\n\t"
    "v_sub_f32 v244, v144, v242\n\t"
    "v_sub_f32 v245, v145, v242\n\t"
    "v_exp_f32 v244, v244\n\t"
    "v_exp_f32 v245, v245\n\t"
    "s_nop 0\n\t"
    "v_cvt_pk_bf16_f32 v184, v244, v245\n\t"
    "v_sub_f32 v244, v146, v242\n\t"
    "v_sub_f32 v245, v147, v242\n\t"
    "v_exp_f32 v244, v244\n\t"
    "v_exp_f32 v245, v245\n\t"
    "s_nop 0\n\t"
    "v_cvt_pk_bf16_f32 v185, v244, v245\n\t"
    "v_sub_f32 v244, v148, v242\n\t"
    "v_sub_f32 v245, v149, v242\n\t"
    "v_exp_f32 v244, v244\n\t"
    "v_exp_f32 v245, v245\n\t"
    "s_nop 0\n\t"
    "v_cvt_pk_bf16_f32 v186, v244, v245\n\t"
    "v_sub_f32 v244, v150, v242\n\t"
    "v_sub_f32 v245, v151, v242\n\t"
    "v_exp_f32 v244, v244\n\t"
    "v_exp_f32 v245, v245\n\t"
    "s_nop 0\n\t"
    "v_cvt_pk_bf16_f32 v187, v244, v245\n\t"
    "s_nop 1\n\t"
    "v_mfma_f32_16x16x32_bf16 v[252:255], a[252:255], v[184:187], 0\n\t"
    "v_mul_f32 v8, v8, v243\n\t"
    "v_mul_f32 v9, v9, v243\n\t"
    "v_mul_f32 v10, v10, v243\n\t"
    "v_mul_f32 v11, v11, v243\n\t"
    "v_mul_f32 v24, v24, v243\n\t"
    "v_mul_f32 v25, v25, v243\n\t"
    "v_mul_f32 v26, v26, v243\n\t"
    "v_mul_f32 v27, v27, v243\n\t"
    "v_mul_f32 v40, v40, v243\n\t"
    "v_mul_f32 v41, v41, v243\n\t"
    "v_mul_f32 v42, v42, v243\n\t"
    "v_mul_f32 v43, v43, v243\n\t"
    "v_mul_f32 v56, v56, v243\n\t"
    "v_mul_f32 v57, v57, v243\n\t"
    "v_mul_f32 v58, v58, v243\n\t"
    "v_mul_f32 v59, v59, v243\n\t"
    "v_mul_f32 v72, v72, v243\n\t"
    "v_mul_f32 v73, v73, v243\n\t"
    "v_mul_f32 v74, v74, v243\n\t"
    "v_mul_f32 v75, v75, v243\n\t"
    "v_mul_f32 v88, v88, v243\n\t"
    "v_mul_f32 v89, v89, v243\n\t"
    "v_mul_f32 v90, v90, v243\n\t"
    "v_mul_f32 v91, v91, v243\n\t"
    "v_mul_f32 v104, v104, v243\n\t"
    "v_mul_f32 v105, v105, v243\n\t"
    "v_mul_f32 v106, v106, v243\n\t"
    "v_mul_f32 v107, v107, v243\n\t"
    "v_mul_f32 v120, v120, v243\n\t"
    "v_mul_f32 v121, v121, v243\n\t"
    "v_mul_f32 v122, v122, v243\n\t"
    "v_mul_f32 v123, v123, v243\n\t"
    "v_mul_f32 v238, v238, v243\n\t"
    "v_max3_f32 v240, v152, v153, v154\n\t"
    "v_max3_f32 v240, v240, v155, v156\n\t"
    "v_max3_f32 v240, v240, v157, v158\n\t"
    "v_max_f32 v240, v240, v159\n\t"
    "v_mov_b32 v241, v240\n\t"
    "s_nop 1\n\t"
    "v_permlane32_swap_b32 v240, v241\n\t"
    "v_max_f32 v240, v240, v241\n\t"
    "v_mov_b32 v241, v240\n\t"
    "s_nop 1\n\t"
    "v_permlane16_swap_b32 v240, v241\n\t"
    "v_max_f32 v240, v240, v241\n\t"
    "v_add_f32 v242, s69, v240\n\t"
    "v_max_f32 v242, 0, v242\n\t"
    "v_exp_f32 v243, -v242\n\t"
    "v_sub_f32 v204, v204, v242\n\t"
    "v_sub_f32 v205, v205, v242\n\t"
    "v_sub_f32 v206, v206, v242\n\t"
    "v_sub_f32 v207, v207, v242\n\t"
    "v_sub_f32 v244, v152, v242\n\t"
    "v_sub_f32 v245, v153, v242\n\t"
    "v_exp_f32 v244, v244\n\t"
    "v_exp_f32 v245, v245\n\t"
    "s_nop 0\n\t"
    "v_add_f32 v233, v244, v245\n\t"
    "v_cvt_pk_bf16_f32 v188, v244, v245\n\t"
    "v_sub_f32 v244, v154, v242\n\t"
    "v_sub_f32 v245, v155, v242\n\t"
    "v_exp_f32 v244, v244\n\t"
    "v_exp_f32 v245, v245\n\t"
    "s_nop 0\n\t"
    "v_add_f32 v233, v233, v244\n\t"
    "v_add_f32 v233, v233, v245\n\t"
    "v_cvt_pk_bf16_f32 v189, v244, v245\n\t"
    "v_sub_f32 v244, v156, v242\n\t"
    "v_sub_f32 v245, v157, v242\n\t"
    "v_exp_f32 v244, v244\n\t"
    "v_exp_f32 v245, v245\n\t"
    "s_nop 0\n\t"
    "v_add_f32 v233, v233, v244\n\t"
    "v_add_f32 v233, v233, v245\n\t"
    "v_cvt_pk_bf16_f32 v190, v244, v245\n\t"
    "v_sub_f32 v244, v158, v242\n\t"
    "v_sub_f32 v245, v159, v242\n\t"
    "v_exp_f32 v244, v244\n\t"
    "v_exp_f32 v245, v245\n\t"
    "s_nop 0\n\t"
    "v_add_f32 v233, v233, v244\n\t"
    "v_add_f32 v233, v233, v245\n\t"
    "v_cvt_pk_bf16_f32 v191, v244, v245\n\t"
    "v_mul_f32 v12, v12, v243\n\t"
    "v_mul_f32 v13, v13, v243\n\t"
    "v_mul_f32 v14, v14, v243\n\t"
    "v_mul_f32 v15, v15, v243\n\t"
    "v_mul_f32 v28, v28, v243\n\t"
    "v_mul_f32 v29, v29, v243\n\t"
    "v_mul_f32 v30, v30, v243\n\t"
    "v_mul_f32 v31, v31, v243\n\t"
    "v_mul_f32 v44, v44, v243\n\t"
    "v_mul_f32 v45, v45, v243\n\t"
    "v_mul_f32 v46, v46, v243\n\t"
    "v_mul_f32 v47, v47, v243\n\t"
    "v_mul_f32 v60, v60, v243\n\t"
    "v_mul_f32 v61, v61, v243\n\t"
    "v_mul_f32 v62, v62, v243\n\t"
    "v_mul_f32 v63, v63, v243\n\t"
    "v_mul_f32 v76, v76, v243\n\t"
    "v_mul_f32 v77, v77, v243\n\t"
    "v_mul_f32 v78, v78, v243\n\t"
    "v_mul_f32 v79, v79, v243\n\t"
    "v_mul_f32 v92, v92, v243\n\t"
    "v_mul_f32 v93, v93, v243\n\t"
    "v_mul_f32 v94, v94, v243\n\t"
    "v_mul_f32 v95, v95, v243\n\t"
    "v_mul_f32 v108, v108, v243\n\t"
    "v_mul_f32 v109, v109, v243\n\t"
    "v_mul_f32 v110, v110, v243\n\t"
    "v_mul_f32 v111, v111, v243\n\t"
    "v_mul_f32 v124, v124, v243\n\t"
    "v_mul_f32 v125, v125, v243\n\t"
    "v_mul_f32 v126, v126, v243\n\t"
    "v_mul_f32 v127, v127, v243\n\t"
    "v_mul_f32 v239, v239, v243\n\t"
    "s_nop 1\n\t"
    "s_cmp_eq_u32 s67, 3\n\t"
    "s_cbranch_scc1 L_ret3_%=\n\t"
    "s_cmp_eq_u32 s67, 7\n\t"
    "s_cbranch_scc1 L_ret7_%=\n\t"
    "s_cmp_eq_u32 s67, 11\n\t"
    "s_cbranch_scc1 L_ret11_%=\n\t"
    "s_cmp_eq_u32 s67, 15\n\t"
    "s_cbranch_scc1 L_ret15_%=\n\t"
    "s_cmp_eq_u32 s67, 19\n\t"
    "s_cbranch_scc1 L_ret19_%=\n\t"
    "s_cmp_eq_u32 s67, 23\n\t"
    "s_cbranch_scc1 L_ret23_%=\n\t"
    "s_cmp_eq_u32 s67, 27\n\t"
    "s_cbranch_scc1 L_ret27_%=\n\t"
    "s_branch L_ret31_%=\n\t"
    "L_end_%=:\n\t"
    :
    : "s"(ws + WS_H), "s"(wq), "s"(kc), "s"(vc), "s"(ws + WS_OF), "s"(bid), "s"(wid), "s"(ldsb), "s"(ws), "s"(wo), "s"(wd), "s"(wg), "s"(wu), "s"(ws + WS_TAB)
    :
      "v0", "v1", "v2", "v3", "v4", "v5", "v6", "v7", "v8", "v9", "v10", "v11", "v12", "v13", "v14", "v15", "v16", "v17", "v18", "v19", "v20", "v21", "v22", "v23",
      "v24", "v25", "v26", "v27", "v28", "v29", "v30", "v31", "v32", "v33", "v34", "v35", "v36", "v37", "v38", "v39", "v40", "v41", "v42", "v43", "v44", "v45", "v46", "v47",
      "v48", "v49", "v50", "v51", "v52", "v53", "v54", "v55", "v56", "v57", "v58", "v59", "v60", "v61", "v62", "v63", "v64", "v65", "v66", "v67", "v68", "v69", "v70", "v71",
      "v72", "v73", "v74", "v75", "v76", "v77", "v78", "v79", "v80", "v81", "v82", "v83", "v84", "v85", "v86", "v87", "v88", "v89", "v90", "v91", "v92", "v93", "v94", "v95",
      "v96", "v97", "v98", "v99", "v100", "v101", "v102", "v103", "v104", "v105", "v106", "v107", "v108", "v109", "v110", "v111", "v112", "v113", "v114", "v115", "v116", "v117", "v118", "v119",
      "v120", "v121", "v122", "v123", "v124", "v125", "v126", "v127", "v128", "v129", "v130", "v131", "v132", "v133", "v134", "v135", "v136", "v137", "v138", "v139", "v140", "v141", "v142", "v143",
      "v144", "v145", "v146", "v147", "v148", "v149", "v150", "v151", "v152", "v153", "v154", "v155", "v156", "v157", "v158", "v159", "v160", "v161", "v162", "v163", "v164", "v165", "v166", "v167",
      "v168", "v169", "v170", "v171", "v172", "v173", "v174", "v175", "v176", "v177", "v178", "v179", "v180", "v181", "v182", "v183", "v184", "v185", "v186", "v187", "v188", "v189", "v190", "v191",
      "v192", "v193", "v194", "v195", "v196", "v197", "v198", "v199", "v200", "v201", "v202", "v203", "v204", "v205", "v206", "v207", "v208", "v209", "v210", "v211", "v212", "v213", "v214", "v215",
      "v216", "v217", "v218", "v219", "v220", "v221", "v222", "v223", "v224", "v225", "v226", "v227", "v228", "v229", "v230", "v231", "v232", "v233", "v234", "v235", "v236", "v237", "v238", "v239",
      "v240", "v241", "v242", "v243", "v244", "v245", "v246", "v247", "v248", "v249", "v250", "v251", "v252", "v253", "v254", "v255", "a0", "a1", "a2", "a3", "a4", "a5", "a6", "a7",
      "a8", "a9", "a10", "a11", "a12", "a13", "a14", "a15", "a16", "a17", "a18", "a19", "a20", "a21", "a22", "a23", "a24", "a25", "a26", "a27", "a28", "a29", "a30", "a31",
      "a32", "a33", "a34", "a35", "a36", "a37", "a38", "a39", "a40", "a41", "a42", "a43", "a44", "a45", "a46", "a47", "a48", "a49", "a50", "a51", "a52", "a53", "a54", "a55",
      "a56", "a57", "a58", "a59", "a60", "a61", "a62", "a63", "a64", "a65", "a66", "a67", "a68", "a69", "a70", "a71", "a72", "a73", "a74", "a75", "a76", "a77", "a78", "a79",
      "a80", "a81", "a82", "a83", "a84", "a85", "a86", "a87", "a88", "a89", "a90", "a91", "a92", "a93", "a94", "a95", "a96", "a97", "a98", "a99", "a100", "a101", "a102", "a103",
      "a104", "a105", "a106", "a107", "a108", "a109", "a110", "a111", "a112", "a113", "a114", "a115", "a116", "a117", "a118", "a119", "a120", "a121", "a122", "a123", "a124", "a125", "a126", "a127",
      "a128", "a129", "a130", "a131", "a132", "a133", "a134", "a135", "a136", "a137", "a138", "a139", "a140", "a141", "a142", "a143", "a144", "a145", "a146", "a147", "a148", "a149", "a150", "a151",
      "a152", "a153", "a154", "a155", "a156", "a157", "a158", "a159", "a160", "a161", "a162", "a163", "a164", "a165", "a166", "a167", "a168", "a169", "a170", "a171", "a172", "a173", "a174", "a175",
      "a176", "a177", "a178", "a179", "a180", "a181", "a182", "a183", "a184", "a185", "a186", "a187", "a188", "a189", "a190", "a191", "a192", "a193", "a194", "a195", "a196", "a197", "a198", "a199",
      "a200", "a201", "a202", "a203", "a204", "a205", "a206", "a207", "a208", "a209", "a210", "a211", "a212", "a213", "a214", "a215", "a216", "a217", "a218", "a219", "a220", "a221", "a222", "a223",
      "a224", "a225", "a226", "a227", "a228", "a229", "a230", "a231", "a232", "a233", "a234", "a235", "a236", "a237", "a238", "a239", "a240", "a241", "a242", "a243", "a244", "a245", "a246", "a247",
      "a248", "a249", "a250", "a251", "a252", "a253", "a254", "a255", "s36", "s37", "s38", "s39", "s40", "s41", "s42", "s43", "s44", "s45", "s46", "s47", "s48", "s49", "s50", "s51",
      "s52", "s53", "s54", "s55", "s56", "s57", "s58", "s59", "s60", "s61", "s62", "s63", "s64", "s65", "s66", "s67", "s68", "s69", "s70", "s71", "s72", "s73", "s74", "s75",
      "s76", "s77", "s78", "s79", "s80", "s81", "s82", "s83", "vcc", "scc", "memory"
  );
  arrive((unsigned*)(ws + WS_CNT) + 3072 + ((bid >> 5) * 4 + (xc >> 1)) * 64, wid);
  const unsigned lane = lane_id(), m8 = lane & 7, g = (lane >> 3) & 3, h = lane >> 5;
  const unsigned mt16 = mt128 * 8 + (idx >> 1), fl = g * 16 + (idx & 1) * 8 + m8, w1 = (mt16 >> 1) & 3, mt = mt16 & 1, wv = 2 * wid + h, n0 = wv * 64 + 4 * g;
  bf16x4 xr[4]; f32x4 ga[4], cb[4], sb[4];
#pragma unroll
  for (int nt = 0; nt < 4; ++nt) {
    xr[nt] = *(const bf16x4*)&x[(mt16 * 16 + (fl & 15)) * D + n0 + 16 * nt];
    ga[nt] = *(const f32x4*)&mod[1024 + n0 + 16 * nt]; sb[nt] = *(const f32x4*)&mod[1536 + n0 + 16 * nt]; cb[nt] = *(const f32x4*)&mod[2048 + n0 + 16 * nt];
  }
  PRE_G1(G1P0); if (wid != 0) PRE_G1(G1P1);
  wait_ge<false>((unsigned*)(ws + WS_CNT) + 3072 + ((mtp >> 1) * 4 + kq) * 64, 8, wid);
  if (wid == 0) PRE_G1(G1P1);
  asm volatile(
    "v_mbcnt_lo_u32_b32 v196, -1, 0\n\t"
    "v_mbcnt_hi_u32_b32 v196, -1, v196\n\t"
    "s_lshl_b32 s60, %5, 8\n\t"
    "s_lshl_b32 s61, %6, 20\n\t"
    "s_add_u32 s60, s60, s61\n\t"
    "s_mov_b64 s[40:41], %1\n\t"
    "s_add_u32 s40, s40, s60\n\t"
    "s_addc_u32 s41, s41, 0\n\t"
    "s_and_b32 s41, s41, 0xffff\n\t"
    "s_mov_b32 s42, 1047808\n\t"
    "s_mov_b32 s43, 0x20000\n\t"
    "v_lshrrev_b32 v220, 3, v196\n\t"
    "s_lshl_b32 s60, %7, 4\n\t"
    "v_add_u32 v220, s60, v220\n\t"
    "s_mov_b32 s60, 1024\n\t"
    "v_mul_lo_u32 v198, v220, s60\n\t"
    "v_bfe_u32 v221, v196, 4, 1\n\t"
    "v_lshlrev_b32 v221, 2, v221\n\t"
    "v_bfe_u32 v222, v196, 5, 1\n\t"
    "v_lshl_add_u32 v221, v222, 1, v221\n\t"
    "v_and_b32 v222, 7, v196\n\t"
    "v_xor_b32 v222, v222, v221\n\t"
    "v_lshl_add_u32 v198, v222, 4, v198\n\t"
    "s_lshl_b32 s68, %7, 11\n\t"
    "s_add_u32 s68, s68, %8\n\t"
    "v_lshlrev_b32 v197, 4, v196\n\t"
    "s_lshl_b32 s60, %4, 3\n\t"
    "s_lshl_b32 s61, %7, 1\n\t"
    "s_add_u32 s62, s60, s61\n\t"
    "s_mul_i32 s60, s62, 131072\n\t"
    "s_lshl_b32 s61, %6, 15\n\t"
    "s_add_u32 s60, s60, s61\n\t"
    "s_mov_b64 s[36:37], %0\n\t"
    "s_add_u32 s36, s36, s60\n\t"
    "s_addc_u32 s37, s37, 0\n\t"
    "s_and_b32 s37, s37, 0xffff\n\t"
    "s_mov_b32 s38, 163840\n\t"
    "s_mov_b32 s39, 0x20000\n\t"
    "s_mov_b32 s69, 0\n\t"
    "buffer_load_dwordx4 a[0:3], v197, s[36:39], s69 offen offset:0 sc1\n\t"
    "s_mov_b32 s69, 131072\n\t"
    "buffer_load_dwordx4 a[4:7], v197, s[36:39], s69 offen offset:0 sc1\n\t"
    "s_mov_b32 s69, 0\n\t"
    "buffer_load_dwordx4 a[8:11], v197, s[36:39], s69 offen offset:1024 sc1\n\t"
    "s_mov_b32 s69, 131072\n\t"
    "buffer_load_dwordx4 a[12:15], v197, s[36:39], s69 offen offset:1024 sc1\n\t"
    "s_mov_b32 s69, 0\n\t"
    "buffer_load_dwordx4 a[16:19], v197, s[36:39], s69 offen offset:2048 sc1\n\t"
    "s_mov_b32 s69, 131072\n\t"
    "buffer_load_dwordx4 a[20:23], v197, s[36:39], s69 offen offset:2048 sc1\n\t"
    "s_mov_b32 s69, 0\n\t"
    "buffer_load_dwordx4 a[24:27], v197, s[36:39], s69 offen offset:3072 sc1\n\t"
    "s_mov_b32 s69, 131072\n\t"
    "buffer_load_dwordx4 a[28:31], v197, s[36:39], s69 offen offset:3072 sc1\n\t"
    "s_lshl_b32 s60, %6, 4\n\t"
    "s_add_u32 s60, s60, %4\n\t"
    "s_lshl_b32 s60, s60, 2\n\t"
    "s_add_u32 s60, s60, %5\n\t"
    "s_lshl_b32 s60, s60, 2\n\t"
    "s_add_u32 s60, s60, %7\n\t"
    "s_mul_i32 s60, s60, 8192\n\t"
    "s_mov_b64 s[48:49], %3\n\t"
    "s_add_u32 s48, s48, s60\n\t"
    "s_addc_u32 s49, s49, 0\n\t"
    "s_and_b32 s49, s49, 0xffff\n\t"
    "s_mov_b32 s50, 8192\n\t"
    "s_mov_b32 s51, 0x20000\n\t"
    "v_and_b32 v221, 1, v196\n\t"
    "v_lshlrev_b32 v223, 3, v221\n\t"
    "v_bfe_u32 v221, v196, 2, 3\n\t"
    "v_lshl_add_u32 v223, v221, 7, v223\n\t"
    "v_lshrrev_b32 v221, 5, v196\n\t"
    "v_lshl_add_u32 v223, v221, 10, v223\n\t"
    "v_add_u32 v223, %8, v223\n\t"
    "v_bfe_u32 v221, v196, 3, 1\n\t"
    "v_lshlrev_b32 v221, 2, v221\n\t"
    "v_bfe_u32 v222, v196, 4, 1\n\t"
    "v_lshl_add_u32 v221, v222, 1, v221\n\t"
    "v_bfe_u32 v222, v196, 1, 1\n\t"
    "v_xor_b32 v221, v221, v222\n\t"
    "v_xor_b32 v222, 0, v221\n\t"
    "v_lshl_add_u32 v200, v222, 4, v223\n\t"
    "v_add_u32 v204, 0x10000, v200\n\t"
    "v_add_u32 v208, 0x20000, v200\n\t"
    "v_xor_b32 v222, 2, v221\n\t"
    "v_lshl_add_u32 v201, v222, 4, v223\n\t"
    "v_add_u32 v205, 0x10000, v201\n\t"
    "v_add_u32 v209, 0x20000, v201\n\t"
    "v_xor_b32 v222, 4, v221\n\t"
    "v_lshl_add_u32 v202, v222, 4, v223\n\t"
    "v_add_u32 v206, 0x10000, v202\n\t"
    "v_add_u32 v210, 0x20000, v202\n\t"
    "v_xor_b32 v222, 6, v221\n\t"
    "v_lshl_add_u32 v203, v222, 4, v223\n\t"
    "v_add_u32 v207, 0x10000, v203\n\t"
    "v_add_u32 v211, 0x20000, v203\n\t"
    "s_lshl_b32 s60, %11, 8\n\t"
    "s_mov_b64 s[52:53], %9\n\t"
    "s_add_u32 s52, s52, s60\n\t"
    "s_addc_u32 s53, s53, 0\n\t"
    "s_and_b32 s53, s53, 0xffff\n\t"
    "s_mov_b32 s54, 1831680\n\t"
    "s_mov_b32 s55, 0x20000\n\t"
    "s_mov_b64 s[56:57], %10\n\t"
    "s_add_u32 s56, s56, s60\n\t"
    "s_addc_u32 s57, s57, 0\n\t"
    "s_and_b32 s57, s57, 0xffff\n\t"
    "s_mov_b32 s58, 1831680\n\t"
    "s_mov_b32 s59, 0x20000\n\t"
    "v_lshrrev_b32 v220, 3, v196\n\t"
    "s_lshl_b32 s60, %7, 4\n\t"
    "v_add_u32 v220, s60, v220\n\t"
    "s_mov_b32 s60, 3584\n\t"
    "v_mul_lo_u32 v199, v220, s60\n\t"
    "v_bfe_u32 v221, v196, 4, 1\n\t"
    "v_lshlrev_b32 v221, 2, v221\n\t"
    "v_bfe_u32 v222, v196, 5, 1\n\t"
    "v_lshl_add_u32 v221, v222, 1, v221\n\t"
    "v_and_b32 v222, 7, v196\n\t"
    "v_xor_b32 v222, v222, v221\n\t"
    "v_lshl_add_u32 v199, v222, 4, v199\n\t"
    "s_waitcnt vmcnt(32)\n\t"
    "s_barrier\n\t"
    "ds_read_b64_tr_b16 a[128:129], v200 offset:0\n\t"
    "ds_read_b64_tr_b16 a[130:131], v200 offset:2048\n\t"
    "ds_read_b64_tr_b16 a[132:133], v201 offset:0\n\t"
    "ds_read_b64_tr_b16 a[134:135], v201 offset:2048\n\t"
    "ds_read_b64_tr_b16 a[136:137], v202 offset:0\n\t"
    "ds_read_b64_tr_b16 a[138:139], v202 offset:2048\n\t"
    "ds_read_b64_tr_b16 a[140:141], v203 offset:0\n\t"
    "ds_read_b64_tr_b16 a[142:143], v203 offset:2048\n\t"
    "ds_read_b64_tr_b16 a[144:145], v200 offset:8192\n\t"
    "ds_read_b64_tr_b16 a[146:147], v200 offset:10240\n\t"
    "ds_read_b64_tr_b16 a[148:149], v201 offset:8192\n\t"
    "ds_read_b64_tr_b16 a[150:151], v201 offset:10240\n\t"
    "s_waitcnt vmcnt(6) lgkmcnt(10)\n\t"
    "s_mov_b32 s69, 4096\n\t"
    "v_mfma_f32_16x16x32_bf16 v[0:3], a[128:131], a[0:3], 0\n\t"
    "buffer_load_dwordx4 a[32:35], v197, s[36:39], s69 offen offset:0 sc1\n\t"
    "ds_read_b64_tr_b16 a[152:153], v202 offset:8192\n\t"
    "v_mfma_f32_16x16x32_bf16 v[4:7], a[128:131], a[4:7], 0\n\t"
    "ds_read_b64_tr_b16 a[154:155], v202 offset:10240\n\t"
    "s_waitcnt lgkmcnt(10)\n\t"
    "v_mfma_f32_16x16x32_bf16 v[8:11], a[132:135], a[0:3], 0\n\t"
    "ds_read_b64_tr_b16 a[156:157], v203 offset:8192\n\t"
    "v_mfma_f32_16x16x32_bf16 v[12:15], a[132:135], a[4:7], 0\n\t"
    "ds_read_b64_tr_b16 a[158:159], v203 offset:10240\n\t"
    "s_waitcnt lgkmcnt(10)\n\t"
    "s_mov_b32 s69, 135168\n\t"
    "v_mfma_f32_16x16x32_bf16 v[16:19], a[136:139], a[0:3], 0\n\t"
    "buffer_load_dwordx4 a[36:39], v197, s[36:39], s69 offen offset:0 sc1\n\t"
    "ds_read_b64_tr_b16 a[160:161], v200 offset:4096\n\t"
    "v_mfma_f32_16x16x32_bf16 v[20:23], a[136:139], a[4:7], 0\n\t"
    "ds_read_b64_tr_b16 a[162:163], v200 offset:6144\n\t"
    "s_waitcnt lgkmcnt(10)\n\t"
    "v_mfma_f32_16x16x32_bf16 v[24:27], a[140:143], a[0:3], 0\n\t"
    "ds_read_b64_tr_b16 a[164:165], v201 offset:4096\n\t"
    "v_mfma_f32_16x16x32_bf16 v[28:31], a[140:143], a[4:7], 0\n\t"
    "ds_read_b64_tr_b16 a[166:167], v201 offset:6144\n\t"
    "s_waitcnt lgkmcnt(10)\n\t"
    "s_mov_b32 s69, 4096\n\t"
    "v_mfma_f32_16x16x32_bf16 v[32:35], a[144:147], a[0:3], 0\n\t"
    "buffer_load_dwordx4 a[40:43], v197, s[36:39], s69 offen offset:1024 sc1\n\t"
    "ds_read_b64_tr_b16 a[168:169], v202 offset:4096\n\t"
    "v_mfma_f32_16x16x32_bf16 v[36:39], a[144:147], a[4:7], 0\n\t"
    "ds_read_b64_tr_b16 a[170:171], v202 offset:6144\n\t"
    "s_waitcnt lgkmcnt(10)\n\t"
    "v_mfma_f32_16x16x32_bf16 v[40:43], a[148:151], a[0:3], 0\n\t"
    "ds_read_b64_tr_b16 a[172:173], v203 offset:4096\n\t"
    "v_mfma_f32_16x16x32_bf16 v[44:47], a[148:151], a[4:7], 0\n\t"
    "ds_read_b64_tr_b16 a[174:175], v203 offset:6144\n\t"
    "s_waitcnt lgkmcnt(10)\n\t"
    "s_mov_b32 s69, 135168\n\t"
    "v_mfma_f32_16x16x32_bf16 v[48:51], a[152:155], a[0:3], 0\n\t"
    "buffer_load_dwordx4 a[44:47], v197, s[36:39], s69 offen offset:1024 sc1\n\t"
    "ds_read_b64_tr_b16 a[176:177], v200 offset:12288\n\t"
    "v_mfma_f32_16x16x32_bf16 v[52:55], a[152:155], a[4:7], 0\n\t"
    "ds_read_b64_tr_b16 a[178:179], v200 offset:14336\n\t"
    "s_waitcnt lgkmcnt(10)\n\t"
    "v_mfma_f32_16x16x32_bf16 v[56:59], a[156:159], a[0:3], 0\n\t"
    "ds_read_b64_tr_b16 a[180:181], v201 offset:12288\n\t"
    "v_mfma_f32_16x16x32_bf16 v[60:63], a[156:159], a[4:7], 0\n\t"
    "ds_read_b64_tr_b16 a[182:183], v201 offset:14336\n\t"
    "s_waitcnt vmcnt(8) lgkmcnt(10)\n\t"
    "s_mov_b32 s69, 4096\n\t"
    "v_mfma_f32_16x16x32_bf16 v[0:3], a[160:163], a[8:11], v[0:3]\n\t"
    "buffer_load_dwordx4 a[48:51], v197, s[36:39], s69 offen offset:2048 sc1\n\t"
    "ds_read_b64_tr_b16 a[184:185], v202 offset:12288\n\t"
    "v_mfma_f32_16x16x32_bf16 v[4:7], a[160:163], a[12:15], v[4:7]\n\t"
    "ds_read_b64_tr_b16 a[186:187], v202 offset:14336\n\t"
    "s_waitcnt lgkmcnt(10)\n\t"
    "v_mfma_f32_16x16x32_bf16 v[8:11], a[164:167], a[8:11], v[8:11]\n\t"
    "ds_read_b64_tr_b16 a[188:189], v203 offset:12288\n\t"
    "v_mfma_f32_16x16x32_bf16 v[12:15], a[164:167], a[12:15], v[12:15]\n\t"
    "ds_read_b64_tr_b16 a[190:191], v203 offset:14336\n\t"
    "s_barrier\n\t"
    "s_waitcnt lgkmcnt(10)\n\t"
    "s_mov_b32 s69, 135168\n\t"
    "v_mfma_f32_16x16x32_bf16 v[16:19], a[168:171], a[8:11], v[16:19]\n\t"
    "buffer_load_dwordx4 a[52:55], v197, s[36:39], s69 offen offset:2048 sc1\n\t"
    "ds_read_b64_tr_b16 a[192:193], v200 offset:16384\n\t"
    "v_mfma_f32_16x16x32_bf16 v[20:23], a[168:171], a[12:15], v[20:23]\n\t"
    "ds_read_b64_tr_b16 a[194:195], v200 offset:18432\n\t"
    "s_waitcnt lgkmcnt(10)\n\t"
    "v_mfma_f32_16x16x32_bf16 v[24:27], a[172:175], a[8:11], v[24:27]\n\t"
    "ds_read_b64_tr_b16 a[196:197], v201 offset:16384\n\t"
    "v_mfma_f32_16x16x32_bf16 v[28:31], a[172:175], a[12:15], v[28:31]\n\t"
    "ds_read_b64_tr_b16 a[198:199], v201 offset:18432\n\t"
    "s_waitcnt lgkmcnt(10)\n\t"
    "s_mov_b32 s69, 4096\n\t"
    "v_mfma_f32_16x16x32_bf16 v[32:35], a[176:179], a[8:11], v[32:35]\n\t"
    "buffer_load_dwordx4 a[56:59], v197, s[36:39], s69 offen offset:3072 sc1\n\t"
    "ds_read_b64_tr_b16 a[200:201], v202 offset:16384\n\t"
    "v_mfma_f32_16x16x32_bf16 v[36:39], a[176:179], a[12:15], v[36:39]\n\t"
    "ds_read_b64_tr_b16 a[202:203], v202 offset:18432\n\t"
    "s_waitcnt lgkmcnt(10)\n\t"
    "v_mfma_f32_16x16x32_bf16 v[40:43], a[180:183], a[8:11], v[40:43]\n\t"
    "ds_read_b64_tr_b16 a[204:205], v203 offset:16384\n\t"
    "v_mfma_f32_16x16x32_bf16 v[44:47], a[180:183], a[12:15], v[44:47]\n\t"
    "ds_read_b64_tr_b16 a[206:207], v203 offset:18432\n\t"
    "s_waitcnt lgkmcnt(10)\n\t"
    "s_mov_b32 s69, 135168\n\t"
    "v_mfma_f32_16x16x32_bf16 v[48:51], a[184:187], a[8:11], v[48:51]\n\t"
    "buffer_load_dwordx4 a[60:63], v197, s[36:39], s69 offen offset:3072 sc1\n\t"
    "ds_read_b64_tr_b16 a[208:209], v200 offset:24576\n\t"
    "v_mfma_f32_16x16x32_bf16 v[52:55], a[184:187], a[12:15], v[52:55]\n\t"
    "ds_read_b64_tr_b16 a[210:211], v200 offset:26624\n\t"
    "s_waitcnt lgkmcnt(10)\n\t"
    "v_mfma_f32_16x16x32_bf16 v[56:59], a[188:191], a[8:11], v[56:59]\n\t"
    "ds_read_b64_tr_b16 a[212:213], v201 offset:24576\n\t"
    "v_mfma_f32_16x16x32_bf16 v[60:63], a[188:191], a[12:15], v[60:63]\n\t"
    "ds_read_b64_tr_b16 a[214:215], v201 offset:26624\n\t"
    "s_waitcnt vmcnt(10) lgkmcnt(10)\n\t"
    "s_mov_b32 s69, 8192\n\t"
    "v_mfma_f32_16x16x32_bf16 v[0:3], a[192:195], a[16:19], v[0:3]\n\t"
    "buffer_load_dwordx4 a[64:67], v197, s[36:39], s69 offen offset:0 sc1\n\t"
    "ds_read_b64_tr_b16 a[216:217], v202 offset:24576\n\t"
    "v_mfma_f32_16x16x32_bf16 v[4:7], a[192:195], a[20:23], v[4:7]\n\t"
    "ds_read_b64_tr_b16 a[218:219], v202 offset:26624\n\t"
    "s_waitcnt lgkmcnt(10)\n\t"
    "v_mfma_f32_16x16x32_bf16 v[8:11], a[196:199], a[16:19], v[8:11]\n\t"
    "ds_read_b64_tr_b16 a[220:221], v203 offset:24576\n\t"
    "v_mfma_f32_16x16x32_bf16 v[12:15], a[196:199], a[20:23], v[12:15]\n\t"
    "ds_read_b64_tr_b16 a[222:223], v203 offset:26624\n\t"
    "s_waitcnt lgkmcnt(10)\n\t"
    "s_mov_b32 s69, 139264\n\t"
    "v_mfma_f32_16x16x32_bf16 v[16:19], a[200:203], a[16:19], v[16:19]\n\t"
    "buffer_load_dwordx4 a[68:71], v197, s[36:39], s69 offen offset:0 sc1\n\t"
    "ds_read_b64_tr_b16 a[224:225], v200 offset:20480\n\t"
    "v_mfma_f32_16x16x32_bf16 v[20:23], a[200:203], a[20:23], v[20:23]\n\t"
    "ds_read_b64_tr_b16 a[226:227], v200 offset:22528\n\t"
    "s_waitcnt lgkmcnt(10)\n\t"
    "v_mfma_f32_16x16x32_bf16 v[24:27], a[204:207], a[16:19], v[24:27]\n\t"
    "ds_read_b64_tr_b16 a[228:229], v201 offset:20480\n\t"
    "v_mfma_f32_16x16x32_bf16 v[28:31], a[204:207], a[20:23], v[28:31]\n\t"
    "ds_read_b64_tr_b16 a[230:231], v201 offset:22528\n\t"
    "s_waitcnt lgkmcnt(10)\n\t"
    "s_mov_b32 s69, 8192\n\t"
    "v_mfma_f32_16x16x32_bf16 v[32:35], a[208:211], a[16:19], v[32:35]\n\t"
    "buffer_load_dwordx4 a[72:75], v197, s[36:39], s69 offen offset:1024 sc1\n\t"
    "ds_read_b64_tr_b16 a[232:233], v202 offset:20480\n\t"
    "v_mfma_f32_16x16x32_bf16 v[36:39], a[208:211], a[20:23], v[36:39]\n\t"
    "ds_read_b64_tr_b16 a[234:235], v202 offset:22528\n\t"
    "s_waitcnt lgkmcnt(10)\n\t"
    "v_mfma_f32_16x16x32_bf16 v[40:43], a[212:215], a[16:19], v[40:43]\n\t"
    "ds_read_b64_tr_b16 a[236:237], v203 offset:20480\n\t"
    "v_mfma_f32_16x16x32_bf16 v[44:47], a[212:215], a[20:23], v[44:47]\n\t"
    "ds_read_b64_tr_b16 a[238:239], v203 offset:22528\n\t"
    "s_waitcnt lgkmcnt(10)\n\t"
    "s_mov_b32 s69, 139264\n\t"
    "v_mfma_f32_16x16x32_bf16 v[48:51], a[216:219], a[16:19], v[48:51]\n\t"
    "buffer_load_dwordx4 a[76:79], v197, s[36:39], s69 offen offset:1024 sc1\n\t"
    "ds_read_b64_tr_b16 a[240:241], v200 offset:28672\n\t"
    "v_mfma_f32_16x16x32_bf16 v[52:55], a[216:219], a[20:23], v[52:55]\n\t"
    "ds_read_b64_tr_b16 a[242:243], v200 offset:30720\n\t"
    "s_waitcnt lgkmcnt(10)\n\t"
    "v_mfma_f32_16x16x32_bf16 v[56:59], a[220:223], a[16:19], v[56:59]\n\t"
    "ds_read_b64_tr_b16 a[244:245], v201 offset:28672\n\t"
    "v_mfma_f32_16x16x32_bf16 v[60:63], a[220:223], a[20:23], v[60:63]\n\t"
    "ds_read_b64_tr_b16 a[246:247], v201 offset:30720\n\t"
    "s_waitcnt vmcnt(12) lgkmcnt(10)\n\t"
    "s_mov_b32 s69, 8192\n\t"
    "v_mfma_f32_16x16x32_bf16 v[0:3], a[224:227], a[24:27], v[0:3]\n\t"
    "buffer_load_dwordx4 a[80:83], v197, s[36:39], s69 offen offset:2048 sc1\n\t"
    "ds_read_b64_tr_b16 a[248:249], v202 offset:28672\n\t"
    "v_mfma_f32_16x16x32_bf16 v[4:7], a[224:227], a[28:31], v[4:7]\n\t"
    "ds_read_b64_tr_b16 a[250:251], v202 offset:30720\n\t"
    "s_waitcnt lgkmcnt(10)\n\t"
    "v_mfma_f32_16x16x32_bf16 v[8:11], a[228:231], a[24:27], v[8:11]\n\t"
    "ds_read_b64_tr_b16 a[252:253], v203 offset:28672\n\t"
    "v_mfma_f32_16x16x32_bf16 v[12:15], a[228:231], a[28:31], v[12:15]\n\t"
    "ds_read_b64_tr_b16 a[254:255], v203 offset:30720\n\t"
    "s_barrier\n\t"
    "s_waitcnt lgkmcnt(10)\n\t"
    "s_mov_b32 s69, 139264\n\t"
    "v_mfma_f32_16x16x32_bf16 v[16:19], a[232:235], a[24:27], v[16:19]\n\t"
    "buffer_load_dwordx4 a[84:87], v197, s[36:39], s69 offen offset:2048 sc1\n\t"
    "ds_read_b64_tr_b16 a[128:129], v200 offset:32768\n\t"
    "v_mfma_f32_16x16x32_bf16 v[20:23], a[232:235], a[28:31], v[20:23]\n\t"
    "ds_read_b64_tr_b16 a[130:131], v200 offset:34816\n\t"
    "s_waitcnt lgkmcnt(10)\n\t"
    "v_mfma_f32_16x16x32_bf16 v[24:27], a[236:239], a[24:27], v[24:27]\n\t"
    "ds_read_b64_tr_b16 a[132:133], v201 offset:32768\n\t"
    "v_mfma_f32_16x16x32_bf16 v[28:31], a[236:239], a[28:31], v[28:31]\n\t"
    "ds_read_b64_tr_b16 a[134:135], v201 offset:34816\n\t"
    "s_waitcnt lgkmcnt(10)\n\t"
    "s_mov_b32 s69, 8192\n\t"
    "v_mfma_f32_16x16x32_bf16 v[32:35], a[240:243], a[24:27], v[32:35]\n\t"
    "buffer_load_dwordx4 a[88:91], v197, s[36:39], s69 offen offset:3072 sc1\n\t"
    "ds_read_b64_tr_b16 a[136:137], v202 offset:32768\n\t"
    "v_mfma_f32_16x16x32_bf16 v[36:39], a[240:243], a[28:31], v[36:39]\n\t"
    "ds_read_b64_tr_b16 a[138:139], v202 offset:34816\n\t"
    "s_waitcnt lgkmcnt(10)\n\t"
    "v_mfma_f32_16x16x32_bf16 v[40:43], a[244:247], a[24:27], v[40:43]\n\t"
    "ds_read_b64_tr_b16 a[140:141], v203 offset:32768\n\t"
    "v_mfma_f32_16x16x32_bf16 v[44:47], a[244:247], a[28:31], v[44:47]\n\t"
    "ds_read_b64_tr_b16 a[142:143], v203 offset:34816\n\t"
    "s_waitcnt lgkmcnt(10)\n\t"
    "s_mov_b32 s69, 139264\n\t"
    "v_mfma_f32_16x16x32_bf16 v[48:51], a[248:251], a[24:27], v[48:51]\n\t"
    "buffer_load_dwordx4 a[92:95], v197, s[36:39], s69 offen offset:3072 sc1\n\t"
    "ds_read_b64_tr_b16 a[144:145], v200 offset:40960\n\t"
    "v_mfma_f32_16x16x32_bf16 v[52:55], a[248:251], a[28:31], v[52:55]\n\t"
    "ds_read_b64_tr_b16 a[146:147], v200 offset:43008\n\t"
    "s_waitcnt lgkmcnt(10)\n\t"
    "v_mfma_f32_16x16x32_bf16 v[56:59], a[252:255], a[24:27], v[56:59]\n\t"
    "ds_read_b64_tr_b16 a[148:149], v201 offset:40960\n\t"
    "v_mfma_f32_16x16x32_bf16 v[60:63], a[252:255], a[28:31], v[60:63]\n\t"
    "ds_read_b64_tr_b16 a[150:151], v201 offset:43008\n\t"
    "s_waitcnt vmcnt(14) lgkmcnt(10)\n\t"
    "s_mov_b32 s69, 12288\n\t"
    "v_mfma_f32_16x16x32_bf16 v[0:3], a[128:131], a[32:35], v[0:3]\n\t"
    "buffer_load_dwordx4 a[96:99], v197, s[36:39], s69 offen offset:0 sc1\n\t"
    "ds_read_b64_tr_b16 a[152:153], v202 offset:40960\n\t"
    "v_mfma_f32_16x16x32_bf16 v[4:7], a[128:131], a[36:39], v[4:7]\n\t"
    "ds_read_b64_tr_b16 a[154:155], v202 offset:43008\n\t"
    "s_waitcnt lgkmcnt(10)\n\t"
    "v_mfma_f32_16x16x32_bf16 v[8:11], a[132:135], a[32:35], v[8:11]\n\t"
    "ds_read_b64_tr_b16 a[156:157], v203 offset:40960\n\t"
    "v_mfma_f32_16x16x32_bf16 v[12:15], a[132:135], a[36:39], v[12:15]\n\t"
    "ds_read_b64_tr_b16 a[158:159], v203 offset:43008\n\t"
    "s_waitcnt lgkmcnt(10)\n\t"
    "s_mov_b32 s69, 143360\n\t"
    "v_mfma_f32_16x16x32_bf16 v[16:19], a[136:139], a[32:35], v[16:19]\n\t"
    "buffer_load_dwordx4 a[100:103], v197, s[36:39], s69 offen offset:0 sc1\n\t"
    "ds_read_b64_tr_b16 a[160:161], v200 offset:36864\n\t"
    "v_mfma_f32_16x16x32_bf16 v[20:23], a[136:139], a[36:39], v[20:23]\n\t"
    "ds_read_b64_tr_b16 a[162:163], v200 offset:38912\n\t"
    "s_waitcnt lgkmcnt(10)\n\t"
    "v_mfma_f32_16x16x32_bf16 v[24:27], a[140:143], a[32:35], v[24:27]\n\t"
    "ds_read_b64_tr_b16 a[164:165], v201 offset:36864\n\t"
    "v_mfma_f32_16x16x32_bf16 v[28:31], a[140:143], a[36:39], v[28:31]\n\t"
    "ds_read_b64_tr_b16 a[166:167], v201 offset:38912\n\t"
    "s_waitcnt lgkmcnt(10)\n\t"
    "s_mov_b32 s69, 12288\n\t"
    "v_mfma_f32_16x16x32_bf16 v[32:35], a[144:147], a[32:35], v[32:35]\n\t"
    "buffer_load_dwordx4 a[104:107], v197, s[36:39], s69 offen offset:1024 sc1\n\t"
    "ds_read_b64_tr_b16 a[168:169], v202 offset:36864\n\t"
    "v_mfma_f32_16x16x32_bf16 v[36:39], a[144:147], a[36:39], v[36:39]\n\t"
    "ds_read_b64_tr_b16 a[170:171], v202 offset:38912\n\t"
    "s_waitcnt lgkmcnt(10)\n\t"
    "v_mfma_f32_16x16x32_bf16 v[40:43], a[148:151], a[32:35], v[40:43]\n\t"
    "ds_read_b64_tr_b16 a[172:173], v203 offset:36864\n\t"
    "v_mfma_f32_16x16x32_bf16 v[44:47], a[148:151], a[36:39], v[44:47]\n\t"
    "ds_read_b64_tr_b16 a[174:175], v203 offset:38912\n\t"
    "s_waitcnt lgkmcnt(10)\n\t"
    "s_mov_b32 s69, 143360\n\t"
    "v_mfma_f32_16x16x32_bf16 v[48:51], a[152:155], a[32:35], v[48:51]\n\t"
    "buffer_load_dwordx4 a[108:111], v197, s[36:39], s69 offen offset:1024 sc1\n\t"
    "ds_read_b64_tr_b16 a[176:177], v200 offset:45056\n\t"
    "v_mfma_f32_16x16x32_bf16 v[52:55], a[152:155], a[36:39], v[52:55]\n\t"
    "ds_read_b64_tr_b16 a[178:179], v200 offset:47104\n\t"
    "s_waitcnt lgkmcnt(10)\n\t"
    "v_mfma_f32_16x16x32_bf16 v[56:59], a[156:159], a[32:35], v[56:59]\n\t"
    "ds_read_b64_tr_b16 a[180:181], v201 offset:45056\n\t"
    "v_mfma_f32_16x16x32_bf16 v[60:63], a[156:159], a[36:39], v[60:63]\n\t"
    "ds_read_b64_tr_b16 a[182:183], v201 offset:47104\n\t"
    "s_waitcnt vmcnt(16) lgkmcnt(10)\n\t"
    "s_add_u32 m0, s68, 114688\n\t"
    "s_mov_b32 s69, 458752\n\t"
    "v_mfma_f32_16x16x32_bf16 v[0:3], a[160:163], a[40:43], v[0:3]\n\t"
    "buffer_load_dwordx4 v198, s[40:43], s69 offen lds\n\t"
    "ds_read_b64_tr_b16 a[184:185], v202 offset:45056\n\t"
    "v_mfma_f32_16x16x32_bf16 v[4:7], a[160:163], a[44:47], v[4:7]\n\t"
    "ds_read_b64_tr_b16 a[186:187], v202 offset:47104\n\t"
    "s_waitcnt lgkmcnt(10)\n\t"
    "v_mfma_f32_16x16x32_bf16 v[8:11], a[164:167], a[40:43], v[8:11]\n\t"
    "ds_read_b64_tr_b16 a[188:189], v203 offset:45056\n\t"
    "v_mfma_f32_16x16x32_bf16 v[12:15], a[164:167], a[44:47], v[12:15]\n\t"
    "ds_read_b64_tr_b16 a[190:191], v203 offset:47104\n\t"
    "s_barrier\n\t"
    "s_waitcnt lgkmcnt(10)\n\t"
    "s_add_u32 m0, s68, 115712\n\t"
    "s_mov_b32 s69, 466944\n\t"
    "v_mfma_f32_16x16x32_bf16 v[16:19], a[168:171], a[40:43], v[16:19]\n\t"
    "buffer_load_dwordx4 v198, s[40:43], s69 offen lds\n\t"
    "ds_read_b64_tr_b16 a[192:193], v200 offset:49152\n\t"
    "v_mfma_f32_16x16x32_bf16 v[20:23], a[168:171], a[44:47], v[20:23]\n\t"
    "ds_read_b64_tr_b16 a[194:195], v200 offset:51200\n\t"
    "s_waitcnt lgkmcnt(10)\n\t"
    "v_mfma_f32_16x16x32_bf16 v[24:27], a[172:175], a[40:43], v[24:27]\n\t"
    "ds_read_b64_tr_b16 a[196:197], v201 offset:49152\n\t"
    "v_mfma_f32_16x16x32_bf16 v[28:31], a[172:175], a[44:47], v[28:31]\n\t"
    "ds_read_b64_tr_b16 a[198:199], v201 offset:51200\n\t"
    "s_waitcnt lgkmcnt(10)\n\t"
    "s_add_u32 m0, s68, 122880\n\t"
    "s_mov_b32 s69, 458880\n\t"
    "v_mfma_f32_16x16x32_bf16 v[32:35], a[176:179], a[40:43], v[32:35]\n\t"
    "buffer_load_dwordx4 v198, s[40:43], s69 offen lds\n\t"
    "ds_read_b64_tr_b16 a[200:201], v202 offset:49152\n\t"
    "v_mfma_f32_16x16x32_bf16 v[36:39], a[176:179], a[44:47], v[36:39]\n\t"
    "ds_read_b64_tr_b16 a[202:203], v202 offset:51200\n\t"
    "s_waitcnt lgkmcnt(10)\n\t"
    "v_mfma_f32_16x16x32_bf16 v[40:43], a[180:183], a[40:43], v[40:43]\n\t"
    "ds_read_b64_tr_b16 a[204:205], v203 offset:49152\n\t"
    "v_mfma_f32_16x16x32_bf16 v[44:47], a[180:183], a[44:47], v[44:47]\n\t"
    "ds_read_b64_tr_b16 a[206:207], v203 offset:51200\n\t"
    "s_waitcnt lgkmcnt(10)\n\t"
    "s_add_u32 m0, s68, 123904\n\t"
    "s_mov_b32 s69, 467072\n\t"
    "v_mfma_f32_16x16x32_bf16 v[48:51], a[184:187], a[40:43], v[48:51]\n\t"
    "buffer_load_dwordx4 v198, s[40:43], s69 offen lds\n\t"
    "ds_read_b64_tr_b16 a[208:209], v200 offset:57344\n\t"
    "v_mfma_f32_16x16x32_bf16 v[52:55], a[184:187], a[44:47], v[52:55]\n\t"
    "ds_read_b64_tr_b16 a[210:211], v200 offset:59392\n\t"
    "s_waitcnt lgkmcnt(10)\n\t"
    "v_mfma_f32_16x16x32_bf16 v[56:59], a[188:191], a[40:43], v[56:59]\n\t"
    "ds_read_b64_tr_b16 a[212:213], v201 offset:57344\n\t"
    "v_mfma_f32_16x16x32_bf16 v[60:63], a[188:191], a[44:47], v[60:63]\n\t"
    "ds_read_b64_tr_b16 a[214:215], v201 offset:59392\n\t"
    "s_waitcnt vmcnt(18) lgkmcnt(10)\n\t"
    "s_mov_b32 s69, 12288\n\t"
    "v_mfma_f32_16x16x32_bf16 v[0:3], a[192:195], a[48:51], v[0:3]\n\t"
    "buffer_load_dwordx4 a[112:115], v197, s[36:39], s69 offen offset:2048 sc1\n\t"
    "ds_read_b64_tr_b16 a[216:217], v202 offset:57344\n\t"
    "v_mfma_f32_16x16x32_bf16 v[4:7], a[192:195], a[52:55], v[4:7]\n\t"
    "ds_read_b64_tr_b16 a[218:219], v202 offset:59392\n\t"
    "s_waitcnt lgkmcnt(10)\n\t"
    "v_mfma_f32_16x16x32_bf16 v[8:11], a[196:199], a[48:51], v[8:11]\n\t"
    "ds_read_b64_tr_b16 a[220:221], v203 offset:57344\n\t"
    "v_mfma_f32_16x16x32_bf16 v[12:15], a[196:199], a[52:55], v[12:15]\n\t"
    "ds_read_b64_tr_b16 a[222:223], v203 offset:59392\n\t"
    "s_waitcnt lgkmcnt(10)\n\t"
    "s_mov_b32 s69, 143360\n\t"
    "v_mfma_f32_16x16x32_bf16 v[16:19], a[200:203], a[48:51], v[16:19]\n\t"
    "buffer_load_dwordx4 a[116:119], v197, s[36:39], s69 offen offset:2048 sc1\n\t"
    "ds_read_b64_tr_b16 a[224:225], v200 offset:53248\n\t"
    "v_mfma_f32_16x16x32_bf16 v[20:23], a[200:203], a[52:55], v[20:23]\n\t"
    "ds_read_b64_tr_b16 a[226:227], v200 offset:55296\n\t"
    "s_waitcnt lgkmcnt(10)\n\t"
    "v_mfma_f32_16x16x32_bf16 v[24:27], a[204:207], a[48:51], v[24:27]\n\t"
    "ds_read_b64_tr_b16 a[228:229], v201 offset:53248\n\t"
    "v_mfma_f32_16x16x32_bf16 v[28:31], a[204:207], a[52:55], v[28:31]\n\t"
    "ds_read_b64_tr_b16 a[230:231], v201 offset:55296\n\t"
    "s_waitcnt lgkmcnt(10)\n\t"
    "s_mov_b32 s69, 12288\n\t"
    "v_mfma_f32_16x16x32_bf16 v[32:35], a[208:211], a[48:51], v[32:35]\n\t"
    "buffer_load_dwordx4 a[120:123], v197, s[36:39], s69 offen offset:3072 sc1\n\t"
    "ds_read_b64_tr_b16 a[232:233], v202 offset:53248\n\t"
    "v_mfma_f32_16x16x32_bf16 v[36:39], a[208:211], a[52:55], v[36:39]\n\t"
    "ds_read_b64_tr_b16 a[234:235], v202 offset:55296\n\t"
    "s_waitcnt lgkmcnt(10)\n\t"
    "v_mfma_f32_16x16x32_bf16 v[40:43], a[212:215], a[48:51], v[40:43]\n\t"
    "ds_read_b64_tr_b16 a[236:237], v203 offset:53248\n\t"
    "v_mfma_f32_16x16x32_bf16 v[44:47], a[212:215], a[52:55], v[44:47]\n\t"
    "ds_read_b64_tr_b16 a[238:239], v203 offset:55296\n\t"
    "s_waitcnt lgkmcnt(10)\n\t"
    "s_mov_b32 s69, 143360\n\t"
    "v_mfma_f32_16x16x32_bf16 v[48:51], a[216:219], a[48:51], v[48:51]\n\t"
    "buffer_load_dwordx4 a[124:127], v197, s[36:39], s69 offen offset:3072 sc1\n\t"
    "ds_read_b64_tr_b16 a[240:241], v200 offset:61440\n\t"
    "v_mfma_f32_16x16x32_bf16 v[52:55], a[216:219], a[52:55], v[52:55]\n\t"
    "ds_read_b64_tr_b16 a[242:243], v200 offset:63488\n\t"
    "s_waitcnt lgkmcnt(10)\n\t"
    "v_mfma_f32_16x16x32_bf16 v[56:59], a[220:223], a[48:51], v[56:59]\n\t"
    "ds_read_b64_tr_b16 a[244:245], v201 offset:61440\n\t"
    "v_mfma_f32_16x16x32_bf16 v[60:63], a[220:223], a[52:55], v[60:63]\n\t"
    "ds_read_b64_tr_b16 a[246:247], v201 offset:63488\n\t"
    "s_waitcnt vmcnt(20) lgkmcnt(10)\n\t"
    "s_add_u32 m0, s68, 131072\n\t"
    "s_mov_b32 s69, 524288\n\t"
    "v_mfma_f32_16x16x32_bf16 v[0:3], a[224:227], a[56:59], v[0:3]\n\t"
    "buffer_load_dwordx4 v198, s[40:43], s69 offen lds\n\t"
    "ds_read_b64_tr_b16 a[248:249], v202 offset:61440\n\t"
    "v_mfma_f32_16x16x32_bf16 v[4:7], a[224:227], a[60:63], v[4:7]\n\t"
    "ds_read_b64_tr_b16 a[250:251], v202 offset:63488\n\t"
    "s_waitcnt lgkmcnt(10)\n\t"
    "v_mfma_f32_16x16x32_bf16 v[8:11], a[228:231], a[56:59], v[8:11]\n\t"
    "ds_read_b64_tr_b16 a[252:253], v203 offset:61440\n\t"
    "v_mfma_f32_16x16x32_bf16 v[12:15], a[228:231], a[60:63], v[12:15]\n\t"
    "ds_read_b64_tr_b16 a[254:255], v203 offset:63488\n\t"
    "s_barrier\n\t"
    "s_waitcnt lgkmcnt(10)\n\t"
    "s_add_u32 m0, s68, 132096\n\t"
    "s_mov_b32 s69, 532480\n\t"
    "v_mfma_f32_16x16x32_bf16 v[16:19], a[232:235], a[56:59], v[16:19]\n\t"
    "buffer_load_dwordx4 v198, s[40:43], s69 offen lds\n\t"
    "ds_read_b64_tr_b16 a[128:129], v204 offset:0\n\t"
    "v_mfma_f32_16x16x32_bf16 v[20:23], a[232:235], a[60:63], v[20:23]\n\t"
    "ds_read_b64_tr_b16 a[130:131], v204 offset:2048\n\t"
    "s_waitcnt lgkmcnt(10)\n\t"
    "v_mfma_f32_16x16x32_bf16 v[24:27], a[236:239], a[56:59], v[24:27]\n\t"
    "ds_read_b64_tr_b16 a[132:133], v205 offset:0\n\t"
    "v_mfma_f32_16x16x32_bf16 v[28:31], a[236:239], a[60:63], v[28:31]\n\t"
    "ds_read_b64_tr_b16 a[134:135], v205 offset:2048\n\t"
    "s_waitcnt lgkmcnt(10)\n\t"
    "s_add_u32 m0, s68, 139264\n\t"
    "s_mov_b32 s69, 524416\n\t"
    "v_mfma_f32_16x16x32_bf16 v[32:35], a[240:243], a[56:59], v[32:35]\n\t"
    "buffer_load_dwordx4 v198, s[40:43], s69 offen lds\n\t"
    "ds_read_b64_tr_b16 a[136:137], v206 offset:0\n\t"
    "v_mfma_f32_16x16x32_bf16 v[36:39], a[240:243], a[60:63], v[36:39]\n\t"
    "ds_read_b64_tr_b16 a[138:139], v206 offset:2048\n\t"
    "s_waitcnt lgkmcnt(10)\n\t"
    "v_mfma_f32_16x16x32_bf16 v[40:43], a[244:247], a[56:59], v[40:43]\n\t"
    "ds_read_b64_tr_b16 a[140:141], v207 offset:0\n\t"
    "v_mfma_f32_16x16x32_bf16 v[44:47], a[244:247], a[60:63], v[44:47]\n\t"
    "ds_read_b64_tr_b16 a[142:143], v207 offset:2048\n\t"
    "s_waitcnt lgkmcnt(10)\n\t"
    "s_add_u32 m0, s68, 140288\n\t"
    "s_mov_b32 s69, 532608\n\t"
    "v_mfma_f32_16x16x32_bf16 v[48:51], a[248:251], a[56:59], v[48:51]\n\t"
    "buffer_load_dwordx4 v198, s[40:43], s69 offen lds\n\t"
    "ds_read_b64_tr_b16 a[144:145], v204 offset:8192\n\t"
    "v_mfma_f32_16x16x32_bf16 v[52:55], a[248:251], a[60:63], v[52:55]\n\t"
    "ds_read_b64_tr_b16 a[146:147], v204 offset:10240\n\t"
    "s_waitcnt lgkmcnt(10)\n\t"
    "v_mfma_f32_16x16x32_bf16 v[56:59], a[252:255], a[56:59], v[56:59]\n\t"
    "ds_read_b64_tr_b16 a[148:149], v205 offset:8192\n\t"
    "v_mfma_f32_16x16x32_bf16 v[60:63], a[252:255], a[60:63], v[60:63]\n\t"
    "ds_read_b64_tr_b16 a[150:151], v205 offset:10240\n\t"
    "s_waitcnt vmcnt(22) lgkmcnt(10)\n\t"
    "s_mov_b32 s69, 16384\n\t"
    "v_mfma_f32_16x16x32_bf16 v[0:3], a[128:131], a[64:67], v[0:3]\n\t"
    "buffer_load_dwordx4 a[0:3], v197, s[36:39], s69 offen offset:0 sc1\n\t"
    "ds_read_b64_tr_b16 a[152:153], v206 offset:8192\n\t"
    "v_mfma_f32_16x16x32_bf16 v[4:7], a[128:131], a[68:71], v[4:7]\n\t"
    "ds_read_b64_tr_b16 a[154:155], v206 offset:10240\n\t"
    "s_waitcnt lgkmcnt(10)\n\t"
    "v_mfma_f32_16x16x32_bf16 v[8:11], a[132:135], a[64:67], v[8:11]\n\t"
    "ds_read_b64_tr_b16 a[156:157], v207 offset:8192\n\t"
    "v_mfma_f32_16x16x32_bf16 v[12:15], a[132:135], a[68:71], v[12:15]\n\t"
    "ds_read_b64_tr_b16 a[158:159], v207 offset:10240\n\t"
    "s_waitcnt lgkmcnt(10)\n\t"
    "s_mov_b32 s69, 147456\n\t"
    "v_mfma_f32_16x16x32_bf16 v[16:19], a[136:139], a[64:67], v[16:19]\n\t"
    "buffer_load_dwordx4 a[4:7], v197, s[36:39], s69 offen offset:0 sc1\n\t"
    "ds_read_b64_tr_b16 a[160:161], v204 offset:4096\n\t"
    "v_mfma_f32_16x16x32_bf16 v[20:23], a[136:139], a[68:71], v[20:23]\n\t"
    "ds_read_b64_tr_b16 a[162:163], v204 offset:6144\n\t"
    "s_waitcnt lgkmcnt(10)\n\t"
    "v_mfma_f32_16x16x32_bf16 v[24:27], a[140:143], a[64:67], v[24:27]\n\t"
    "ds_read_b64_tr_b16 a[164:165], v205 offset:4096\n\t"
    "v_mfma_f32_16x16x32_bf16 v[28:31], a[140:143], a[68:71], v[28:31]\n\t"
    "ds_read_b64_tr_b16 a[166:167], v205 offset:6144\n\t"
    "s_waitcnt lgkmcnt(10)\n\t"
    "s_mov_b32 s69, 16384\n\t"
    "v_mfma_f32_16x16x32_bf16 v[32:35], a[144:147], a[64:67], v[32:35]\n\t"
    "buffer_load_dwordx4 a[8:11], v197, s[36:39], s69 offen offset:1024 sc1\n\t"
    "ds_read_b64_tr_b16 a[168:169], v206 offset:4096\n\t"
    "v_mfma_f32_16x16x32_bf16 v[36:39], a[144:147], a[68:71], v[36:39]\n\t"
    "ds_read_b64_tr_b16 a[170:171], v206 offset:6144\n\t"
    "s_waitcnt lgkmcnt(10)\n\t"
    "v_mfma_f32_16x16x32_bf16 v[40:43], a[148:151], a[64:67], v[40:43]\n\t"
    "ds_read_b64_tr_b16 a[172:173], v207 offset:4096\n\t"
    "v_mfma_f32_16x16x32_bf16 v[44:47], a[148:151], a[68:71], v[44:47]\n\t"
    "ds_read_b64_tr_b16 a[174:175], v207 offset:6144\n\t"
    "s_waitcnt lgkmcnt(10)\n\t"
    "s_mov_b32 s69, 147456\n\t"
    "v_mfma_f32_16x16x32_bf16 v[48:51], a[152:155], a[64:67], v[48:51]\n\t"
    "buffer_load_dwordx4 a[12:15], v197, s[36:39], s69 offen offset:1024 sc1\n\t"
    "ds_read_b64_tr_b16 a[176:177], v204 offset:12288\n\t"
    "v_mfma_f32_16x16x32_bf16 v[52:55], a[152:155], a[68:71], v[52:55]\n\t"
    "ds_read_b64_tr_b16 a[178:179], v204 offset:14336\n\t"
    "s_waitcnt lgkmcnt(10)\n\t"
    "v_mfma_f32_16x16x32_bf16 v[56:59], a[156:159], a[64:67], v[56:59]\n\t"
    "ds_read_b64_tr_b16 a[180:181], v205 offset:12288\n\t"
    "v_mfma_f32_16x16x32_bf16 v[60:63], a[156:159], a[68:71], v[60:63]\n\t"
    "ds_read_b64_tr_b16 a[182:183], v205 offset:14336\n\t"
    "s_waitcnt vmcnt(24) lgkmcnt(10)\n\t"
    "s_add_u32 m0, s68, 0\n\t"
    "s_mov_b32 s69, 589824\n\t"
    "v_mfma_f32_16x16x32_bf16 v[0:3], a[160:163], a[72:75], v[0:3]\n\t"
    "buffer_load_dwordx4 v198, s[40:43], s69 offen lds\n\t"
    "ds_read_b64_tr_b16 a[184:185], v206 offset:12288\n\t"
    "v_mfma_f32_16x16x32_bf16 v[4:7], a[160:163], a[76:79], v[4:7]\n\t"
    "ds_read_b64_tr_b16 a[186:187], v206 offset:14336\n\t"
    "s_waitcnt lgkmcnt(10)\n\t"
    "v_mfma_f32_16x16x32_bf16 v[8:11], a[164:167], a[72:75], v[8:11]\n\t"
    "ds_read_b64_tr_b16 a[188:189], v207 offset:12288\n\t"
    "v_mfma_f32_16x16x32_bf16 v[12:15], a[164:167], a[76:79], v[12:15]\n\t"
    "ds_read_b64_tr_b16 a[190:191], v207 offset:14336\n\t"
    "s_barrier\n\t"
    "s_waitcnt lgkmcnt(10)\n\t"
    "s_add_u32 m0, s68, 1024\n\t"
    "s_mov_b32 s69, 598016\n\t"
    "v_mfma_f32_16x16x32_bf16 v[16:19], a[168:171], a[72:75], v[16:19]\n\t"
    "buffer_load_dwordx4 v198, s[40:43], s69 offen lds\n\t"
    "ds_read_b64_tr_b16 a[192:193], v204 offset:16384\n\t"
    "v_mfma_f32_16x16x32_bf16 v[20:23], a[168:171], a[76:79], v[20:23]\n\t"
    "ds_read_b64_tr_b16 a[194:195], v204 offset:18432\n\t"
    "s_waitcnt lgkmcnt(10)\n\t"
    "v_mfma_f32_16x16x32_bf16 v[24:27], a[172:175], a[72:75], v[24:27]\n\t"
    "ds_read_b64_tr_b16 a[196:197], v205 offset:16384\n\t"
    "v_mfma_f32_16x16x32_bf16 v[28:31], a[172:175], a[76:79], v[28:31]\n\t"
    "ds_read_b64_tr_b16 a[198:199], v205 offset:18432\n\t"
    "s_waitcnt lgkmcnt(10)\n\t"
    "s_add_u32 m0, s68, 8192\n\t"
    "s_mov_b32 s69, 589952\n\t"
    "v_mfma_f32_16x16x32_bf16 v[32:35], a[176:179], a[72:75], v[32:35]\n\t"
    "buffer_load_dwordx4 v198, s[40:43], s69 offen lds\n\t"
    "ds_read_b64_tr_b16 a[200:201], v206 offset:16384\n\t"
    "v_mfma_f32_16x16x32_bf16 v[36:39], a[176:179], a[76:79], v[36:39]\n\t"
    "ds_read_b64_tr_b16 a[202:203], v206 offset:18432\n\t"
    "s_waitcnt lgkmcnt(10)\n\t"
    "v_mfma_f32_16x16x32_bf16 v[40:43], a[180:183], a[72:75], v[40:43]\n\t"
    "ds_read_b64_tr_b16 a[204:205], v207 offset:16384\n\t"
    "v_mfma_f32_16x16x32_bf16 v[44:47], a[180:183], a[76:79], v[44:47]\n\t"
    "ds_read_b64_tr_b16 a[206:207], v207 offset:18432\n\t"
    "s_waitcnt lgkmcnt(10)\n\t"
    "s_add_u32 m0, s68, 9216\n\t"
    "s_mov_b32 s69, 598144\n\t"
    "v_mfma_f32_16x16x32_bf16 v[48:51], a[184:187], a[72:75], v[48:51]\n\t"
    "buffer_load_dwordx4 v198, s[40:43], s69 offen lds\n\t"
    "ds_read_b64_tr_b16 a[208:209], v204 offset:24576\n\t"
    "v_mfma_f32_16x16x32_bf16 v[52:55], a[184:187], a[76:79], v[52:55]\n\t"
    "ds_read_b64_tr_b16 a[210:211], v204 offset:26624\n\t"
    "s_waitcnt lgkmcnt(10)\n\t"
    "v_mfma_f32_16x16x32_bf16 v[56:59], a[188:191], a[72:75], v[56:59]\n\t"
    "ds_read_b64_tr_b16 a[212:213], v205 offset:24576\n\t"
    "v_mfma_f32_16x16x32_bf16 v[60:63], a[188:191], a[76:79], v[60:63]\n\t"
    "ds_read_b64_tr_b16 a[214:215], v205 offset:26624\n\t"
    "s_waitcnt vmcnt(26) lgkmcnt(10)\n\t"
    "s_mov_b32 s69, 16384\n\t"
    "v_mfma_f32_16x16x32_bf16 v[0:3], a[192:195], a[80:83], v[0:3]\n\t"
    "buffer_load_dwordx4 a[16:19], v197, s[36:39], s69 offen offset:2048 sc1\n\t"
    "ds_read_b64_tr_b16 a[216:217], v206 offset:24576\n\t"
    "v_mfma_f32_16x16x32_bf16 v[4:7], a[192:195], a[84:87], v[4:7]\n\t"
    "ds_read_b64_tr_b16 a[218:219], v206 offset:26624\n\t"
    "s_waitcnt lgkmcnt(10)\n\t"
    "v_mfma_f32_16x16x32_bf16 v[8:11], a[196:199], a[80:83], v[8:11]\n\t"
    "ds_read_b64_tr_b16 a[220:221], v207 offset:24576\n\t"
    "v_mfma_f32_16x16x32_bf16 v[12:15], a[196:199], a[84:87], v[12:15]\n\t"
    "ds_read_b64_tr_b16 a[222:223], v207 offset:26624\n\t"
    "s_waitcnt lgkmcnt(10)\n\t"
    "s_mov_b32 s69, 147456\n\t"
    "v_mfma_f32_16x16x32_bf16 v[16:19], a[200:203], a[80:83], v[16:19]\n\t"
    "buffer_load_dwordx4 a[20:23], v197, s[36:39], s69 offen offset:2048 sc1\n\t"
    "ds_read_b64_tr_b16 a[224:225], v204 offset:20480\n\t"
    "v_mfma_f32_16x16x32_bf16 v[20:23], a[200:203], a[84:87], v[20:23]\n\t"
    "ds_read_b64_tr_b16 a[226:227], v204 offset:22528\n\t"
    "s_waitcnt lgkmcnt(10)\n\t"
    "v_mfma_f32_16x16x32_bf16 v[24:27], a[204:207], a[80:83], v[24:27]\n\t"
    "ds_read_b64_tr_b16 a[228:229], v205 offset:20480\n\t"
    "v_mfma_f32_16x16x32_bf16 v[28:31], a[204:207], a[84:87], v[28:31]\n\t"
    "ds_read_b64_tr_b16 a[230:231], v205 offset:22528\n\t"
    "s_waitcnt lgkmcnt(10)\n\t"
    "s_mov_b32 s69, 16384\n\t"
    "v_mfma_f32_16x16x32_bf16 v[32:35], a[208:211], a[80:83], v[32:35]\n\t"
    "buffer_load_dwordx4 a[24:27], v197, s[36:39], s69 offen offset:3072 sc1\n\t"
    "ds_read_b64_tr_b16 a[232:233], v206 offset:20480\n\t"
    "v_mfma_f32_16x16x32_bf16 v[36:39], a[208:211], a[84:87], v[36:39]\n\t"
    "ds_read_b64_tr_b16 a[234:235], v206 offset:22528\n\t"
    "s_waitcnt lgkmcnt(10)\n\t"
    "v_mfma_f32_16x16x32_bf16 v[40:43], a[212:215], a[80:83], v[40:43]\n\t"
    "ds_read_b64_tr_b16 a[236:237], v207 offset:20480\n\t"
    "v_mfma_f32_16x16x32_bf16 v[44:47], a[212:215], a[84:87], v[44:47]\n\t"
    "ds_read_b64_tr_b16 a[238:239], v207 offset:22528\n\t"
    "s_waitcnt lgkmcnt(10)\n\t"
    "s_mov_b32 s69, 147456\n\t"
    "v_mfma_f32_16x16x32_bf16 v[48:51], a[216:219], a[80:83], v[48:51]\n\t"
    "buffer_load_dwordx4 a[28:31], v197, s[36:39], s69 offen offset:3072 sc1\n\t"
    "ds_read_b64_tr_b16 a[240:241], v204 offset:28672\n\t"
    "v_mfma_f32_16x16x32_bf16 v[52:55], a[216:219], a[84:87], v[52:55]\n\t"
    "ds_read_b64_tr_b16 a[242:243], v204 offset:30720\n\t"
    "s_waitcnt lgkmcnt(10)\n\t"
    "v_mfma_f32_16x16x32_bf16 v[56:59], a[220:223], a[80:83], v[56:59]\n\t"
    "ds_read_b64_tr_b16 a[244:245], v205 offset:28672\n\t"
    "v_mfma_f32_16x16x32_bf16 v[60:63], a[220:223], a[84:87], v[60:63]\n\t"
    "ds_read_b64_tr_b16 a[246:247], v205 offset:30720\n\t"
    "s_waitcnt vmcnt(28) lgkmcnt(10)\n\t"
    "s_add_u32 m0, s68, 16384\n\t"
    "s_mov_b32 s69, 655360\n\t"
    "v_mfma_f32_16x16x32_bf16 v[0:3], a[224:227], a[88:91], v[0:3]\n\t"
    "buffer_load_dwordx4 v198, s[40:43], s69 offen lds\n\t"
    "ds_read_b64_tr_b16 a[248:249], v206 offset:28672\n\t"
    "v_mfma_f32_16x16x32_bf16 v[4:7], a[224:227], a[92:95], v[4:7]\n\t"
    "ds_read_b64_tr_b16 a[250:251], v206 offset:30720\n\t"
    "s_waitcnt lgkmcnt(10)\n\t"
    "v_mfma_f32_16x16x32_bf16 v[8:11], a[228:231], a[88:91], v[8:11]\n\t"
    "ds_read_b64_tr_b16 a[252:253], v207 offset:28672\n\t"
    "v_mfma_f32_16x16x32_bf16 v[12:15], a[228:231], a[92:95], v[12:15]\n\t"
    "ds_read_b64_tr_b16 a[254:255], v207 offset:30720\n\t"
    "s_barrier\n\t"
    "s_waitcnt lgkmcnt(10)\n\t"
    "s_add_u32 m0, s68, 17408\n\t"
    "s_mov_b32 s69, 663552\n\t"
    "v_mfma_f32_16x16x32_bf16 v[16:19], a[232:235], a[88:91], v[16:19]\n\t"
    "buffer_load_dwordx4 v198, s[40:43], s69 offen lds\n\t"
    "ds_read_b64_tr_b16 a[128:129], v204 offset:32768\n\t"
    "v_mfma_f32_16x16x32_bf16 v[20:23], a[232:235], a[92:95], v[20:23]\n\t"
    "ds_read_b64_tr_b16 a[130:131], v204 offset:34816\n\t"
    "s_waitcnt lgkmcnt(10)\n\t"
    "v_mfma_f32_16x16x32_bf16 v[24:27], a[236:239], a[88:91], v[24:27]\n\t"
    "ds_read_b64_tr_b16 a[132:133], v205 offset:32768\n\t"
    "v_mfma_f32_16x16x32_bf16 v[28:31], a[236:239], a[92:95], v[28:31]\n\t"
    "ds_read_b64_tr_b16 a[134:135], v205 offset:34816\n\t"
    "s_waitcnt lgkmcnt(10)\n\t"
    "s_add_u32 m0, s68, 24576\n\t"
    "s_mov_b32 s69, 655488\n\t"
    "v_mfma_f32_16x16x32_bf16 v[32:35], a[240:243], a[88:91], v[32:35]\n\t"
    "buffer_load_dwordx4 v198, s[40:43], s69 offen lds\n\t"
    "ds_read_b64_tr_b16 a[136:137], v206 offset:32768\n\t"
    "v_mfma_f32_16x16x32_bf16 v[36:39], a[240:243], a[92:95], v[36:39]\n\t"
    "ds_read_b64_tr_b16 a[138:139], v206 offset:34816\n\t"
    "s_waitcnt lgkmcnt(10)\n\t"
    "v_mfma_f32_16x16x32_bf16 v[40:43], a[244:247], a[88:91], v[40:43]\n\t"
    "ds_read_b64_tr_b16 a[140:141], v207 offset:32768\n\t"
    "v_mfma_f32_16x16x32_bf16 v[44:47], a[244:247], a[92:95], v[44:47]\n\t"
    "ds_read_b64_tr_b16 a[142:143], v207 offset:34816\n\t"
    "s_waitcnt lgkmcnt(10)\n\t"
    "s_add_u32 m0, s68, 25600\n\t"
    "s_mov_b32 s69, 663680\n\t"
    "v_mfma_f32_16x16x32_bf16 v[48:51], a[248:251], a[88:91], v[48:51]\n\t"
    "buffer_load_dwordx4 v198, s[40:43], s69 offen lds\n\t"
    "ds_read_b64_tr_b16 a[144:145], v204 offset:40960\n\t"
    "v_mfma_f32_16x16x32_bf16 v[52:55], a[248:251], a[92:95], v[52:55]\n\t"
    "ds_read_b64_tr_b16 a[146:147], v204 offset:43008\n\t"
    "s_waitcnt lgkmcnt(10)\n\t"
    "v_mfma_f32_16x16x32_bf16 v[56:59], a[252:255], a[88:91], v[56:59]\n\t"
    "ds_read_b64_tr_b16 a[148:149], v205 offset:40960\n\t"
    "v_mfma_f32_16x16x32_bf16 v[60:63], a[252:255], a[92:95], v[60:63]\n\t"
    "ds_read_b64_tr_b16 a[150:151], v205 offset:43008\n\t"
    "s_waitcnt vmcnt(30) lgkmcnt(10)\n\t"
    "s_mov_b32 s69, 20480\n\t"
    "v_mfma_f32_16x16x32_bf16 v[0:3], a[128:131], a[96:99], v[0:3]\n\t"
    "buffer_load_dwordx4 a[32:35], v197, s[36:39], s69 offen offset:0 sc1\n\t"
    "ds_read_b64_tr_b16 a[152:153], v206 offset:40960\n\t"
    "v_mfma_f32_16x16x32_bf16 v[4:7], a[128:131], a[100:103], v[4:7]\n\t"
    "ds_read_b64_tr_b16 a[154:155], v206 offset:43008\n\t"
    "s_waitcnt lgkmcnt(10)\n\t"
    "v_mfma_f32_16x16x32_bf16 v[8:11], a[132:135], a[96:99], v[8:11]\n\t"
    "ds_read_b64_tr_b16 a[156:157], v207 offset:40960\n\t"
    "v_mfma_f32_16x16x32_bf16 v[12:15], a[132:135], a[100:103], v[12:15]\n\t"
    "ds_read_b64_tr_b16 a[158:159], v207 offset:43008\n\t"
    "s_waitcnt lgkmcnt(10)\n\t"
    "s_mov_b32 s69, 151552\n\t"
    "v_mfma_f32_16x16x32_bf16 v[16:19], a[136:139], a[96:99], v[16:19]\n\t"
    "buffer_load_dwordx4 a[36:39], v197, s[36:39], s69 offen offset:0 sc1\n\t"
    "ds_read_b64_tr_b16 a[160:161], v204 offset:36864\n\t"
    "v_mfma_f32_16x16x32_bf16 v[20:23], a[136:139], a[100:103], v[20:23]\n\t"
    "ds_read_b64_tr_b16 a[162:163], v204 offset:38912\n\t"
    "s_waitcnt lgkmcnt(10)\n\t"
    "v_mfma_f32_16x16x32_bf16 v[24:27], a[140:143], a[96:99], v[24:27]\n\t"
    "ds_read_b64_tr_b16 a[164:165], v205 offset:36864\n\t"
    "v_mfma_f32_16x16x32_bf16 v[28:31], a[140:143], a[100:103], v[28:31]\n\t"
    "ds_read_b64_tr_b16 a[166:167], v205 offset:38912\n\t"
    "s_waitcnt lgkmcnt(10)\n\t"
    "s_mov_b32 s69, 20480\n\t"
    "v_mfma_f32_16x16x32_bf16 v[32:35], a[144:147], a[96:99], v[32:35]\n\t"
    "buffer_load_dwordx4 a[40:43], v197, s[36:39], s69 offen offset:1024 sc1\n\t"
    "ds_read_b64_tr_b16 a[168:169], v206 offset:36864\n\t"
    "v_mfma_f32_16x16x32_bf16 v[36:39], a[144:147], a[100:103], v[36:39]\n\t"
    "ds_read_b64_tr_b16 a[170:171], v206 offset:38912\n\t"
    "s_waitcnt lgkmcnt(10)\n\t"
    "v_mfma_f32_16x16x32_bf16 v[40:43], a[148:151], a[96:99], v[40:43]\n\t"
    "ds_read_b64_tr_b16 a[172:173], v207 offset:36864\n\t"
    "v_mfma_f32_16x16x32_bf16 v[44:47], a[148:151], a[100:103], v[44:47]\n\t"
    "ds_read_b64_tr_b16 a[174:175], v207 offset:38912\n\t"
    "s_waitcnt lgkmcnt(10)\n\t"
    "s_mov_b32 s69, 151552\n\t"
    "v_mfma_f32_16x16x32_bf16 v[48:51], a[152:155], a[96:99], v[48:51]\n\t"
    "buffer_load_dwordx4 a[44:47], v197, s[36:39], s69 offen offset:1024 sc1\n\t"
    "ds_read_b64_tr_b16 a[176:177], v204 offset:45056\n\t"
    "v_mfma_f32_16x16x32_bf16 v[52:55], a[152:155], a[100:103], v[52:55]\n\t"
    "ds_read_b64_tr_b16 a[178:179], v204 offset:47104\n\t"
    "s_waitcnt lgkmcnt(10)\n\t"
    "v_mfma_f32_16x16x32_bf16 v[56:59], a[156:159], a[96:99], v[56:59]\n\t"
    "ds_read_b64_tr_b16 a[180:181], v205 offset:45056\n\t"
    "v_mfma_f32_16x16x32_bf16 v[60:63], a[156:159], a[100:103], v[60:63]\n\t"
    "ds_read_b64_tr_b16 a[182:183], v205 offset:47104\n\t"
    "s_waitcnt vmcnt(32) lgkmcnt(10)\n\t"
    "s_add_u32 m0, s68, 32768\n\t"
    "s_mov_b32 s69, 720896\n\t"
    "v_mfma_f32_16x16x32_bf16 v[0:3], a[160:163], a[104:107], v[0:3]\n\t"
    "buffer_load_dwordx4 v198, s[40:43], s69 offen lds\n\t"
    "ds_read_b64_tr_b16 a[184:185], v206 offset:45056\n\t"
    "v_mfma_f32_16x16x32_bf16 v[4:7], a[160:163], a[108:111], v[4:7]\n\t"
    "ds_read_b64_tr_b16 a[186:187], v206 offset:47104\n\t"
    "s_waitcnt lgkmcnt(10)\n\t"
    "v_mfma_f32_16x16x32_bf16 v[8:11], a[164:167], a[104:107], v[8:11]\n\t"
    "ds_read_b64_tr_b16 a[188:189], v207 offset:45056\n\t"
    "v_mfma_f32_16x16x32_bf16 v[12:15], a[164:167], a[108:111], v[12:15]\n\t"
    "ds_read_b64_tr_b16 a[190:191], v207 offset:47104\n\t"
    "s_waitcnt vmcnt(29)\n\t"
    "s_barrier\n\t"
    "s_waitcnt lgkmcnt(10)\n\t"
    "s_add_u32 m0, s68, 33792\n\t"
    "s_mov_b32 s69, 729088\n\t"
    "v_mfma_f32_16x16x32_bf16 v[16:19], a[168:171], a[104:107], v[16:19]\n\t"
    "buffer_load_dwordx4 v198, s[40:43], s69 offen lds\n\t"
    "ds_read_b64_tr_b16 a[192:193], v204 offset:49152\n\t"
    "v_mfma_f32_16x16x32_bf16 v[20:23], a[168:171], a[108:111], v[20:23]\n\t"
    "ds_read_b64_tr_b16 a[194:195], v204 offset:51200\n\t"
    "s_waitcnt lgkmcnt(10)\n\t"
    "v_mfma_f32_16x16x32_bf16 v[24:27], a[172:175], a[104:107], v[24:27]\n\t"
    "ds_read_b64_tr_b16 a[196:197], v205 offset:49152\n\t"
    "v_mfma_f32_16x16x32_bf16 v[28:31], a[172:175], a[108:111], v[28:31]\n\t"
    "ds_read_b64_tr_b16 a[198:199], v205 offset:51200\n\t"
    "s_waitcnt lgkmcnt(10)\n\t"
    "s_add_u32 m0, s68, 40960\n\t"
    "s_mov_b32 s69, 721024\n\t"
    "v_mfma_f32_16x16x32_bf16 v[32:35], a[176:179], a[104:107], v[32:35]\n\t"
    "buffer_load_dwordx4 v198, s[40:43], s69 offen lds\n\t"
    "ds_read_b64_tr_b16 a[200:201], v206 offset:49152\n\t"
    "v_mfma_f32_16x16x32_bf16 v[36:39], a[176:179], a[108:111], v[36:39]\n\t"
    "ds_read_b64_tr_b16 a[202:203], v206 offset:51200\n\t"
    "s_waitcnt lgkmcnt(10)\n\t"
    "v_mfma_f32_16x16x32_bf16 v[40:43], a[180:183], a[104:107], v[40:43]\n\t"
    "ds_read_b64_tr_b16 a[204:205], v207 offset:49152\n\t"
    "v_mfma_f32_16x16x32_bf16 v[44:47], a[180:183], a[108:111], v[44:47]\n\t"
    "ds_read_b64_tr_b16 a[206:207], v207 offset:51200\n\t"
    "s_waitcnt lgkmcnt(10)\n\t"
    "s_add_u32 m0, s68, 41984\n\t"
    "s_mov_b32 s69, 729216\n\t"
    "v_mfma_f32_16x16x32_bf16 v[48:51], a[184:187], a[104:107], v[48:51]\n\t"
    "buffer_load_dwordx4 v198, s[40:43], s69 offen lds\n\t"
    "ds_read_b64_tr_b16 a[208:209], v204 offset:57344\n\t"
    "v_mfma_f32_16x16x32_bf16 v[52:55], a[184:187], a[108:111], v[52:55]\n\t"
    "ds_read_b64_tr_b16 a[210:211], v204 offset:59392\n\t"
    "s_waitcnt lgkmcnt(10)\n\t"
    "v_mfma_f32_16x16x32_bf16 v[56:59], a[188:191], a[104:107], v[56:59]\n\t"
    "ds_read_b64_tr_b16 a[212:213], v205 offset:57344\n\t"
    "v_mfma_f32_16x16x32_bf16 v[60:63], a[188:191], a[108:111], v[60:63]\n\t"
    "ds_read_b64_tr_b16 a[214:215], v205 offset:59392\n\t"
    "s_waitcnt vmcnt(30) lgkmcnt(10)\n\t"
    "s_mov_b32 s69, 20480\n\t"
    "v_mfma_f32_16x16x32_bf16 v[0:3], a[192:195], a[112:115], v[0:3]\n\t"
    "buffer_load_dwordx4 a[48:51], v197, s[36:39], s69 offen offset:2048 sc1\n\t"
    "ds_read_b64_tr_b16 a[216:217], v206 offset:57344\n\t"
    "v_mfma_f32_16x16x32_bf16 v[4:7], a[192:195], a[116:119], v[4:7]\n\t"
    "ds_read_b64_tr_b16 a[218:219], v206 offset:59392\n\t"
    "s_waitcnt lgkmcnt(10)\n\t"
    "v_mfma_f32_16x16x32_bf16 v[8:11], a[196:199], a[112:115], v[8:11]\n\t"
    "ds_read_b64_tr_b16 a[220:221], v207 offset:57344\n\t"
    "v_mfma_f32_16x16x32_bf16 v[12:15], a[196:199], a[116:119], v[12:15]\n\t"
    "ds_read_b64_tr_b16 a[222:223], v207 offset:59392\n\t"
    "s_waitcnt lgkmcnt(10)\n\t"
    "s_mov_b32 s69, 151552\n\t"
    "v_mfma_f32_16x16x32_bf16 v[16:19], a[200:203], a[112:115], v[16:19]\n\t"
    "buffer_load_dwordx4 a[52:55], v197, s[36:39], s69 offen offset:2048 sc1\n\t"
    "ds_read_b64_tr_b16 a[224:225], v204 offset:53248\n\t"
    "v_mfma_f32_16x16x32_bf16 v[20:23], a[200:203], a[116:119], v[20:23]\n\t"
    "ds_read_b64_tr_b16 a[226:227], v204 offset:55296\n\t"
    "s_waitcnt lgkmcnt(10)\n\t"
    "v_mfma_f32_16x16x32_bf16 v[24:27], a[204:207], a[112:115], v[24:27]\n\t"
    "ds_read_b64_tr_b16 a[228:229], v205 offset:53248\n\t"
    "v_mfma_f32_16x16x32_bf16 v[28:31], a[204:207], a[116:119], v[28:31]\n\t"
    "ds_read_b64_tr_b16 a[230:231], v205 offset:55296\n\t"
    "s_waitcnt lgkmcnt(10)\n\t"
    "s_mov_b32 s69, 20480\n\t"
    "v_mfma_f32_16x16x32_bf16 v[32:35], a[208:211], a[112:115], v[32:35]\n\t"
    "buffer_load_dwordx4 a[56:59], v197, s[36:39], s69 offen offset:3072 sc1\n\t"
    "ds_read_b64_tr_b16 a[232:233], v206 offset:53248\n\t"
    "v_mfma_f32_16x16x32_bf16 v[36:39], a[208:211], a[116:119], v[36:39]\n\t"
    "ds_read_b64_tr_b16 a[234:235], v206 offset:55296\n\t"
    "s_waitcnt lgkmcnt(10)\n\t"
    "v_mfma_f32_16x16x32_bf16 v[40:43], a[212:215], a[112:115], v[40:43]\n\t"
    "ds_read_b64_tr_b16 a[236:237], v207 offset:53248\n\t"
    "v_mfma_f32_16x16x32_bf16 v[44:47], a[212:215], a[116:119], v[44:47]\n\t"
    "ds_read_b64_tr_b16 a[238:239], v207 offset:55296\n\t"
    "s_waitcnt lgkmcnt(10)\n\t"
    "s_mov_b32 s69, 151552\n\t"
    "v_mfma_f32_16x16x32_bf16 v[48:51], a[216:219], a[112:115], v[48:51]\n\t"
    "buffer_load_dwordx4 a[60:63], v197, s[36:39], s69 offen offset:3072 sc1\n\t"
    "ds_read_b64_tr_b16 a[240:241], v204 offset:61440\n\t"
    "v_mfma_f32_16x16x32_bf16 v[52:55], a[216:219], a[116:119], v[52:55]\n\t"
    "ds_read_b64_tr_b16 a[242:243], v204 offset:63488\n\t"
    "s_waitcnt lgkmcnt(10)\n\t"
    "v_mfma_f32_16x16x32_bf16 v[56:59], a[220:223], a[112:115], v[56:59]\n\t"
    "ds_read_b64_tr_b16 a[244:245], v205 offset:61440\n\t"
    "v_mfma_f32_16x16x32_bf16 v[60:63], a[220:223], a[116:119], v[60:63]\n\t"
    "ds_read_b64_tr_b16 a[246:247], v205 offset:63488\n\t"
    "s_waitcnt vmcnt(32) lgkmcnt(10)\n\t"
    "s_add_u32 m0, s68, 49152\n\t"
    "s_mov_b32 s69, 786432\n\t"
    "v_mfma_f32_16x16x32_bf16 v[0:3], a[224:227], a[120:123], v[0:3]\n\t"
    "buffer_load_dwordx4 v198, s[40:43], s69 offen lds\n\t"
    "ds_read_b64_tr_b16 a[248:249], v206 offset:61440\n\t"
    "v_mfma_f32_16x16x32_bf16 v[4:7], a[224:227], a[124:127], v[4:7]\n\t"
    "ds_read_b64_tr_b16 a[250:251], v206 offset:63488\n\t"
    "s_waitcnt lgkmcnt(10)\n\t"
    "v_mfma_f32_16x16x32_bf16 v[8:11], a[228:231], a[120:123], v[8:11]\n\t"
    "ds_read_b64_tr_b16 a[252:253], v207 offset:61440\n\t"
    "v_mfma_f32_16x16x32_bf16 v[12:15], a[228:231], a[124:127], v[12:15]\n\t"
    "ds_read_b64_tr_b16 a[254:255], v207 offset:63488\n\t"
    "s_waitcnt vmcnt(29)\n\t"
    "s_barrier\n\t"
    "s_waitcnt lgkmcnt(10)\n\t"
    "s_add_u32 m0, s68, 50176\n\t"
    "s_mov_b32 s69, 794624\n\t"
    "v_mfma_f32_16x16x32_bf16 v[16:19], a[232:235], a[120:123], v[16:19]\n\t"
    "buffer_load_dwordx4 v198, s[40:43], s69 offen lds\n\t"
    "ds_read_b64_tr_b16 a[128:129], v208 offset:0\n\t"
    "v_mfma_f32_16x16x32_bf16 v[20:23], a[232:235], a[124:127], v[20:23]\n\t"
    "ds_read_b64_tr_b16 a[130:131], v208 offset:2048\n\t"
    "s_waitcnt lgkmcnt(10)\n\t"
    "v_mfma_f32_16x16x32_bf16 v[24:27], a[236:239], a[120:123], v[24:27]\n\t"
    "ds_read_b64_tr_b16 a[132:133], v209 offset:0\n\t"
    "v_mfma_f32_16x16x32_bf16 v[28:31], a[236:239], a[124:127], v[28:31]\n\t"
    "ds_read_b64_tr_b16 a[134:135], v209 offset:2048\n\t"
    "s_waitcnt lgkmcnt(10)\n\t"
    "s_add_u32 m0, s68, 57344\n\t"
    "s_mov_b32 s69, 786560\n\t"
    "v_mfma_f32_16x16x32_bf16 v[32:35], a[240:243], a[120:123], v[32:35]\n\t"
    "buffer_load_dwordx4 v198, s[40:43], s69 offen lds\n\t"
    "ds_read_b64_tr_b16 a[136:137], v210 offset:0\n\t"
    "v_mfma_f32_16x16x32_bf16 v[36:39], a[240:243], a[124:127], v[36:39]\n\t"
    "ds_read_b64_tr_b16 a[138:139], v210 offset:2048\n\t"
    "s_waitcnt lgkmcnt(10)\n\t"
    "v_mfma_f32_16x16x32_bf16 v[40:43], a[244:247], a[120:123], v[40:43]\n\t"
    "ds_read_b64_tr_b16 a[140:141], v211 offset:0\n\t"
    "v_mfma_f32_16x16x32_bf16 v[44:47], a[244:247], a[124:127], v[44:47]\n\t"
    "ds_read_b64_tr_b16 a[142:143], v211 offset:2048\n\t"
    "s_waitcnt lgkmcnt(10)\n\t"
    "s_add_u32 m0, s68, 58368\n\t"
    "s_mov_b32 s69, 794752\n\t"
    "v_mfma_f32_16x16x32_bf16 v[48:51], a[248:251], a[120:123], v[48:51]\n\t"
    "buffer_load_dwordx4 v198, s[40:43], s69 offen lds\n\t"
    "ds_read_b64_tr_b16 a[144:145], v208 offset:8192\n\t"
    "v_mfma_f32_16x16x32_bf16 v[52:55], a[248:251], a[124:127], v[52:55]\n\t"
    "ds_read_b64_tr_b16 a[146:147], v208 offset:10240\n\t"
    "s_waitcnt lgkmcnt(10)\n\t"
    "v_mfma_f32_16x16x32_bf16 v[56:59], a[252:255], a[120:123], v[56:59]\n\t"
    "ds_read_b64_tr_b16 a[148:149], v209 offset:8192\n\t"
    "v_mfma_f32_16x16x32_bf16 v[60:63], a[252:255], a[124:127], v[60:63]\n\t"
    "ds_read_b64_tr_b16 a[150:151], v209 offset:10240\n\t"
    "s_waitcnt vmcnt(30) lgkmcnt(10)\n\t"
    "s_mov_b32 s69, 24576\n\t"
    "v_mfma_f32_16x16x32_bf16 v[0:3], a[128:131], a[0:3], v[0:3]\n\t"
    "buffer_load_dwordx4 a[64:67], v197, s[36:39], s69 offen offset:0 sc1\n\t"
    "ds_read_b64_tr_b16 a[152:153], v210 offset:8192\n\t"
    "v_mfma_f32_16x16x32_bf16 v[4:7], a[128:131], a[4:7], v[4:7]\n\t"
    "ds_read_b64_tr_b16 a[154:155], v210 offset:10240\n\t"
    "s_waitcnt lgkmcnt(10)\n\t"
    "v_mfma_f32_16x16x32_bf16 v[8:11], a[132:135], a[0:3], v[8:11]\n\t"
    "ds_read_b64_tr_b16 a[156:157], v211 offset:8192\n\t"
    "v_mfma_f32_16x16x32_bf16 v[12:15], a[132:135], a[4:7], v[12:15]\n\t"
    "ds_read_b64_tr_b16 a[158:159], v211 offset:10240\n\t"
    "s_waitcnt lgkmcnt(10)\n\t"
    "s_mov_b32 s69, 155648\n\t"
    "v_mfma_f32_16x16x32_bf16 v[16:19], a[136:139], a[0:3], v[16:19]\n\t"
    "buffer_load_dwordx4 a[68:71], v197, s[36:39], s69 offen offset:0 sc1\n\t"
    "ds_read_b64_tr_b16 a[160:161], v208 offset:4096\n\t"
    "v_mfma_f32_16x16x32_bf16 v[20:23], a[136:139], a[4:7], v[20:23]\n\t"
    "ds_read_b64_tr_b16 a[162:163], v208 offset:6144\n\t"
    "s_waitcnt lgkmcnt(10)\n\t"
    "v_mfma_f32_16x16x32_bf16 v[24:27], a[140:143], a[0:3], v[24:27]\n\t"
    "ds_read_b64_tr_b16 a[164:165], v209 offset:4096\n\t"
    "v_mfma_f32_16x16x32_bf16 v[28:31], a[140:143], a[4:7], v[28:31]\n\t"
    "ds_read_b64_tr_b16 a[166:167], v209 offset:6144\n\t"
    "s_waitcnt lgkmcnt(10)\n\t"
    "s_mov_b32 s69, 24576\n\t"
    "v_mfma_f32_16x16x32_bf16 v[32:35], a[144:147], a[0:3], v[32:35]\n\t"
    "buffer_load_dwordx4 a[72:75], v197, s[36:39], s69 offen offset:1024 sc1\n\t"
    "ds_read_b64_tr_b16 a[168:169], v210 offset:4096\n\t"
    "v_mfma_f32_16x16x32_bf16 v[36:39], a[144:147], a[4:7], v[36:39]\n\t"
    "ds_read_b64_tr_b16 a[170:171], v210 offset:6144\n\t"
    "s_waitcnt lgkmcnt(10)\n\t"
    "v_mfma_f32_16x16x32_bf16 v[40:43], a[148:151], a[0:3], v[40:43]\n\t"
    "ds_read_b64_tr_b16 a[172:173], v211 offset:4096\n\t"
    "v_mfma_f32_16x16x32_bf16 v[44:47], a[148:151], a[4:7], v[44:47]\n\t"
    "ds_read_b64_tr_b16 a[174:175], v211 offset:6144\n\t"
    "s_waitcnt lgkmcnt(10)\n\t"
    "s_mov_b32 s69, 155648\n\t"
    "v_mfma_f32_16x16x32_bf16 v[48:51], a[152:155], a[0:3], v[48:51]\n\t"
    "buffer_load_dwordx4 a[76:79], v197, s[36:39], s69 offen offset:1024 sc1\n\t"
    "ds_read_b64_tr_b16 a[176:177], v208 offset:12288\n\t"
    "v_mfma_f32_16x16x32_bf16 v[52:55], a[152:155], a[4:7], v[52:55]\n\t"
    "ds_read_b64_tr_b16 a[178:179], v208 offset:14336\n\t"
    "s_waitcnt lgkmcnt(10)\n\t"
    "v_mfma_f32_16x16x32_bf16 v[56:59], a[156:159], a[0:3], v[56:59]\n\t"
    "ds_read_b64_tr_b16 a[180:181], v209 offset:12288\n\t"
    "v_mfma_f32_16x16x32_bf16 v[60:63], a[156:159], a[4:7], v[60:63]\n\t"
    "ds_read_b64_tr_b16 a[182:183], v209 offset:14336\n\t"
    "s_waitcnt vmcnt(32) lgkmcnt(10)\n\t"
    "s_add_u32 m0, s68, 65536\n\t"
    "s_mov_b32 s69, 851968\n\t"
    "v_mfma_f32_16x16x32_bf16 v[0:3], a[160:163], a[8:11], v[0:3]\n\t"
    "buffer_load_dwordx4 v198, s[40:43], s69 offen lds\n\t"
    "ds_read_b64_tr_b16 a[184:185], v210 offset:12288\n\t"
    "v_mfma_f32_16x16x32_bf16 v[4:7], a[160:163], a[12:15], v[4:7]\n\t"
    "ds_read_b64_tr_b16 a[186:187], v210 offset:14336\n\t"
    "s_waitcnt lgkmcnt(10)\n\t"
    "v_mfma_f32_16x16x32_bf16 v[8:11], a[164:167], a[8:11], v[8:11]\n\t"
    "ds_read_b64_tr_b16 a[188:189], v211 offset:12288\n\t"
    "v_mfma_f32_16x16x32_bf16 v[12:15], a[164:167], a[12:15], v[12:15]\n\t"
    "ds_read_b64_tr_b16 a[190:191], v211 offset:14336\n\t"
    "s_waitcnt vmcnt(29)\n\t"
    "s_barrier\n\t"
    "s_waitcnt lgkmcnt(10)\n\t"
    "s_add_u32 m0, s68, 66560\n\t"
    "s_mov_b32 s69, 860160\n\t"
    "v_mfma_f32_16x16x32_bf16 v[16:19], a[168:171], a[8:11], v[16:19]\n\t"
    "buffer_load_dwordx4 v198, s[40:43], s69 offen lds\n\t"
    "ds_read_b64_tr_b16 a[192:193], v200 offset:0\n\t"
    "v_mfma_f32_16x16x32_bf16 v[20:23], a[168:171], a[12:15], v[20:23]\n\t"
    "ds_read_b64_tr_b16 a[194:195], v200 offset:2048\n\t"
    "s_waitcnt lgkmcnt(10)\n\t"
    "s_add_u32 m0, s68, 114688\n\t"
    "s_mov_b32 s69, 0\n\t"
    "v_mfma_f32_16x16x32_bf16 v[24:27], a[172:175], a[8:11], v[24:27]\n\t"
    "buffer_load_dwordx4 v199, s[52:55], s69 offen lds\n\t"
    "ds_read_b64_tr_b16 a[196:197], v201 offset:0\n\t"
    "v_mfma_f32_16x16x32_bf16 v[28:31], a[172:175], a[12:15], v[28:31]\n\t"
    "ds_read_b64_tr_b16 a[198:199], v201 offset:2048\n\t"
    "s_waitcnt lgkmcnt(10)\n\t"
    "s_add_u32 m0, s68, 73728\n\t"
    "s_mov_b32 s69, 852096\n\t"
    "v_mfma_f32_16x16x32_bf16 v[32:35], a[176:179], a[8:11], v[32:35]\n\t"
    "buffer_load_dwordx4 v198, s[40:43], s69 offen lds\n\t"
    "ds_read_b64_tr_b16 a[200:201], v202 offset:0\n\t"
    "v_mfma_f32_16x16x32_bf16 v[36:39], a[176:179], a[12:15], v[36:39]\n\t"
    "ds_read_b64_tr_b16 a[202:203], v202 offset:2048\n\t"
    "s_waitcnt lgkmcnt(10)\n\t"
    "s_add_u32 m0, s68, 115712\n\t"
    "s_mov_b32 s69, 28672\n\t"
    "v_mfma_f32_16x16x32_bf16 v[40:43], a[180:183], a[8:11], v[40:43]\n\t"
    "buffer_load_dwordx4 v199, s[52:55], s69 offen lds\n\t"
    "ds_read_b64_tr_b16 a[204:205], v203 offset:0\n\t"
    "v_mfma_f32_16x16x32_bf16 v[44:47], a[180:183], a[12:15], v[44:47]\n\t"
    "ds_read_b64_tr_b16 a[206:207], v203 offset:2048\n\t"
    "s_waitcnt lgkmcnt(10)\n\t"
    "s_add_u32 m0, s68, 74752\n\t"
    "s_mov_b32 s69, 860288\n\t"
    "v_mfma_f32_16x16x32_bf16 v[48:51], a[184:187], a[8:11], v[48:51]\n\t"
    "buffer_load_dwordx4 v198, s[40:43], s69 offen lds\n\t"
    "ds_read_b64_tr_b16 a[208:209], v200 offset:8192\n\t"
    "v_mfma_f32_16x16x32_bf16 v[52:55], a[184:187], a[12:15], v[52:55]\n\t"
    "ds_read_b64_tr_b16 a[210:211], v200 offset:10240\n\t"
    "s_waitcnt lgkmcnt(10)\n\t"
    "s_add_u32 m0, s68, 122880\n\t"
    "s_mov_b32 s69, 0\n\t"
    "v_mfma_f32_16x16x32_bf16 v[56:59], a[188:191], a[8:11], v[56:59]\n\t"
    "buffer_load_dwordx4 v199, s[56:59], s69 offen lds\n\t"
    "ds_read_b64_tr_b16 a[212:213], v201 offset:8192\n\t"
    "v_mfma_f32_16x16x32_bf16 v[60:63], a[188:191], a[12:15], v[60:63]\n\t"
    "ds_read_b64_tr_b16 a[214:215], v201 offset:10240\n\t"
    "s_waitcnt vmcnt(33) lgkmcnt(10)\n\t"
    "s_mov_b32 s69, 24576\n\t"
    "v_mfma_f32_16x16x32_bf16 v[0:3], a[192:195], a[16:19], v[0:3]\n\t"
    "buffer_load_dwordx4 a[80:83], v197, s[36:39], s69 offen offset:2048 sc1\n\t"
    "ds_read_b64_tr_b16 a[216:217], v202 offset:8192\n\t"
    "v_mfma_f32_16x16x32_bf16 v[4:7], a[192:195], a[20:23], v[4:7]\n\t"
    "ds_read_b64_tr_b16 a[218:219], v202 offset:10240\n\t"
    "s_waitcnt lgkmcnt(10)\n\t"
    "s_add_u32 m0, s68, 123904\n\t"
    "s_mov_b32 s69, 28672\n\t"
    "v_mfma_f32_16x16x32_bf16 v[8:11], a[196:199], a[16:19], v[8:11]\n\t"
    "buffer_load_dwordx4 v199, s[56:59], s69 offen lds\n\t"
    "ds_read_b64_tr_b16 a[220:221], v203 offset:8192\n\t"
    "v_mfma_f32_16x16x32_bf16 v[12:15], a[196:199], a[20:23], v[12:15]\n\t"
    "ds_read_b64_tr_b16 a[222:223], v203 offset:10240\n\t"
    "s_waitcnt lgkmcnt(10)\n\t"
    "s_mov_b32 s69, 155648\n\t"
    "v_mfma_f32_16x16x32_bf16 v[16:19], a[200:203], a[16:19], v[16:19]\n\t"
    "buffer_load_dwordx4 a[84:87], v197, s[36:39], s69 offen offset:2048 sc1\n\t"
    "ds_read_b64_tr_b16 a[224:225], v200 offset:4096\n\t"
    "v_mfma_f32_16x16x32_bf16 v[20:23], a[200:203], a[20:23], v[20:23]\n\t"
    "ds_read_b64_tr_b16 a[226:227], v200 offset:6144\n\t"
    "s_waitcnt lgkmcnt(10)\n\t"
    "v_mfma_f32_16x16x32_bf16 v[24:27], a[204:207], a[16:19], v[24:27]\n\t"
    "ds_read_b64_tr_b16 a[228:229], v201 offset:4096\n\t"
    "v_mfma_f32_16x16x32_bf16 v[28:31], a[204:207], a[20:23], v[28:31]\n\t"
    "ds_read_b64_tr_b16 a[230:231], v201 offset:6144\n\t"
    "s_waitcnt lgkmcnt(10)\n\t"
    "s_mov_b32 s69, 24576\n\t"
    "v_mfma_f32_16x16x32_bf16 v[32:35], a[208:211], a[16:19], v[32:35]\n\t"
    "buffer_load_dwordx4 a[88:91], v197, s[36:39], s69 offen offset:3072 sc1\n\t"
    "ds_read_b64_tr_b16 a[232:233], v202 offset:4096\n\t"
    "v_mfma_f32_16x16x32_bf16 v[36:39], a[208:211], a[20:23], v[36:39]\n\t"
    "ds_read_b64_tr_b16 a[234:235], v202 offset:6144\n\t"
    "s_waitcnt lgkmcnt(10)\n\t"
    "v_mfma_f32_16x16x32_bf16 v[40:43], a[212:215], a[16:19], v[40:43]\n\t"
    "ds_read_b64_tr_b16 a[236:237], v203 offset:4096\n\t"
    "v_mfma_f32_16x16x32_bf16 v[44:47], a[212:215], a[20:23], v[44:47]\n\t"
    "ds_read_b64_tr_b16 a[238:239], v203 offset:6144\n\t"
    "s_waitcnt lgkmcnt(10)\n\t"
    "s_mov_b32 s69, 155648\n\t"
    "v_mfma_f32_16x16x32_bf16 v[48:51], a[216:219], a[16:19], v[48:51]\n\t"
    "buffer_load_dwordx4 a[92:95], v197, s[36:39], s69 offen offset:3072 sc1\n\t"
    "ds_read_b64_tr_b16 a[240:241], v200 offset:12288\n\t"
    "v_mfma_f32_16x16x32_bf16 v[52:55], a[216:219], a[20:23], v[52:55]\n\t"
    "ds_read_b64_tr_b16 a[242:243], v200 offset:14336\n\t"
    "s_waitcnt lgkmcnt(10)\n\t"
    "v_mfma_f32_16x16x32_bf16 v[56:59], a[220:223], a[16:19], v[56:59]\n\t"
    "ds_read_b64_tr_b16 a[244:245], v201 offset:12288\n\t"
    "v_mfma_f32_16x16x32_bf16 v[60:63], a[220:223], a[20:23], v[60:63]\n\t"
    "ds_read_b64_tr_b16 a[246:247], v201 offset:14336\n\t"
    "s_waitcnt vmcnt(36) lgkmcnt(10)\n\t"
    "s_add_u32 m0, s68, 81920\n\t"
    "s_mov_b32 s69, 917504\n\t"
    "v_mfma_f32_16x16x32_bf16 v[0:3], a[224:227], a[24:27], v[0:3]\n\t"
    "buffer_load_dwordx4 v198, s[40:43], s69 offen lds\n\t"
    "ds_read_b64_tr_b16 a[248:249], v202 offset:12288\n\t"
    "v_mfma_f32_16x16x32_bf16 v[4:7], a[224:227], a[28:31], v[4:7]\n\t"
    "ds_read_b64_tr_b16 a[250:251], v202 offset:14336\n\t"
    "s_waitcnt lgkmcnt(10)\n\t"
    "v_mfma_f32_16x16x32_bf16 v[8:11], a[228:231], a[24:27], v[8:11]\n\t"
    "ds_read_b64_tr_b16 a[252:253], v203 offset:12288\n\t"
    "v_mfma_f32_16x16x32_bf16 v[12:15], a[228:231], a[28:31], v[12:15]\n\t"
    "ds_read_b64_tr_b16 a[254:255], v203 offset:14336\n\t"
    "s_waitcnt vmcnt(33)\n\t"
    "s_barrier\n\t"
    "s_waitcnt lgkmcnt(10)\n\t"
    "s_add_u32 m0, s68, 82944\n\t"
    "s_mov_b32 s69, 925696\n\t"
    "v_mfma_f32_16x16x32_bf16 v[16:19], a[232:235], a[24:27], v[16:19]\n\t"
    "buffer_load_dwordx4 v198, s[40:43], s69 offen lds\n\t"
    "ds_read_b64_tr_b16 a[128:129], v200 offset:16384\n\t"
    "v_mfma_f32_16x16x32_bf16 v[20:23], a[232:235], a[28:31], v[20:23]\n\t"
    "ds_read_b64_tr_b16 a[130:131], v200 offset:18432\n\t"
    "s_waitcnt lgkmcnt(10)\n\t"
    "s_add_u32 m0, s68, 131072\n\t"
    "s_mov_b32 s69, 229376\n\t"
    "v_mfma_f32_16x16x32_bf16 v[24:27], a[236:239], a[24:27], v[24:27]\n\t"
    "buffer_load_dwordx4 v199, s[52:55], s69 offen lds\n\t"
    "ds_read_b64_tr_b16 a[132:133], v201 offset:16384\n\t"
    "v_mfma_f32_16x16x32_bf16 v[28:31], a[236:239], a[28:31], v[28:31]\n\t"
    "ds_read_b64_tr_b16 a[134:135], v201 offset:18432\n\t"
    "s_waitcnt lgkmcnt(10)\n\t"
    "s_add_u32 m0, s68, 90112\n\t"
    "s_mov_b32 s69, 917632\n\t"
    "v_mfma_f32_16x16x32_bf16 v[32:35], a[240:243], a[24:27], v[32:35]\n\t"
    "buffer_load_dwordx4 v198, s[40:43], s69 offen lds\n\t"
    "ds_read_b64_tr_b16 a[136:137], v202 offset:16384\n\t"
    "v_mfma_f32_16x16x32_bf16 v[36:39], a[240:243], a[28:31], v[36:39]\n\t"
    "ds_read_b64_tr_b16 a[138:139], v202 offset:18432\n\t"
    "s_waitcnt lgkmcnt(10)\n\t"
    "s_add_u32 m0, s68, 132096\n\t"
    "s_mov_b32 s69, 258048\n\t"
    "v_mfma_f32_16x16x32_bf16 v[40:43], a[244:247], a[24:27], v[40:43]\n\t"
    "buffer_load_dwordx4 v199, s[52:55], s69 offen lds\n\t"
    "ds_read_b64_tr_b16 a[140:141], v203 offset:16384\n\t"
    "v_mfma_f32_16x16x32_bf16 v[44:47], a[244:247], a[28:31], v[44:47]\n\t"
    "ds_read_b64_tr_b16 a[142:143], v203 offset:18432\n\t"
    "s_waitcnt lgkmcnt(10)\n\t"
    "s_add_u32 m0, s68, 91136\n\t"
    "s_mov_b32 s69, 925824\n\t"
    "v_mfma_f32_16x16x32_bf16 v[48:51], a[248:251], a[24:27], v[48:51]\n\t"
    "buffer_load_dwordx4 v198, s[40:43], s69 offen lds\n\t"
    "ds_read_b64_tr_b16 a[144:145], v200 offset:24576\n\t"
    "v_mfma_f32_16x16x32_bf16 v[52:55], a[248:251], a[28:31], v[52:55]\n\t"
    "ds_read_b64_tr_b16 a[146:147], v200 offset:26624\n\t"
    "s_waitcnt lgkmcnt(10)\n\t"
    "s_add_u32 m0, s68, 139264\n\t"
    "s_mov_b32 s69, 229376\n\t"
    "v_mfma_f32_16x16x32_bf16 v[56:59], a[252:255], a[24:27], v[56:59]\n\t"
    "buffer_load_dwordx4 v199, s[56:59], s69 offen lds\n\t"
    "ds_read_b64_tr_b16 a[148:149], v201 offset:24576\n\t"
    "v_mfma_f32_16x16x32_bf16 v[60:63], a[252:255], a[28:31], v[60:63]\n\t"
    "ds_read_b64_tr_b16 a[150:151], v201 offset:26624\n\t"
    "s_waitcnt vmcnt(37) lgkmcnt(10)\n\t"
    "s_mov_b32 s69, 28672\n\t"
    "v_mfma_f32_16x16x32_bf16 v[0:3], a[128:131], a[32:35], v[0:3]\n\t"
    "buffer_load_dwordx4 a[96:99], v197, s[36:39], s69 offen offset:0 sc1\n\t"
    "ds_read_b64_tr_b16 a[152:153], v202 offset:24576\n\t"
    "v_mfma_f32_16x16x32_bf16 v[4:7], a[128:131], a[36:39], v[4:7]\n\t"
    "ds_read_b64_tr_b16 a[154:155], v202 offset:26624\n\t"
    "s_waitcnt lgkmcnt(10)\n\t"
    "s_add_u32 m0, s68, 140288\n\t"
    "s_mov_b32 s69, 258048\n\t"
    "v_mfma_f32_16x16x32_bf16 v[8:11], a[132:135], a[32:35], v[8:11]\n\t"
    "buffer_load_dwordx4 v199, s[56:59], s69 offen lds\n\t"
    "ds_read_b64_tr_b16 a[156:157], v203 offset:24576\n\t"
    "v_mfma_f32_16x16x32_bf16 v[12:15], a[132:135], a[36:39], v[12:15]\n\t"
    "ds_read_b64_tr_b16 a[158:159], v203 offset:26624\n\t"
    "s_waitcnt lgkmcnt(10)\n\t"
    "s_mov_b32 s69, 159744\n\t"
    "v_mfma_f32_16x16x32_bf16 v[16:19], a[136:139], a[32:35], v[16:19]\n\t"
    "buffer_load_dwordx4 a[100:103], v197, s[36:39], s69 offen offset:0 sc1\n\t"
    "ds_read_b64_tr_b16 a[160:161], v200 offset:20480\n\t"
    "v_mfma_f32_16x16x32_bf16 v[20:23], a[136:139], a[36:39], v[20:23]\n\t"
    "ds_read_b64_tr_b16 a[162:163], v200 offset:22528\n\t"
    "s_waitcnt lgkmcnt(10)\n\t"
    "v_mfma_f32_16x16x32_bf16 v[24:27], a[140:143], a[32:35], v[24:27]\n\t"
    "ds_read_b64_tr_b16 a[164:165], v201 offset:20480\n\t"
    "v_mfma_f32_16x16x32_bf16 v[28:31], a[140:143], a[36:39], v[28:31]\n\t"
    "ds_read_b64_tr_b16 a[166:167], v201 offset:22528\n\t"
    "s_waitcnt lgkmcnt(10)\n\t"
    "s_mov_b32 s69, 28672\n\t"
    "v_mfma_f32_16x16x32_bf16 v[32:35], a[144:147], a[32:35], v[32:35]\n\t"
    "buffer_load_dwordx4 a[104:107], v197, s[36:39], s69 offen offset:1024 sc1\n\t"
    "ds_read_b64_tr_b16 a[168:169], v202 offset:20480\n\t"
    "v_mfma_f32_16x16x32_bf16 v[36:39], a[144:147], a[36:39], v[36:39]\n\t"
    "ds_read_b64_tr_b16 a[170:171], v202 offset:22528\n\t"
    "s_waitcnt lgkmcnt(10)\n\t"
    "v_mfma_f32_16x16x32_bf16 v[40:43], a[148:151], a[32:35], v[40:43]\n\t"
    "ds_read_b64_tr_b16 a[172:173], v203 offset:20480\n\t"
    "v_mfma_f32_16x16x32_bf16 v[44:47], a[148:151], a[36:39], v[44:47]\n\t"
    "ds_read_b64_tr_b16 a[174:175], v203 offset:22528\n\t"
    "s_waitcnt lgkmcnt(10)\n\t"
    "s_mov_b32 s69, 159744\n\t"
    "v_mfma_f32_16x16x32_bf16 v[48:51], a[152:155], a[32:35], v[48:51]\n\t"
    "buffer_load_dwordx4 a[108:111], v197, s[36:39], s69 offen offset:1024 sc1\n\t"
    "ds_read_b64_tr_b16 a[176:177], v200 offset:28672\n\t"
    "v_mfma_f32_16x16x32_bf16 v[52:55], a[152:155], a[36:39], v[52:55]\n\t"
    "ds_read_b64_tr_b16 a[178:179], v200 offset:30720\n\t"
    "s_waitcnt lgkmcnt(10)\n\t"
    "v_mfma_f32_16x16x32_bf16 v[56:59], a[156:159], a[32:35], v[56:59]\n\t"
    "ds_read_b64_tr_b16 a[180:181], v201 offset:28672\n\t"
    "v_mfma_f32_16x16x32_bf16 v[60:63], a[156:159], a[36:39], v[60:63]\n\t"
    "ds_read_b64_tr_b16 a[182:183], v201 offset:30720\n\t"
    "s_waitcnt vmcnt(40) lgkmcnt(10)\n\t"
    "s_add_u32 m0, s68, 98304\n\t"
    "s_mov_b32 s69, 983040\n\t"
    "v_mfma_f32_16x16x32_bf16 v[0:3], a[160:163], a[40:43], v[0:3]\n\t"
    "buffer_load_dwordx4 v198, s[40:43], s69 offen lds\n\t"
    "ds_read_b64_tr_b16 a[184:185], v202 offset:28672\n\t"
    "v_mfma_f32_16x16x32_bf16 v[4:7], a[160:163], a[44:47], v[4:7]\n\t"
    "ds_read_b64_tr_b16 a[186:187], v202 offset:30720\n\t"
    "s_waitcnt lgkmcnt(10)\n\t"
    "v_mfma_f32_16x16x32_bf16 v[8:11], a[164:167], a[40:43], v[8:11]\n\t"
    "ds_read_b64_tr_b16 a[188:189], v203 offset:28672\n\t"
    "v_mfma_f32_16x16x32_bf16 v[12:15], a[164:167], a[44:47], v[12:15]\n\t"
    "ds_read_b64_tr_b16 a[190:191], v203 offset:30720\n\t"
    "s_waitcnt vmcnt(37)\n\t"
    "s_barrier\n\t"
    "s_waitcnt lgkmcnt(10)\n\t"
    "s_add_u32 m0, s68, 99328\n\t"
    "s_mov_b32 s69, 991232\n\t"
    "v_mfma_f32_16x16x32_bf16 v[16:19], a[168:171], a[40:43], v[16:19]\n\t"
    "buffer_load_dwordx4 v198, s[40:43], s69 offen lds\n\t"
    "ds_read_b64_tr_b16 a[192:193], v200 offset:32768\n\t"
    "v_mfma_f32_16x16x32_bf16 v[20:23], a[168:171], a[44:47], v[20:23]\n\t"
    "ds_read_b64_tr_b16 a[194:195], v200 offset:34816\n\t"
    "s_waitcnt lgkmcnt(10)\n\t"
    "s_add_u32 m0, s68, 0\n\t"
    "s_mov_b32 s69, 458752\n\t"
    "v_mfma_f32_16x16x32_bf16 v[24:27], a[172:175], a[40:43], v[24:27]\n\t"
    "buffer_load_dwordx4 v199, s[52:55], s69 offen lds\n\t"
    "ds_read_b64_tr_b16 a[196:197], v201 offset:32768\n\t"
    "v_mfma_f32_16x16x32_bf16 v[28:31], a[172:175], a[44:47], v[28:31]\n\t"
    "ds_read_b64_tr_b16 a[198:199], v201 offset:34816\n\t"
    "s_waitcnt lgkmcnt(10)\n\t"
    "s_add_u32 m0, s68, 106496\n\t"
    "s_mov_b32 s69, 983168\n\t"
    "v_mfma_f32_16x16x32_bf16 v[32:35], a[176:179], a[40:43], v[32:35]\n\t"
    "buffer_load_dwordx4 v198, s[40:43], s69 offen lds\n\t"
    "ds_read_b64_tr_b16 a[200:201], v202 offset:32768\n\t"
    "v_mfma_f32_16x16x32_bf16 v[36:39], a[176:179], a[44:47], v[36:39]\n\t"
    "ds_read_b64_tr_b16 a[202:203], v202 offset:34816\n\t"
    "s_waitcnt lgkmcnt(10)\n\t"
    "s_add_u32 m0, s68, 1024\n\t"
    "s_mov_b32 s69, 487424\n\t"
    "v_mfma_f32_16x16x32_bf16 v[40:43], a[180:183], a[40:43], v[40:43]\n\t"
    "buffer_load_dwordx4 v199, s[52:55], s69 offen lds\n\t"
    "ds_read_b64_tr_b16 a[204:205], v203 offset:32768\n\t"
    "v_mfma_f32_16x16x32_bf16 v[44:47], a[180:183], a[44:47], v[44:47]\n\t"
    "ds_read_b64_tr_b16 a[206:207], v203 offset:34816\n\t"
    "s_waitcnt lgkmcnt(10)\n\t"
    "s_add_u32 m0, s68, 107520\n\t"
    "s_mov_b32 s69, 991360\n\t"
    "v_mfma_f32_16x16x32_bf16 v[48:51], a[184:187], a[40:43], v[48:51]\n\t"
    "buffer_load_dwordx4 v198, s[40:43], s69 offen lds\n\t"
    "ds_read_b64_tr_b16 a[208:209], v200 offset:40960\n\t"
    "v_mfma_f32_16x16x32_bf16 v[52:55], a[184:187], a[44:47], v[52:55]\n\t"
    "ds_read_b64_tr_b16 a[210:211], v200 offset:43008\n\t"
    "s_waitcnt lgkmcnt(10)\n\t"
    "s_add_u32 m0, s68, 8192\n\t"
    "s_mov_b32 s69, 458752\n\t"
    "v_mfma_f32_16x16x32_bf16 v[56:59], a[188:191], a[40:43], v[56:59]\n\t"
    "buffer_load_dwordx4 v199, s[56:59], s69 offen lds\n\t"
    "ds_read_b64_tr_b16 a[212:213], v201 offset:40960\n\t"
    "v_mfma_f32_16x16x32_bf16 v[60:63], a[188:191], a[44:47], v[60:63]\n\t"
    "ds_read_b64_tr_b16 a[214:215], v201 offset:43008\n\t"
    "s_waitcnt vmcnt(41) lgkmcnt(10)\n\t"
    "s_mov_b32 s69, 28672\n\t"
    "v_mfma_f32_16x16x32_bf16 v[0:3], a[192:195], a[48:51], v[0:3]\n\t"
    "buffer_load_dwordx4 a[112:115], v197, s[36:39], s69 offen offset:2048 sc1\n\t"
    "ds_read_b64_tr_b16 a[216:217], v202 offset:40960\n\t"
    "v_mfma_f32_16x16x32_bf16 v[4:7], a[192:195], a[52:55], v[4:7]\n\t"
    "ds_read_b64_tr_b16 a[218:219], v202 offset:43008\n\t"
    "s_waitcnt lgkmcnt(10)\n\t"
    "s_add_u32 m0, s68, 9216\n\t"
    "s_mov_b32 s69, 487424\n\t"
    "v_mfma_f32_16x16x32_bf16 v[8:11], a[196:199], a[48:51], v[8:11]\n\t"
    "buffer_load_dwordx4 v199, s[56:59], s69 offen lds\n\t"
    "ds_read_b64_tr_b16 a[220:221], v203 offset:40960\n\t"
    "v_mfma_f32_16x16x32_bf16 v[12:15], a[196:199], a[52:55], v[12:15]\n\t"
    "ds_read_b64_tr_b16 a[222:223], v203 offset:43008\n\t"
    "s_waitcnt lgkmcnt(10)\n\t"
    "s_mov_b32 s69, 159744\n\t"
    "v_mfma_f32_16x16x32_bf16 v[16:19], a[200:203], a[48:51], v[16:19]\n\t"
    "buffer_load_dwordx4 a[116:119], v197, s[36:39], s69 offen offset:2048 sc1\n\t"
    "ds_read_b64_tr_b16 a[224:225], v200 offset:36864\n\t"
    "v_mfma_f32_16x16x32_bf16 v[20:23], a[200:203], a[52:55], v[20:23]\n\t"
    "ds_read_b64_tr_b16 a[226:227], v200 offset:38912\n\t"
    "s_waitcnt lgkmcnt(10)\n\t"
    "v_mfma_f32_16x16x32_bf16 v[24:27], a[204:207], a[48:51], v[24:27]\n\t"
    "ds_read_b64_tr_b16 a[228:229], v201 offset:36864\n\t"
    "v_mfma_f32_16x16x32_bf16 v[28:31], a[204:207], a[52:55], v[28:31]\n\t"
    "ds_read_b64_tr_b16 a[230:231], v201 offset:38912\n\t"
    "s_waitcnt lgkmcnt(10)\n\t"
    "s_mov_b32 s69, 28672\n\t"
    "v_mfma_f32_16x16x32_bf16 v[32:35], a[208:211], a[48:51], v[32:35]\n\t"
    "buffer_load_dwordx4 a[120:123], v197, s[36:39], s69 offen offset:3072 sc1\n\t"
    "ds_read_b64_tr_b16 a[232:233], v202 offset:36864\n\t"
    "v_mfma_f32_16x16x32_bf16 v[36:39], a[208:211], a[52:55], v[36:39]\n\t"
    "ds_read_b64_tr_b16 a[234:235], v202 offset:38912\n\t"
    "s_waitcnt lgkmcnt(10)\n\t"
    "v_mfma_f32_16x16x32_bf16 v[40:43], a[212:215], a[48:51], v[40:43]\n\t"
    "ds_read_b64_tr_b16 a[236:237], v203 offset:36864\n\t"
    "v_mfma_f32_16x16x32_bf16 v[44:47], a[212:215], a[52:55], v[44:47]\n\t"
    "ds_read_b64_tr_b16 a[238:239], v203 offset:38912\n\t"
    "s_waitcnt lgkmcnt(10)\n\t"
    "s_mov_b32 s69, 159744\n\t"
    "v_mfma_f32_16x16x32_bf16 v[48:51], a[216:219], a[48:51], v[48:51]\n\t"
    "buffer_load_dwordx4 a[124:127], v197, s[36:39], s69 offen offset:3072 sc1\n\t"
    "ds_read_b64_tr_b16 a[240:241], v200 offset:45056\n\t"
    "v_mfma_f32_16x16x32_bf16 v[52:55], a[216:219], a[52:55], v[52:55]\n\t"
    "ds_read_b64_tr_b16 a[242:243], v200 offset:47104\n\t"
    "s_waitcnt lgkmcnt(10)\n\t"
    "v_mfma_f32_16x16x32_bf16 v[56:59], a[220:223], a[48:51], v[56:59]\n\t"
    "ds_read_b64_tr_b16 a[244:245], v201 offset:45056\n\t"
    "v_mfma_f32_16x16x32_bf16 v[60:63], a[220:223], a[52:55], v[60:63]\n\t"
    "ds_read_b64_tr_b16 a[246:247], v201 offset:47104\n\t"
    "s_waitcnt vmcnt(44) lgkmcnt(10)\n\t"
    "v_mfma_f32_16x16x32_bf16 v[0:3], a[224:227], a[56:59], v[0:3]\n\t"
    "ds_read_b64_tr_b16 a[248:249], v202 offset:45056\n\t"
    "v_mfma_f32_16x16x32_bf16 v[4:7], a[224:227], a[60:63], v[4:7]\n\t"
    "ds_read_b64_tr_b16 a[250:251], v202 offset:47104\n\t"
    "s_waitcnt lgkmcnt(10)\n\t"
    "v_mfma_f32_16x16x32_bf16 v[8:11], a[228:231], a[56:59], v[8:11]\n\t"
    "ds_read_b64_tr_b16 a[252:253], v203 offset:45056\n\t"
    "v_mfma_f32_16x16x32_bf16 v[12:15], a[228:231], a[60:63], v[12:15]\n\t"
    "ds_read_b64_tr_b16 a[254:255], v203 offset:47104\n\t"
    "s_waitcnt vmcnt(40)\n\t"
    "s_barrier\n\t"
    "s_waitcnt lgkmcnt(10)\n\t"
    "v_mfma_f32_16x16x32_bf16 v[16:19], a[232:235], a[56:59], v[16:19]\n\t"
    "ds_read_b64_tr_b16 a[128:129], v200 offset:49152\n\t"
    "v_mfma_f32_16x16x32_bf16 v[20:23], a[232:235], a[60:63], v[20:23]\n\t"
    "ds_read_b64_tr_b16 a[130:131], v200 offset:51200\n\t"
    "s_waitcnt lgkmcnt(10)\n\t"
    "s_add_u32 m0, s68, 16384\n\t"
    "s_mov_b32 s69, 688128\n\t"
    "v_mfma_f32_16x16x32_bf16 v[24:27], a[236:239], a[56:59], v[24:27]\n\t"
    "buffer_load_dwordx4 v199, s[52:55], s69 offen lds\n\t"
    "ds_read_b64_tr_b16 a[132:133], v201 offset:49152\n\t"
    "v_mfma_f32_16x16x32_bf16 v[28:31], a[236:239], a[60:63], v[28:31]\n\t"
    "ds_read_b64_tr_b16 a[134:135], v201 offset:51200\n\t"
    "s_waitcnt lgkmcnt(10)\n\t"
    "v_mfma_f32_16x16x32_bf16 v[32:35], a[240:243], a[56:59], v[32:35]\n\t"
    "ds_read_b64_tr_b16 a[136:137], v202 offset:49152\n\t"
    "v_mfma_f32_16x16x32_bf16 v[36:39], a[240:243], a[60:63], v[36:39]\n\t"
    "ds_read_b64_tr_b16 a[138:139], v202 offset:51200\n\t"
    "s_waitcnt lgkmcnt(10)\n\t"
    "s_add_u32 m0, s68, 17408\n\t"
    "s_mov_b32 s69, 716800\n\t"
    "v_mfma_f32_16x16x32_bf16 v[40:43], a[244:247], a[56:59], v[40:43]\n\t"
    "buffer_load_dwordx4 v199, s[52:55], s69 offen lds\n\t"
    "ds_read_b64_tr_b16 a[140:141], v203 offset:49152\n\t"
    "v_mfma_f32_16x16x32_bf16 v[44:47], a[244:247], a[60:63], v[44:47]\n\t"
    "ds_read_b64_tr_b16 a[142:143], v203 offset:51200\n\t"
    "s_waitcnt lgkmcnt(10)\n\t"
    "v_mfma_f32_16x16x32_bf16 v[48:51], a[248:251], a[56:59], v[48:51]\n\t"
    "ds_read_b64_tr_b16 a[144:145], v200 offset:57344\n\t"
    "v_mfma_f32_16x16x32_bf16 v[52:55], a[248:251], a[60:63], v[52:55]\n\t"
    "ds_read_b64_tr_b16 a[146:147], v200 offset:59392\n\t"
    "s_waitcnt lgkmcnt(10)\n\t"
    "s_add_u32 m0, s68, 24576\n\t"
    "s_mov_b32 s69, 688128\n\t"
    "v_mfma_f32_16x16x32_bf16 v[56:59], a[252:255], a[56:59], v[56:59]\n\t"
    "buffer_load_dwordx4 v199, s[56:59], s69 offen lds\n\t"
    "ds_read_b64_tr_b16 a[148:149], v201 offset:57344\n\t"
    "v_mfma_f32_16x16x32_bf16 v[60:63], a[252:255], a[60:63], v[60:63]\n\t"
    "ds_read_b64_tr_b16 a[150:151], v201 offset:59392\n\t"
    "s_waitcnt vmcnt(41) lgkmcnt(10)\n\t"
    "v_mfma_f32_16x16x32_bf16 v[0:3], a[128:131], a[64:67], v[0:3]\n\t"
    "ds_read_b64_tr_b16 a[152:153], v202 offset:57344\n\t"
    "v_mfma_f32_16x16x32_bf16 v[4:7], a[128:131], a[68:71], v[4:7]\n\t"
    "ds_read_b64_tr_b16 a[154:155], v202 offset:59392\n\t"
    "s_waitcnt lgkmcnt(10)\n\t"
    "s_add_u32 m0, s68, 25600\n\t"
    "s_mov_b32 s69, 716800\n\t"
    "v_mfma_f32_16x16x32_bf16 v[8:11], a[132:135], a[64:67], v[8:11]\n\t"
    "buffer_load_dwordx4 v199, s[56:59], s69 offen lds\n\t"
    "ds_read_b64_tr_b16 a[156:157], v203 offset:57344\n\t"
    "v_mfma_f32_16x16x32_bf16 v[12:15], a[132:135], a[68:71], v[12:15]\n\t"
    "ds_read_b64_tr_b16 a[158:159], v203 offset:59392\n\t"
    "s_waitcnt lgkmcnt(10)\n\t"
    "v_mfma_f32_16x16x32_bf16 v[16:19], a[136:139], a[64:67], v[16:19]\n\t"
    "ds_read_b64_tr_b16 a[160:161], v200 offset:53248\n\t"
    "v_mfma_f32_16x16x32_bf16 v[20:23], a[136:139], a[68:71], v[20:23]\n\t"
    "ds_read_b64_tr_b16 a[162:163], v200 offset:55296\n\t"
    "s_waitcnt lgkmcnt(10)\n\t"
    "v_mfma_f32_16x16x32_bf16 v[24:27], a[140:143], a[64:67], v[24:27]\n\t"
    "ds_read_b64_tr_b16 a[164:165], v201 offset:53248\n\t"
    "v_mfma_f32_16x16x32_bf16 v[28:31], a[140:143], a[68:71], v[28:31]\n\t"
    "ds_read_b64_tr_b16 a[166:167], v201 offset:55296\n\t"
    "s_waitcnt lgkmcnt(10)\n\t"
    "v_mfma_f32_16x16x32_bf16 v[32:35], a[144:147], a[64:67], v[32:35]\n\t"
    "ds_read_b64_tr_b16 a[168:169], v202 offset:53248\n\t"
    "v_mfma_f32_16x16x32_bf16 v[36:39], a[144:147], a[68:71], v[36:39]\n\t"
    "ds_read_b64_tr_b16 a[170:171], v202 offset:55296\n\t"
    "s_waitcnt lgkmcnt(10)\n\t"
    "v_mfma_f32_16x16x32_bf16 v[40:43], a[148:151], a[64:67], v[40:43]\n\t"
    "ds_read_b64_tr_b16 a[172:173], v203 offset:53248\n\t"
    "v_mfma_f32_16x16x32_bf16 v[44:47], a[148:151], a[68:71], v[44:47]\n\t"
    "ds_read_b64_tr_b16 a[174:175], v203 offset:55296\n\t"
    "s_waitcnt lgkmcnt(10)\n\t"
    "v_mfma_f32_16x16x32_bf16 v[48:51], a[152:155], a[64:67], v[48:51]\n\t"
    "ds_read_b64_tr_b16 a[176:177], v200 offset:61440\n\t"
    "v_mfma_f32_16x16x32_bf16 v[52:55], a[152:155], a[68:71], v[52:55]\n\t"
    "ds_read_b64_tr_b16 a[178:179], v200 offset:63488\n\t"
    "s_waitcnt lgkmcnt(10)\n\t"
    "v_mfma_f32_16x16x32_bf16 v[56:59], a[156:159], a[64:67], v[56:59]\n\t"
    "ds_read_b64_tr_b16 a[180:181], v201 offset:61440\n\t"
    "v_mfma_f32_16x16x32_bf16 v[60:63], a[156:159], a[68:71], v[60:63]\n\t"
    "ds_read_b64_tr_b16 a[182:183], v201 offset:63488\n\t"
    "s_waitcnt vmcnt(40) lgkmcnt(10)\n\t"
    "v_mfma_f32_16x16x32_bf16 v[0:3], a[160:163], a[72:75], v[0:3]\n\t"
    "ds_read_b64_tr_b16 a[184:185], v202 offset:61440\n\t"
    "v_mfma_f32_16x16x32_bf16 v[4:7], a[160:163], a[76:79], v[4:7]\n\t"
    "ds_read_b64_tr_b16 a[186:187], v202 offset:63488\n\t"
    "s_waitcnt lgkmcnt(10)\n\t"
    "v_mfma_f32_16x16x32_bf16 v[8:11], a[164:167], a[72:75], v[8:11]\n\t"
    "ds_read_b64_tr_b16 a[188:189], v203 offset:61440\n\t"
    "v_mfma_f32_16x16x32_bf16 v[12:15], a[164:167], a[76:79], v[12:15]\n\t"
    "ds_read_b64_tr_b16 a[190:191], v203 offset:63488\n\t"
    "s_waitcnt vmcnt(34)\n\t"
    "s_barrier\n\t"
    "s_waitcnt lgkmcnt(10)\n\t"
    "v_mfma_f32_16x16x32_bf16 v[16:19], a[168:171], a[72:75], v[16:19]\n\t"
    "ds_read_b64_tr_b16 a[192:193], v204 offset:0\n\t"
    "v_mfma_f32_16x16x32_bf16 v[20:23], a[168:171], a[76:79], v[20:23]\n\t"
    "ds_read_b64_tr_b16 a[194:195], v204 offset:2048\n\t"
    "s_waitcnt lgkmcnt(10)\n\t"
    "s_add_u32 m0, s68, 32768\n\t"
    "s_mov_b32 s69, 917504\n\t"
    "v_mfma_f32_16x16x32_bf16 v[24:27], a[172:175], a[72:75], v[24:27]\n\t"
    "buffer_load_dwordx4 v199, s[52:55], s69 offen lds\n\t"
    "ds_read_b64_tr_b16 a[196:197], v205 offset:0\n\t"
    "v_mfma_f32_16x16x32_bf16 v[28:31], a[172:175], a[76:79], v[28:31]\n\t"
    "ds_read_b64_tr_b16 a[198:199], v205 offset:2048\n\t"
    "s_waitcnt lgkmcnt(10)\n\t"
    "v_mfma_f32_16x16x32_bf16 v[32:35], a[176:179], a[72:75], v[32:35]\n\t"
    "ds_read_b64_tr_b16 a[200:201], v206 offset:0\n\t"
    "v_mfma_f32_16x16x32_bf16 v[36:39], a[176:179], a[76:79], v[36:39]\n\t"
    "ds_read_b64_tr_b16 a[202:203], v206 offset:2048\n\t"
    "s_waitcnt lgkmcnt(10)\n\t"
    "s_add_u32 m0, s68, 33792\n\t"
    "s_mov_b32 s69, 946176\n\t"
    "v_mfma_f32_16x16x32_bf16 v[40:43], a[180:183], a[72:75], v[40:43]\n\t"
    "buffer_load_dwordx4 v199, s[52:55], s69 offen lds\n\t"
    "ds_read_b64_tr_b16 a[204:205], v207 offset:0\n\t"
    "v_mfma_f32_16x16x32_bf16 v[44:47], a[180:183], a[76:79], v[44:47]\n\t"
    "ds_read_b64_tr_b16 a[206:207], v207 offset:2048\n\t"
    "s_waitcnt lgkmcnt(10)\n\t"
    "v_mfma_f32_16x16x32_bf16 v[48:51], a[184:187], a[72:75], v[48:51]\n\t"
    "ds_read_b64_tr_b16 a[208:209], v204 offset:8192\n\t"
    "v_mfma_f32_16x16x32_bf16 v[52:55], a[184:187], a[76:79], v[52:55]\n\t"
    "ds_read_b64_tr_b16 a[210:211], v204 offset:10240\n\t"
    "s_waitcnt lgkmcnt(10)\n\t"
    "s_add_u32 m0, s68, 40960\n\t"
    "s_mov_b32 s69, 917504\n\t"
    "v_mfma_f32_16x16x32_bf16 v[56:59], a[188:191], a[72:75], v[56:59]\n\t"
    "buffer_load_dwordx4 v199, s[56:59], s69 offen lds\n\t"
    "ds_read_b64_tr_b16 a[212:213], v205 offset:8192\n\t"
    "v_mfma_f32_16x16x32_bf16 v[60:63], a[188:191], a[76:79], v[60:63]\n\t"
    "ds_read_b64_tr_b16 a[214:215], v205 offset:10240\n\t"
    "s_waitcnt vmcnt(33) lgkmcnt(10)\n\t"
    "v_mfma_f32_16x16x32_bf16 v[0:3], a[192:195], a[80:83], v[0:3]\n\t"
    "ds_read_b64_tr_b16 a[216:217], v206 offset:8192\n\t"
    "v_mfma_f32_16x16x32_bf16 v[4:7], a[192:195], a[84:87], v[4:7]\n\t"
    "ds_read_b64_tr_b16 a[218:219], v206 offset:10240\n\t"
    "s_waitcnt lgkmcnt(10)\n\t"
    "s_add_u32 m0, s68, 41984\n\t"
    "s_mov_b32 s69, 946176\n\t"
    "v_mfma_f32_16x16x32_bf16 v[8:11], a[196:199], a[80:83], v[8:11]\n\t"
    "buffer_load_dwordx4 v199, s[56:59], s69 offen lds\n\t"
    "ds_read_b64_tr_b16 a[220:221], v207 offset:8192\n\t"
    "v_mfma_f32_16x16x32_bf16 v[12:15], a[196:199], a[84:87], v[12:15]\n\t"
    "ds_read_b64_tr_b16 a[222:223], v207 offset:10240\n\t"
    "s_waitcnt lgkmcnt(10)\n\t"
    "v_mfma_f32_16x16x32_bf16 v[16:19], a[200:203], a[80:83], v[16:19]\n\t"
    "ds_read_b64_tr_b16 a[224:225], v204 offset:4096\n\t"
    "v_mfma_f32_16x16x32_bf16 v[20:23], a[200:203], a[84:87], v[20:23]\n\t"
    "ds_read_b64_tr_b16 a[226:227], v204 offset:6144\n\t"
    "s_waitcnt lgkmcnt(10)\n\t"
    "v_mfma_f32_16x16x32_bf16 v[24:27], a[204:207], a[80:83], v[24:27]\n\t"
    "ds_read_b64_tr_b16 a[228:229], v205 offset:4096\n\t"
    "v_mfma_f32_16x16x32_bf16 v[28:31], a[204:207], a[84:87], v[28:31]\n\t"
    "ds_read_b64_tr_b16 a[230:231], v205 offset:6144\n\t"
    "s_waitcnt lgkmcnt(10)\n\t"
    "v_mfma_f32_16x16x32_bf16 v[32:35], a[208:211], a[80:83], v[32:35]\n\t"
    "ds_read_b64_tr_b16 a[232:233], v206 offset:4096\n\t"
    "v_mfma_f32_16x16x32_bf16 v[36:39], a[208:211], a[84:87], v[36:39]\n\t"
    "ds_read_b64_tr_b16 a[234:235], v206 offset:6144\n\t"
    "s_waitcnt lgkmcnt(10)\n\t"
    "v_mfma_f32_16x16x32_bf16 v[40:43], a[212:215], a[80:83], v[40:43]\n\t"
    "ds_read_b64_tr_b16 a[236:237], v207 offset:4096\n\t"
    "v_mfma_f32_16x16x32_bf16 v[44:47], a[212:215], a[84:87], v[44:47]\n\t"
    "ds_read_b64_tr_b16 a[238:239], v207 offset:6144\n\t"
    "s_waitcnt lgkmcnt(10)\n\t"
    "v_mfma_f32_16x16x32_bf16 v[48:51], a[216:219], a[80:83], v[48:51]\n\t"
    "ds_read_b64_tr_b16 a[240:241], v204 offset:12288\n\t"
    "v_mfma_f32_16x16x32_bf16 v[52:55], a[216:219], a[84:87], v[52:55]\n\t"
    "ds_read_b64_tr_b16 a[242:243], v204 offset:14336\n\t"
    "s_waitcnt lgkmcnt(10)\n\t"
    "v_mfma_f32_16x16x32_bf16 v[56:59], a[220:223], a[80:83], v[56:59]\n\t"
    "ds_read_b64_tr_b16 a[244:245], v205 offset:12288\n\t"
    "v_mfma_f32_16x16x32_bf16 v[60:63], a[220:223], a[84:87], v[60:63]\n\t"
    "ds_read_b64_tr_b16 a[246:247], v205 offset:14336\n\t"
    "s_waitcnt vmcnt(32) lgkmcnt(10)\n\t"
    "v_mfma_f32_16x16x32_bf16 v[0:3], a[224:227], a[88:91], v[0:3]\n\t"
    "ds_read_b64_tr_b16 a[248:249], v206 offset:12288\n\t"
    "v_mfma_f32_16x16x32_bf16 v[4:7], a[224:227], a[92:95], v[4:7]\n\t"
    "ds_read_b64_tr_b16 a[250:251], v206 offset:14336\n\t"
    "s_waitcnt lgkmcnt(10)\n\t"
    "v_mfma_f32_16x16x32_bf16 v[8:11], a[228:231], a[88:91], v[8:11]\n\t"
    "ds_read_b64_tr_b16 a[252:253], v207 offset:12288\n\t"
    "v_mfma_f32_16x16x32_bf16 v[12:15], a[228:231], a[92:95], v[12:15]\n\t"
    "ds_read_b64_tr_b16 a[254:255], v207 offset:14336\n\t"
    "s_waitcnt vmcnt(26)\n\t"
    "s_barrier\n\t"
    "s_waitcnt lgkmcnt(10)\n\t"
    "v_mfma_f32_16x16x32_bf16 v[16:19], a[232:235], a[88:91], v[16:19]\n\t"
    "ds_read_b64_tr_b16 a[128:129], v204 offset:16384\n\t"
    "v_mfma_f32_16x16x32_bf16 v[20:23], a[232:235], a[92:95], v[20:23]\n\t"
    "ds_read_b64_tr_b16 a[130:131], v204 offset:18432\n\t"
    "s_waitcnt lgkmcnt(10)\n\t"
    "s_add_u32 m0, s68, 49152\n\t"
    "s_mov_b32 s69, 1146880\n\t"
    "v_mfma_f32_16x16x32_bf16 v[24:27], a[236:239], a[88:91], v[24:27]\n\t"
    "buffer_load_dwordx4 v199, s[52:55], s69 offen lds\n\t"
    "ds_read_b64_tr_b16 a[132:133], v205 offset:16384\n\t"
    "v_mfma_f32_16x16x32_bf16 v[28:31], a[236:239], a[92:95], v[28:31]\n\t"
    "ds_read_b64_tr_b16 a[134:135], v205 offset:18432\n\t"
    "s_waitcnt lgkmcnt(10)\n\t"
    "v_mfma_f32_16x16x32_bf16 v[32:35], a[240:243], a[88:91], v[32:35]\n\t"
    "ds_read_b64_tr_b16 a[136:137], v206 offset:16384\n\t"
    "v_mfma_f32_16x16x32_bf16 v[36:39], a[240:243], a[92:95], v[36:39]\n\t"
    "ds_read_b64_tr_b16 a[138:139], v206 offset:18432\n\t"
    "s_waitcnt lgkmcnt(10)\n\t"
    "s_add_u32 m0, s68, 50176\n\t"
    "s_mov_b32 s69, 1175552\n\t"
    "v_mfma_f32_16x16x32_bf16 v[40:43], a[244:247], a[88:91], v[40:43]\n\t"
    "buffer_load_dwordx4 v199, s[52:55], s69 offen lds\n\t"
    "ds_read_b64_tr_b16 a[140:141], v207 offset:16384\n\t"
    "v_mfma_f32_16x16x32_bf16 v[44:47], a[244:247], a[92:95], v[44:47]\n\t"
    "ds_read_b64_tr_b16 a[142:143], v207 offset:18432\n\t"
    "s_waitcnt lgkmcnt(10)\n\t"
    "v_mfma_f32_16x16x32_bf16 v[48:51], a[248:251], a[88:91], v[48:51]\n\t"
    "ds_read_b64_tr_b16 a[144:145], v204 offset:24576\n\t"
    "v_mfma_f32_16x16x32_bf16 v[52:55], a[248:251], a[92:95], v[52:55]\n\t"
    "ds_read_b64_tr_b16 a[146:147], v204 offset:26624\n\t"
    "s_waitcnt lgkmcnt(10)\n\t"
    "s_add_u32 m0, s68, 57344\n\t"
    "s_mov_b32 s69, 1146880\n\t"
    "v_mfma_f32_16x16x32_bf16 v[56:59], a[252:255], a[88:91], v[56:59]\n\t"
    "buffer_load_dwordx4 v199, s[56:59], s69 offen lds\n\t"
    "ds_read_b64_tr_b16 a[148:149], v205 offset:24576\n\t"
    "v_mfma_f32_16x16x32_bf16 v[60:63], a[252:255], a[92:95], v[60:63]\n\t"
    "ds_read_b64_tr_b16 a[150:151], v205 offset:26624\n\t"
    "s_waitcnt vmcnt(25) lgkmcnt(10)\n\t"
    "v_mfma_f32_16x16x32_bf16 v[0:3], a[128:131], a[96:99], v[0:3]\n\t"
    "ds_read_b64_tr_b16 a[152:153], v206 offset:24576\n\t"
    "v_mfma_f32_16x16x32_bf16 v[4:7], a[128:131], a[100:103], v[4:7]\n\t"
    "ds_read_b64_tr_b16 a[154:155], v206 offset:26624\n\t"
    "s_waitcnt lgkmcnt(10)\n\t"
    "s_add_u32 m0, s68, 58368\n\t"
    "s_mov_b32 s69, 1175552\n\t"
    "v_mfma_f32_16x16x32_bf16 v[8:11], a[132:135], a[96:99], v[8:11]\n\t"
    "buffer_load_dwordx4 v199, s[56:59], s69 offen lds\n\t"
    "ds_read_b64_tr_b16 a[156:157], v207 offset:24576\n\t"
    "v_mfma_f32_16x16x32_bf16 v[12:15], a[132:135], a[100:103], v[12:15]\n\t"
    "ds_read_b64_tr_b16 a[158:159], v207 offset:26624\n\t"
    "s_waitcnt lgkmcnt(10)\n\t"
    "v_mfma_f32_16x16x32_bf16 v[16:19], a[136:139], a[96:99], v[16:19]\n\t"
    "ds_read_b64_tr_b16 a[160:161], v204 offset:20480\n\t"
    "v_mfma_f32_16x16x32_bf16 v[20:23], a[136:139], a[100:103], v[20:23]\n\t"
    "ds_read_b64_tr_b16 a[162:163], v204 offset:22528\n\t"
    "s_waitcnt lgkmcnt(10)\n\t"
    "v_mfma_f32_16x16x32_bf16 v[24:27], a[140:143], a[96:99], v[24:27]\n\t"
    "ds_read_b64_tr_b16 a[164:165], v205 offset:20480\n\t"
    "v_mfma_f32_16x16x32_bf16 v[28:31], a[140:143], a[100:103], v[28:31]\n\t"
    "ds_read_b64_tr_b16 a[166:167], v205 offset:22528\n\t"
    "s_waitcnt lgkmcnt(10)\n\t"
    "v_mfma_f32_16x16x32_bf16 v[32:35], a[144:147], a[96:99], v[32:35]\n\t"
    "ds_read_b64_tr_b16 a[168:169], v206 offset:20480\n\t"
    "v_mfma_f32_16x16x32_bf16 v[36:39], a[144:147], a[100:103], v[36:39]\n\t"
    "ds_read_b64_tr_b16 a[170:171], v206 offset:22528\n\t"
    "s_waitcnt lgkmcnt(10)\n\t"
    "v_mfma_f32_16x16x32_bf16 v[40:43], a[148:151], a[96:99], v[40:43]\n\t"
    "ds_read_b64_tr_b16 a[172:173], v207 offset:20480\n\t"
    "v_mfma_f32_16x16x32_bf16 v[44:47], a[148:151], a[100:103], v[44:47]\n\t"
    "ds_read_b64_tr_b16 a[174:175], v207 offset:22528\n\t"
    "s_waitcnt lgkmcnt(10)\n\t"
    "v_mfma_f32_16x16x32_bf16 v[48:51], a[152:155], a[96:99], v[48:51]\n\t"
    "ds_read_b64_tr_b16 a[176:177], v204 offset:28672\n\t"
    "v_mfma_f32_16x16x32_bf16 v[52:55], a[152:155], a[100:103], v[52:55]\n\t"
    "ds_read_b64_tr_b16 a[178:179], v204 offset:30720\n\t"
    "s_waitcnt lgkmcnt(10)\n\t"
    "v_mfma_f32_16x16x32_bf16 v[56:59], a[156:159], a[96:99], v[56:59]\n\t"
    "ds_read_b64_tr_b16 a[180:181], v205 offset:28672\n\t"
    "v_mfma_f32_16x16x32_bf16 v[60:63], a[156:159], a[100:103], v[60:63]\n\t"
    "ds_read_b64_tr_b16 a[182:183], v205 offset:30720\n\t"
    "s_waitcnt vmcnt(24) lgkmcnt(10)\n\t"
    "v_mfma_f32_16x16x32_bf16 v[0:3], a[160:163], a[104:107], v[0:3]\n\t"
    "ds_read_b64_tr_b16 a[184:185], v206 offset:28672\n\t"
    "v_mfma_f32_16x16x32_bf16 v[4:7], a[160:163], a[108:111], v[4:7]\n\t"
    "ds_read_b64_tr_b16 a[186:187], v206 offset:30720\n\t"
    "s_waitcnt lgkmcnt(10)\n\t"
    "v_mfma_f32_16x16x32_bf16 v[8:11], a[164:167], a[104:107], v[8:11]\n\t"
    "ds_read_b64_tr_b16 a[188:189], v207 offset:28672\n\t"
    "v_mfma_f32_16x16x32_bf16 v[12:15], a[164:167], a[108:111], v[12:15]\n\t"
    "ds_read_b64_tr_b16 a[190:191], v207 offset:30720\n\t"
    "s_waitcnt vmcnt(18)\n\t"
    "s_barrier\n\t"
    "s_waitcnt lgkmcnt(10)\n\t"
    "v_mfma_f32_16x16x32_bf16 v[16:19], a[168:171], a[104:107], v[16:19]\n\t"
    "ds_read_b64_tr_b16 a[192:193], v204 offset:32768\n\t"
    "v_mfma_f32_16x16x32_bf16 v[20:23], a[168:171], a[108:111], v[20:23]\n\t"
    "ds_read_b64_tr_b16 a[194:195], v204 offset:34816\n\t"
    "s_waitcnt lgkmcnt(10)\n\t"
    "s_add_u32 m0, s68, 65536\n\t"
    "s_mov_b32 s69, 1376256\n\t"
    "v_mfma_f32_16x16x32_bf16 v[24:27], a[172:175], a[104:107], v[24:27]\n\t"
    "buffer_load_dwordx4 v199, s[52:55], s69 offen lds\n\t"
    "ds_read_b64_tr_b16 a[196:197], v205 offset:32768\n\t"
    "v_mfma_f32_16x16x32_bf16 v[28:31], a[172:175], a[108:111], v[28:31]\n\t"
    "ds_read_b64_tr_b16 a[198:199], v205 offset:34816\n\t"
    "s_waitcnt lgkmcnt(10)\n\t"
    "v_mfma_f32_16x16x32_bf16 v[32:35], a[176:179], a[104:107], v[32:35]\n\t"
    "ds_read_b64_tr_b16 a[200:201], v206 offset:32768\n\t"
    "v_mfma_f32_16x16x32_bf16 v[36:39], a[176:179], a[108:111], v[36:39]\n\t"
    "ds_read_b64_tr_b16 a[202:203], v206 offset:34816\n\t"
    "s_waitcnt lgkmcnt(10)\n\t"
    "s_add_u32 m0, s68, 66560\n\t"
    "s_mov_b32 s69, 1404928\n\t"
    "v_mfma_f32_16x16x32_bf16 v[40:43], a[180:183], a[104:107], v[40:43]\n\t"
    "buffer_load_dwordx4 v199, s[52:55], s69 offen lds\n\t"
    "ds_read_b64_tr_b16 a[204:205], v207 offset:32768\n\t"
    "v_mfma_f32_16x16x32_bf16 v[44:47], a[180:183], a[108:111], v[44:47]\n\t"
    "ds_read_b64_tr_b16 a[206:207], v207 offset:34816\n\t"
    "s_waitcnt lgkmcnt(10)\n\t"
    "v_mfma_f32_16x16x32_bf16 v[48:51], a[184:187], a[104:107], v[48:51]\n\t"
    "ds_read_b64_tr_b16 a[208:209], v204 offset:40960\n\t"
    "v_mfma_f32_16x16x32_bf16 v[52:55], a[184:187], a[108:111], v[52:55]\n\t"
    "ds_read_b64_tr_b16 a[210:211], v204 offset:43008\n\t"
    "s_waitcnt lgkmcnt(10)\n\t"
    "s_add_u32 m0, s68, 73728\n\t"
    "s_mov_b32 s69, 1376256\n\t"
    "v_mfma_f32_16x16x32_bf16 v[56:59], a[188:191], a[104:107], v[56:59]\n\t"
    "buffer_load_dwordx4 v199, s[56:59], s69 offen lds\n\t"
    "ds_read_b64_tr_b16 a[212:213], v205 offset:40960\n\t"
    "v_mfma_f32_16x16x32_bf16 v[60:63], a[188:191], a[108:111], v[60:63]\n\t"
    "ds_read_b64_tr_b16 a[214:215], v205 offset:43008\n\t"
    "s_waitcnt vmcnt(17) lgkmcnt(10)\n\t"
    "v_mfma_f32_16x16x32_bf16 v[0:3], a[192:195], a[112:115], v[0:3]\n\t"
    "ds_read_b64_tr_b16 a[216:217], v206 offset:40960\n\t"
    "v_mfma_f32_16x16x32_bf16 v[4:7], a[192:195], a[116:119], v[4:7]\n\t"
    "ds_read_b64_tr_b16 a[218:219], v206 offset:43008\n\t"
    "s_waitcnt lgkmcnt(10)\n\t"
    "s_add_u32 m0, s68, 74752\n\t"
    "s_mov_b32 s69, 1404928\n\t"
    "v_mfma_f32_16x16x32_bf16 v[8:11], a[196:199], a[112:115], v[8:11]\n\t"
    "buffer_load_dwordx4 v199, s[56:59], s69 offen lds\n\t"
    "ds_read_b64_tr_b16 a[220:221], v207 offset:40960\n\t"
    "v_mfma_f32_16x16x32_bf16 v[12:15], a[196:199], a[116:119], v[12:15]\n\t"
    "ds_read_b64_tr_b16 a[222:223], v207 offset:43008\n\t"
    "s_waitcnt lgkmcnt(10)\n\t"
    "v_mfma_f32_16x16x32_bf16 v[16:19], a[200:203], a[112:115], v[16:19]\n\t"
    "ds_read_b64_tr_b16 a[224:225], v204 offset:36864\n\t"
    "v_mfma_f32_16x16x32_bf16 v[20:23], a[200:203], a[116:119], v[20:23]\n\t"
    "ds_read_b64_tr_b16 a[226:227], v204 offset:38912\n\t"
    "s_waitcnt lgkmcnt(10)\n\t"
    "v_mfma_f32_16x16x32_bf16 v[24:27], a[204:207], a[112:115], v[24:27]\n\t"
    "ds_read_b64_tr_b16 a[228:229], v205 offset:36864\n\t"
    "v_mfma_f32_16x16x32_bf16 v[28:31], a[204:207], a[116:119], v[28:31]\n\t"
    "ds_read_b64_tr_b16 a[230:231], v205 offset:38912\n\t"
    "s_waitcnt lgkmcnt(10)\n\t"
    "v_mfma_f32_16x16x32_bf16 v[32:35], a[208:211], a[112:115], v[32:35]\n\t"
    "ds_read_b64_tr_b16 a[232:233], v206 offset:36864\n\t"
    "v_mfma_f32_16x16x32_bf16 v[36:39], a[208:211], a[116:119], v[36:39]\n\t"
    "ds_read_b64_tr_b16 a[234:235], v206 offset:38912\n\t"
    "s_waitcnt lgkmcnt(10)\n\t"
    "v_mfma_f32_16x16x32_bf16 v[40:43], a[212:215], a[112:115], v[40:43]\n\t"
    "ds_read_b64_tr_b16 a[236:237], v207 offset:36864\n\t"
    "v_mfma_f32_16x16x32_bf16 v[44:47], a[212:215], a[116:119], v[44:47]\n\t"
    "ds_read_b64_tr_b16 a[238:239], v207 offset:38912\n\t"
    "s_waitcnt lgkmcnt(10)\n\t"
    "v_mfma_f32_16x16x32_bf16 v[48:51], a[216:219], a[112:115], v[48:51]\n\t"
    "ds_read_b64_tr_b16 a[240:241], v204 offset:45056\n\t"
    "v_mfma_f32_16x16x32_bf16 v[52:55], a[216:219], a[116:119], v[52:55]\n\t"
    "ds_read_b64_tr_b16 a[242:243], v204 offset:47104\n\t"
    "s_waitcnt lgkmcnt(10)\n\t"
    "v_mfma_f32_16x16x32_bf16 v[56:59], a[220:223], a[112:115], v[56:59]\n\t"
    "ds_read_b64_tr_b16 a[244:245], v205 offset:45056\n\t"
    "v_mfma_f32_16x16x32_bf16 v[60:63], a[220:223], a[116:119], v[60:63]\n\t"
    "ds_read_b64_tr_b16 a[246:247], v205 offset:47104\n\t"
    "s_waitcnt vmcnt(16) lgkmcnt(10)\n\t"
    "v_mfma_f32_16x16x32_bf16 v[0:3], a[224:227], a[120:123], v[0:3]\n\t"
    "ds_read_b64_tr_b16 a[248:249], v206 offset:45056\n\t"
    "v_mfma_f32_16x16x32_bf16 v[4:7], a[224:227], a[124:127], v[4:7]\n\t"
    "ds_read_b64_tr_b16 a[250:251], v206 offset:47104\n\t"
    "s_waitcnt lgkmcnt(10)\n\t"
    "v_mfma_f32_16x16x32_bf16 v[8:11], a[228:231], a[120:123], v[8:11]\n\t"
    "ds_read_b64_tr_b16 a[252:253], v207 offset:45056\n\t"
    "v_mfma_f32_16x16x32_bf16 v[12:15], a[228:231], a[124:127], v[12:15]\n\t"
    "ds_read_b64_tr_b16 a[254:255], v207 offset:47104\n\t"
    "s_waitcnt lgkmcnt(10)\n\t"
    "v_mfma_f32_16x16x32_bf16 v[16:19], a[232:235], a[120:123], v[16:19]\n\t"
    "v_mfma_f32_16x16x32_bf16 v[20:23], a[232:235], a[124:127], v[20:23]\n\t"
    "s_waitcnt lgkmcnt(8)\n\t"
    "v_mfma_f32_16x16x32_bf16 v[24:27], a[236:239], a[120:123], v[24:27]\n\t"
    "v_mfma_f32_16x16x32_bf16 v[28:31], a[236:239], a[124:127], v[28:31]\n\t"
    "s_waitcnt lgkmcnt(6)\n\t"
    "v_mfma_f32_16x16x32_bf16 v[32:35], a[240:243], a[120:123], v[32:35]\n\t"
    "v_mfma_f32_16x16x32_bf16 v[36:39], a[240:243], a[124:127], v[36:39]\n\t"
    "s_waitcnt lgkmcnt(4)\n\t"
    "v_mfma_f32_16x16x32_bf16 v[40:43], a[244:247], a[120:123], v[40:43]\n\t"
    "v_mfma_f32_16x16x32_bf16 v[44:47], a[244:247], a[124:127], v[44:47]\n\t"
    "s_waitcnt lgkmcnt(2)\n\t"
    "v_mfma_f32_16x16x32_bf16 v[48:51], a[248:251], a[120:123], v[48:51]\n\t"
    "v_mfma_f32_16x16x32_bf16 v[52:55], a[248:251], a[124:127], v[52:55]\n\t"
    "s_waitcnt lgkmcnt(0)\n\t"
    "v_mfma_f32_16x16x32_bf16 v[56:59], a[252:255], a[120:123], v[56:59]\n\t"
    "v_mfma_f32_16x16x32_bf16 v[60:63], a[252:255], a[124:127], v[60:63]\n\t"
    "v_cvt_pk_bf16_f32 v220, v0, v1\n\t"
    "v_cvt_pk_bf16_f32 v221, v2, v3\n\t"
    "v_cvt_pk_bf16_f32 v222, v8, v9\n\t"
    "v_cvt_pk_bf16_f32 v223, v10, v11\n\t"
    "s_mov_b32 s69, 0\n\t"
    "buffer_store_dwordx4 v[220:223], v197, s[48:51], s69 offen offset:0 sc1\n\t"
    "v_cvt_pk_bf16_f32 v224, v4, v5\n\t"
    "v_cvt_pk_bf16_f32 v225, v6, v7\n\t"
    "v_cvt_pk_bf16_f32 v226, v12, v13\n\t"
    "v_cvt_pk_bf16_f32 v227, v14, v15\n\t"
    "buffer_store_dwordx4 v[224:227], v197, s[48:51], s69 offen offset:1024 sc1\n\t"
    "s_nop 1\n\t"
    "v_cvt_pk_bf16_f32 v224, v16, v17\n\t"
    "v_cvt_pk_bf16_f32 v225, v18, v19\n\t"
    "v_cvt_pk_bf16_f32 v226, v24, v25\n\t"
    "v_cvt_pk_bf16_f32 v227, v26, v27\n\t"
    "buffer_store_dwordx4 v[224:227], v197, s[48:51], s69 offen offset:2048 sc1\n\t"
    "v_cvt_pk_bf16_f32 v228, v20, v21\n\t"
    "v_cvt_pk_bf16_f32 v229, v22, v23\n\t"
    "v_cvt_pk_bf16_f32 v230, v28, v29\n\t"
    "v_cvt_pk_bf16_f32 v231, v30, v31\n\t"
    "buffer_store_dwordx4 v[228:231], v197, s[48:51], s69 offen offset:3072 sc1\n\t"
    "v_cvt_pk_bf16_f32 v220, v32, v33\n\t"
    "v_cvt_pk_bf16_f32 v221, v34, v35\n\t"
    "v_cvt_pk_bf16_f32 v222, v40, v41\n\t"
    "v_cvt_pk_bf16_f32 v223, v42, v43\n\t"
    "s_mov_b32 s69, 4096\n\t"
    "buffer_store_dwordx4 v[220:223], v197, s[48:51], s69 offen offset:0 sc1\n\t"
    "v_cvt_pk_bf16_f32 v224, v36, v37\n\t"
    "v_cvt_pk_bf16_f32 v225, v38, v39\n\t"
    "v_cvt_pk_bf16_f32 v226, v44, v45\n\t"
    "v_cvt_pk_bf16_f32 v227, v46, v47\n\t"
    "buffer_store_dwordx4 v[224:227], v197, s[48:51], s69 offen offset:1024 sc1\n\t"
    "s_nop 1\n\t"
    "v_cvt_pk_bf16_f32 v224, v48, v49\n\t"
    "v_cvt_pk_bf16_f32 v225, v50, v51\n\t"
    "v_cvt_pk_bf16_f32 v226, v56, v57\n\t"
    "v_cvt_pk_bf16_f32 v227, v58, v59\n\t"
    "buffer_store_dwordx4 v[224:227], v197, s[48:51], s69 offen offset:2048 sc1\n\t"
    "v_cvt_pk_bf16_f32 v228, v52, v53\n\t"
    "v_cvt_pk_bf16_f32 v229, v54, v55\n\t"
    "v_cvt_pk_bf16_f32 v230, v60, v61\n\t"
    "v_cvt_pk_bf16_f32 v231, v62, v63\n\t"
    "buffer_store_dwordx4 v[228:231], v197, s[48:51], s69 offen offset:3072 sc1\n\t"
    "s_waitcnt vmcnt(0)\n\t"
    "s_barrier\n\t"
    :
    : "s"(ws + WS_OF), "s"(wo), "s"(wo), "s"(ws + WS_PART), "s"(mtp), "s"(nt128), "s"(kq), "s"(wid), "s"(ldsb), "s"(wg), "s"(wu), "s"(idx < 14 ? idx : 13u)
    :
"v0", "v1", "v2", "v3", "v4", "v5", "v6", "v7", "v8", "v9", "v10", "v11", "v12", "v13", "v14", "v15", "v16", "v17", "v18", "v19", "v20", "v21", "v22", "v23", "v24", "v25", "v26", "v27", "v28", "v29", "v30", "v31", "v32", "v33", "v34", "v35", "v36", "v37", "v38", "v39", "v40", "v41", "v42", "v43", "v44", "v45", "v46", "v47", "v48", "v49", "v50", "v51", "v52", "v53", "v54", "v55", "v56", "v57", "v58", "v59", "v60", "v61", "v62", "v63", "v196", "v197", "v198", "v199", "v200", "v201", "v202", "v203", "v204", "v205", "v206", "v207", "v208", "v209", "v210", "v211", "v212", "v213", "v214", "v215", "v216", "v217", "v218", "v219", "v220", "v221", "v222", "v223", "v224", "v225", "v226", "v227", "v228", "v229", "v230", "v231", "v232", "v233", "v234", "v235", "v236", "v237", "v238", "v239", "v240", "v241", "v242", "v243", "v244", "v245", "v246", "v247", "v248", "v249", "v250", "v251", "v252", "v253", "v254", "v255", "a0", "a1", "a2", "a3", "a4", "a5", "a6", "a7", "a8", "a9", "a10", "a11", "a12", "a13", "a14", "a15", "a16", "a17", "a18", "a19", "a20", "a21", "a22", "a23", "a24", "a25", "a26", "a27", "a28", "a29", "a30", "a31", "a32", "a33", "a34", "a35", "a36", "a37", "a38", "a39", "a40", "a41", "a42", "a43", "a44", "a45", "a46", "a47", "a48", "a49", "a50", "a51", "a52", "a53", "a54", "a55", "a56", "a57", "a58", "a59", "a60", "a61", "a62", "a63", "a64", "a65", "a66", "a67", "a68", "a69", "a70", "a71", "a72", "a73", "a74", "a75", "a76", "a77", "a78", "a79", "a80", "a81", "a82", "a83", "a84", "a85", "a86", "a87", "a88", "a89", "a90", "a91", "a92", "a93", "a94", "a95", "a96", "a97", "a98", "a99", "a100", "a101", "a102", "a103", "a104", "a105", "a106", "a107", "a108", "a109", "a110", "a111", "a112", "a113", "a114", "a115", "a116", "a117", "a118", "a119", "a120", "a121", "a122", "a123", "a124", "a125", "a126", "a127", "a128", "a129", "a130", "a131", "a132", "a133", "a134", "a135", "a136", "a137", "a138", "a139", "a140", "a141", "a142", "a143", "a144", "a145", "a146", "a147", "a148", "a149", "a150", "a151", "a152", "a153", "a154", "a155", "a156", "a157", "a158", "a159", "a160", "a161", "a162", "a163", "a164", "a165", "a166", "a167", "a168", "a169", "a170", "a171", "a172", "a173", "a174", "a175", "a176", "a177", "a178", "a179", "a180", "a181", "a182", "a183", "a184", "a185", "a186", "a187", "a188", "a189", "a190", "a191", "a192", "a193", "a194", "a195", "a196", "a197", "a198", "a199", "a200", "a201", "a202", "a203", "a204", "a205", "a206", "a207", "a208", "a209", "a210", "a211", "a212", "a213", "a214", "a215", "a216", "a217", "a218", "a219", "a220", "a221", "a222", "a223", "a224", "a225", "a226", "a227", "a228", "a229", "a230", "a231", "a232", "a233", "a234", "a235", "a236", "a237", "a238", "a239", "a240", "a241", "a242", "a243", "a244", "a245", "a246", "a247", "a248", "a249", "a250", "a251", "a252", "a253", "a254", "a255", "s36", "s37", "s38", "s39", "s40", "s41", "s42", "s43", "s44", "s45", "s46", "s47", "s48", "s49", "s50", "s51", "s52", "s53", "s54", "s55", "s56", "s57", "s58", "s59", "s60", "s61", "s62", "s63", "s64", "s65", "s66", "s67", "s68", "s69", "s70", "s71", "vcc", "scc", "memory"
  );
  arrive((unsigned*)(ws + WS_CNT) + mtp * 192, wid);
  {
    wait_ge<false>(cnt, 16, wid);
    const __amdgpu_buffer_rsrc_t rp = __builtin_amdgcn_make_buffer_rsrc((void*)(ws + WS_PART), (short)0, 8 << 20, 0x00020000);
    u32x4 p[4][2];
#pragma unroll
    for (int k4 = 0; k4 < 4; ++k4)
#pragma unroll
      for (int u = 0; u < 2; ++u)
        p[k4][u] = __builtin_amdgcn_raw_buffer_load_b128(rp, ((2 * h + u) * 2 + mt) * 1024 + fl * 16, (((k4 * 16 + mt128) * 4 + wid) * 4 + w1) * 8192, 16);
    float v[16];
#pragma unroll
    for (int nt = 0; nt < 4; ++nt)
#pragma unroll
      for (int r = 0; r < 4; ++r) {
        float a = 0.f;
#pragma unroll
        for (int k4 = 0; k4 < 4; ++k4) { const unsigned wd_ = p[k4][nt >> 1][2 * (nt & 1) + (r >> 1)]; a += __uint_as_float((r & 1) ? (wd_ & 0xffff0000u) : (wd_ << 16)); }
        v[4 * nt + r] = bf2f((u16)xr[nt][r]) + ga[nt][r] * a;
      }
    float s = 0.f;
#pragma unroll
    for (int j = 0; j < 16; ++j) s += v[j];
    float ml = s * (1.f / 16), q = 0.f;
#pragma unroll
    for (int j = 0; j < 16; ++j) { const float d = v[j] - ml; q += d * d; }
    { const float m2 = __uint_as_float(__builtin_amdgcn_update_dpp(0u, __float_as_uint(ml), 0x128, 0xf, 0xf, false)), q2 = __uint_as_float(__builtin_amdgcn_update_dpp(0u, __float_as_uint(q), 0x128, 0xf, 0xf, false));
      const float d = m2 - ml; q = q + q2 + 8.f * d * d; ml = 0.5f * (ml + m2); }
    { auto a = __builtin_amdgcn_permlane16_swap(__float_as_uint(ml), __float_as_uint(ml), false, false); auto b = __builtin_amdgcn_permlane16_swap(__float_as_uint(q), __float_as_uint(q), false, false);
      const float d = __uint_as_float(a[0]) - __uint_as_float(a[1]); q = __uint_as_float(b[0]) + __uint_as_float(b[1]) + 16.f * d * d; ml = 0.5f * (__uint_as_float(a[0]) + __uint_as_float(a[1])); }
    { auto a = __builtin_amdgcn_permlane32_swap(__float_as_uint(ml), __float_as_uint(ml), false, false); auto b = __builtin_amdgcn_permlane32_swap(__float_as_uint(q), __float_as_uint(q), false, false);
      const float d = __uint_as_float(a[0]) - __uint_as_float(a[1]); q = __uint_as_float(b[0]) + __uint_as_float(b[1]) + 32.f * d * d; ml = 0.5f * (__uint_as_float(a[0]) + __uint_as_float(a[1])); }
    float* red = (float*)(lds + 147456);
    if (lane < 8) { red[(wid * 8 + m8) * 2] = ml; red[(wid * 8 + m8) * 2 + 1] = q; }
    const __amdgpu_buffer_rsrc_t rx = __builtin_amdgcn_make_buffer_rsrc((void*)(ws + WS_X1F), (short)0, 4 << 20, 0x00020000);
    const unsigned fo = ((mt16 * 16 + 2 * wv) * 64 + fl) * 16;
    __builtin_amdgcn_raw_buffer_store_b128((u32x4){cvtpk(v[0], v[1]), cvtpk(v[2], v[3]), cvtpk(v[4], v[5]), cvtpk(v[6], v[7])}, rx, fo, 0, 16);
    __builtin_amdgcn_raw_buffer_store_b128((u32x4){cvtpk(v[8], v[9]), cvtpk(v[10], v[11]), cvtpk(v[12], v[13]), cvtpk(v[14], v[15])}, rx, fo + 1024, 0, 16);
    asm volatile("s_waitcnt lgkmcnt(0)" ::: "memory"); __builtin_amdgcn_s_barrier(); asm volatile("" ::: "memory");
    const float m0 = red[m8 * 2], m1 = red[(8 + m8) * 2], m2 = red[(16 + m8) * 2], m3 = red[(24 + m8) * 2];
    const float mean = 0.25f * ((m0 + m1) + (m2 + m3));
    const float M2 = (red[m8 * 2 + 1] + red[(8 + m8) * 2 + 1]) + (red[(16 + m8) * 2 + 1] + red[(24 + m8) * 2 + 1])
                   + 128.f * ((m0 - mean) * (m0 - mean) + (m1 - mean) * (m1 - mean) + (m2 - mean) * (m2 - mean) + (m3 - mean) * (m3 - mean));
    const float rstd = rsqrtf(M2 * (1.f / D) + 1e-5f);
    float o[16];
#pragma unroll
    for (int nt = 0; nt < 4; ++nt)
#pragma unroll
      for (int r = 0; r < 4; ++r) o[4 * nt + r] = (v[4 * nt + r] - mean) * rstd * rbf(1.f + cb[nt][r]) + sb[nt][r];
    __builtin_amdgcn_raw_buffer_store_b128((u32x4){cvtpk(o[0], o[1]), cvtpk(o[2], o[3]), cvtpk(o[4], o[5]), cvtpk(o[6], o[7])}, rx, fo, 2 << 20, 16);
    __builtin_amdgcn_raw_buffer_store_b128((u32x4){cvtpk(o[8], o[9]), cvtpk(o[10], o[11]), cvtpk(o[12], o[13]), cvtpk(o[14], o[15])}, rx, fo + 1024, 2 << 20, 16);
    asm volatile("s_waitcnt vmcnt(0)" ::: "memory"); __builtin_amdgcn_s_barrier(); asm volatile("" ::: "memory");
    seen = arrive_ret(cnt + 64, wid);
  }
  if (idx < 14) {
    if (wid != 0) PRE_G2(G2P1);
    wait_ge2(cnt + 64, 16, wid, seen);
    if (wid == 0) PRE_G2(G2P1);
    asm volatile(
    "v_mbcnt_lo_u32_b32 v196, -1, 0\n\t"
    "v_mbcnt_hi_u32_b32 v196, -1, v196\n\t"
    "s_lshl_b32 s60, %5, 8\n\t"
    "s_mov_b64 s[40:41], %1\n\t"
    "s_add_u32 s40, s40, s60\n\t"
    "s_addc_u32 s41, s41, 0\n\t"
    "s_and_b32 s41, s41, 0xffff\n\t"
    "s_mov_b32 s42, 1831680\n\t"
    "s_mov_b32 s43, 0x20000\n\t"
    "s_mov_b64 s[44:45], %2\n\t"
    "s_add_u32 s44, s44, s60\n\t"
    "s_addc_u32 s45, s45, 0\n\t"
    "s_and_b32 s45, s45, 0xffff\n\t"
    "s_mov_b32 s46, 1831680\n\t"
    "s_mov_b32 s47, 0x20000\n\t"
    "v_lshrrev_b32 v220, 3, v196\n\t"
    "s_lshl_b32 s60, %7, 4\n\t"
    "v_add_u32 v220, s60, v220\n\t"
    "s_mov_b32 s60, 3584\n\t"
    "v_mul_lo_u32 v198, v220, s60\n\t"
    "v_bfe_u32 v221, v196, 4, 1\n\t"
    "v_lshlrev_b32 v221, 2, v221\n\t"
    "v_bfe_u32 v222, v196, 5, 1\n\t"
    "v_lshl_add_u32 v221, v222, 1, v221\n\t"
    "v_and_b32 v222, 7, v196\n\t"
    "v_xor_b32 v222, v222, v221\n\t"
    "v_lshl_add_u32 v198, v222, 4, v198\n\t"
    "s_lshl_b32 s68, %7, 11\n\t"
    "s_add_u32 s68, s68, %8\n\t"
    "v_lshlrev_b32 v197, 4, v196\n\t"
    "s_lshl_b32 s60, %4, 3\n\t"
    "s_lshl_b32 s61, %7, 1\n\t"
    "s_add_u32 s62, s60, s61\n\t"
    "s_mul_i32 s60, s62, 16384\n\t"
    "s_mov_b64 s[36:37], %0\n\t"
    "s_add_u32 s36, s36, s60\n\t"
    "s_addc_u32 s37, s37, 0\n\t"
    "s_and_b32 s37, s37, 0xffff\n\t"
    "s_mov_b32 s38, 32768\n\t"
    "s_mov_b32 s39, 0x20000\n\t"
    "s_mov_b32 s69, 0\n\t"
    "buffer_load_dwordx4 a[0:3], v197, s[36:39], s69 offen offset:0 sc1\n\t"
    "s_mov_b32 s69, 16384\n\t"
    "buffer_load_dwordx4 a[4:7], v197, s[36:39], s69 offen offset:0 sc1\n\t"
    "s_mov_b32 s69, 0\n\t"
    "buffer_load_dwordx4 a[8:11], v197, s[36:39], s69 offen offset:1024 sc1\n\t"
    "s_mov_b32 s69, 16384\n\t"
    "buffer_load_dwordx4 a[12:15], v197, s[36:39], s69 offen offset:1024 sc1\n\t"
    "s_mov_b32 s69, 0\n\t"
    "buffer_load_dwordx4 a[16:19], v197, s[36:39], s69 offen offset:2048 sc1\n\t"
    "s_mov_b32 s69, 16384\n\t"
    "buffer_load_dwordx4 a[20:23], v197, s[36:39], s69 offen offset:2048 sc1\n\t"
    "s_mov_b32 s69, 0\n\t"
    "buffer_load_dwordx4 a[24:27], v197, s[36:39], s69 offen offset:3072 sc1\n\t"
    "s_mov_b32 s69, 16384\n\t"
    "buffer_load_dwordx4 a[28:31], v197, s[36:39], s69 offen offset:3072 sc1\n\t"
    "s_mul_i32 s60, s62, 56\n\t"
    "s_lshl_b32 s61, %5, 2\n\t"
    "s_add_u32 s60, s60, s61\n\t"
    "s_lshl_b32 s60, s60, 10\n\t"
    "s_mov_b64 s[48:49], %3\n\t"
    "s_add_u32 s48, s48, s60\n\t"
    "s_addc_u32 s49, s49, 0\n\t"
    "s_and_b32 s49, s49, 0xffff\n\t"
    "s_mov_b32 s50, 61440\n\t"
    "s_mov_b32 s51, 0x20000\n\t"
    "v_and_b32 v221, 1, v196\n\t"
    "v_lshlrev_b32 v223, 3, v221\n\t"
    "v_bfe_u32 v221, v196, 2, 3\n\t"
    "v_lshl_add_u32 v223, v221, 7, v223\n\t"
    "v_lshrrev_b32 v221, 5, v196\n\t"
    "v_lshl_add_u32 v223, v221, 10, v223\n\t"
    "v_add_u32 v223, %8, v223\n\t"
    "v_bfe_u32 v221, v196, 3, 1\n\t"
    "v_lshlrev_b32 v221, 2, v221\n\t"
    "v_bfe_u32 v222, v196, 4, 1\n\t"
    "v_lshl_add_u32 v221, v222, 1, v221\n\t"
    "v_bfe_u32 v222, v196, 1, 1\n\t"
    "v_xor_b32 v221, v221, v222\n\t"
    "v_xor_b32 v222, 0, v221\n\t"
    "v_lshl_add_u32 v200, v222, 4, v223\n\t"
    "v_add_u32 v204, 0x10000, v200\n\t"
    "v_add_u32 v208, 0x20000, v200\n\t"
    "v_xor_b32 v222, 2, v221\n\t"
    "v_lshl_add_u32 v201, v222, 4, v223\n\t"
    "v_add_u32 v205, 0x10000, v201\n\t"
    "v_add_u32 v209, 0x20000, v201\n\t"
    "v_xor_b32 v222, 4, v221\n\t"
    "v_lshl_add_u32 v202, v222, 4, v223\n\t"
    "v_add_u32 v206, 0x10000, v202\n\t"
    "v_add_u32 v210, 0x20000, v202\n\t"
    "v_xor_b32 v222, 6, v221\n\t"
    "v_lshl_add_u32 v203, v222, 4, v223\n\t"
    "v_add_u32 v207, 0x10000, v203\n\t"
    "v_add_u32 v211, 0x20000, v203\n\t"
    "v_mov_b32 v212, 0xc0135761\n\t"
    "v_mov_b32 v214, 0xbdd2d3e8\n\t"
    "v_mov_b32 v216, 1.0\n\t"
    "v_mov_b32 v213, 0xc0135761\n\t"
    "v_mov_b32 v215, 0xbdd2d3e8\n\t"
    "v_mov_b32 v217, 1.0\n\t"
    "s_mul_i32 s60, %7, 458752\n\t"
    "s_lshl_b32 s61, %10, 7\n\t"
    "s_add_u32 s60, s60, s61\n\t"
    "s_mov_b64 s[52:53], %9\n\t"
    "s_add_u32 s52, s52, s60\n\t"
    "s_addc_u32 s53, s53, 0\n\t"
    "s_and_b32 s53, s53, 0xffff\n\t"
    "s_mov_b32 s54, 457856\n\t"
    "s_mov_b32 s55, 0x20000\n\t"
    "v_bfe_u32 v221, v196, 4, 1\n\t"
    "v_lshlrev_b32 v221, 2, v221\n\t"
    "v_bfe_u32 v222, v196, 5, 1\n\t"
    "v_lshl_add_u32 v221, v222, 1, v221\n\t"
    "v_and_b32 v222, 7, v196\n\t"
    "v_xor_b32 v222, v222, v221\n\t"
    "v_lshrrev_b32 v221, 3, v196\n\t"
    "v_lshlrev_b32 v221, 10, v221\n\t"
    "v_lshl_add_u32 v199, v222, 4, v221\n\t"
    "s_lshl_b32 s70, %7, 12\n\t"
    "s_add_u32 s70, s70, %8\n\t"
    "s_barrier\n\t"
    "ds_read_b64_tr_b16 a[128:129], v204 offset:49152\n\t"
    "ds_read_b64_tr_b16 a[130:131], v204 offset:51200\n\t"
    "ds_read_b64_tr_b16 a[132:133], v205 offset:49152\n\t"
    "ds_read_b64_tr_b16 a[134:135], v205 offset:51200\n\t"
    "ds_read_b64_tr_b16 a[136:137], v206 offset:49152\n\t"
    "ds_read_b64_tr_b16 a[138:139], v206 offset:51200\n\t"
    "ds_read_b64_tr_b16 a[140:141], v207 offset:49152\n\t"
    "ds_read_b64_tr_b16 a[142:143], v207 offset:51200\n\t"
    "ds_read_b64_tr_b16 a[144:145], v204 offset:57344\n\t"
    "ds_read_b64_tr_b16 a[146:147], v204 offset:59392\n\t"
    "ds_read_b64_tr_b16 a[148:149], v205 offset:57344\n\t"
    "ds_read_b64_tr_b16 a[150:151], v205 offset:59392\n\t"
    "s_waitcnt vmcnt(6) lgkmcnt(10)\n\t"
    "s_mov_b32 s69, 4096\n\t"
    "v_mfma_f32_16x16x32_bf16 v[0:3], a[128:131], a[0:3], 0\n\t"
    "buffer_load_dwordx4 a[32:35], v197, s[36:39], s69 offen offset:0 sc1\n\t"
    "ds_read_b64_tr_b16 a[152:153], v206 offset:57344\n\t"
    "v_mfma_f32_16x16x32_bf16 v[4:7], a[128:131], a[4:7], 0\n\t"
    "ds_read_b64_tr_b16 a[154:155], v206 offset:59392\n\t"
    "s_waitcnt lgkmcnt(10)\n\t"
    "v_mfma_f32_16x16x32_bf16 v[8:11], a[132:135], a[0:3], 0\n\t"
    "ds_read_b64_tr_b16 a[156:157], v207 offset:57344\n\t"
    "v_mfma_f32_16x16x32_bf16 v[12:15], a[132:135], a[4:7], 0\n\t"
    "ds_read_b64_tr_b16 a[158:159], v207 offset:59392\n\t"
    "s_waitcnt lgkmcnt(10)\n\t"
    "v_mfma_f32_16x16x32_bf16 v[16:19], a[136:139], a[0:3], 0\n\t"
    "ds_read_b64_tr_b16 a[160:161], v204 offset:53248\n\t"
    "v_mfma_f32_16x16x32_bf16 v[20:23], a[136:139], a[4:7], 0\n\t"
    "ds_read_b64_tr_b16 a[162:163], v204 offset:55296\n\t"
    "s_waitcnt lgkmcnt(10)\n\t"
    "s_mov_b32 s69, 20480\n\t"
    "v_mfma_f32_16x16x32_bf16 v[24:27], a[140:143], a[0:3], 0\n\t"
    "buffer_load_dwordx4 a[36:39], v197, s[36:39], s69 offen offset:0 sc1\n\t"
    "ds_read_b64_tr_b16 a[164:165], v205 offset:53248\n\t"
    "v_mfma_f32_16x16x32_bf16 v[28:31], a[140:143], a[4:7], 0\n\t"
    "ds_read_b64_tr_b16 a[166:167], v205 offset:55296\n\t"
    "s_waitcnt lgkmcnt(10)\n\t"
    "v_mfma_f32_16x16x32_bf16 v[32:35], a[144:147], a[0:3], 0\n\t"
    "ds_read_b64_tr_b16 a[168:169], v206 offset:53248\n\t"
    "v_mfma_f32_16x16x32_bf16 v[36:39], a[144:147], a[4:7], 0\n\t"
    "ds_read_b64_tr_b16 a[170:171], v206 offset:55296\n\t"
    "s_waitcnt lgkmcnt(10)\n\t"
    "v_mfma_f32_16x16x32_bf16 v[40:43], a[148:151], a[0:3], 0\n\t"
    "ds_read_b64_tr_b16 a[172:173], v207 offset:53248\n\t"
    "v_mfma_f32_16x16x32_bf16 v[44:47], a[148:151], a[4:7], 0\n\t"
    "ds_read_b64_tr_b16 a[174:175], v207 offset:55296\n\t"
    "s_waitcnt lgkmcnt(10)\n\t"
    "s_mov_b32 s69, 4096\n\t"
    "v_mfma_f32_16x16x32_bf16 v[48:51], a[152:155], a[0:3], 0\n\t"
    "buffer_load_dwordx4 a[40:43], v197, s[36:39], s69 offen offset:1024 sc1\n\t"
    "ds_read_b64_tr_b16 a[176:177], v204 offset:61440\n\t"
    "v_mfma_f32_16x16x32_bf16 v[52:55], a[152:155], a[4:7], 0\n\t"
    "ds_read_b64_tr_b16 a[178:179], v204 offset:63488\n\t"
    "s_waitcnt lgkmcnt(10)\n\t"
    "v_mfma_f32_16x16x32_bf16 v[56:59], a[156:159], a[0:3], 0\n\t"
    "ds_read_b64_tr_b16 a[180:181], v205 offset:61440\n\t"
    "v_mfma_f32_16x16x32_bf16 v[60:63], a[156:159], a[4:7], 0\n\t"
    "ds_read_b64_tr_b16 a[182:183], v205 offset:63488\n\t"
    "s_waitcnt vmcnt(7) lgkmcnt(10)\n\t"
    "v_mfma_f32_16x16x32_bf16 v[0:3], a[160:163], a[8:11], v[0:3]\n\t"
    "ds_read_b64_tr_b16 a[184:185], v206 offset:61440\n\t"
    "v_mfma_f32_16x16x32_bf16 v[4:7], a[160:163], a[12:15], v[4:7]\n\t"
    "ds_read_b64_tr_b16 a[186:187], v206 offset:63488\n\t"
    "s_waitcnt lgkmcnt(10)\n\t"
    "s_mov_b32 s69, 20480\n\t"
    "v_mfma_f32_16x16x32_bf16 v[8:11], a[164:167], a[8:11], v[8:11]\n\t"
    "buffer_load_dwordx4 a[44:47], v197, s[36:39], s69 offen offset:1024 sc1\n\t"
    "ds_read_b64_tr_b16 a[188:189], v207 offset:61440\n\t"
    "v_mfma_f32_16x16x32_bf16 v[12:15], a[164:167], a[12:15], v[12:15]\n\t"
    "ds_read_b64_tr_b16 a[190:191], v207 offset:63488\n\t"
    "s_barrier\n\t"
    "s_waitcnt lgkmcnt(10)\n\t"
    "v_mfma_f32_16x16x32_bf16 v[16:19], a[168:171], a[8:11], v[16:19]\n\t"
    "ds_read_b64_tr_b16 a[192:193], v208 offset:0\n\t"
    "v_mfma_f32_16x16x32_bf16 v[20:23], a[168:171], a[12:15], v[20:23]\n\t"
    "ds_read_b64_tr_b16 a[194:195], v208 offset:2048\n\t"
    "s_waitcnt lgkmcnt(10)\n\t"
    "v_mfma_f32_16x16x32_bf16 v[24:27], a[172:175], a[8:11], v[24:27]\n\t"
    "ds_read_b64_tr_b16 a[196:197], v209 offset:0\n\t"
    "v_mfma_f32_16x16x32_bf16 v[28:31], a[172:175], a[12:15], v[28:31]\n\t"
    "ds_read_b64_tr_b16 a[198:199], v209 offset:2048\n\t"
    "s_waitcnt lgkmcnt(10)\n\t"
    "s_mov_b32 s69, 4096\n\t"
    "v_mfma_f32_16x16x32_bf16 v[32:35], a[176:179], a[8:11], v[32:35]\n\t"
    "buffer_load_dwordx4 a[48:51], v197, s[36:39], s69 offen offset:2048 sc1\n\t"
    "ds_read_b64_tr_b16 a[200:201], v210 offset:0\n\t"
    "v_mfma_f32_16x16x32_bf16 v[36:39], a[176:179], a[12:15], v[36:39]\n\t"
    "ds_read_b64_tr_b16 a[202:203], v210 offset:2048\n\t"
    "s_waitcnt lgkmcnt(10)\n\t"
    "v_mfma_f32_16x16x32_bf16 v[40:43], a[180:183], a[8:11], v[40:43]\n\t"
    "ds_read_b64_tr_b16 a[204:205], v211 offset:0\n\t"
    "v_mfma_f32_16x16x32_bf16 v[44:47], a[180:183], a[12:15], v[44:47]\n\t"
    "ds_read_b64_tr_b16 a[206:207], v211 offset:2048\n\t"
    "s_waitcnt lgkmcnt(10)\n\t"
    "v_mfma_f32_16x16x32_bf16 v[48:51], a[184:187], a[8:11], v[48:51]\n\t"
    "ds_read_b64_tr_b16 a[208:209], v208 offset:8192\n\t"
    "v_mfma_f32_16x16x32_bf16 v[52:55], a[184:187], a[12:15], v[52:55]\n\t"
    "ds_read_b64_tr_b16 a[210:211], v208 offset:10240\n\t"
    "s_waitcnt lgkmcnt(10)\n\t"
    "s_mov_b32 s69, 20480\n\t"
    "v_mfma_f32_16x16x32_bf16 v[56:59], a[188:191], a[8:11], v[56:59]\n\t"
    "buffer_load_dwordx4 a[52:55], v197, s[36:39], s69 offen offset:2048 sc1\n\t"
    "ds_read_b64_tr_b16 a[212:213], v209 offset:8192\n\t"
    "v_mfma_f32_16x16x32_bf16 v[60:63], a[188:191], a[12:15], v[60:63]\n\t"
    "ds_read_b64_tr_b16 a[214:215], v209 offset:10240\n\t"
    "s_waitcnt vmcnt(8) lgkmcnt(10)\n\t"
    "v_mfma_f32_16x16x32_bf16 v[0:3], a[192:195], a[16:19], v[0:3]\n\t"
    "ds_read_b64_tr_b16 a[216:217], v210 offset:8192\n\t"
    "v_mfma_f32_16x16x32_bf16 v[4:7], a[192:195], a[20:23], v[4:7]\n\t"
    "ds_read_b64_tr_b16 a[218:219], v210 offset:10240\n\t"
    "s_waitcnt lgkmcnt(10)\n\t"
    "v_mfma_f32_16x16x32_bf16 v[8:11], a[196:199], a[16:19], v[8:11]\n\t"
    "ds_read_b64_tr_b16 a[220:221], v211 offset:8192\n\t"
    "v_mfma_f32_16x16x32_bf16 v[12:15], a[196:199], a[20:23], v[12:15]\n\t"
    "ds_read_b64_tr_b16 a[222:223], v211 offset:10240\n\t"
    "s_waitcnt lgkmcnt(10)\n\t"
    "s_mov_b32 s69, 4096\n\t"
    "v_mfma_f32_16x16x32_bf16 v[16:19], a[200:203], a[16:19], v[16:19]\n\t"
    "buffer_load_dwordx4 a[56:59], v197, s[36:39], s69 offen offset:3072 sc1\n\t"
    "ds_read_b64_tr_b16 a[224:225], v208 offset:4096\n\t"
    "v_mfma_f32_16x16x32_bf16 v[20:23], a[200:203], a[20:23], v[20:23]\n\t"
    "ds_read_b64_tr_b16 a[226:227], v208 offset:6144\n\t"
    "s_waitcnt lgkmcnt(10)\n\t"
    "v_mfma_f32_16x16x32_bf16 v[24:27], a[204:207], a[16:19], v[24:27]\n\t"
    "ds_read_b64_tr_b16 a[228:229], v209 offset:4096\n\t"
    "v_mfma_f32_16x16x32_bf16 v[28:31], a[204:207], a[20:23], v[28:31]\n\t"
    "ds_read_b64_tr_b16 a[230:231], v209 offset:6144\n\t"
    "s_waitcnt lgkmcnt(10)\n\t"
    "v_mfma_f32_16x16x32_bf16 v[32:35], a[208:211], a[16:19], v[32:35]\n\t"
    "ds_read_b64_tr_b16 a[232:233], v210 offset:4096\n\t"
    "v_mfma_f32_16x16x32_bf16 v[36:39], a[208:211], a[20:23], v[36:39]\n\t"
    "ds_read_b64_tr_b16 a[234:235], v210 offset:6144\n\t"
    "s_waitcnt lgkmcnt(10)\n\t"
    "s_mov_b32 s69, 20480\n\t"
    "v_mfma_f32_16x16x32_bf16 v[40:43], a[212:215], a[16:19], v[40:43]\n\t"
    "buffer_load_dwordx4 a[60:63], v197, s[36:39], s69 offen offset:3072 sc1\n\t"
    "ds_read_b64_tr_b16 a[236:237], v211 offset:4096\n\t"
    "v_mfma_f32_16x16x32_bf16 v[44:47], a[212:215], a[20:23], v[44:47]\n\t"
    "ds_read_b64_tr_b16 a[238:239], v211 offset:6144\n\t"
    "s_waitcnt lgkmcnt(10)\n\t"
    "v_mfma_f32_16x16x32_bf16 v[48:51], a[216:219], a[16:19], v[48:51]\n\t"
    "ds_read_b64_tr_b16 a[240:241], v208 offset:12288\n\t"
    "v_mfma_f32_16x16x32_bf16 v[52:55], a[216:219], a[20:23], v[52:55]\n\t"
    "ds_read_b64_tr_b16 a[242:243], v208 offset:14336\n\t"
    "s_waitcnt lgkmcnt(10)\n\t"
    "v_mfma_f32_16x16x32_bf16 v[56:59], a[220:223], a[16:19], v[56:59]\n\t"
    "ds_read_b64_tr_b16 a[244:245], v209 offset:12288\n\t"
    "v_mfma_f32_16x16x32_bf16 v[60:63], a[220:223], a[20:23], v[60:63]\n\t"
    "ds_read_b64_tr_b16 a[246:247], v209 offset:14336\n\t"
    "s_waitcnt vmcnt(8) lgkmcnt(10)\n\t"
    "s_mov_b32 s69, 8192\n\t"
    "v_mfma_f32_16x16x32_bf16 v[0:3], a[224:227], a[24:27], v[0:3]\n\t"
    "buffer_load_dwordx4 a[64:67], v197, s[36:39], s69 offen offset:0 sc1\n\t"
    "ds_read_b64_tr_b16 a[248:249], v210 offset:12288\n\t"
    "v_mfma_f32_16x16x32_bf16 v[4:7], a[224:227], a[28:31], v[4:7]\n\t"
    "ds_read_b64_tr_b16 a[250:251], v210 offset:14336\n\t"
    "s_waitcnt lgkmcnt(10)\n\t"
    "v_mfma_f32_16x16x32_bf16 v[8:11], a[228:231], a[24:27], v[8:11]\n\t"
    "ds_read_b64_tr_b16 a[252:253], v211 offset:12288\n\t"
    "v_mfma_f32_16x16x32_bf16 v[12:15], a[228:231], a[28:31], v[12:15]\n\t"
    "ds_read_b64_tr_b16 a[254:255], v211 offset:14336\n\t"
    "s_barrier\n\t"
    "s_waitcnt lgkmcnt(10)\n\t"
    "v_mfma_f32_16x16x32_bf16 v[16:19], a[232:235], a[24:27], v[16:19]\n\t"
    "ds_read_b64_tr_b16 a[128:129], v200 offset:0\n\t"
    "v_mfma_f32_16x16x32_bf16 v[20:23], a[232:235], a[28:31], v[20:23]\n\t"
    "ds_read_b64_tr_b16 a[130:131], v200 offset:2048\n\t"
    "s_waitcnt lgkmcnt(10)\n\t"
    "s_mov_b32 s69, 24576\n\t"
    "v_mfma_f32_16x16x32_bf16 v[24:27], a[236:239], a[24:27], v[24:27]\n\t"
    "buffer_load_dwordx4 a[68:71], v197, s[36:39], s69 offen offset:0 sc1\n\t"
    "ds_read_b64_tr_b16 a[132:133], v201 offset:0\n\t"
    "v_mfma_f32_16x16x32_bf16 v[28:31], a[236:239], a[28:31], v[28:31]\n\t"
    "ds_read_b64_tr_b16 a[134:135], v201 offset:2048\n\t"
    "s_waitcnt lgkmcnt(10)\n\t"
    "v_mfma_f32_16x16x32_bf16 v[32:35], a[240:243], a[24:27], v[32:35]\n\t"
    "ds_read_b64_tr_b16 a[136:137], v202 offset:0\n\t"
    "v_mfma_f32_16x16x32_bf16 v[36:39], a[240:243], a[28:31], v[36:39]\n\t"
    "ds_read_b64_tr_b16 a[138:139], v202 offset:2048\n\t"
    "s_waitcnt lgkmcnt(10)\n\t"
    "v_mfma_f32_16x16x32_bf16 v[40:43], a[244:247], a[24:27], v[40:43]\n\t"
    "ds_read_b64_tr_b16 a[140:141], v203 offset:0\n\t"
    "v_mfma_f32_16x16x32_bf16 v[44:47], a[244:247], a[28:31], v[44:47]\n\t"
    "ds_read_b64_tr_b16 a[142:143], v203 offset:2048\n\t"
    "s_waitcnt lgkmcnt(10)\n\t"
    "s_mov_b32 s69, 8192\n\t"
    "v_mfma_f32_16x16x32_bf16 v[48:51], a[248:251], a[24:27], v[48:51]\n\t"
    "buffer_load_dwordx4 a[72:75], v197, s[36:39], s69 offen offset:1024 sc1\n\t"
    "ds_read_b64_tr_b16 a[144:145], v200 offset:8192\n\t"
    "v_mfma_f32_16x16x32_bf16 v[52:55], a[248:251], a[28:31], v[52:55]\n\t"
    "ds_read_b64_tr_b16 a[146:147], v200 offset:10240\n\t"
    "s_waitcnt lgkmcnt(10)\n\t"
    "v_mfma_f32_16x16x32_bf16 v[56:59], a[252:255], a[24:27], v[56:59]\n\t"
    "ds_read_b64_tr_b16 a[148:149], v201 offset:8192\n\t"
    "v_mfma_f32_16x16x32_bf16 v[60:63], a[252:255], a[28:31], v[60:63]\n\t"
    "ds_read_b64_tr_b16 a[150:151], v201 offset:10240\n\t"
    "s_waitcnt vmcnt(9) lgkmcnt(10)\n\t"
    "v_mfma_f32_16x16x32_bf16 v[0:3], a[128:131], a[32:35], v[0:3]\n\t"
    "ds_read_b64_tr_b16 a[152:153], v202 offset:8192\n\t"
    "v_mfma_f32_16x16x32_bf16 v[4:7], a[128:131], a[36:39], v[4:7]\n\t"
    "ds_read_b64_tr_b16 a[154:155], v202 offset:10240\n\t"
    "s_waitcnt lgkmcnt(10)\n\t"
    "s_mov_b32 s69, 24576\n\t"
    "v_mfma_f32_16x16x32_bf16 v[8:11], a[132:135], a[32:35], v[8:11]\n\t"
    "buffer_load_dwordx4 a[76:79], v197, s[36:39], s69 offen offset:1024 sc1\n\t"
    "ds_read_b64_tr_b16 a[156:157], v203 offset:8192\n\t"
    "v_mfma_f32_16x16x32_bf16 v[12:15], a[132:135], a[36:39], v[12:15]\n\t"
    "ds_read_b64_tr_b16 a[158:159], v203 offset:10240\n\t"
    "s_waitcnt lgkmcnt(10)\n\t"
    "v_mfma_f32_16x16x32_bf16 v[16:19], a[136:139], a[32:35], v[16:19]\n\t"
    "ds_read_b64_tr_b16 a[160:161], v200 offset:4096\n\t"
    "v_mfma_f32_16x16x32_bf16 v[20:23], a[136:139], a[36:39], v[20:23]\n\t"
    "ds_read_b64_tr_b16 a[162:163], v200 offset:6144\n\t"
    "s_waitcnt lgkmcnt(10)\n\t"
    "v_mfma_f32_16x16x32_bf16 v[24:27], a[140:143], a[32:35], v[24:27]\n\t"
    "ds_read_b64_tr_b16 a[164:165], v201 offset:4096\n\t"
    "v_mfma_f32_16x16x32_bf16 v[28:31], a[140:143], a[36:39], v[28:31]\n\t"
    "ds_read_b64_tr_b16 a[166:167], v201 offset:6144\n\t"
    "s_waitcnt lgkmcnt(10)\n\t"
    "s_mov_b32 s69, 8192\n\t"
    "v_mfma_f32_16x16x32_bf16 v[32:35], a[144:147], a[32:35], v[32:35]\n\t"
    "buffer_load_dwordx4 a[80:83], v197, s[36:39], s69 offen offset:2048 sc1\n\t"
    "ds_read_b64_tr_b16 a[168:169], v202 offset:4096\n\t"
    "v_mfma_f32_16x16x32_bf16 v[36:39], a[144:147], a[36:39], v[36:39]\n\t"
    "ds_read_b64_tr_b16 a[170:171], v202 offset:6144\n\t"
    "s_waitcnt lgkmcnt(10)\n\t"
    "v_mfma_f32_16x16x32_bf16 v[40:43], a[148:151], a[32:35], v[40:43]\n\t"
    "ds_read_b64_tr_b16 a[172:173], v203 offset:4096\n\t"
    "v_mfma_f32_16x16x32_bf16 v[44:47], a[148:151], a[36:39], v[44:47]\n\t"
    "ds_read_b64_tr_b16 a[174:175], v203 offset:6144\n\t"
    "s_waitcnt lgkmcnt(10)\n\t"
    "v_mfma_f32_16x16x32_bf16 v[48:51], a[152:155], a[32:35], v[48:51]\n\t"
    "ds_read_b64_tr_b16 a[176:177], v200 offset:12288\n\t"
    "v_mfma_f32_16x16x32_bf16 v[52:55], a[152:155], a[36:39], v[52:55]\n\t"
    "ds_read_b64_tr_b16 a[178:179], v200 offset:14336\n\t"
    "s_waitcnt lgkmcnt(10)\n\t"
    "s_mov_b32 s69, 24576\n\t"
    "v_mfma_f32_16x16x32_bf16 v[56:59], a[156:159], a[32:35], v[56:59]\n\t"
    "buffer_load_dwordx4 a[84:87], v197, s[36:39], s69 offen offset:2048 sc1\n\t"
    "ds_read_b64_tr_b16 a[180:181], v201 offset:12288\n\t"
    "v_mfma_f32_16x16x32_bf16 v[60:63], a[156:159], a[36:39], v[60:63]\n\t"
    "ds_read_b64_tr_b16 a[182:183], v201 offset:14336\n\t"
    "s_waitcnt vmcnt(10) lgkmcnt(10)\n\t"
    "v_mfma_f32_16x16x32_bf16 v[0:3], a[160:163], a[40:43], v[0:3]\n\t"
    "ds_read_b64_tr_b16 a[184:185], v202 offset:12288\n\t"
    "v_mfma_f32_16x16x32_bf16 v[4:7], a[160:163], a[44:47], v[4:7]\n\t"
    "ds_read_b64_tr_b16 a[186:187], v202 offset:14336\n\t"
    "s_waitcnt lgkmcnt(10)\n\t"
    "v_mfma_f32_16x16x32_bf16 v[8:11], a[164:167], a[40:43], v[8:11]\n\t"
    "ds_read_b64_tr_b16 a[188:189], v203 offset:12288\n\t"
    "v_mfma_f32_16x16x32_bf16 v[12:15], a[164:167], a[44:47], v[12:15]\n\t"
    "ds_read_b64_tr_b16 a[190:191], v203 offset:14336\n\t"
    "s_barrier\n\t"
    "s_waitcnt lgkmcnt(10)\n\t"
    "s_mov_b32 s69, 8192\n\t"
    "v_mfma_f32_16x16x32_bf16 v[16:19], a[168:171], a[40:43], v[16:19]\n\t"
    "buffer_load_dwordx4 a[88:91], v197, s[36:39], s69 offen offset:3072 sc1\n\t"
    "ds_read_b64_tr_b16 a[192:193], v200 offset:16384\n\t"
    "v_mfma_f32_16x16x32_bf16 v[20:23], a[168:171], a[44:47], v[20:23]\n\t"
    "ds_read_b64_tr_b16 a[194:195], v200 offset:18432\n\t"
    "s_waitcnt lgkmcnt(10)\n\t"
    "v_mfma_f32_16x16x32_bf16 v[24:27], a[172:175], a[40:43], v[24:27]\n\t"
    "ds_read_b64_tr_b16 a[196:197], v201 offset:16384\n\t"
    "v_mfma_f32_16x16x32_bf16 v[28:31], a[172:175], a[44:47], v[28:31]\n\t"
    "ds_read_b64_tr_b16 a[198:199], v201 offset:18432\n\t"
    "s_waitcnt lgkmcnt(10)\n\t"
    "v_mfma_f32_16x16x32_bf16 v[32:35], a[176:179], a[40:43], v[32:35]\n\t"
    "ds_read_b64_tr_b16 a[200:201], v202 offset:16384\n\t"
    "v_mfma_f32_16x16x32_bf16 v[36:39], a[176:179], a[44:47], v[36:39]\n\t"
    "ds_read_b64_tr_b16 a[202:203], v202 offset:18432\n\t"
    "s_waitcnt lgkmcnt(10)\n\t"
    "s_mov_b32 s69, 24576\n\t"
    "v_mfma_f32_16x16x32_bf16 v[40:43], a[180:183], a[40:43], v[40:43]\n\t"
    "buffer_load_dwordx4 a[92:95], v197, s[36:39], s69 offen offset:3072 sc1\n\t"
    "ds_read_b64_tr_b16 a[204:205], v203 offset:16384\n\t"
    "v_mfma_f32_16x16x32_bf16 v[44:47], a[180:183], a[44:47], v[44:47]\n\t"
    "ds_read_b64_tr_b16 a[206:207], v203 offset:18432\n\t"
    "s_waitcnt lgkmcnt(10)\n\t"
    "v_mfma_f32_16x16x32_bf16 v[48:51], a[184:187], a[40:43], v[48:51]\n\t"
    "ds_read_b64_tr_b16 a[208:209], v200 offset:24576\n\t"
    "v_mfma_f32_16x16x32_bf16 v[52:55], a[184:187], a[44:47], v[52:55]\n\t"
    "ds_read_b64_tr_b16 a[210:211], v200 offset:26624\n\t"
    "s_waitcnt lgkmcnt(10)\n\t"
    "v_mfma_f32_16x16x32_bf16 v[56:59], a[188:191], a[40:43], v[56:59]\n\t"
    "ds_read_b64_tr_b16 a[212:213], v201 offset:24576\n\t"
    "v_mfma_f32_16x16x32_bf16 v[60:63], a[188:191], a[44:47], v[60:63]\n\t"
    "ds_read_b64_tr_b16 a[214:215], v201 offset:26624\n\t"
    "s_waitcnt vmcnt(10) lgkmcnt(10)\n\t"
    "s_mov_b32 s69, 12288\n\t"
    "v_mfma_f32_16x16x32_bf16 v[0:3], a[192:195], a[48:51], v[0:3]\n\t"
    "buffer_load_dwordx4 a[96:99], v197, s[36:39], s69 offen offset:0 sc1\n\t"
    "ds_read_b64_tr_b16 a[216:217], v202 offset:24576\n\t"
    "v_mfma_f32_16x16x32_bf16 v[4:7], a[192:195], a[52:55], v[4:7]\n\t"
    "ds_read_b64_tr_b16 a[218:219], v202 offset:26624\n\t"
    "s_waitcnt lgkmcnt(10)\n\t"
    "v_mfma_f32_16x16x32_bf16 v[8:11], a[196:199], a[48:51], v[8:11]\n\t"
    "ds_read_b64_tr_b16 a[220:221], v203 offset:24576\n\t"
    "v_mfma_f32_16x16x32_bf16 v[12:15], a[196:199], a[52:55], v[12:15]\n\t"
    "ds_read_b64_tr_b16 a[222:223], v203 offset:26624\n\t"
    "s_waitcnt lgkmcnt(10)\n\t"
    "v_mfma_f32_16x16x32_bf16 v[16:19], a[200:203], a[48:51], v[16:19]\n\t"
    "ds_read_b64_tr_b16 a[224:225], v200 offset:20480\n\t"
    "v_mfma_f32_16x16x32_bf16 v[20:23], a[200:203], a[52:55], v[20:23]\n\t"
    "ds_read_b64_tr_b16 a[226:227], v200 offset:22528\n\t"
    "s_waitcnt lgkmcnt(10)\n\t"
    "s_mov_b32 s69, 28672\n\t"
    "v_mfma_f32_16x16x32_bf16 v[24:27], a[204:207], a[48:51], v[24:27]\n\t"
    "buffer_load_dwordx4 a[100:103], v197, s[36:39], s69 offen offset:0 sc1\n\t"
    "ds_read_b64_tr_b16 a[228:229], v201 offset:20480\n\t"
    "v_mfma_f32_16x16x32_bf16 v[28:31], a[204:207], a[52:55], v[28:31]\n\t"
    "ds_read_b64_tr_b16 a[230:231], v201 offset:22528\n\t"
    "s_waitcnt lgkmcnt(10)\n\t"
    "v_mfma_f32_16x16x32_bf16 v[32:35], a[208:211], a[48:51], v[32:35]\n\t"
    "ds_read_b64_tr_b16 a[232:233], v202 offset:20480\n\t"
    "v_mfma_f32_16x16x32_bf16 v[36:39], a[208:211], a[52:55], v[36:39]\n\t"
    "ds_read_b64_tr_b16 a[234:235], v202 offset:22528\n\t"
    "s_waitcnt lgkmcnt(10)\n\t"
    "v_mfma_f32_16x16x32_bf16 v[40:43], a[212:215], a[48:51], v[40:43]\n\t"
    "ds_read_b64_tr_b16 a[236:237], v203 offset:20480\n\t"
    "v_mfma_f32_16x16x32_bf16 v[44:47], a[212:215], a[52:55], v[44:47]\n\t"
    "ds_read_b64_tr_b16 a[238:239], v203 offset:22528\n\t"
    "s_waitcnt lgkmcnt(10)\n\t"
    "s_mov_b32 s69, 12288\n\t"
    "v_mfma_f32_16x16x32_bf16 v[48:51], a[216:219], a[48:51], v[48:51]\n\t"
    "buffer_load_dwordx4 a[104:107], v197, s[36:39], s69 offen offset:1024 sc1\n\t"
    "ds_read_b64_tr_b16 a[240:241], v200 offset:28672\n\t"
    "v_mfma_f32_16x16x32_bf16 v[52:55], a[216:219], a[52:55], v[52:55]\n\t"
    "ds_read_b64_tr_b16 a[242:243], v200 offset:30720\n\t"
    "s_waitcnt lgkmcnt(10)\n\t"
    "v_mfma_f32_16x16x32_bf16 v[56:59], a[220:223], a[48:51], v[56:59]\n\t"
    "ds_read_b64_tr_b16 a[244:245], v201 offset:28672\n\t"
    "v_mfma_f32_16x16x32_bf16 v[60:63], a[220:223], a[52:55], v[60:63]\n\t"
    "ds_read_b64_tr_b16 a[246:247], v201 offset:30720\n\t"
    "s_waitcnt vmcnt(11) lgkmcnt(10)\n\t"
    "v_mfma_f32_16x16x32_bf16 v[0:3], a[224:227], a[56:59], v[0:3]\n\t"
    "ds_read_b64_tr_b16 a[248:249], v202 offset:28672\n\t"
    "v_mfma_f32_16x16x32_bf16 v[4:7], a[224:227], a[60:63], v[4:7]\n\t"
    "ds_read_b64_tr_b16 a[250:251], v202 offset:30720\n\t"
    "s_waitcnt lgkmcnt(10)\n\t"
    "s_mov_b32 s69, 28672\n\t"
    "v_mfma_f32_16x16x32_bf16 v[8:11], a[228:231], a[56:59], v[8:11]\n\t"
    "buffer_load_dwordx4 a[108:111], v197, s[36:39], s69 offen offset:1024 sc1\n\t"
    "ds_read_b64_tr_b16 a[252:253], v203 offset:28672\n\t"
    "v_mfma_f32_16x16x32_bf16 v[12:15], a[228:231], a[60:63], v[12:15]\n\t"
    "ds_read_b64_tr_b16 a[254:255], v203 offset:30720\n\t"
    "s_barrier\n\t"
    "s_waitcnt lgkmcnt(10)\n\t"
    "v_mfma_f32_16x16x32_bf16 v[16:19], a[232:235], a[56:59], v[16:19]\n\t"
    "ds_read_b64_tr_b16 a[128:129], v200 offset:32768\n\t"
    "v_mfma_f32_16x16x32_bf16 v[20:23], a[232:235], a[60:63], v[20:23]\n\t"
    "ds_read_b64_tr_b16 a[130:131], v200 offset:34816\n\t"
    "s_waitcnt lgkmcnt(10)\n\t"
    "v_mfma_f32_16x16x32_bf16 v[24:27], a[236:239], a[56:59], v[24:27]\n\t"
    "ds_read_b64_tr_b16 a[132:133], v201 offset:32768\n\t"
    "v_mfma_f32_16x16x32_bf16 v[28:31], a[236:239], a[60:63], v[28:31]\n\t"
    "ds_read_b64_tr_b16 a[134:135], v201 offset:34816\n\t"
    "s_waitcnt lgkmcnt(10)\n\t"
    "s_mov_b32 s69, 12288\n\t"
    "v_mfma_f32_16x16x32_bf16 v[32:35], a[240:243], a[56:59], v[32:35]\n\t"
    "buffer_load_dwordx4 a[112:115], v197, s[36:39], s69 offen offset:2048 sc1\n\t"
    "ds_read_b64_tr_b16 a[136:137], v202 offset:32768\n\t"
    "v_mfma_f32_16x16x32_bf16 v[36:39], a[240:243], a[60:63], v[36:39]\n\t"
    "ds_read_b64_tr_b16 a[138:139], v202 offset:34816\n\t"
    "s_waitcnt lgkmcnt(10)\n\t"
    "v_mfma_f32_16x16x32_bf16 v[40:43], a[244:247], a[56:59], v[40:43]\n\t"
    "ds_read_b64_tr_b16 a[140:141], v203 offset:32768\n\t"
    "v_mfma_f32_16x16x32_bf16 v[44:47], a[244:247], a[60:63], v[44:47]\n\t"
    "ds_read_b64_tr_b16 a[142:143], v203 offset:34816\n\t"
    "s_waitcnt lgkmcnt(10)\n\t"
    "v_mfma_f32_16x16x32_bf16 v[48:51], a[248:251], a[56:59], v[48:51]\n\t"
    "ds_read_b64_tr_b16 a[144:145], v200 offset:40960\n\t"
    "v_mfma_f32_16x16x32_bf16 v[52:55], a[248:251], a[60:63], v[52:55]\n\t"
    "ds_read_b64_tr_b16 a[146:147], v200 offset:43008\n\t"
    "s_waitcnt lgkmcnt(10)\n\t"
    "s_mov_b32 s69, 28672\n\t"
    "v_mfma_f32_16x16x32_bf16 v[56:59], a[252:255], a[56:59], v[56:59]\n\t"
    "buffer_load_dwordx4 a[116:119], v197, s[36:39], s69 offen offset:2048 sc1\n\t"
    "ds_read_b64_tr_b16 a[148:149], v201 offset:40960\n\t"
    "v_mfma_f32_16x16x32_bf16 v[60:63], a[252:255], a[60:63], v[60:63]\n\t"
    "ds_read_b64_tr_b16 a[150:151], v201 offset:43008\n\t"
    "s_waitcnt vmcnt(12) lgkmcnt(10)\n\t"
    "v_mfma_f32_16x16x32_bf16 v[0:3], a[128:131], a[64:67], v[0:3]\n\t"
    "ds_read_b64_tr_b16 a[152:153], v202 offset:40960\n\t"
    "v_mfma_f32_16x16x32_bf16 v[4:7], a[128:131], a[68:71], v[4:7]\n\t"
    "ds_read_b64_tr_b16 a[154:155], v202 offset:43008\n\t"
    "s_waitcnt lgkmcnt(10)\n\t"
    "v_mfma_f32_16x16x32_bf16 v[8:11], a[132:135], a[64:67], v[8:11]\n\t"
    "ds_read_b64_tr_b16 a[156:157], v203 offset:40960\n\t"
    "v_mfma_f32_16x16x32_bf16 v[12:15], a[132:135], a[68:71], v[12:15]\n\t"
    "ds_read_b64_tr_b16 a[158:159], v203 offset:43008\n\t"
    "s_waitcnt lgkmcnt(10)\n\t"
    "s_mov_b32 s69, 12288\n\t"
    "v_mfma_f32_16x16x32_bf16 v[16:19], a[136:139], a[64:67], v[16:19]\n\t"
    "buffer_load_dwordx4 a[120:123], v197, s[36:39], s69 offen offset:3072 sc1\n\t"
    "ds_read_b64_tr_b16 a[160:161], v200 offset:36864\n\t"
    "v_mfma_f32_16x16x32_bf16 v[20:23], a[136:139], a[68:71], v[20:23]\n\t"
    "ds_read_b64_tr_b16 a[162:163], v200 offset:38912\n\t"
    "s_waitcnt lgkmcnt(10)\n\t"
    "v_mfma_f32_16x16x32_bf16 v[24:27], a[140:143], a[64:67], v[24:27]\n\t"
    "ds_read_b64_tr_b16 a[164:165], v201 offset:36864\n\t"
    "v_mfma_f32_16x16x32_bf16 v[28:31], a[140:143], a[68:71], v[28:31]\n\t"
    "ds_read_b64_tr_b16 a[166:167], v201 offset:38912\n\t"
    "s_waitcnt lgkmcnt(10)\n\t"
    "v_mfma_f32_16x16x32_bf16 v[32:35], a[144:147], a[64:67], v[32:35]\n\t"
    "ds_read_b64_tr_b16 a[168:169], v202 offset:36864\n\t"
    "v_mfma_f32_16x16x32_bf16 v[36:39], a[144:147], a[68:71], v[36:39]\n\t"
    "ds_read_b64_tr_b16 a[170:171], v202 offset:38912\n\t"
    "s_waitcnt lgkmcnt(10)\n\t"
    "s_mov_b32 s69, 28672\n\t"
    "v_mfma_f32_16x16x32_bf16 v[40:43], a[148:151], a[64:67], v[40:43]\n\t"
    "buffer_load_dwordx4 a[124:127], v197, s[36:39], s69 offen offset:3072 sc1\n\t"
    "ds_read_b64_tr_b16 a[172:173], v203 offset:36864\n\t"
    "v_mfma_f32_16x16x32_bf16 v[44:47], a[148:151], a[68:71], v[44:47]\n\t"
    "ds_read_b64_tr_b16 a[174:175], v203 offset:38912\n\t"
    "s_waitcnt lgkmcnt(10)\n\t"
    "v_mfma_f32_16x16x32_bf16 v[48:51], a[152:155], a[64:67], v[48:51]\n\t"
    "ds_read_b64_tr_b16 a[176:177], v200 offset:45056\n\t"
    "v_mfma_f32_16x16x32_bf16 v[52:55], a[152:155], a[68:71], v[52:55]\n\t"
    "ds_read_b64_tr_b16 a[178:179], v200 offset:47104\n\t"
    "s_waitcnt lgkmcnt(10)\n\t"
    "v_mfma_f32_16x16x32_bf16 v[56:59], a[156:159], a[64:67], v[56:59]\n\t"
    "ds_read_b64_tr_b16 a[180:181], v201 offset:45056\n\t"
    "v_mfma_f32_16x16x32_bf16 v[60:63], a[156:159], a[68:71], v[60:63]\n\t"
    "ds_read_b64_tr_b16 a[182:183], v201 offset:47104\n\t"
    "s_waitcnt vmcnt(12) lgkmcnt(10)\n\t"
    "s_add_u32 m0, s68, 114688\n\t"
    "s_mov_b32 s69, 229504\n\t"
    "v_mfma_f32_16x16x32_bf16 v[0:3], a[160:163], a[72:75], v[0:3]\n\t"
    "buffer_load_dwordx4 v198, s[40:43], s69 offen lds\n\t"
    "ds_read_b64_tr_b16 a[184:185], v202 offset:45056\n\t"
    "v_mfma_f32_16x16x32_bf16 v[4:7], a[160:163], a[76:79], v[4:7]\n\t"
    "ds_read_b64_tr_b16 a[186:187], v202 offset:47104\n\t"
    "s_waitcnt lgkmcnt(10)\n\t"
    "v_mfma_f32_16x16x32_bf16 v[8:11], a[164:167], a[72:75], v[8:11]\n\t"
    "ds_read_b64_tr_b16 a[188:189], v203 offset:45056\n\t"
    "v_mfma_f32_16x16x32_bf16 v[12:15], a[164:167], a[76:79], v[12:15]\n\t"
    "ds_read_b64_tr_b16 a[190:191], v203 offset:47104\n\t"
    "s_barrier\n\t"
    "s_waitcnt lgkmcnt(10)\n\t"
    "v_mfma_f32_16x16x32_bf16 v[16:19], a[168:171], a[72:75], v[16:19]\n\t"
    "ds_read_b64_tr_b16 a[192:193], v200 offset:49152\n\t"
    "v_mfma_f32_16x16x32_bf16 v[20:23], a[168:171], a[76:79], v[20:23]\n\t"
    "ds_read_b64_tr_b16 a[194:195], v200 offset:51200\n\t"
    "s_waitcnt lgkmcnt(10)\n\t"
    "s_add_u32 m0, s68, 115712\n\t"
    "s_mov_b32 s69, 258176\n\t"
    "v_mfma_f32_16x16x32_bf16 v[24:27], a[172:175], a[72:75], v[24:27]\n\t"
    "buffer_load_dwordx4 v198, s[40:43], s69 offen lds\n\t"
    "ds_read_b64_tr_b16 a[196:197], v201 offset:49152\n\t"
    "v_mfma_f32_16x16x32_bf16 v[28:31], a[172:175], a[76:79], v[28:31]\n\t"
    "ds_read_b64_tr_b16 a[198:199], v201 offset:51200\n\t"
    "s_waitcnt lgkmcnt(10)\n\t"
    "v_mfma_f32_16x16x32_bf16 v[32:35], a[176:179], a[72:75], v[32:35]\n\t"
    "ds_read_b64_tr_b16 a[200:201], v202 offset:49152\n\t"
    "v_mfma_f32_16x16x32_bf16 v[36:39], a[176:179], a[76:79], v[36:39]\n\t"
    "ds_read_b64_tr_b16 a[202:203], v202 offset:51200\n\t"
    "s_waitcnt lgkmcnt(10)\n\t"
    "v_mfma_f32_16x16x32_bf16 v[40:43], a[180:183], a[72:75], v[40:43]\n\t"
    "ds_read_b64_tr_b16 a[204:205], v203 offset:49152\n\t"
    "v_mfma_f32_16x16x32_bf16 v[44:47], a[180:183], a[76:79], v[44:47]\n\t"
    "ds_read_b64_tr_b16 a[206:207], v203 offset:51200\n\t"
    "s_waitcnt lgkmcnt(10)\n\t"
    "s_add_u32 m0, s68, 122880\n\t"
    "s_mov_b32 s69, 229504\n\t"
    "v_mfma_f32_16x16x32_bf16 v[48:51], a[184:187], a[72:75], v[48:51]\n\t"
    "buffer_load_dwordx4 v198, s[44:47], s69 offen lds\n\t"
    "ds_read_b64_tr_b16 a[208:209], v200 offset:57344\n\t"
    "v_mfma_f32_16x16x32_bf16 v[52:55], a[184:187], a[76:79], v[52:55]\n\t"
    "ds_read_b64_tr_b16 a[210:211], v200 offset:59392\n\t"
    "s_waitcnt lgkmcnt(10)\n\t"
    "v_mfma_f32_16x16x32_bf16 v[56:59], a[188:191], a[72:75], v[56:59]\n\t"
    "ds_read_b64_tr_b16 a[212:213], v201 offset:57344\n\t"
    "v_mfma_f32_16x16x32_bf16 v[60:63], a[188:191], a[76:79], v[60:63]\n\t"
    "ds_read_b64_tr_b16 a[214:215], v201 offset:59392\n\t"
    "s_waitcnt vmcnt(13) lgkmcnt(10)\n\t"
    "v_mfma_f32_16x16x32_bf16 v[0:3], a[192:195], a[80:83], v[0:3]\n\t"
    "ds_read_b64_tr_b16 a[216:217], v202 offset:57344\n\t"
    "v_mfma_f32_16x16x32_bf16 v[4:7], a[192:195], a[84:87], v[4:7]\n\t"
    "ds_read_b64_tr_b16 a[218:219], v202 offset:59392\n\t"
    "s_waitcnt lgkmcnt(10)\n\t"
    "s_add_u32 m0, s68, 123904\n\t"
    "s_mov_b32 s69, 258176\n\t"
    "v_mfma_f32_16x16x32_bf16 v[8:11], a[196:199], a[80:83], v[8:11]\n\t"
    "buffer_load_dwordx4 v198, s[44:47], s69 offen lds\n\t"
    "ds_read_b64_tr_b16 a[220:221], v203 offset:57344\n\t"
    "v_mfma_f32_16x16x32_bf16 v[12:15], a[196:199], a[84:87], v[12:15]\n\t"
    "ds_read_b64_tr_b16 a[222:223], v203 offset:59392\n\t"
    "s_waitcnt lgkmcnt(10)\n\t"
    "v_mfma_f32_16x16x32_bf16 v[16:19], a[200:203], a[80:83], v[16:19]\n\t"
    "ds_read_b64_tr_b16 a[224:225], v200 offset:53248\n\t"
    "v_mfma_f32_16x16x32_bf16 v[20:23], a[200:203], a[84:87], v[20:23]\n\t"
    "ds_read_b64_tr_b16 a[226:227], v200 offset:55296\n\t"
    "s_waitcnt lgkmcnt(10)\n\t"
    "v_mfma_f32_16x16x32_bf16 v[24:27], a[204:207], a[80:83], v[24:27]\n\t"
    "ds_read_b64_tr_b16 a[228:229], v201 offset:53248\n\t"
    "v_mfma_f32_16x16x32_bf16 v[28:31], a[204:207], a[84:87], v[28:31]\n\t"
    "ds_read_b64_tr_b16 a[230:231], v201 offset:55296\n\t"
    "s_waitcnt lgkmcnt(10)\n\t"
    "s_add_u32 m0, s68, 131072\n\t"
    "s_mov_b32 s69, 458880\n\t"
    "v_mfma_f32_16x16x32_bf16 v[32:35], a[208:211], a[80:83], v[32:35]\n\t"
    "buffer_load_dwordx4 v198, s[40:43], s69 offen lds\n\t"
    "ds_read_b64_tr_b16 a[232:233], v202 offset:53248\n\t"
    "v_mfma_f32_16x16x32_bf16 v[36:39], a[208:211], a[84:87], v[36:39]\n\t"
    "ds_read_b64_tr_b16 a[234:235], v202 offset:55296\n\t"
    "s_waitcnt lgkmcnt(10)\n\t"
    "v_mfma_f32_16x16x32_bf16 v[40:43], a[212:215], a[80:83], v[40:43]\n\t"
    "ds_read_b64_tr_b16 a[236:237], v203 offset:53248\n\t"
    "v_mfma_f32_16x16x32_bf16 v[44:47], a[212:215], a[84:87], v[44:47]\n\t"
    "ds_read_b64_tr_b16 a[238:239], v203 offset:55296\n\t"
    "s_waitcnt lgkmcnt(10)\n\t"
    "v_mfma_f32_16x16x32_bf16 v[48:51], a[216:219], a[80:83], v[48:51]\n\t"
    "ds_read_b64_tr_b16 a[240:241], v200 offset:61440\n\t"
    "v_mfma_f32_16x16x32_bf16 v[52:55], a[216:219], a[84:87], v[52:55]\n\t"
    "ds_read_b64_tr_b16 a[242:243], v200 offset:63488\n\t"
    "s_waitcnt lgkmcnt(10)\n\t"
    "s_add_u32 m0, s68, 132096\n\t"
    "s_mov_b32 s69, 487552\n\t"
    "v_mfma_f32_16x16x32_bf16 v[56:59], a[220:223], a[80:83], v[56:59]\n\t"
    "buffer_load_dwordx4 v198, s[40:43], s69 offen lds\n\t"
    "ds_read_b64_tr_b16 a[244:245], v201 offset:61440\n\t"
    "v_mfma_f32_16x16x32_bf16 v[60:63], a[220:223], a[84:87], v[60:63]\n\t"
    "ds_read_b64_tr_b16 a[246:247], v201 offset:63488\n\t"
    "s_waitcnt vmcnt(14) lgkmcnt(10)\n\t"
    "v_mfma_f32_16x16x32_bf16 v[0:3], a[224:227], a[88:91], v[0:3]\n\t"
    "ds_read_b64_tr_b16 a[248:249], v202 offset:61440\n\t"
    "v_mfma_f32_16x16x32_bf16 v[4:7], a[224:227], a[92:95], v[4:7]\n\t"
    "ds_read_b64_tr_b16 a[250:251], v202 offset:63488\n\t"
    "s_waitcnt lgkmcnt(10)\n\t"
    "v_mfma_f32_16x16x32_bf16 v[8:11], a[228:231], a[88:91], v[8:11]\n\t"
    "ds_read_b64_tr_b16 a[252:253], v203 offset:61440\n\t"
    "v_mfma_f32_16x16x32_bf16 v[12:15], a[228:231], a[92:95], v[12:15]\n\t"
    "ds_read_b64_tr_b16 a[254:255], v203 offset:63488\n\t"
    "s_barrier\n\t"
    "s_waitcnt lgkmcnt(10)\n\t"
    "s_add_u32 m0, s68, 139264\n\t"
    "s_mov_b32 s69, 458880\n\t"
    "v_mfma_f32_16x16x32_bf16 v[16:19], a[232:235], a[88:91], v[16:19]\n\t"
    "buffer_load_dwordx4 v198, s[44:47], s69 offen lds\n\t"
    "ds_read_b64_tr_b16 a[128:129], v204 offset:0\n\t"
    "v_mfma_f32_16x16x32_bf16 v[20:23], a[232:235], a[92:95], v[20:23]\n\t"
    "ds_read_b64_tr_b16 a[130:131], v204 offset:2048\n\t"
    "s_waitcnt lgkmcnt(10)\n\t"
    "v_mfma_f32_16x16x32_bf16 v[24:27], a[236:239], a[88:91], v[24:27]\n\t"
    "ds_read_b64_tr_b16 a[132:133], v205 offset:0\n\t"
    "v_mfma_f32_16x16x32_bf16 v[28:31], a[236:239], a[92:95], v[28:31]\n\t"
    "ds_read_b64_tr_b16 a[134:135], v205 offset:2048\n\t"
    "s_waitcnt lgkmcnt(10)\n\t"
    "v_mfma_f32_16x16x32_bf16 v[32:35], a[240:243], a[88:91], v[32:35]\n\t"
    "ds_read_b64_tr_b16 a[136:137], v206 offset:0\n\t"
    "v_mfma_f32_16x16x32_bf16 v[36:39], a[240:243], a[92:95], v[36:39]\n\t"
    "ds_read_b64_tr_b16 a[138:139], v206 offset:2048\n\t"
    "s_waitcnt lgkmcnt(10)\n\t"
    "s_add_u32 m0, s68, 140288\n\t"
    "s_mov_b32 s69, 487552\n\t"
    "v_mfma_f32_16x16x32_bf16 v[40:43], a[244:247], a[88:91], v[40:43]\n\t"
    "buffer_load_dwordx4 v198, s[44:47], s69 offen lds\n\t"
    "ds_read_b64_tr_b16 a[140:141], v207 offset:0\n\t"
    "v_mfma_f32_16x16x32_bf16 v[44:47], a[244:247], a[92:95], v[44:47]\n\t"
    "ds_read_b64_tr_b16 a[142:143], v207 offset:2048\n\t"
    "s_waitcnt lgkmcnt(10)\n\t"
    "v_mfma_f32_16x16x32_bf16 v[48:51], a[248:251], a[88:91], v[48:51]\n\t"
    "ds_read_b64_tr_b16 a[144:145], v204 offset:8192\n\t"
    "v_mfma_f32_16x16x32_bf16 v[52:55], a[248:251], a[92:95], v[52:55]\n\t"
    "ds_read_b64_tr_b16 a[146:147], v204 offset:10240\n\t"
    "s_waitcnt lgkmcnt(10)\n\t"
    "v_mfma_f32_16x16x32_bf16 v[56:59], a[252:255], a[88:91], v[56:59]\n\t"
    "ds_read_b64_tr_b16 a[148:149], v205 offset:8192\n\t"
    "v_mfma_f32_16x16x32_bf16 v[60:63], a[252:255], a[92:95], v[60:63]\n\t"
    "ds_read_b64_tr_b16 a[150:151], v205 offset:10240\n\t"
    "s_waitcnt vmcnt(14) lgkmcnt(10)\n\t"
    "s_add_u32 m0, s68, 0\n\t"
    "s_mov_b32 s69, 688256\n\t"
    "v_mfma_f32_16x16x32_bf16 v[0:3], a[128:131], a[96:99], v[0:3]\n\t"
    "buffer_load_dwordx4 v198, s[40:43], s69 offen lds\n\t"
    "ds_read_b64_tr_b16 a[152:153], v206 offset:8192\n\t"
    "v_mfma_f32_16x16x32_bf16 v[4:7], a[128:131], a[100:103], v[4:7]\n\t"
    "ds_read_b64_tr_b16 a[154:155], v206 offset:10240\n\t"
    "s_waitcnt lgkmcnt(10)\n\t"
    "v_mfma_f32_16x16x32_bf16 v[8:11], a[132:135], a[96:99], v[8:11]\n\t"
    "ds_read_b64_tr_b16 a[156:157], v207 offset:8192\n\t"
    "v_mfma_f32_16x16x32_bf16 v[12:15], a[132:135], a[100:103], v[12:15]\n\t"
    "ds_read_b64_tr_b16 a[158:159], v207 offset:10240\n\t"
    "s_waitcnt lgkmcnt(10)\n\t"
    "v_mfma_f32_16x16x32_bf16 v[16:19], a[136:139], a[96:99], v[16:19]\n\t"
    "ds_read_b64_tr_b16 a[160:161], v204 offset:4096\n\t"
    "v_mfma_f32_16x16x32_bf16 v[20:23], a[136:139], a[100:103], v[20:23]\n\t"
    "ds_read_b64_tr_b16 a[162:163], v204 offset:6144\n\t"
    "s_waitcnt lgkmcnt(10)\n\t"
    "s_add_u32 m0, s68, 1024\n\t"
    "s_mov_b32 s69, 716928\n\t"
    "v_mfma_f32_16x16x32_bf16 v[24:27], a[140:143], a[96:99], v[24:27]\n\t"
    "buffer_load_dwordx4 v198, s[40:43], s69 offen lds\n\t"
    "ds_read_b64_tr_b16 a[164:165], v205 offset:4096\n\t"
    "v_mfma_f32_16x16x32_bf16 v[28:31], a[140:143], a[100:103], v[28:31]\n\t"
    "ds_read_b64_tr_b16 a[166:167], v205 offset:6144\n\t"
    "s_waitcnt lgkmcnt(10)\n\t"
    "v_mfma_f32_16x16x32_bf16 v[32:35], a[144:147], a[96:99], v[32:35]\n\t"
    "ds_read_b64_tr_b16 a[168:169], v206 offset:4096\n\t"
    "v_mfma_f32_16x16x32_bf16 v[36:39], a[144:147], a[100:103], v[36:39]\n\t"
    "ds_read_b64_tr_b16 a[170:171], v206 offset:6144\n\t"
    "s_waitcnt lgkmcnt(10)\n\t"
    "v_mfma_f32_16x16x32_bf16 v[40:43], a[148:151], a[96:99], v[40:43]\n\t"
    "ds_read_b64_tr_b16 a[172:173], v207 offset:4096\n\t"
    "v_mfma_f32_16x16x32_bf16 v[44:47], a[148:151], a[100:103], v[44:47]\n\t"
    "ds_read_b64_tr_b16 a[174:175], v207 offset:6144\n\t"
    "s_waitcnt lgkmcnt(10)\n\t"
    "s_add_u32 m0, s68, 8192\n\t"
    "s_mov_b32 s69, 688256\n\t"
    "v_mfma_f32_16x16x32_bf16 v[48:51], a[152:155], a[96:99], v[48:51]\n\t"
    "buffer_load_dwordx4 v198, s[44:47], s69 offen lds\n\t"
    "ds_read_b64_tr_b16 a[176:177], v204 offset:12288\n\t"
    "v_mfma_f32_16x16x32_bf16 v[52:55], a[152:155], a[100:103], v[52:55]\n\t"
    "ds_read_b64_tr_b16 a[178:179], v204 offset:14336\n\t"
    "s_waitcnt lgkmcnt(10)\n\t"
    "v_mfma_f32_16x16x32_bf16 v[56:59], a[156:159], a[96:99], v[56:59]\n\t"
    "ds_read_b64_tr_b16 a[180:181], v205 offset:12288\n\t"
    "v_mfma_f32_16x16x32_bf16 v[60:63], a[156:159], a[100:103], v[60:63]\n\t"
    "ds_read_b64_tr_b16 a[182:183], v205 offset:14336\n\t"
    "s_waitcnt vmcnt(15) lgkmcnt(10)\n\t"
    "v_mfma_f32_16x16x32_bf16 v[0:3], a[160:163], a[104:107], v[0:3]\n\t"
    "ds_read_b64_tr_b16 a[184:185], v206 offset:12288\n\t"
    "v_mfma_f32_16x16x32_bf16 v[4:7], a[160:163], a[108:111], v[4:7]\n\t"
    "ds_read_b64_tr_b16 a[186:187], v206 offset:14336\n\t"
    "s_waitcnt lgkmcnt(10)\n\t"
    "s_add_u32 m0, s68, 9216\n\t"
    "s_mov_b32 s69, 716928\n\t"
    "v_mfma_f32_16x16x32_bf16 v[8:11], a[164:167], a[104:107], v[8:11]\n\t"
    "buffer_load_dwordx4 v198, s[44:47], s69 offen lds\n\t"
    "ds_read_b64_tr_b16 a[188:189], v207 offset:12288\n\t"
    "v_mfma_f32_16x16x32_bf16 v[12:15], a[164:167], a[108:111], v[12:15]\n\t"
    "ds_read_b64_tr_b16 a[190:191], v207 offset:14336\n\t"
    "s_barrier\n\t"
    "s_waitcnt lgkmcnt(10)\n\t"
    "v_mfma_f32_16x16x32_bf16 v[16:19], a[168:171], a[104:107], v[16:19]\n\t"
    "ds_read_b64_tr_b16 a[192:193], v204 offset:16384\n\t"
    "v_mfma_f32_16x16x32_bf16 v[20:23], a[168:171], a[108:111], v[20:23]\n\t"
    "ds_read_b64_tr_b16 a[194:195], v204 offset:18432\n\t"
    "s_waitcnt lgkmcnt(10)\n\t"
    "v_mfma_f32_16x16x32_bf16 v[24:27], a[172:175], a[104:107], v[24:27]\n\t"
    "ds_read_b64_tr_b16 a[196:197], v205 offset:16384\n\t"
    "v_mfma_f32_16x16x32_bf16 v[28:31], a[172:175], a[108:111], v[28:31]\n\t"
    "ds_read_b64_tr_b16 a[198:199], v205 offset:18432\n\t"
    "s_waitcnt lgkmcnt(10)\n\t"
    "s_add_u32 m0, s68, 16384\n\t"
    "s_mov_b32 s69, 917632\n\t"
    "v_mfma_f32_16x16x32_bf16 v[32:35], a[176:179], a[104:107], v[32:35]\n\t"
    "buffer_load_dwordx4 v198, s[40:43], s69 offen lds\n\t"
    "ds_read_b64_tr_b16 a[200:201], v206 offset:16384\n\t"
    "v_mfma_f32_16x16x32_bf16 v[36:39], a[176:179], a[108:111], v[36:39]\n\t"
    "ds_read_b64_tr_b16 a[202:203], v206 offset:18432\n\t"
    "s_waitcnt lgkmcnt(10)\n\t"
    "v_mfma_f32_16x16x32_bf16 v[40:43], a[180:183], a[104:107], v[40:43]\n\t"
    "ds_read_b64_tr_b16 a[204:205], v207 offset:16384\n\t"
    "v_mfma_f32_16x16x32_bf16 v[44:47], a[180:183], a[108:111], v[44:47]\n\t"
    "ds_read_b64_tr_b16 a[206:207], v207 offset:18432\n\t"
    "s_waitcnt lgkmcnt(10)\n\t"
    "v_mfma_f32_16x16x32_bf16 v[48:51], a[184:187], a[104:107], v[48:51]\n\t"
    "ds_read_b64_tr_b16 a[208:209], v204 offset:24576\n\t"
    "v_mfma_f32_16x16x32_bf16 v[52:55], a[184:187], a[108:111], v[52:55]\n\t"
    "ds_read_b64_tr_b16 a[210:211], v204 offset:26624\n\t"
    "s_waitcnt lgkmcnt(10)\n\t"
    "s_add_u32 m0, s68, 17408\n\t"
    "s_mov_b32 s69, 946304\n\t"
    "v_mfma_f32_16x16x32_bf16 v[56:59], a[188:191], a[104:107], v[56:59]\n\t"
    "buffer_load_dwordx4 v198, s[40:43], s69 offen lds\n\t"
    "ds_read_b64_tr_b16 a[212:213], v205 offset:24576\n\t"
    "v_mfma_f32_16x16x32_bf16 v[60:63], a[188:191], a[108:111], v[60:63]\n\t"
    "ds_read_b64_tr_b16 a[214:215], v205 offset:26624\n\t"
    "s_waitcnt vmcnt(16) lgkmcnt(10)\n\t"
    "v_mfma_f32_16x16x32_bf16 v[0:3], a[192:195], a[112:115], v[0:3]\n\t"
    "ds_read_b64_tr_b16 a[216:217], v206 offset:24576\n\t"
    "v_mfma_f32_16x16x32_bf16 v[4:7], a[192:195], a[116:119], v[4:7]\n\t"
    "ds_read_b64_tr_b16 a[218:219], v206 offset:26624\n\t"
    "s_waitcnt lgkmcnt(10)\n\t"
    "v_mfma_f32_16x16x32_bf16 v[8:11], a[196:199], a[112:115], v[8:11]\n\t"
    "ds_read_b64_tr_b16 a[220:221], v207 offset:24576\n\t"
    "v_mfma_f32_16x16x32_bf16 v[12:15], a[196:199], a[116:119], v[12:15]\n\t"
    "ds_read_b64_tr_b16 a[222:223], v207 offset:26624\n\t"
    "s_waitcnt lgkmcnt(10)\n\t"
    "s_add_u32 m0, s68, 24576\n\t"
    "s_mov_b32 s69, 917632\n\t"
    "v_mfma_f32_16x16x32_bf16 v[16:19], a[200:203], a[112:115], v[16:19]\n\t"
    "buffer_load_dwordx4 v198, s[44:47], s69 offen lds\n\t"
    "ds_read_b64_tr_b16 a[224:225], v204 offset:20480\n\t"
    "v_mfma_f32_16x16x32_bf16 v[20:23], a[200:203], a[116:119], v[20:23]\n\t"
    "ds_read_b64_tr_b16 a[226:227], v204 offset:22528\n\t"
    "s_waitcnt lgkmcnt(10)\n\t"
    "v_mfma_f32_16x16x32_bf16 v[24:27], a[204:207], a[112:115], v[24:27]\n\t"
    "ds_read_b64_tr_b16 a[228:229], v205 offset:20480\n\t"
    "v_mfma_f32_16x16x32_bf16 v[28:31], a[204:207], a[116:119], v[28:31]\n\t"
    "ds_read_b64_tr_b16 a[230:231], v205 offset:22528\n\t"
    "s_waitcnt lgkmcnt(10)\n\t"
    "v_mfma_f32_16x16x32_bf16 v[32:35], a[208:211], a[112:115], v[32:35]\n\t"
    "ds_read_b64_tr_b16 a[232:233], v206 offset:20480\n\t"
    "v_mfma_f32_16x16x32_bf16 v[36:39], a[208:211], a[116:119], v[36:39]\n\t"
    "ds_read_b64_tr_b16 a[234:235], v206 offset:22528\n\t"
    "s_waitcnt lgkmcnt(10)\n\t"
    "s_add_u32 m0, s68, 25600\n\t"
    "s_mov_b32 s69, 946304\n\t"
    "v_mfma_f32_16x16x32_bf16 v[40:43], a[212:215], a[112:115], v[40:43]\n\t"
    "buffer_load_dwordx4 v198, s[44:47], s69 offen lds\n\t"
    "ds_read_b64_tr_b16 a[236:237], v207 offset:20480\n\t"
    "v_mfma_f32_16x16x32_bf16 v[44:47], a[212:215], a[116:119], v[44:47]\n\t"
    "ds_read_b64_tr_b16 a[238:239], v207 offset:22528\n\t"
    "s_waitcnt lgkmcnt(10)\n\t"
    "v_mfma_f32_16x16x32_bf16 v[48:51], a[216:219], a[112:115], v[48:51]\n\t"
    "ds_read_b64_tr_b16 a[240:241], v204 offset:28672\n\t"
    "v_mfma_f32_16x16x32_bf16 v[52:55], a[216:219], a[116:119], v[52:55]\n\t"
    "ds_read_b64_tr_b16 a[242:243], v204 offset:30720\n\t"
    "s_waitcnt lgkmcnt(10)\n\t"
    "v_mfma_f32_16x16x32_bf16 v[56:59], a[220:223], a[112:115], v[56:59]\n\t"
    "ds_read_b64_tr_b16 a[244:245], v205 offset:28672\n\t"
    "v_mfma_f32_16x16x32_bf16 v[60:63], a[220:223], a[116:119], v[60:63]\n\t"
    "ds_read_b64_tr_b16 a[246:247], v205 offset:30720\n\t"
    "s_waitcnt vmcnt(16) lgkmcnt(10)\n\t"
    "s_add_u32 m0, s68, 32768\n\t"
    "s_mov_b32 s69, 1147008\n\t"
    "v_mfma_f32_16x16x32_bf16 v[0:3], a[224:227], a[120:123], v[0:3]\n\t"
    "buffer_load_dwordx4 v198, s[40:43], s69 offen lds\n\t"
    "ds_read_b64_tr_b16 a[248:249], v206 offset:28672\n\t"
    "v_mfma_f32_16x16x32_bf16 v[4:7], a[224:227], a[124:127], v[4:7]\n\t"
    "ds_read_b64_tr_b16 a[250:251], v206 offset:30720\n\t"
    "s_waitcnt lgkmcnt(10)\n\t"
    "v_mfma_f32_16x16x32_bf16 v[8:11], a[228:231], a[120:123], v[8:11]\n\t"
    "ds_read_b64_tr_b16 a[252:253], v207 offset:28672\n\t"
    "v_mfma_f32_16x16x32_bf16 v[12:15], a[228:231], a[124:127], v[12:15]\n\t"
    "ds_read_b64_tr_b16 a[254:255], v207 offset:30720\n\t"
    "s_barrier\n\t"
    "s_waitcnt lgkmcnt(10)\n\t"
    "v_mfma_f32_16x16x32_bf16 v[16:19], a[232:235], a[120:123], v[16:19]\n\t"
    "ds_read_b64_tr_b16 a[128:129], v204 offset:32768\n\t"
    "v_mfma_f32_16x16x32_bf16 v[20:23], a[232:235], a[124:127], v[20:23]\n\t"
    "ds_read_b64_tr_b16 a[130:131], v204 offset:34816\n\t"
    "s_waitcnt lgkmcnt(10)\n\t"
    "s_add_u32 m0, s68, 33792\n\t"
    "s_mov_b32 s69, 1175680\n\t"
    "v_mfma_f32_16x16x32_bf16 v[24:27], a[236:239], a[120:123], v[24:27]\n\t"
    "buffer_load_dwordx4 v198, s[40:43], s69 offen lds\n\t"
    "ds_read_b64_tr_b16 a[132:133], v205 offset:32768\n\t"
    "v_mfma_f32_16x16x32_bf16 v[28:31], a[236:239], a[124:127], v[28:31]\n\t"
    "ds_read_b64_tr_b16 a[134:135], v205 offset:34816\n\t"
    "s_waitcnt lgkmcnt(10)\n\t"
    "v_mfma_f32_16x16x32_bf16 v[32:35], a[240:243], a[120:123], v[32:35]\n\t"
    "ds_read_b64_tr_b16 a[136:137], v206 offset:32768\n\t"
    "v_mfma_f32_16x16x32_bf16 v[36:39], a[240:243], a[124:127], v[36:39]\n\t"
    "ds_read_b64_tr_b16 a[138:139], v206 offset:34816\n\t"
    "s_waitcnt lgkmcnt(10)\n\t"
    "v_mfma_f32_16x16x32_bf16 v[40:43], a[244:247], a[120:123], v[40:43]\n\t"
    "ds_read_b64_tr_b16 a[140:141], v207 offset:32768\n\t"
    "v_mfma_f32_16x16x32_bf16 v[44:47], a[244:247], a[124:127], v[44:47]\n\t"
    "ds_read_b64_tr_b16 a[142:143], v207 offset:34816\n\t"
    "s_waitcnt lgkmcnt(10)\n\t"
    "s_add_u32 m0, s68, 40960\n\t"
    "s_mov_b32 s69, 1147008\n\t"
    "v_mfma_f32_16x16x32_bf16 v[48:51], a[248:251], a[120:123], v[48:51]\n\t"
    "buffer_load_dwordx4 v198, s[44:47], s69 offen lds\n\t"
    "ds_read_b64_tr_b16 a[144:145], v204 offset:40960\n\t"
    "v_mfma_f32_16x16x32_bf16 v[52:55], a[248:251], a[124:127], v[52:55]\n\t"
    "ds_read_b64_tr_b16 a[146:147], v204 offset:43008\n\t"
    "s_waitcnt lgkmcnt(10)\n\t"
    "v_mfma_f32_16x16x32_bf16 v[56:59], a[252:255], a[120:123], v[56:59]\n\t"
    "ds_read_b64_tr_b16 a[148:149], v205 offset:40960\n\t"
    "v_mfma_f32_16x16x32_bf16 v[60:63], a[252:255], a[124:127], v[60:63]\n\t"
    "ds_read_b64_tr_b16 a[150:151], v205 offset:43008\n\t"
    "s_waitcnt lgkmcnt(10)\n\t"
    "v_mfma_f32_16x16x32_bf16 v[64:67], a[128:131], a[0:3], 0\n\t"
    "ds_read_b64_tr_b16 a[152:153], v206 offset:40960\n\t"
    "v_mfma_f32_16x16x32_bf16 v[68:71], a[128:131], a[4:7], 0\n\t"
    "ds_read_b64_tr_b16 a[154:155], v206 offset:43008\n\t"
    "s_waitcnt lgkmcnt(10)\n\t"
    "s_add_u32 m0, s68, 41984\n\t"
    "s_mov_b32 s69, 1175680\n\t"
    "v_mfma_f32_16x16x32_bf16 v[72:75], a[132:135], a[0:3], 0\n\t"
    "buffer_load_dwordx4 v198, s[44:47], s69 offen lds\n\t"
    "ds_read_b64_tr_b16 a[156:157], v207 offset:40960\n\t"
    "v_mfma_f32_16x16x32_bf16 v[76:79], a[132:135], a[4:7], 0\n\t"
    "ds_read_b64_tr_b16 a[158:159], v207 offset:43008\n\t"
    "s_waitcnt lgkmcnt(10)\n\t"
    "v_mfma_f32_16x16x32_bf16 v[80:83], a[136:139], a[0:3], 0\n\t"
    "ds_read_b64_tr_b16 a[160:161], v204 offset:36864\n\t"
    "v_mfma_f32_16x16x32_bf16 v[84:87], a[136:139], a[4:7], 0\n\t"
    "ds_read_b64_tr_b16 a[162:163], v204 offset:38912\n\t"
    "s_waitcnt lgkmcnt(10)\n\t"
    "v_mfma_f32_16x16x32_bf16 v[88:91], a[140:143], a[0:3], 0\n\t"
    "ds_read_b64_tr_b16 a[164:165], v205 offset:36864\n\t"
    "v_mul_f32 v224, v0, v0\n\t"
    "v_mfma_f32_16x16x32_bf16 v[92:95], a[140:143], a[4:7], 0\n\t"
    "ds_read_b64_tr_b16 a[166:167], v205 offset:38912\n\t"
    "v_mul_f32 v225, v1, v1\n\t"
    "s_waitcnt lgkmcnt(10)\n\t"
    "s_add_u32 m0, s68, 49152\n\t"
    "s_mov_b32 s69, 1376384\n\t"
    "v_mfma_f32_16x16x32_bf16 v[96:99], a[144:147], a[0:3], 0\n\t"
    "buffer_load_dwordx4 v198, s[40:43], s69 offen lds\n\t"
    "ds_read_b64_tr_b16 a[168:169], v206 offset:36864\n\t"
    "v_mfma_f32_16x16x32_bf16 v[100:103], a[144:147], a[4:7], 0\n\t"
    "ds_read_b64_tr_b16 a[170:171], v206 offset:38912\n\t"
    "v_mul_f32 v226, v2, v2\n\t"
    "s_waitcnt lgkmcnt(10)\n\t"
    "v_mfma_f32_16x16x32_bf16 v[104:107], a[148:151], a[0:3], 0\n\t"
    "ds_read_b64_tr_b16 a[172:173], v207 offset:36864\n\t"
    "v_mul_f32 v227, v3, v3\n\t"
    "v_mfma_f32_16x16x32_bf16 v[108:111], a[148:151], a[4:7], 0\n\t"
    "ds_read_b64_tr_b16 a[174:175], v207 offset:38912\n\t"
    "v_fma_f32 v220, v224, v214, v212\n\t"
    "s_waitcnt lgkmcnt(10)\n\t"
    "v_mfma_f32_16x16x32_bf16 v[112:115], a[152:155], a[0:3], 0\n\t"
    "ds_read_b64_tr_b16 a[176:177], v204 offset:45056\n\t"
    "v_fma_f32 v221, v225, v214, v212\n\t"
    "v_mfma_f32_16x16x32_bf16 v[116:119], a[152:155], a[4:7], 0\n\t"
    "ds_read_b64_tr_b16 a[178:179], v204 offset:47104\n\t"
    "v_fma_f32 v222, v226, v214, v212\n\t"
    "s_waitcnt lgkmcnt(10)\n\t"
    "s_add_u32 m0, s68, 50176\n\t"
    "s_mov_b32 s69, 1405056\n\t"
    "v_mfma_f32_16x16x32_bf16 v[120:123], a[156:159], a[0:3], 0\n\t"
    "buffer_load_dwordx4 v198, s[40:43], s69 offen lds\n\t"
    "ds_read_b64_tr_b16 a[180:181], v205 offset:45056\n\t"
    "v_mfma_f32_16x16x32_bf16 v[124:127], a[156:159], a[4:7], 0\n\t"
    "ds_read_b64_tr_b16 a[182:183], v205 offset:47104\n\t"
    "v_fma_f32 v223, v227, v214, v212\n\t"
    "s_waitcnt lgkmcnt(10)\n\t"
    "v_mfma_f32_16x16x32_bf16 v[64:67], a[160:163], a[8:11], v[64:67]\n\t"
    "ds_read_b64_tr_b16 a[184:185], v206 offset:45056\n\t"
    "v_mul_f32 v220, v220, v0\n\t"
    "v_mfma_f32_16x16x32_bf16 v[68:71], a[160:163], a[12:15], v[68:71]\n\t"
    "ds_read_b64_tr_b16 a[186:187], v206 offset:47104\n\t"
    "v_mul_f32 v221, v221, v1\n\t"
    "s_waitcnt lgkmcnt(10)\n\t"
    "v_mfma_f32_16x16x32_bf16 v[72:75], a[164:167], a[8:11], v[72:75]\n\t"
    "ds_read_b64_tr_b16 a[188:189], v207 offset:45056\n\t"
    "v_mul_f32 v222, v222, v2\n\t"
    "v_mfma_f32_16x16x32_bf16 v[76:79], a[164:167], a[12:15], v[76:79]\n\t"
    "ds_read_b64_tr_b16 a[190:191], v207 offset:47104\n\t"
    "v_mul_f32 v223, v223, v3\n\t"
    "s_waitcnt vmcnt(18)\n\t"
    "s_barrier\n\t"
    "s_waitcnt lgkmcnt(10)\n\t"
    "s_add_u32 m0, s68, 57344\n\t"
    "s_mov_b32 s69, 1376384\n\t"
    "v_mfma_f32_16x16x32_bf16 v[80:83], a[168:171], a[8:11], v[80:83]\n\t"
    "buffer_load_dwordx4 v198, s[44:47], s69 offen lds\n\t"
    "ds_read_b64_tr_b16 a[192:193], v204 offset:49152\n\t"
    "v_mfma_f32_16x16x32_bf16 v[84:87], a[168:171], a[12:15], v[84:87]\n\t"
    "ds_read_b64_tr_b16 a[194:195], v204 offset:51200\n\t"
    "v_exp_f32 v220, v220\n\t"
    "s_waitcnt lgkmcnt(10)\n\t"
    "v_mfma_f32_16x16x32_bf16 v[88:91], a[172:175], a[8:11], v[88:91]\n\t"
    "ds_read_b64_tr_b16 a[196:197], v205 offset:49152\n\t"
    "v_exp_f32 v221, v221\n\t"
    "v_mfma_f32_16x16x32_bf16 v[92:95], a[172:175], a[12:15], v[92:95]\n\t"
    "ds_read_b64_tr_b16 a[198:199], v205 offset:51200\n\t"
    "v_exp_f32 v222, v222\n\t"
    "s_waitcnt lgkmcnt(10)\n\t"
    "v_mfma_f32_16x16x32_bf16 v[96:99], a[176:179], a[8:11], v[96:99]\n\t"
    "ds_read_b64_tr_b16 a[200:201], v206 offset:49152\n\t"
    "v_exp_f32 v223, v223\n\t"
    "v_mfma_f32_16x16x32_bf16 v[100:103], a[176:179], a[12:15], v[100:103]\n\t"
    "ds_read_b64_tr_b16 a[202:203], v206 offset:51200\n\t"
    "v_mul_f32 v32, v32, v0\n\t"
    "s_waitcnt lgkmcnt(10)\n\t"
    "s_add_u32 m0, s68, 58368\n\t"
    "s_mov_b32 s69, 1405056\n\t"
    "v_mfma_f32_16x16x32_bf16 v[104:107], a[180:183], a[8:11], v[104:107]\n\t"
    "buffer_load_dwordx4 v198, s[44:47], s69 offen lds\n\t"
    "ds_read_b64_tr_b16 a[204:205], v207 offset:49152\n\t"
    "v_mfma_f32_16x16x32_bf16 v[108:111], a[180:183], a[12:15], v[108:111]\n\t"
    "ds_read_b64_tr_b16 a[206:207], v207 offset:51200\n\t"
    "v_mul_f32 v33, v33, v1\n\t"
    "s_waitcnt lgkmcnt(10)\n\t"
    "v_mfma_f32_16x16x32_bf16 v[112:115], a[184:187], a[8:11], v[112:115]\n\t"
    "ds_read_b64_tr_b16 a[208:209], v204 offset:57344\n\t"
    "v_mul_f32 v34, v34, v2\n\t"
    "v_mfma_f32_16x16x32_bf16 v[116:119], a[184:187], a[12:15], v[116:119]\n\t"
    "ds_read_b64_tr_b16 a[210:211], v204 offset:59392\n\t"
    "v_mul_f32 v35, v35, v3\n\t"
    "s_waitcnt lgkmcnt(10)\n\t"
    "v_mfma_f32_16x16x32_bf16 v[120:123], a[188:191], a[8:11], v[120:123]\n\t"
    "ds_read_b64_tr_b16 a[212:213], v205 offset:57344\n\t"
    "v_add_f32 v220, 1.0, v220\n\t"
    "v_mfma_f32_16x16x32_bf16 v[124:127], a[188:191], a[12:15], v[124:127]\n\t"
    "ds_read_b64_tr_b16 a[214:215], v205 offset:59392\n\t"
    "v_add_f32 v221, 1.0, v221\n\t"
    "s_waitcnt lgkmcnt(10)\n\t"
    "s_add_u32 m0, s68, 65536\n\t"
    "s_mov_b32 s69, 1605760\n\t"
    "v_mfma_f32_16x16x32_bf16 v[64:67], a[192:195], a[16:19], v[64:67]\n\t"
    "buffer_load_dwordx4 v198, s[40:43], s69 offen lds\n\t"
    "ds_read_b64_tr_b16 a[216:217], v206 offset:57344\n\t"
    "v_mfma_f32_16x16x32_bf16 v[68:71], a[192:195], a[20:23], v[68:71]\n\t"
    "ds_read_b64_tr_b16 a[218:219], v206 offset:59392\n\t"
    "v_add_f32 v222, 1.0, v222\n\t"
    "s_waitcnt lgkmcnt(10)\n\t"
    "v_mfma_f32_16x16x32_bf16 v[72:75], a[196:199], a[16:19], v[72:75]\n\t"
    "ds_read_b64_tr_b16 a[220:221], v207 offset:57344\n\t"
    "v_add_f32 v223, 1.0, v223\n\t"
    "v_mfma_f32_16x16x32_bf16 v[76:79], a[196:199], a[20:23], v[76:79]\n\t"
    "ds_read_b64_tr_b16 a[222:223], v207 offset:59392\n\t"
    "v_rcp_f32 v220, v220\n\t"
    "s_waitcnt lgkmcnt(10)\n\t"
    "v_mfma_f32_16x16x32_bf16 v[80:83], a[200:203], a[16:19], v[80:83]\n\t"
    "ds_read_b64_tr_b16 a[224:225], v204 offset:53248\n\t"
    "v_rcp_f32 v221, v221\n\t"
    "v_mfma_f32_16x16x32_bf16 v[84:87], a[200:203], a[20:23], v[84:87]\n\t"
    "ds_read_b64_tr_b16 a[226:227], v204 offset:55296\n\t"
    "v_rcp_f32 v222, v222\n\t"
    "s_waitcnt lgkmcnt(10)\n\t"
    "s_add_u32 m0, s68, 66560\n\t"
    "s_mov_b32 s69, 1634432\n\t"
    "v_mfma_f32_16x16x32_bf16 v[88:91], a[204:207], a[16:19], v[88:91]\n\t"
    "buffer_load_dwordx4 v198, s[40:43], s69 offen lds\n\t"
    "ds_read_b64_tr_b16 a[228:229], v205 offset:53248\n\t"
    "v_mfma_f32_16x16x32_bf16 v[92:95], a[204:207], a[20:23], v[92:95]\n\t"
    "ds_read_b64_tr_b16 a[230:231], v205 offset:55296\n\t"
    "v_rcp_f32 v223, v223\n\t"
    "s_waitcnt lgkmcnt(10)\n\t"
    "v_mfma_f32_16x16x32_bf16 v[96:99], a[208:211], a[16:19], v[96:99]\n\t"
    "ds_read_b64_tr_b16 a[232:233], v206 offset:53248\n\t"
    "v_mul_f32 v32, v32, v220\n\t"
    "v_mfma_f32_16x16x32_bf16 v[100:103], a[208:211], a[20:23], v[100:103]\n\t"
    "ds_read_b64_tr_b16 a[234:235], v206 offset:55296\n\t"
    "v_mul_f32 v33, v33, v221\n\t"
    "s_waitcnt lgkmcnt(10)\n\t"
    "v_mfma_f32_16x16x32_bf16 v[104:107], a[212:215], a[16:19], v[104:107]\n\t"
    "ds_read_b64_tr_b16 a[236:237], v207 offset:53248\n\t"
    "v_mul_f32 v34, v34, v222\n\t"
    "v_mfma_f32_16x16x32_bf16 v[108:111], a[212:215], a[20:23], v[108:111]\n\t"
    "ds_read_b64_tr_b16 a[238:239], v207 offset:55296\n\t"
    "v_mul_f32 v35, v35, v223\n\t"
    "s_waitcnt lgkmcnt(10)\n\t"
    "s_add_u32 m0, s68, 73728\n\t"
    "s_mov_b32 s69, 1605760\n\t"
    "v_mfma_f32_16x16x32_bf16 v[112:115], a[216:219], a[16:19], v[112:115]\n\t"
    "buffer_load_dwordx4 v198, s[44:47], s69 offen lds\n\t"
    "ds_read_b64_tr_b16 a[240:241], v204 offset:61440\n\t"
    "v_mfma_f32_16x16x32_bf16 v[116:119], a[216:219], a[20:23], v[116:119]\n\t"
    "ds_read_b64_tr_b16 a[242:243], v204 offset:63488\n\t"
    "v_cvt_pk_bf16_f32 v236, v32, v33\n\t"
    "s_waitcnt lgkmcnt(10)\n\t"
    "v_mfma_f32_16x16x32_bf16 v[120:123], a[220:223], a[16:19], v[120:123]\n\t"
    "ds_read_b64_tr_b16 a[244:245], v205 offset:61440\n\t"
    "v_cvt_pk_bf16_f32 v237, v34, v35\n\t"
    "v_mfma_f32_16x16x32_bf16 v[124:127], a[220:223], a[20:23], v[124:127]\n\t"
    "ds_read_b64_tr_b16 a[246:247], v205 offset:63488\n\t"
    "v_mul_f32 v224, v8, v8\n\t"
    "s_waitcnt lgkmcnt(10)\n\t"
    "v_mfma_f32_16x16x32_bf16 v[64:67], a[224:227], a[24:27], v[64:67]\n\t"
    "ds_read_b64_tr_b16 a[248:249], v206 offset:61440\n\t"
    "v_mul_f32 v225, v9, v9\n\t"
    "v_mfma_f32_16x16x32_bf16 v[68:71], a[224:227], a[28:31], v[68:71]\n\t"
    "ds_read_b64_tr_b16 a[250:251], v206 offset:63488\n\t"
    "v_mul_f32 v226, v10, v10\n\t"
    "s_waitcnt lgkmcnt(10)\n\t"
    "s_add_u32 m0, s68, 74752\n\t"
    "s_mov_b32 s69, 1634432\n\t"
    "v_mfma_f32_16x16x32_bf16 v[72:75], a[228:231], a[24:27], v[72:75]\n\t"
    "buffer_load_dwordx4 v198, s[44:47], s69 offen lds\n\t"
    "ds_read_b64_tr_b16 a[252:253], v207 offset:61440\n\t"
    "v_mfma_f32_16x16x32_bf16 v[76:79], a[228:231], a[28:31], v[76:79]\n\t"
    "ds_read_b64_tr_b16 a[254:255], v207 offset:63488\n\t"
    "v_mul_f32 v227, v11, v11\n\t"
    "s_waitcnt vmcnt(20)\n\t"
    "s_barrier\n\t"
    "s_waitcnt lgkmcnt(10)\n\t"
    "v_mfma_f32_16x16x32_bf16 v[80:83], a[232:235], a[24:27], v[80:83]\n\t"
    "ds_read_b64_tr_b16 a[128:129], v208 offset:0\n\t"
    "v_fma_f32 v220, v224, v214, v212\n\t"
    "v_mfma_f32_16x16x32_bf16 v[84:87], a[232:235], a[28:31], v[84:87]\n\t"
    "ds_read_b64_tr_b16 a[130:131], v208 offset:2048\n\t"
    "v_fma_f32 v221, v225, v214, v212\n\t"
    "s_waitcnt lgkmcnt(10)\n\t"
    "v_mfma_f32_16x16x32_bf16 v[88:91], a[236:239], a[24:27], v[88:91]\n\t"
    "ds_read_b64_tr_b16 a[132:133], v209 offset:0\n\t"
    "v_fma_f32 v222, v226, v214, v212\n\t"
    "v_mfma_f32_16x16x32_bf16 v[92:95], a[236:239], a[28:31], v[92:95]\n\t"
    "ds_read_b64_tr_b16 a[134:135], v209 offset:2048\n\t"
    "v_fma_f32 v223, v227, v214, v212\n\t"
    "s_waitcnt lgkmcnt(10)\n\t"
    "s_add_u32 m0, s70, 81920\n\t"
    "s_mov_b32 s69, 0\n\t"
    "v_mfma_f32_16x16x32_bf16 v[96:99], a[240:243], a[24:27], v[96:99]\n\t"
    "buffer_load_dwordx4 v199, s[52:55], s69 offen lds\n\t"
    "ds_read_b64_tr_b16 a[136:137], v210 offset:0\n\t"
    "v_mfma_f32_16x16x32_bf16 v[100:103], a[240:243], a[28:31], v[100:103]\n\t"
    "ds_read_b64_tr_b16 a[138:139], v210 offset:2048\n\t"
    "v_mul_f32 v220, v220, v8\n\t"
    "s_waitcnt lgkmcnt(10)\n\t"
    "v_mfma_f32_16x16x32_bf16 v[104:107], a[244:247], a[24:27], v[104:107]\n\t"
    "ds_read_b64_tr_b16 a[140:141], v211 offset:0\n\t"
    "v_mul_f32 v221, v221, v9\n\t"
    "v_mfma_f32_16x16x32_bf16 v[108:111], a[244:247], a[28:31], v[108:111]\n\t"
    "ds_read_b64_tr_b16 a[142:143], v211 offset:2048\n\t"
    "v_mul_f32 v222, v222, v10\n\t"
    "s_waitcnt lgkmcnt(10)\n\t"
    "v_mfma_f32_16x16x32_bf16 v[112:115], a[248:251], a[24:27], v[112:115]\n\t"
    "ds_read_b64_tr_b16 a[144:145], v208 offset:8192\n\t"
    "v_mul_f32 v223, v223, v11\n\t"
    "v_mfma_f32_16x16x32_bf16 v[116:119], a[248:251], a[28:31], v[116:119]\n\t"
    "ds_read_b64_tr_b16 a[146:147], v208 offset:10240\n\t"
    "v_exp_f32 v220, v220\n\t"
    "s_waitcnt lgkmcnt(10)\n\t"
    "s_add_u32 m0, s70, 82944\n\t"
    "s_mov_b32 s69, 8192\n\t"
    "v_mfma_f32_16x16x32_bf16 v[120:123], a[252:255], a[24:27], v[120:123]\n\t"
    "buffer_load_dwordx4 v199, s[52:55], s69 offen lds\n\t"
    "ds_read_b64_tr_b16 a[148:149], v209 offset:8192\n\t"
    "v_mfma_f32_16x16x32_bf16 v[124:127], a[252:255], a[28:31], v[124:127]\n\t"
    "ds_read_b64_tr_b16 a[150:151], v209 offset:10240\n\t"
    "v_exp_f32 v221, v221\n\t"
    "s_waitcnt lgkmcnt(10)\n\t"
    "v_mfma_f32_16x16x32_bf16 v[64:67], a[128:131], a[32:35], v[64:67]\n\t"
    "ds_read_b64_tr_b16 a[152:153], v210 offset:8192\n\t"
    "v_exp_f32 v222, v222\n\t"
    "v_mfma_f32_16x16x32_bf16 v[68:71], a[128:131], a[36:39], v[68:71]\n\t"
    "ds_read_b64_tr_b16 a[154:155], v210 offset:10240\n\t"
    "v_exp_f32 v223, v223\n\t"
    "s_waitcnt lgkmcnt(10)\n\t"
    "v_mfma_f32_16x16x32_bf16 v[72:75], a[132:135], a[32:35], v[72:75]\n\t"
    "ds_read_b64_tr_b16 a[156:157], v211 offset:8192\n\t"
    "v_mul_f32 v40, v40, v8\n\t"
    "v_mfma_f32_16x16x32_bf16 v[76:79], a[132:135], a[36:39], v[76:79]\n\t"
    "ds_read_b64_tr_b16 a[158:159], v211 offset:10240\n\t"
    "v_mul_f32 v41, v41, v9\n\t"
    "s_waitcnt lgkmcnt(10)\n\t"
    "s_add_u32 m0, s70, 83968\n\t"
    "s_mov_b32 s69, 16384\n\t"
    "v_mfma_f32_16x16x32_bf16 v[80:83], a[136:139], a[32:35], v[80:83]\n\t"
    "buffer_load_dwordx4 v199, s[52:55], s69 offen lds\n\t"
    "ds_read_b64_tr_b16 a[160:161], v208 offset:4096\n\t"
    "v_mfma_f32_16x16x32_bf16 v[84:87], a[136:139], a[36:39], v[84:87]\n\t"
    "ds_read_b64_tr_b16 a[162:163], v208 offset:6144\n\t"
    "v_mul_f32 v42, v42, v10\n\t"
    "s_waitcnt lgkmcnt(10)\n\t"
    "v_mfma_f32_16x16x32_bf16 v[88:91], a[140:143], a[32:35], v[88:91]\n\t"
    "ds_read_b64_tr_b16 a[164:165], v209 offset:4096\n\t"
    "v_mul_f32 v43, v43, v11\n\t"
    "v_mfma_f32_16x16x32_bf16 v[92:95], a[140:143], a[36:39], v[92:95]\n\t"
    "ds_read_b64_tr_b16 a[166:167], v209 offset:6144\n\t"
    "v_add_f32 v220, 1.0, v220\n\t"
    "s_waitcnt lgkmcnt(10)\n\t"
    "v_mfma_f32_16x16x32_bf16 v[96:99], a[144:147], a[32:35], v[96:99]\n\t"
    "ds_read_b64_tr_b16 a[168:169], v210 offset:4096\n\t"
    "v_add_f32 v221, 1.0, v221\n\t"
    "v_mfma_f32_16x16x32_bf16 v[100:103], a[144:147], a[36:39], v[100:103]\n\t"
    "ds_read_b64_tr_b16 a[170:171], v210 offset:6144\n\t"
    "v_add_f32 v222, 1.0, v222\n\t"
    "s_waitcnt lgkmcnt(10)\n\t"
    "s_add_u32 m0, s70, 84992\n\t"
    "s_mov_b32 s69, 24576\n\t"
    "v_mfma_f32_16x16x32_bf16 v[104:107], a[148:151], a[32:35], v[104:107]\n\t"
    "buffer_load_dwordx4 v199, s[52:55], s69 offen lds\n\t"
    "ds_read_b64_tr_b16 a[172:173], v211 offset:4096\n\t"
    "v_mfma_f32_16x16x32_bf16 v[108:111], a[148:151], a[36:39], v[108:111]\n\t"
    "ds_read_b64_tr_b16 a[174:175], v211 offset:6144\n\t"
    "v_add_f32 v223, 1.0, v223\n\t"
    "s_waitcnt lgkmcnt(10)\n\t"
    "v_mfma_f32_16x16x32_bf16 v[112:115], a[152:155], a[32:35], v[112:115]\n\t"
    "ds_read_b64_tr_b16 a[176:177], v208 offset:12288\n\t"
    "v_rcp_f32 v220, v220\n\t"
    "v_mfma_f32_16x16x32_bf16 v[116:119], a[152:155], a[36:39], v[116:119]\n\t"
    "ds_read_b64_tr_b16 a[178:179], v208 offset:14336\n\t"
    "v_rcp_f32 v221, v221\n\t"
    "s_waitcnt lgkmcnt(10)\n\t"
    "v_mfma_f32_16x16x32_bf16 v[120:123], a[156:159], a[32:35], v[120:123]\n\t"
    "ds_read_b64_tr_b16 a[180:181], v209 offset:12288\n\t"
    "v_rcp_f32 v222, v222\n\t"
    "v_mfma_f32_16x16x32_bf16 v[124:127], a[156:159], a[36:39], v[124:127]\n\t"
    "ds_read_b64_tr_b16 a[182:183], v209 offset:14336\n\t"
    "v_rcp_f32 v223, v223\n\t"
    "s_waitcnt lgkmcnt(10)\n\t"
    "s_add_u32 m0, s70, 98304\n\t"
    "s_mov_b32 s69, 32768\n\t"
    "v_mfma_f32_16x16x32_bf16 v[64:67], a[160:163], a[40:43], v[64:67]\n\t"
    "buffer_load_dwordx4 v199, s[52:55], s69 offen lds\n\t"
    "ds_read_b64_tr_b16 a[184:185], v210 offset:12288\n\t"
    "v_mfma_f32_16x16x32_bf16 v[68:71], a[160:163], a[44:47], v[68:71]\n\t"
    "ds_read_b64_tr_b16 a[186:187], v210 offset:14336\n\t"
    "v_mul_f32 v40, v40, v220\n\t"
    "s_waitcnt lgkmcnt(10)\n\t"
    "v_mfma_f32_16x16x32_bf16 v[72:75], a[164:167], a[40:43], v[72:75]\n\t"
    "ds_read_b64_tr_b16 a[188:189], v211 offset:12288\n\t"
    "v_mul_f32 v41, v41, v221\n\t"
    "v_mfma_f32_16x16x32_bf16 v[76:79], a[164:167], a[44:47], v[76:79]\n\t"
    "ds_read_b64_tr_b16 a[190:191], v211 offset:14336\n\t"
    "v_mul_f32 v42, v42, v222\n\t"
    "s_waitcnt vmcnt(21)\n\t"
    "s_barrier\n\t"
    "s_waitcnt lgkmcnt(10)\n\t"
    "v_mfma_f32_16x16x32_bf16 v[80:83], a[168:171], a[40:43], v[80:83]\n\t"
    "ds_read_b64_tr_b16 a[192:193], v200 offset:0\n\t"
    "v_mul_f32 v43, v43, v223\n\t"
    "v_mfma_f32_16x16x32_bf16 v[84:87], a[168:171], a[44:47], v[84:87]\n\t"
    "ds_read_b64_tr_b16 a[194:195], v200 offset:2048\n\t"
    "v_cvt_pk_bf16_f32 v238, v40, v41\n\t"
    "s_waitcnt lgkmcnt(10)\n\t"
    "s_add_u32 m0, s70, 99328\n\t"
    "s_mov_b32 s69, 40960\n\t"
    "v_mfma_f32_16x16x32_bf16 v[88:91], a[172:175], a[40:43], v[88:91]\n\t"
    "buffer_load_dwordx4 v199, s[52:55], s69 offen lds\n\t"
    "ds_read_b64_tr_b16 a[196:197], v201 offset:0\n\t"
    "v_mfma_f32_16x16x32_bf16 v[92:95], a[172:175], a[44:47], v[92:95]\n\t"
    "ds_read_b64_tr_b16 a[198:199], v201 offset:2048\n\t"
    "v_cvt_pk_bf16_f32 v239, v42, v43\n\t"
    "s_waitcnt lgkmcnt(10)\n\t"
    "v_mfma_f32_16x16x32_bf16 v[96:99], a[176:179], a[40:43], v[96:99]\n\t"
    "ds_read_b64_tr_b16 a[200:201], v202 offset:0\n\t"
    "s_mov_b32 s69, 0\n\t"
    "buffer_store_dwordx4 v[236:239], v197, s[48:51], s69 offen offset:0 sc1\n\t"
    "v_mfma_f32_16x16x32_bf16 v[100:103], a[176:179], a[44:47], v[100:103]\n\t"
    "ds_read_b64_tr_b16 a[202:203], v202 offset:2048\n\t"
    "v_mul_f32 v224, v4, v4\n\t"
    "s_waitcnt lgkmcnt(10)\n\t"
    "v_mfma_f32_16x16x32_bf16 v[104:107], a[180:183], a[40:43], v[104:107]\n\t"
    "ds_read_b64_tr_b16 a[204:205], v203 offset:0\n\t"
    "v_mul_f32 v225, v5, v5\n\t"
    "v_mfma_f32_16x16x32_bf16 v[108:111], a[180:183], a[44:47], v[108:111]\n\t"
    "ds_read_b64_tr_b16 a[206:207], v203 offset:2048\n\t"
    "v_mul_f32 v226, v6, v6\n\t"
    "s_waitcnt lgkmcnt(10)\n\t"
    "s_add_u32 m0, s70, 100352\n\t"
    "s_mov_b32 s69, 49152\n\t"
    "v_mfma_f32_16x16x32_bf16 v[112:115], a[184:187], a[40:43], v[112:115]\n\t"
    "buffer_load_dwordx4 v199, s[52:55], s69 offen lds\n\t"
    "ds_read_b64_tr_b16 a[208:209], v200 offset:8192\n\t"
    "v_mfma_f32_16x16x32_bf16 v[116:119], a[184:187], a[44:47], v[116:119]\n\t"
    "ds_read_b64_tr_b16 a[210:211], v200 offset:10240\n\t"
    "v_mul_f32 v227, v7, v7\n\t"
    "s_waitcnt lgkmcnt(10)\n\t"
    "v_mfma_f32_16x16x32_bf16 v[120:123], a[188:191], a[40:43], v[120:123]\n\t"
    "ds_read_b64_tr_b16 a[212:213], v201 offset:8192\n\t"
    "v_fma_f32 v220, v224, v214, v212\n\t"
    "v_mfma_f32_16x16x32_bf16 v[124:127], a[188:191], a[44:47], v[124:127]\n\t"
    "ds_read_b64_tr_b16 a[214:215], v201 offset:10240\n\t"
    "v_fma_f32 v221, v225, v214, v212\n\t"
    "s_waitcnt lgkmcnt(10)\n\t"
    "v_mfma_f32_16x16x32_bf16 v[64:67], a[192:195], a[48:51], v[64:67]\n\t"
    "ds_read_b64_tr_b16 a[216:217], v202 offset:8192\n\t"
    "v_fma_f32 v222, v226, v214, v212\n\t"
    "v_mfma_f32_16x16x32_bf16 v[68:71], a[192:195], a[52:55], v[68:71]\n\t"
    "ds_read_b64_tr_b16 a[218:219], v202 offset:10240\n\t"
    "v_fma_f32 v223, v227, v214, v212\n\t"
    "s_waitcnt lgkmcnt(10)\n\t"
    "s_add_u32 m0, s70, 101376\n\t"
    "s_mov_b32 s69, 57344\n\t"
    "v_mfma_f32_16x16x32_bf16 v[72:75], a[196:199], a[48:51], v[72:75]\n\t"
    "buffer_load_dwordx4 v199, s[52:55], s69 offen lds\n\t"
    "ds_read_b64_tr_b16 a[220:221], v203 offset:8192\n\t"
    "v_mfma_f32_16x16x32_bf16 v[76:79], a[196:199], a[52:55], v[76:79]\n\t"
    "ds_read_b64_tr_b16 a[222:223], v203 offset:10240\n\t"
    "v_mul_f32 v220, v220, v4\n\t"
    "s_waitcnt lgkmcnt(10)\n\t"
    "v_mfma_f32_16x16x32_bf16 v[80:83], a[200:203], a[48:51], v[80:83]\n\t"
    "ds_read_b64_tr_b16 a[224:225], v200 offset:4096\n\t"
    "v_mul_f32 v221, v221, v5\n\t"
    "v_mfma_f32_16x16x32_bf16 v[84:87], a[200:203], a[52:55], v[84:87]\n\t"
    "ds_read_b64_tr_b16 a[226:227], v200 offset:6144\n\t"
    "v_mul_f32 v222, v222, v6\n\t"
    "s_waitcnt lgkmcnt(10)\n\t"
    "v_mfma_f32_16x16x32_bf16 v[88:91], a[204:207], a[48:51], v[88:91]\n\t"
    "ds_read_b64_tr_b16 a[228:229], v201 offset:4096\n\t"
    "v_mul_f32 v223, v223, v7\n\t"
    "v_mfma_f32_16x16x32_bf16 v[92:95], a[204:207], a[52:55], v[92:95]\n\t"
    "ds_read_b64_tr_b16 a[230:231], v201 offset:6144\n\t"
    "v_exp_f32 v220, v220\n\t"
    "s_waitcnt lgkmcnt(10)\n\t"
    "s_add_u32 m0, s70, 114688\n\t"
    "s_mov_b32 s69, 65536\n\t"
    "v_mfma_f32_16x16x32_bf16 v[96:99], a[208:211], a[48:51], v[96:99]\n\t"
    "buffer_load_dwordx4 v199, s[52:55], s69 offen lds\n\t"
    "ds_read_b64_tr_b16 a[232:233], v202 offset:4096\n\t"
    "v_mfma_f32_16x16x32_bf16 v[100:103], a[208:211], a[52:55], v[100:103]\n\t"
    "ds_read_b64_tr_b16 a[234:235], v202 offset:6144\n\t"
    "v_exp_f32 v221, v221\n\t"
    "s_waitcnt lgkmcnt(10)\n\t"
    "v_mfma_f32_16x16x32_bf16 v[104:107], a[212:215], a[48:51], v[104:107]\n\t"
    "ds_read_b64_tr_b16 a[236:237], v203 offset:4096\n\t"
    "v_exp_f32 v222, v222\n\t"
    "v_mfma_f32_16x16x32_bf16 v[108:111], a[212:215], a[52:55], v[108:111]\n\t"
    "ds_read_b64_tr_b16 a[238:239], v203 offset:6144\n\t"
    "v_exp_f32 v223, v223\n\t"
    "s_waitcnt lgkmcnt(10)\n\t"
    "v_mfma_f32_16x16x32_bf16 v[112:115], a[216:219], a[48:51], v[112:115]\n\t"
    "ds_read_b64_tr_b16 a[240:241], v200 offset:12288\n\t"
    "v_mul_f32 v36, v36, v4\n\t"
    "v_mfma_f32_16x16x32_bf16 v[116:119], a[216:219], a[52:55], v[116:119]\n\t"
    "ds_read_b64_tr_b16 a[242:243], v200 offset:14336\n\t"
    "v_mul_f32 v37, v37, v5\n\t"
    "s_waitcnt lgkmcnt(10)\n\t"
    "s_add_u32 m0, s70, 115712\n\t"
    "s_mov_b32 s69, 73728\n\t"
    "v_mfma_f32_16x16x32_bf16 v[120:123], a[220:223], a[48:51], v[120:123]\n\t"
    "buffer_load_dwordx4 v199, s[52:55], s69 offen lds\n\t"
    "ds_read_b64_tr_b16 a[244:245], v201 offset:12288\n\t"
    "v_mfma_f32_16x16x32_bf16 v[124:127], a[220:223], a[52:55], v[124:127]\n\t"
    "ds_read_b64_tr_b16 a[246:247], v201 offset:14336\n\t"
    "v_mul_f32 v38, v38, v6\n\t"
    "s_waitcnt lgkmcnt(10)\n\t"
    "v_mfma_f32_16x16x32_bf16 v[64:67], a[224:227], a[56:59], v[64:67]\n\t"
    "ds_read_b64_tr_b16 a[248:249], v202 offset:12288\n\t"
    "v_mul_f32 v39, v39, v7\n\t"
    "v_mfma_f32_16x16x32_bf16 v[68:71], a[224:227], a[60:63], v[68:71]\n\t"
    "ds_read_b64_tr_b16 a[250:251], v202 offset:14336\n\t"
    "v_add_f32 v220, 1.0, v220\n\t"
    "s_waitcnt lgkmcnt(10)\n\t"
    "v_mfma_f32_16x16x32_bf16 v[72:75], a[228:231], a[56:59], v[72:75]\n\t"
    "ds_read_b64_tr_b16 a[252:253], v203 offset:12288\n\t"
    "v_add_f32 v221, 1.0, v221\n\t"
    "v_mfma_f32_16x16x32_bf16 v[76:79], a[228:231], a[60:63], v[76:79]\n\t"
    "ds_read_b64_tr_b16 a[254:255], v203 offset:14336\n\t"
    "v_add_f32 v222, 1.0, v222\n\t"
    "s_waitcnt vmcnt(23)\n\t"
    "s_barrier\n\t"
    "s_waitcnt lgkmcnt(10)\n\t"
    "s_add_u32 m0, s70, 116736\n\t"
    "s_mov_b32 s69, 81920\n\t"
    "v_mfma_f32_16x16x32_bf16 v[80:83], a[232:235], a[56:59], v[80:83]\n\t"
    "buffer_load_dwordx4 v199, s[52:55], s69 offen lds\n\t"
    "ds_read_b64_tr_b16 a[128:129], v200 offset:16384\n\t"
    "v_mfma_f32_16x16x32_bf16 v[84:87], a[232:235], a[60:63], v[84:87]\n\t"
    "ds_read_b64_tr_b16 a[130:131], v200 offset:18432\n\t"
    "v_add_f32 v223, 1.0, v223\n\t"
    "s_waitcnt lgkmcnt(10)\n\t"
    "v_mfma_f32_16x16x32_bf16 v[88:91], a[236:239], a[56:59], v[88:91]\n\t"
    "ds_read_b64_tr_b16 a[132:133], v201 offset:16384\n\t"
    "v_rcp_f32 v220, v220\n\t"
    "v_mfma_f32_16x16x32_bf16 v[92:95], a[236:239], a[60:63], v[92:95]\n\t"
    "ds_read_b64_tr_b16 a[134:135], v201 offset:18432\n\t"
    "v_rcp_f32 v221, v221\n\t"
    "s_waitcnt lgkmcnt(10)\n\t"
    "v_mfma_f32_16x16x32_bf16 v[96:99], a[240:243], a[56:59], v[96:99]\n\t"
    "ds_read_b64_tr_b16 a[136:137], v202 offset:16384\n\t"
    "v_rcp_f32 v222, v222\n\t"
    "v_mfma_f32_16x16x32_bf16 v[100:103], a[240:243], a[60:63], v[100:103]\n\t"
    "ds_read_b64_tr_b16 a[138:139], v202 offset:18432\n\t"
    "v_rcp_f32 v223, v223\n\t"
    "s_waitcnt lgkmcnt(10)\n\t"
    "s_add_u32 m0, s70, 117760\n\t"
    "s_mov_b32 s69, 90112\n\t"
    "v_mfma_f32_16x16x32_bf16 v[104:107], a[244:247], a[56:59], v[104:107]\n\t"
    "buffer_load_dwordx4 v199, s[52:55], s69 offen lds\n\t"
    "ds_read_b64_tr_b16 a[140:141], v203 offset:16384\n\t"
    "v_mfma_f32_16x16x32_bf16 v[108:111], a[244:247], a[60:63], v[108:111]\n\t"
    "ds_read_b64_tr_b16 a[142:143], v203 offset:18432\n\t"
    "v_mul_f32 v36, v36, v220\n\t"
    "s_waitcnt lgkmcnt(10)\n\t"
    "v_mfma_f32_16x16x32_bf16 v[112:115], a[248:251], a[56:59], v[112:115]\n\t"
    "ds_read_b64_tr_b16 a[144:145], v200 offset:24576\n\t"
    "v_mul_f32 v37, v37, v221\n\t"
    "v_mfma_f32_16x16x32_bf16 v[116:119], a[248:251], a[60:63], v[116:119]\n\t"
    "ds_read_b64_tr_b16 a[146:147], v200 offset:26624\n\t"
    "v_mul_f32 v38, v38, v222\n\t"
    "s_waitcnt lgkmcnt(10)\n\t"
    "v_mfma_f32_16x16x32_bf16 v[120:123], a[252:255], a[56:59], v[120:123]\n\t"
    "ds_read_b64_tr_b16 a[148:149], v201 offset:24576\n\t"
    "v_mul_f32 v39, v39, v223\n\t"
    "v_mfma_f32_16x16x32_bf16 v[124:127], a[252:255], a[60:63], v[124:127]\n\t"
    "ds_read_b64_tr_b16 a[150:151], v201 offset:26624\n\t"
    "v_cvt_pk_bf16_f32 v240, v36, v37\n\t"
    "s_waitcnt lgkmcnt(10)\n\t"
    "s_add_u32 m0, s70, 131072\n\t"
    "s_mov_b32 s69, 98304\n\t"
    "v_mfma_f32_16x16x32_bf16 v[64:67], a[128:131], a[64:67], v[64:67]\n\t"
    "buffer_load_dwordx4 v199, s[52:55], s69 offen lds\n\t"
    "ds_read_b64_tr_b16 a[152:153], v202 offset:24576\n\t"
    "v_mfma_f32_16x16x32_bf16 v[68:71], a[128:131], a[68:71], v[68:71]\n\t"
    "ds_read_b64_tr_b16 a[154:155], v202 offset:26624\n\t"
    "v_cvt_pk_bf16_f32 v241, v38, v39\n\t"
    "s_waitcnt lgkmcnt(10)\n\t"
    "v_mfma_f32_16x16x32_bf16 v[72:75], a[132:135], a[64:67], v[72:75]\n\t"
    "ds_read_b64_tr_b16 a[156:157], v203 offset:24576\n\t"
    "v_mul_f32 v224, v12, v12\n\t"
    "v_mfma_f32_16x16x32_bf16 v[76:79], a[132:135], a[68:71], v[76:79]\n\t"
    "ds_read_b64_tr_b16 a[158:159], v203 offset:26624\n\t"
    "v_mul_f32 v225, v13, v13\n\t"
    "s_waitcnt lgkmcnt(10)\n\t"
    "v_mfma_f32_16x16x32_bf16 v[80:83], a[136:139], a[64:67], v[80:83]\n\t"
    "ds_read_b64_tr_b16 a[160:161], v200 offset:20480\n\t"
    "v_mul_f32 v226, v14, v14\n\t"
    "v_mfma_f32_16x16x32_bf16 v[84:87], a[136:139], a[68:71], v[84:87]\n\t"
    "ds_read_b64_tr_b16 a[162:163], v200 offset:22528\n\t"
    "v_mul_f32 v227, v15, v15\n\t"
    "s_waitcnt lgkmcnt(10)\n\t"
    "s_add_u32 m0, s70, 132096\n\t"
    "s_mov_b32 s69, 106496\n\t"
    "v_mfma_f32_16x16x32_bf16 v[88:91], a[140:143], a[64:67], v[88:91]\n\t"
    "buffer_load_dwordx4 v199, s[52:55], s69 offen lds\n\t"
    "ds_read_b64_tr_b16 a[164:165], v201 offset:20480\n\t"
    "v_mfma_f32_16x16x32_bf16 v[92:95], a[140:143], a[68:71], v[92:95]\n\t"
    "ds_read_b64_tr_b16 a[166:167], v201 offset:22528\n\t"
    "v_fma_f32 v220, v224, v214, v212\n\t"
    "s_waitcnt lgkmcnt(10)\n\t"
    "v_mfma_f32_16x16x32_bf16 v[96:99], a[144:147], a[64:67], v[96:99]\n\t"
    "ds_read_b64_tr_b16 a[168:169], v202 offset:20480\n\t"
    "v_fma_f32 v221, v225, v214, v212\n\t"
    "v_mfma_f32_16x16x32_bf16 v[100:103], a[144:147], a[68:71], v[100:103]\n\t"
    "ds_read_b64_tr_b16 a[170:171], v202 offset:22528\n\t"
    "v_fma_f32 v222, v226, v214, v212\n\t"
    "s_waitcnt lgkmcnt(10)\n\t"
    "v_mfma_f32_16x16x32_bf16 v[104:107], a[148:151], a[64:67], v[104:107]\n\t"
    "ds_read_b64_tr_b16 a[172:173], v203 offset:20480\n\t"
    "v_fma_f32 v223, v227, v214, v212\n\t"
    "v_mfma_f32_16x16x32_bf16 v[108:111], a[148:151], a[68:71], v[108:111]\n\t"
    "ds_read_b64_tr_b16 a[174:175], v203 offset:22528\n\t"
    "v_mul_f32 v220, v220, v12\n\t"
    "s_waitcnt lgkmcnt(10)\n\t"
    "s_add_u32 m0, s70, 133120\n\t"
    "s_mov_b32 s69, 114688\n\t"
    "v_mfma_f32_16x16x32_bf16 v[112:115], a[152:155], a[64:67], v[112:115]\n\t"
    "buffer_load_dwordx4 v199, s[52:55], s69 offen lds\n\t"
    "ds_read_b64_tr_b16 a[176:177], v200 offset:28672\n\t"
    "v_mfma_f32_16x16x32_bf16 v[116:119], a[152:155], a[68:71], v[116:119]\n\t"
    "ds_read_b64_tr_b16 a[178:179], v200 offset:30720\n\t"
    "v_mul_f32 v221, v221, v13\n\t"
    "s_waitcnt lgkmcnt(10)\n\t"
    "v_mfma_f32_16x16x32_bf16 v[120:123], a[156:159], a[64:67], v[120:123]\n\t"
    "ds_read_b64_tr_b16 a[180:181], v201 offset:28672\n\t"
    "v_mul_f32 v222, v222, v14\n\t"
    "v_mfma_f32_16x16x32_bf16 v[124:127], a[156:159], a[68:71], v[124:127]\n\t"
    "ds_read_b64_tr_b16 a[182:183], v201 offset:30720\n\t"
    "v_mul_f32 v223, v223, v15\n\t"
    "s_waitcnt lgkmcnt(10)\n\t"
    "v_mfma_f32_16x16x32_bf16 v[64:67], a[160:163], a[72:75], v[64:67]\n\t"
    "ds_read_b64_tr_b16 a[184:185], v202 offset:28672\n\t"
    "v_exp_f32 v220, v220\n\t"
    "v_mfma_f32_16x16x32_bf16 v[68:71], a[160:163], a[76:79], v[68:71]\n\t"
    "ds_read_b64_tr_b16 a[186:187], v202 offset:30720\n\t"
    "v_exp_f32 v221, v221\n\t"
    "s_waitcnt lgkmcnt(10)\n\t"
    "s_add_u32 m0, s70, 134144\n\t"
    "s_mov_b32 s69, 122880\n\t"
    "v_mfma_f32_16x16x32_bf16 v[72:75], a[164:167], a[72:75], v[72:75]\n\t"
    "buffer_load_dwordx4 v199, s[52:55], s69 offen lds\n\t"
    "ds_read_b64_tr_b16 a[188:189], v203 offset:28672\n\t"
    "v_mfma_f32_16x16x32_bf16 v[76:79], a[164:167], a[76:79], v[76:79]\n\t"
    "ds_read_b64_tr_b16 a[190:191], v203 offset:30720\n\t"
    "v_exp_f32 v222, v222\n\t"
    "s_waitcnt vmcnt(25)\n\t"
    "s_barrier\n\t"
    "s_waitcnt lgkmcnt(10)\n\t"
    "v_mfma_f32_16x16x32_bf16 v[80:83], a[168:171], a[72:75], v[80:83]\n\t"
    "ds_read_b64_tr_b16 a[192:193], v200 offset:32768\n\t"
    "v_exp_f32 v223, v223\n\t"
    "v_mfma_f32_16x16x32_bf16 v[84:87], a[168:171], a[76:79], v[84:87]\n\t"
    "ds_read_b64_tr_b16 a[194:195], v200 offset:34816\n\t"
    "v_mul_f32 v44, v44, v12\n\t"
    "s_waitcnt lgkmcnt(10)\n\t"
    "v_mfma_f32_16x16x32_bf16 v[88:91], a[172:175], a[72:75], v[88:91]\n\t"
    "ds_read_b64_tr_b16 a[196:197], v201 offset:32768\n\t"
    "v_mul_f32 v45, v45, v13\n\t"
    "v_mfma_f32_16x16x32_bf16 v[92:95], a[172:175], a[76:79], v[92:95]\n\t"
    "ds_read_b64_tr_b16 a[198:199], v201 offset:34816\n\t"
    "v_mul_f32 v46, v46, v14\n\t"
    "s_waitcnt lgkmcnt(10)\n\t"
    "s_add_u32 m0, s70, 0\n\t"
    "s_mov_b32 s69, 131072\n\t"
    "v_mfma_f32_16x16x32_bf16 v[96:99], a[176:179], a[72:75], v[96:99]\n\t"
    "buffer_load_dwordx4 v199, s[52:55], s69 offen lds\n\t"
    "ds_read_b64_tr_b16 a[200:201], v202 offset:32768\n\t"
    "v_mfma_f32_16x16x32_bf16 v[100:103], a[176:179], a[76:79], v[100:103]\n\t"
    "ds_read_b64_tr_b16 a[202:203], v202 offset:34816\n\t"
    "v_mul_f32 v47, v47, v15\n\t"
    "s_waitcnt lgkmcnt(10)\n\t"
    "v_mfma_f32_16x16x32_bf16 v[104:107], a[180:183], a[72:75], v[104:107]\n\t"
    "ds_read_b64_tr_b16 a[204:205], v203 offset:32768\n\t"
    "v_add_f32 v220, 1.0, v220\n\t"
    "v_mfma_f32_16x16x32_bf16 v[108:111], a[180:183], a[76:79], v[108:111]\n\t"
    "ds_read_b64_tr_b16 a[206:207], v203 offset:34816\n\t"
    "v_add_f32 v221, 1.0, v221\n\t"
    "s_waitcnt lgkmcnt(10)\n\t"
    "v_mfma_f32_16x16x32_bf16 v[112:115], a[184:187], a[72:75], v[112:115]\n\t"
    "ds_read_b64_tr_b16 a[208:209], v200 offset:40960\n\t"
    "v_add_f32 v222, 1.0, v222\n\t"
    "v_mfma_f32_16x16x32_bf16 v[116:119], a[184:187], a[76:79], v[116:119]\n\t"
    "ds_read_b64_tr_b16 a[210:211], v200 offset:43008\n\t"
    "v_add_f32 v223, 1.0, v223\n\t"
    "s_waitcnt lgkmcnt(10)\n\t"
    "s_add_u32 m0, s70, 1024\n\t"
    "s_mov_b32 s69, 139264\n\t"
    "v_mfma_f32_16x16x32_bf16 v[120:123], a[188:191], a[72:75], v[120:123]\n\t"
    "buffer_load_dwordx4 v199, s[52:55], s69 offen lds\n\t"
    "ds_read_b64_tr_b16 a[212:213], v201 offset:40960\n\t"
    "v_mfma_f32_16x16x32_bf16 v[124:127], a[188:191], a[76:79], v[124:127]\n\t"
    "ds_read_b64_tr_b16 a[214:215], v201 offset:43008\n\t"
    "v_rcp_f32 v220, v220\n\t"
    "s_waitcnt lgkmcnt(10)\n\t"
    "v_mfma_f32_16x16x32_bf16 v[64:67], a[192:195], a[80:83], v[64:67]\n\t"
    "ds_read_b64_tr_b16 a[216:217], v202 offset:40960\n\t"
    "v_rcp_f32 v221, v221\n\t"
    "v_mfma_f32_16x16x32_bf16 v[68:71], a[192:195], a[84:87], v[68:71]\n\t"
    "ds_read_b64_tr_b16 a[218:219], v202 offset:43008\n\t"
    "v_rcp_f32 v222, v222\n\t"
    "s_waitcnt lgkmcnt(10)\n\t"
    "v_mfma_f32_16x16x32_bf16 v[72:75], a[196:199], a[80:83], v[72:75]\n\t"
    "ds_read_b64_tr_b16 a[220:221], v203 offset:40960\n\t"
    "v_rcp_f32 v223, v223\n\t"
    "v_mfma_f32_16x16x32_bf16 v[76:79], a[196:199], a[84:87], v[76:79]\n\t"
    "ds_read_b64_tr_b16 a[222:223], v203 offset:43008\n\t"
    "v_mul_f32 v44, v44, v220\n\t"
    "s_waitcnt lgkmcnt(10)\n\t"
    "s_add_u32 m0, s70, 2048\n\t"
    "s_mov_b32 s69, 147456\n\t"
    "v_mfma_f32_16x16x32_bf16 v[80:83], a[200:203], a[80:83], v[80:83]\n\t"
    "buffer_load_dwordx4 v199, s[52:55], s69 offen lds\n\t"
    "ds_read_b64_tr_b16 a[224:225], v200 offset:36864\n\t"
    "v_mfma_f32_16x16x32_bf16 v[84:87], a[200:203], a[84:87], v[84:87]\n\t"
    "ds_read_b64_tr_b16 a[226:227], v200 offset:38912\n\t"
    "v_mul_f32 v45, v45, v221\n\t"
    "s_waitcnt lgkmcnt(10)\n\t"
    "v_mfma_f32_16x16x32_bf16 v[88:91], a[204:207], a[80:83], v[88:91]\n\t"
    "ds_read_b64_tr_b16 a[228:229], v201 offset:36864\n\t"
    "v_mul_f32 v46, v46, v222\n\t"
    "v_mfma_f32_16x16x32_bf16 v[92:95], a[204:207], a[84:87], v[92:95]\n\t"
    "ds_read_b64_tr_b16 a[230:231], v201 offset:38912\n\t"
    "v_mul_f32 v47, v47, v223\n\t"
    "s_waitcnt lgkmcnt(10)\n\t"
    "v_mfma_f32_16x16x32_bf16 v[96:99], a[208:211], a[80:83], v[96:99]\n\t"
    "ds_read_b64_tr_b16 a[232:233], v202 offset:36864\n\t"
    "v_cvt_pk_bf16_f32 v242, v44, v45\n\t"
    "v_mfma_f32_16x16x32_bf16 v[100:103], a[208:211], a[84:87], v[100:103]\n\t"
    "ds_read_b64_tr_b16 a[234:235], v202 offset:38912\n\t"
    "v_cvt_pk_bf16_f32 v243, v46, v47\n\t"
    "s_waitcnt lgkmcnt(10)\n\t"
    "s_add_u32 m0, s70, 3072\n\t"
    "s_mov_b32 s69, 155648\n\t"
    "v_mfma_f32_16x16x32_bf16 v[104:107], a[212:215], a[80:83], v[104:107]\n\t"
    "buffer_load_dwordx4 v199, s[52:55], s69 offen lds\n\t"
    "ds_read_b64_tr_b16 a[236:237], v203 offset:36864\n\t"
    "v_mfma_f32_16x16x32_bf16 v[108:111], a[212:215], a[84:87], v[108:111]\n\t"
    "ds_read_b64_tr_b16 a[238:239], v203 offset:38912\n\t"
    "s_mov_b32 s69, 57344\n\t"
    "buffer_store_dwordx4 v[240:243], v197, s[48:51], s69 offen offset:0 sc1\n\t"
    "s_waitcnt lgkmcnt(10)\n\t"
    "v_mfma_f32_16x16x32_bf16 v[112:115], a[216:219], a[80:83], v[112:115]\n\t"
    "ds_read_b64_tr_b16 a[240:241], v200 offset:45056\n\t"
    "v_mul_f32 v224, v16, v16\n\t"
    "v_mfma_f32_16x16x32_bf16 v[116:119], a[216:219], a[84:87], v[116:119]\n\t"
    "ds_read_b64_tr_b16 a[242:243], v200 offset:47104\n\t"
    "v_mul_f32 v225, v17, v17\n\t"
    "s_waitcnt lgkmcnt(10)\n\t"
    "v_mfma_f32_16x16x32_bf16 v[120:123], a[220:223], a[80:83], v[120:123]\n\t"
    "ds_read_b64_tr_b16 a[244:245], v201 offset:45056\n\t"
    "v_mul_f32 v226, v18, v18\n\t"
    "v_mfma_f32_16x16x32_bf16 v[124:127], a[220:223], a[84:87], v[124:127]\n\t"
    "ds_read_b64_tr_b16 a[246:247], v201 offset:47104\n\t"
    "v_mul_f32 v227, v19, v19\n\t"
    "s_waitcnt lgkmcnt(10)\n\t"
    "v_mfma_f32_16x16x32_bf16 v[64:67], a[224:227], a[88:91], v[64:67]\n\t"
    "ds_read_b64_tr_b16 a[248:249], v202 offset:45056\n\t"
    "v_fma_f32 v220, v224, v214, v212\n\t"
    "v_mfma_f32_16x16x32_bf16 v[68:71], a[224:227], a[92:95], v[68:71]\n\t"
    "ds_read_b64_tr_b16 a[250:251], v202 offset:47104\n\t"
    "v_fma_f32 v221, v225, v214, v212\n\t"
    "s_waitcnt lgkmcnt(10)\n\t"
    "v_mfma_f32_16x16x32_bf16 v[72:75], a[228:231], a[88:91], v[72:75]\n\t"
    "ds_read_b64_tr_b16 a[252:253], v203 offset:45056\n\t"
    "v_fma_f32 v222, v226, v214, v212\n\t"
    "v_mfma_f32_16x16x32_bf16 v[76:79], a[228:231], a[92:95], v[76:79]\n\t"
    "ds_read_b64_tr_b16 a[254:255], v203 offset:47104\n\t"
    "v_fma_f32 v223, v227, v214, v212\n\t"
    "s_waitcnt vmcnt(26)\n\t"
    "s_barrier\n\t"
    "s_waitcnt lgkmcnt(10)\n\t"
    "v_mfma_f32_16x16x32_bf16 v[80:83], a[232:235], a[88:91], v[80:83]\n\t"
    "ds_read_b64_tr_b16 a[128:129], v200 offset:49152\n\t"
    "v_mul_f32 v220, v220, v16\n\t"
    "v_mfma_f32_16x16x32_bf16 v[84:87], a[232:235], a[92:95], v[84:87]\n\t"
    "ds_read_b64_tr_b16 a[130:131], v200 offset:51200\n\t"
    "v_mul_f32 v221, v221, v17\n\t"
    "s_waitcnt lgkmcnt(10)\n\t"
    "s_add_u32 m0, s70, 16384\n\t"
    "s_mov_b32 s69, 163840\n\t"
    "v_mfma_f32_16x16x32_bf16 v[88:91], a[236:239], a[88:91], v[88:91]\n\t"
    "buffer_load_dwordx4 v199, s[52:55], s69 offen lds\n\t"
    "ds_read_b64_tr_b16 a[132:133], v201 offset:49152\n\t"
    "v_mfma_f32_16x16x32_bf16 v[92:95], a[236:239], a[92:95], v[92:95]\n\t"
    "ds_read_b64_tr_b16 a[134:135], v201 offset:51200\n\t"
    "v_mul_f32 v222, v222, v18\n\t"
    "s_waitcnt lgkmcnt(10)\n\t"
    "v_mfma_f32_16x16x32_bf16 v[96:99], a[240:243], a[88:91], v[96:99]\n\t"
    "ds_read_b64_tr_b16 a[136:137], v202 offset:49152\n\t"
    "v_mul_f32 v223, v223, v19\n\t"
    "v_mfma_f32_16x16x32_bf16 v[100:103], a[240:243], a[92:95], v[100:103]\n\t"
    "ds_read_b64_tr_b16 a[138:139], v202 offset:51200\n\t"
    "v_exp_f32 v220, v220\n\t"
    "s_waitcnt lgkmcnt(10)\n\t"
    "v_mfma_f32_16x16x32_bf16 v[104:107], a[244:247], a[88:91], v[104:107]\n\t"
    "ds_read_b64_tr_b16 a[140:141], v203 offset:49152\n\t"
    "v_exp_f32 v221, v221\n\t"
    "v_mfma_f32_16x16x32_bf16 v[108:111], a[244:247], a[92:95], v[108:111]\n\t"
    "ds_read_b64_tr_b16 a[142:143], v203 offset:51200\n\t"
    "v_exp_f32 v222, v222\n\t"
    "s_waitcnt lgkmcnt(10)\n\t"
    "s_add_u32 m0, s70, 17408\n\t"
    "s_mov_b32 s69, 172032\n\t"
    "v_mfma_f32_16x16x32_bf16 v[112:115], a[248:251], a[88:91], v[112:115]\n\t"
    "buffer_load_dwordx4 v199, s[52:55], s69 offen lds\n\t"
    "ds_read_b64_tr_b16 a[144:145], v200 offset:57344\n\t"
    "v_mfma_f32_16x16x32_bf16 v[116:119], a[248:251], a[92:95], v[116:119]\n\t"
    "ds_read_b64_tr_b16 a[146:147], v200 offset:59392\n\t"
    "v_exp_f32 v223, v223\n\t"
    "s_waitcnt lgkmcnt(10)\n\t"
    "v_mfma_f32_16x16x32_bf16 v[120:123], a[252:255], a[88:91], v[120:123]\n\t"
    "ds_read_b64_tr_b16 a[148:149], v201 offset:57344\n\t"
    "v_mul_f32 v48, v48, v16\n\t"
    "v_mfma_f32_16x16x32_bf16 v[124:127], a[252:255], a[92:95], v[124:127]\n\t"
    "ds_read_b64_tr_b16 a[150:151], v201 offset:59392\n\t"
    "v_mul_f32 v49, v49, v17\n\t"
    "s_waitcnt lgkmcnt(10)\n\t"
    "v_mfma_f32_16x16x32_bf16 v[64:67], a[128:131], a[96:99], v[64:67]\n\t"
    "ds_read_b64_tr_b16 a[152:153], v202 offset:57344\n\t"
    "v_mul_f32 v50, v50, v18\n\t"
    "v_mfma_f32_16x16x32_bf16 v[68:71], a[128:131], a[100:103], v[68:71]\n\t"
    "ds_read_b64_tr_b16 a[154:155], v202 offset:59392\n\t"
    "v_mul_f32 v51, v51, v19\n\t"
    "s_waitcnt lgkmcnt(10)\n\t"
    "s_add_u32 m0, s70, 18432\n\t"
    "s_mov_b32 s69, 180224\n\t"
    "v_mfma_f32_16x16x32_bf16 v[72:75], a[132:135], a[96:99], v[72:75]\n\t"
    "buffer_load_dwordx4 v199, s[52:55], s69 offen lds\n\t"
    "ds_read_b64_tr_b16 a[156:157], v203 offset:57344\n\t"
    "v_mfma_f32_16x16x32_bf16 v[76:79], a[132:135], a[100:103], v[76:79]\n\t"
    "ds_read_b64_tr_b16 a[158:159], v203 offset:59392\n\t"
    "v_add_f32 v220, 1.0, v220\n\t"
    "s_waitcnt lgkmcnt(10)\n\t"
    "v_mfma_f32_16x16x32_bf16 v[80:83], a[136:139], a[96:99], v[80:83]\n\t"
    "ds_read_b64_tr_b16 a[160:161], v200 offset:53248\n\t"
    "v_add_f32 v221, 1.0, v221\n\t"
    "v_mfma_f32_16x16x32_bf16 v[84:87], a[136:139], a[100:103], v[84:87]\n\t"
    "ds_read_b64_tr_b16 a[162:163], v200 offset:55296\n\t"
    "v_add_f32 v222, 1.0, v222\n\t"
    "s_waitcnt lgkmcnt(10)\n\t"
    "v_mfma_f32_16x16x32_bf16 v[88:91], a[140:143], a[96:99], v[88:91]\n\t"
    "ds_read_b64_tr_b16 a[164:165], v201 offset:53248\n\t"
    "v_add_f32 v223, 1.0, v223\n\t"
    "v_mfma_f32_16x16x32_bf16 v[92:95], a[140:143], a[100:103], v[92:95]\n\t"
    "ds_read_b64_tr_b16 a[166:167], v201 offset:55296\n\t"
    "v_rcp_f32 v220, v220\n\t"
    "s_waitcnt lgkmcnt(10)\n\t"
    "s_add_u32 m0, s70, 19456\n\t"
    "s_mov_b32 s69, 188416\n\t"
    "v_mfma_f32_16x16x32_bf16 v[96:99], a[144:147], a[96:99], v[96:99]\n\t"
    "buffer_load_dwordx4 v199, s[52:55], s69 offen lds\n\t"
    "ds_read_b64_tr_b16 a[168:169], v202 offset:53248\n\t"
    "v_mfma_f32_16x16x32_bf16 v[100:103], a[144:147], a[100:103], v[100:103]\n\t"
    "ds_read_b64_tr_b16 a[170:171], v202 offset:55296\n\t"
    "v_rcp_f32 v221, v221\n\t"
    "s_waitcnt lgkmcnt(10)\n\t"
    "v_mfma_f32_16x16x32_bf16 v[104:107], a[148:151], a[96:99], v[104:107]\n\t"
    "ds_read_b64_tr_b16 a[172:173], v203 offset:53248\n\t"
    "v_rcp_f32 v222, v222\n\t"
    "v_mfma_f32_16x16x32_bf16 v[108:111], a[148:151], a[100:103], v[108:111]\n\t"
    "ds_read_b64_tr_b16 a[174:175], v203 offset:55296\n\t"
    "v_rcp_f32 v223, v223\n\t"
    "s_waitcnt lgkmcnt(10)\n\t"
    "v_mfma_f32_16x16x32_bf16 v[112:115], a[152:155], a[96:99], v[112:115]\n\t"
    "ds_read_b64_tr_b16 a[176:177], v200 offset:61440\n\t"
    "v_mul_f32 v48, v48, v220\n\t"
    "v_mfma_f32_16x16x32_bf16 v[116:119], a[152:155], a[100:103], v[116:119]\n\t"
    "ds_read_b64_tr_b16 a[178:179], v200 offset:63488\n\t"
    "v_mul_f32 v49, v49, v221\n\t"
    "s_waitcnt lgkmcnt(10)\n\t"
    "v_mfma_f32_16x16x32_bf16 v[120:123], a[156:159], a[96:99], v[120:123]\n\t"
    "ds_read_b64_tr_b16 a[180:181], v201 offset:61440\n\t"
    "v_mul_f32 v50, v50, v222\n\t"
    "v_mfma_f32_16x16x32_bf16 v[124:127], a[156:159], a[100:103], v[124:127]\n\t"
    "ds_read_b64_tr_b16 a[182:183], v201 offset:63488\n\t"
    "v_mul_f32 v51, v51, v223\n\t"
    "s_waitcnt lgkmcnt(10)\n\t"
    "v_mfma_f32_16x16x32_bf16 v[64:67], a[160:163], a[104:107], v[64:67]\n\t"
    "ds_read_b64_tr_b16 a[184:185], v202 offset:61440\n\t"
    "v_cvt_pk_bf16_f32 v236, v48, v49\n\t"
    "v_mfma_f32_16x16x32_bf16 v[68:71], a[160:163], a[108:111], v[68:71]\n\t"
    "ds_read_b64_tr_b16 a[186:187], v202 offset:63488\n\t"
    "v_cvt_pk_bf16_f32 v237, v50, v51\n\t"
    "s_waitcnt lgkmcnt(10)\n\t"
    "v_mfma_f32_16x16x32_bf16 v[72:75], a[164:167], a[104:107], v[72:75]\n\t"
    "ds_read_b64_tr_b16 a[188:189], v203 offset:61440\n\t"
    "v_mul_f32 v224, v24, v24\n\t"
    "v_mfma_f32_16x16x32_bf16 v[76:79], a[164:167], a[108:111], v[76:79]\n\t"
    "ds_read_b64_tr_b16 a[190:191], v203 offset:63488\n\t"
    "v_mul_f32 v225, v25, v25\n\t"
    "s_waitcnt vmcnt(26)\n\t"
    "s_barrier\n\t"
    "s_waitcnt lgkmcnt(10)\n\t"
    "s_add_u32 m0, s70, 32768\n\t"
    "s_mov_b32 s69, 196608\n\t"
    "v_mfma_f32_16x16x32_bf16 v[80:83], a[168:171], a[104:107], v[80:83]\n\t"
    "buffer_load_dwordx4 v199, s[52:55], s69 offen lds\n\t"
    "ds_read_b64_tr_b16 a[192:193], v204 offset:0\n\t"
    "v_mfma_f32_16x16x32_bf16 v[84:87], a[168:171], a[108:111], v[84:87]\n\t"
    "ds_read_b64_tr_b16 a[194:195], v204 offset:2048\n\t"
    "v_mul_f32 v226, v26, v26\n\t"
    "s_waitcnt lgkmcnt(10)\n\t"
    "v_mfma_f32_16x16x32_bf16 v[88:91], a[172:175], a[104:107], v[88:91]\n\t"
    "ds_read_b64_tr_b16 a[196:197], v205 offset:0\n\t"
    "v_mul_f32 v227, v27, v27\n\t"
    "v_mfma_f32_16x16x32_bf16 v[92:95], a[172:175], a[108:111], v[92:95]\n\t"
    "ds_read_b64_tr_b16 a[198:199], v205 offset:2048\n\t"
    "v_fma_f32 v220, v224, v214, v212\n\t"
    "s_waitcnt lgkmcnt(10)\n\t"
    "v_mfma_f32_16x16x32_bf16 v[96:99], a[176:179], a[104:107], v[96:99]\n\t"
    "ds_read_b64_tr_b16 a[200:201], v206 offset:0\n\t"
    "v_fma_f32 v221, v225, v214, v212\n\t"
    "v_mfma_f32_16x16x32_bf16 v[100:103], a[176:179], a[108:111], v[100:103]\n\t"
    "ds_read_b64_tr_b16 a[202:203], v206 offset:2048\n\t"
    "v_fma_f32 v222, v226, v214, v212\n\t"
    "s_waitcnt lgkmcnt(10)\n\t"
    "s_add_u32 m0, s70, 33792\n\t"
    "s_mov_b32 s69, 204800\n\t"
    "v_mfma_f32_16x16x32_bf16 v[104:107], a[180:183], a[104:107], v[104:107]\n\t"
    "buffer_load_dwordx4 v199, s[52:55], s69 offen lds\n\t"
    "ds_read_b64_tr_b16 a[204:205], v207 offset:0\n\t"
    "v_mfma_f32_16x16x32_bf16 v[108:111], a[180:183], a[108:111], v[108:111]\n\t"
    "ds_read_b64_tr_b16 a[206:207], v207 offset:2048\n\t"
    "v_fma_f32 v223, v227, v214, v212\n\t"
    "s_waitcnt lgkmcnt(10)\n\t"
    "v_mfma_f32_16x16x32_bf16 v[112:115], a[184:187], a[104:107], v[112:115]\n\t"
    "ds_read_b64_tr_b16 a[208:209], v204 offset:8192\n\t"
    "v_mul_f32 v220, v220, v24\n\t"
    "v_mfma_f32_16x16x32_bf16 v[116:119], a[184:187], a[108:111], v[116:119]\n\t"
    "ds_read_b64_tr_b16 a[210:211], v204 offset:10240\n\t"
    "v_mul_f32 v221, v221, v25\n\t"
    "s_waitcnt lgkmcnt(10)\n\t"
    "v_mfma_f32_16x16x32_bf16 v[120:123], a[188:191], a[104:107], v[120:123]\n\t"
    "ds_read_b64_tr_b16 a[212:213], v205 offset:8192\n\t"
    "v_mul_f32 v222, v222, v26\n\t"
    "v_mfma_f32_16x16x32_bf16 v[124:127], a[188:191], a[108:111], v[124:127]\n\t"
    "ds_read_b64_tr_b16 a[214:215], v205 offset:10240\n\t"
    "v_mul_f32 v223, v223, v27\n\t"
    "s_waitcnt lgkmcnt(10)\n\t"
    "s_add_u32 m0, s70, 34816\n\t"
    "s_mov_b32 s69, 212992\n\t"
    "v_mfma_f32_16x16x32_bf16 v[64:67], a[192:195], a[112:115], v[64:67]\n\t"
    "buffer_load_dwordx4 v199, s[52:55], s69 offen lds\n\t"
    "ds_read_b64_tr_b16 a[216:217], v206 offset:8192\n\t"
    "v_mfma_f32_16x16x32_bf16 v[68:71], a[192:195], a[116:119], v[68:71]\n\t"
    "ds_read_b64_tr_b16 a[218:219], v206 offset:10240\n\t"
    "v_exp_f32 v220, v220\n\t"
    "s_waitcnt lgkmcnt(10)\n\t"
    "v_mfma_f32_16x16x32_bf16 v[72:75], a[196:199], a[112:115], v[72:75]\n\t"
    "ds_read_b64_tr_b16 a[220:221], v207 offset:8192\n\t"
    "v_exp_f32 v221, v221\n\t"
    "v_mfma_f32_16x16x32_bf16 v[76:79], a[196:199], a[116:119], v[76:79]\n\t"
    "ds_read_b64_tr_b16 a[222:223], v207 offset:10240\n\t"
    "v_exp_f32 v222, v222\n\t"
    "s_waitcnt lgkmcnt(10)\n\t"
    "v_mfma_f32_16x16x32_bf16 v[80:83], a[200:203], a[112:115], v[80:83]\n\t"
    "ds_read_b64_tr_b16 a[224:225], v204 offset:4096\n\t"
    "v_exp_f32 v223, v223\n\t"
    "v_mfma_f32_16x16x32_bf16 v[84:87], a[200:203], a[116:119], v[84:87]\n\t"
    "ds_read_b64_tr_b16 a[226:227], v204 offset:6144\n\t"
    "v_mul_f32 v56, v56, v24\n\t"
    "s_waitcnt lgkmcnt(10)\n\t"
    "s_add_u32 m0, s70, 35840\n\t"
    "s_mov_b32 s69, 221184\n\t"
    "v_mfma_f32_16x16x32_bf16 v[88:91], a[204:207], a[112:115], v[88:91]\n\t"
    "buffer_load_dwordx4 v199, s[52:55], s69 offen lds\n\t"
    "ds_read_b64_tr_b16 a[228:229], v205 offset:4096\n\t"
    "v_mfma_f32_16x16x32_bf16 v[92:95], a[204:207], a[116:119], v[92:95]\n\t"
    "ds_read_b64_tr_b16 a[230:231], v205 offset:6144\n\t"
    "v_mul_f32 v57, v57, v25\n\t"
    "s_waitcnt lgkmcnt(10)\n\t"
    "v_mfma_f32_16x16x32_bf16 v[96:99], a[208:211], a[112:115], v[96:99]\n\t"
    "ds_read_b64_tr_b16 a[232:233], v206 offset:4096\n\t"
    "v_mul_f32 v58, v58, v26\n\t"
    "v_mfma_f32_16x16x32_bf16 v[100:103], a[208:211], a[116:119], v[100:103]\n\t"
    "ds_read_b64_tr_b16 a[234:235], v206 offset:6144\n\t"
    "v_mul_f32 v59, v59, v27\n\t"
    "s_waitcnt lgkmcnt(10)\n\t"
    "v_mfma_f32_16x16x32_bf16 v[104:107], a[212:215], a[112:115], v[104:107]\n\t"
    "ds_read_b64_tr_b16 a[236:237], v207 offset:4096\n\t"
    "v_add_f32 v220, 1.0, v220\n\t"
    "v_mfma_f32_16x16x32_bf16 v[108:111], a[212:215], a[116:119], v[108:111]\n\t"
    "ds_read_b64_tr_b16 a[238:239], v207 offset:6144\n\t"
    "v_add_f32 v221, 1.0, v221\n\t"
    "s_waitcnt lgkmcnt(10)\n\t"
    "v_mfma_f32_16x16x32_bf16 v[112:115], a[216:219], a[112:115], v[112:115]\n\t"
    "ds_read_b64_tr_b16 a[240:241], v204 offset:12288\n\t"
    "v_add_f32 v222, 1.0, v222\n\t"
    "v_mfma_f32_16x16x32_bf16 v[116:119], a[216:219], a[116:119], v[116:119]\n\t"
    "ds_read_b64_tr_b16 a[242:243], v204 offset:14336\n\t"
    "v_add_f32 v223, 1.0, v223\n\t"
    "s_waitcnt lgkmcnt(10)\n\t"
    "v_mfma_f32_16x16x32_bf16 v[120:123], a[220:223], a[112:115], v[120:123]\n\t"
    "ds_read_b64_tr_b16 a[244:245], v205 offset:12288\n\t"
    "v_rcp_f32 v220, v220\n\t"
    "v_mfma_f32_16x16x32_bf16 v[124:127], a[220:223], a[116:119], v[124:127]\n\t"
    "ds_read_b64_tr_b16 a[246:247], v205 offset:14336\n\t"
    "v_rcp_f32 v221, v221\n\t"
    "s_waitcnt lgkmcnt(10)\n\t"
    "v_mfma_f32_16x16x32_bf16 v[64:67], a[224:227], a[120:123], v[64:67]\n\t"
    "ds_read_b64_tr_b16 a[248:249], v206 offset:12288\n\t"
    "v_rcp_f32 v222, v222\n\t"
    "v_mfma_f32_16x16x32_bf16 v[68:71], a[224:227], a[124:127], v[68:71]\n\t"
    "ds_read_b64_tr_b16 a[250:251], v206 offset:14336\n\t"
    "v_rcp_f32 v223, v223\n\t"
    "s_waitcnt lgkmcnt(10)\n\t"
    "v_mfma_f32_16x16x32_bf16 v[72:75], a[228:231], a[120:123], v[72:75]\n\t"
    "ds_read_b64_tr_b16 a[252:253], v207 offset:12288\n\t"
    "v_mul_f32 v56, v56, v220\n\t"
    "v_mfma_f32_16x16x32_bf16 v[76:79], a[228:231], a[124:127], v[76:79]\n\t"
    "ds_read_b64_tr_b16 a[254:255], v207 offset:14336\n\t"
    "v_mul_f32 v57, v57, v221\n\t"
    "s_waitcnt lgkmcnt(10)\n\t"
    "v_mfma_f32_16x16x32_bf16 v[80:83], a[232:235], a[120:123], v[80:83]\n\t"
    "v_mul_f32 v58, v58, v222\n\t"
    "v_mfma_f32_16x16x32_bf16 v[84:87], a[232:235], a[124:127], v[84:87]\n\t"
    "v_mul_f32 v59, v59, v223\n\t"
    "s_waitcnt lgkmcnt(8)\n\t"
    "v_mfma_f32_16x16x32_bf16 v[88:91], a[236:239], a[120:123], v[88:91]\n\t"
    "v_cvt_pk_bf16_f32 v238, v56, v57\n\t"
    "v_mfma_f32_16x16x32_bf16 v[92:95], a[236:239], a[124:127], v[92:95]\n\t"
    "v_cvt_pk_bf16_f32 v239, v58, v59\n\t"
    "s_waitcnt lgkmcnt(6)\n\t"
    "v_mfma_f32_16x16x32_bf16 v[96:99], a[240:243], a[120:123], v[96:99]\n\t"
    "s_mov_b32 s69, 0\n\t"
    "buffer_store_dwordx4 v[236:239], v197, s[48:51], s69 offen offset:1024 sc1\n\t"
    "v_mfma_f32_16x16x32_bf16 v[100:103], a[240:243], a[124:127], v[100:103]\n\t"
    "v_mul_f32 v224, v20, v20\n\t"
    "s_waitcnt lgkmcnt(4)\n\t"
    "v_mfma_f32_16x16x32_bf16 v[104:107], a[244:247], a[120:123], v[104:107]\n\t"
    "v_mul_f32 v225, v21, v21\n\t"
    "v_mfma_f32_16x16x32_bf16 v[108:111], a[244:247], a[124:127], v[108:111]\n\t"
    "v_mul_f32 v226, v22, v22\n\t"
    "s_waitcnt lgkmcnt(2)\n\t"
    "v_mfma_f32_16x16x32_bf16 v[112:115], a[248:251], a[120:123], v[112:115]\n\t"
    "v_mul_f32 v227, v23, v23\n\t"
    "v_mfma_f32_16x16x32_bf16 v[116:119], a[248:251], a[124:127], v[116:119]\n\t"
    "v_fma_f32 v220, v224, v214, v212\n\t"
    "s_waitcnt lgkmcnt(0)\n\t"
    "v_mfma_f32_16x16x32_bf16 v[120:123], a[252:255], a[120:123], v[120:123]\n\t"
    "v_fma_f32 v221, v225, v214, v212\n\t"
    "v_mfma_f32_16x16x32_bf16 v[124:127], a[252:255], a[124:127], v[124:127]\n\t"
    "v_fma_f32 v222, v226, v214, v212\n\t"
    "v_fma_f32 v223, v227, v214, v212\n\t"
    "v_mul_f32 v220, v220, v20\n\t"
    "v_mul_f32 v221, v221, v21\n\t"
    "v_mul_f32 v222, v222, v22\n\t"
    "v_mul_f32 v223, v223, v23\n\t"
    "v_exp_f32 v220, v220\n\t"
    "v_exp_f32 v221, v221\n\t"
    "v_exp_f32 v222, v222\n\t"
    "v_exp_f32 v223, v223\n\t"
    "v_mul_f32 v52, v52, v20\n\t"
    "v_mul_f32 v53, v53, v21\n\t"
    "v_mul_f32 v54, v54, v22\n\t"
    "v_mul_f32 v55, v55, v23\n\t"
    "v_add_f32 v220, 1.0, v220\n\t"
    "v_add_f32 v221, 1.0, v221\n\t"
    "v_add_f32 v222, 1.0, v222\n\t"
    "v_add_f32 v223, 1.0, v223\n\t"
    "v_rcp_f32 v220, v220\n\t"
    "v_rcp_f32 v221, v221\n\t"
    "v_rcp_f32 v222, v222\n\t"
    "v_rcp_f32 v223, v223\n\t"
    "v_mul_f32 v52, v52, v220\n\t"
    "v_mul_f32 v53, v53, v221\n\t"
    "v_mul_f32 v54, v54, v222\n\t"
    "v_mul_f32 v55, v55, v223\n\t"
    "v_cvt_pk_bf16_f32 v240, v52, v53\n\t"
    "v_cvt_pk_bf16_f32 v241, v54, v55\n\t"
    "v_mul_f32 v224, v28, v28\n\t"
    "v_mul_f32 v225, v29, v29\n\t"
    "v_mul_f32 v226, v30, v30\n\t"
    "v_mul_f32 v227, v31, v31\n\t"
    "v_fma_f32 v220, v224, v214, v212\n\t"
    "v_fma_f32 v221, v225, v214, v212\n\t"
    "v_fma_f32 v222, v226, v214, v212\n\t"
    "v_fma_f32 v223, v227, v214, v212\n\t"
    "v_mul_f32 v220, v220, v28\n\t"
    "v_mul_f32 v221, v221, v29\n\t"
    "v_mul_f32 v222, v222, v30\n\t"
    "v_mul_f32 v223, v223, v31\n\t"
    "v_exp_f32 v220, v220\n\t"
    "v_exp_f32 v221, v221\n\t"
    "v_exp_f32 v222, v222\n\t"
    "v_exp_f32 v223, v223\n\t"
    "v_mul_f32 v60, v60, v28\n\t"
    "v_mul_f32 v61, v61, v29\n\t"
    "v_mul_f32 v62, v62, v30\n\t"
    "v_mul_f32 v63, v63, v31\n\t"
    "v_add_f32 v220, 1.0, v220\n\t"
    "v_add_f32 v221, 1.0, v221\n\t"
    "v_add_f32 v222, 1.0, v222\n\t"
    "v_add_f32 v223, 1.0, v223\n\t"
    "v_rcp_f32 v220, v220\n\t"
    "v_rcp_f32 v221, v221\n\t"
    "v_rcp_f32 v222, v222\n\t"
    "v_rcp_f32 v223, v223\n\t"
    "v_mul_f32 v60, v60, v220\n\t"
    "v_mul_f32 v61, v61, v221\n\t"
    "v_mul_f32 v62, v62, v222\n\t"
    "v_mul_f32 v63, v63, v223\n\t"
    "v_cvt_pk_bf16_f32 v242, v60, v61\n\t"
    "v_cvt_pk_bf16_f32 v243, v62, v63\n\t"
    "s_mov_b32 s69, 57344\n\t"
    "buffer_store_dwordx4 v[240:243], v197, s[48:51], s69 offen offset:1024 sc1\n\t"
    "v_pk_mul_f32 v[228:229], v[64:65], v[64:65]\n\t"
    "v_pk_mul_f32 v[230:231], v[66:67], v[66:67]\n\t"
    "v_pk_mul_f32 v[232:233], v[72:73], v[72:73]\n\t"
    "v_pk_mul_f32 v[234:235], v[74:75], v[74:75]\n\t"
    "v_pk_fma_f32 v[220:221], v[228:229], v[214:215], v[212:213]\n\t"
    "v_pk_fma_f32 v[222:223], v[230:231], v[214:215], v[212:213]\n\t"
    "v_pk_fma_f32 v[224:225], v[232:233], v[214:215], v[212:213]\n\t"
    "v_pk_fma_f32 v[226:227], v[234:235], v[214:215], v[212:213]\n\t"
    "v_pk_mul_f32 v[220:221], v[220:221], v[64:65]\n\t"
    "v_pk_mul_f32 v[222:223], v[222:223], v[66:67]\n\t"
    "v_pk_mul_f32 v[224:225], v[224:225], v[72:73]\n\t"
    "v_pk_mul_f32 v[226:227], v[226:227], v[74:75]\n\t"
    "v_exp_f32 v220, v220\n\t"
    "v_exp_f32 v222, v222\n\t"
    "v_exp_f32 v224, v224\n\t"
    "v_exp_f32 v226, v226\n\t"
    "v_exp_f32 v221, v221\n\t"
    "v_exp_f32 v223, v223\n\t"
    "v_exp_f32 v225, v225\n\t"
    "v_exp_f32 v227, v227\n\t"
    "v_pk_mul_f32 v[96:97], v[96:97], v[64:65]\n\t"
    "v_pk_mul_f32 v[98:99], v[98:99], v[66:67]\n\t"
    "v_pk_mul_f32 v[104:105], v[104:105], v[72:73]\n\t"
    "v_pk_mul_f32 v[106:107], v[106:107], v[74:75]\n\t"
    "v_pk_add_f32 v[220:221], v[220:221], v[216:217]\n\t"
    "v_pk_add_f32 v[222:223], v[222:223], v[216:217]\n\t"
    "v_pk_add_f32 v[224:225], v[224:225], v[216:217]\n\t"
    "v_pk_add_f32 v[226:227], v[226:227], v[216:217]\n\t"
    "v_rcp_f32 v220, v220\n\t"
    "v_rcp_f32 v222, v222\n\t"
    "v_rcp_f32 v224, v224\n\t"
    "v_rcp_f32 v226, v226\n\t"
    "v_rcp_f32 v221, v221\n\t"
    "v_rcp_f32 v223, v223\n\t"
    "v_rcp_f32 v225, v225\n\t"
    "v_rcp_f32 v227, v227\n\t"
    "v_pk_mul_f32 v[96:97], v[96:97], v[220:221]\n\t"
    "v_pk_mul_f32 v[98:99], v[98:99], v[222:223]\n\t"
    "v_pk_mul_f32 v[104:105], v[104:105], v[224:225]\n\t"
    "v_pk_mul_f32 v[106:107], v[106:107], v[226:227]\n\t"
    "v_cvt_pk_bf16_f32 v236, v96, v97\n\t"
    "v_cvt_pk_bf16_f32 v237, v98, v99\n\t"
    "v_cvt_pk_bf16_f32 v238, v104, v105\n\t"
    "v_cvt_pk_bf16_f32 v239, v106, v107\n\t"
    "s_mov_b32 s69, 0\n\t"
    "buffer_store_dwordx4 v[236:239], v197, s[48:51], s69 offen offset:2048 sc1\n\t"
    "v_pk_mul_f32 v[228:229], v[68:69], v[68:69]\n\t"
    "v_pk_mul_f32 v[230:231], v[70:71], v[70:71]\n\t"
    "v_pk_mul_f32 v[232:233], v[76:77], v[76:77]\n\t"
    "v_pk_mul_f32 v[234:235], v[78:79], v[78:79]\n\t"
    "v_pk_fma_f32 v[220:221], v[228:229], v[214:215], v[212:213]\n\t"
    "v_pk_fma_f32 v[222:223], v[230:231], v[214:215], v[212:213]\n\t"
    "v_pk_fma_f32 v[224:225], v[232:233], v[214:215], v[212:213]\n\t"
    "v_pk_fma_f32 v[226:227], v[234:235], v[214:215], v[212:213]\n\t"
    "v_pk_mul_f32 v[220:221], v[220:221], v[68:69]\n\t"
    "v_pk_mul_f32 v[222:223], v[222:223], v[70:71]\n\t"
    "v_pk_mul_f32 v[224:225], v[224:225], v[76:77]\n\t"
    "v_pk_mul_f32 v[226:227], v[226:227], v[78:79]\n\t"
    "v_exp_f32 v220, v220\n\t"
    "v_exp_f32 v222, v222\n\t"
    "v_exp_f32 v224, v224\n\t"
    "v_exp_f32 v226, v226\n\t"
    "v_exp_f32 v221, v221\n\t"
    "v_exp_f32 v223, v223\n\t"
    "v_exp_f32 v225, v225\n\t"
    "v_exp_f32 v227, v227\n\t"
    "v_pk_mul_f32 v[100:101], v[100:101], v[68:69]\n\t"
    "v_pk_mul_f32 v[102:103], v[102:103], v[70:71]\n\t"
    "v_pk_mul_f32 v[108:109], v[108:109], v[76:77]\n\t"
    "v_pk_mul_f32 v[110:111], v[110:111], v[78:79]\n\t"
    "v_pk_add_f32 v[220:221], v[220:221], v[216:217]\n\t"
    "v_pk_add_f32 v[222:223], v[222:223], v[216:217]\n\t"
    "v_pk_add_f32 v[224:225], v[224:225], v[216:217]\n\t"
    "v_pk_add_f32 v[226:227], v[226:227], v[216:217]\n\t"
    "v_rcp_f32 v220, v220\n\t"
    "v_rcp_f32 v222, v222\n\t"
    "v_rcp_f32 v224, v224\n\t"
    "v_rcp_f32 v226, v226\n\t"
    "v_rcp_f32 v221, v221\n\t"
    "v_rcp_f32 v223, v223\n\t"
    "v_rcp_f32 v225, v225\n\t"
    "v_rcp_f32 v227, v227\n\t"
    "v_pk_mul_f32 v[100:101], v[100:101], v[220:221]\n\t"
    "v_pk_mul_f32 v[102:103], v[102:103], v[222:223]\n\t"
    "v_pk_mul_f32 v[108:109], v[108:109], v[224:225]\n\t"
    "v_pk_mul_f32 v[110:111], v[110:111], v[226:227]\n\t"
    "v_cvt_pk_bf16_f32 v240, v100, v101\n\t"
    "v_cvt_pk_bf16_f32 v241, v102, v103\n\t"
    "v_cvt_pk_bf16_f32 v242, v108, v109\n\t"
    "v_cvt_pk_bf16_f32 v243, v110, v111\n\t"
    "s_mov_b32 s69, 57344\n\t"
    "buffer_store_dwordx4 v[240:243], v197, s[48:51], s69 offen offset:2048 sc1\n\t"
    "v_pk_mul_f32 v[228:229], v[80:81], v[80:81]\n\t"
    "v_pk_mul_f32 v[230:231], v[82:83], v[82:83]\n\t"
    "v_pk_mul_f32 v[232:233], v[88:89], v[88:89]\n\t"
    "v_pk_mul_f32 v[234:235], v[90:91], v[90:91]\n\t"
    "v_pk_fma_f32 v[220:221], v[228:229], v[214:215], v[212:213]\n\t"
    "v_pk_fma_f32 v[222:223], v[230:231], v[214:215], v[212:213]\n\t"
    "v_pk_fma_f32 v[224:225], v[232:233], v[214:215], v[212:213]\n\t"
    "v_pk_fma_f32 v[226:227], v[234:235], v[214:215], v[212:213]\n\t"
    "v_pk_mul_f32 v[220:221], v[220:221], v[80:81]\n\t"
    "v_pk_mul_f32 v[222:223], v[222:223], v[82:83]\n\t"
    "v_pk_mul_f32 v[224:225], v[224:225], v[88:89]\n\t"
    "v_pk_mul_f32 v[226:227], v[226:227], v[90:91]\n\t"
    "v_exp_f32 v220, v220\n\t"
    "v_exp_f32 v222, v222\n\t"
    "v_exp_f32 v224, v224\n\t"
    "v_exp_f32 v226, v226\n\t"
    "v_exp_f32 v221, v221\n\t"
    "v_exp_f32 v223, v223\n\t"
    "v_exp_f32 v225, v225\n\t"
    "v_exp_f32 v227, v227\n\t"
    "v_pk_mul_f32 v[112:113], v[112:113], v[80:81]\n\t"
    "v_pk_mul_f32 v[114:115], v[114:115], v[82:83]\n\t"
    "v_pk_mul_f32 v[120:121], v[120:121], v[88:89]\n\t"
    "v_pk_mul_f32 v[122:123], v[122:123], v[90:91]\n\t"
    "v_pk_add_f32 v[220:221], v[220:221], v[216:217]\n\t"
    "v_pk_add_f32 v[222:223], v[222:223], v[216:217]\n\t"
    "v_pk_add_f32 v[224:225], v[224:225], v[216:217]\n\t"
    "v_pk_add_f32 v[226:227], v[226:227], v[216:217]\n\t"
    "v_rcp_f32 v220, v220\n\t"
    "v_rcp_f32 v222, v222\n\t"
    "v_rcp_f32 v224, v224\n\t"
    "v_rcp_f32 v226, v226\n\t"
    "v_rcp_f32 v221, v221\n\t"
    "v_rcp_f32 v223, v223\n\t"
    "v_rcp_f32 v225, v225\n\t"
    "v_rcp_f32 v227, v227\n\t"
    "v_pk_mul_f32 v[112:113], v[112:113], v[220:221]\n\t"
    "v_pk_mul_f32 v[114:115], v[114:115], v[222:223]\n\t"
    "v_pk_mul_f32 v[120:121], v[120:121], v[224:225]\n\t"
    "v_pk_mul_f32 v[122:123], v[122:123], v[226:227]\n\t"
    "v_cvt_pk_bf16_f32 v236, v112, v113\n\t"
    "v_cvt_pk_bf16_f32 v237, v114, v115\n\t"
    "v_cvt_pk_bf16_f32 v238, v120, v121\n\t"
    "v_cvt_pk_bf16_f32 v239, v122, v123\n\t"
    "s_mov_b32 s69, 0\n\t"
    "buffer_store_dwordx4 v[236:239], v197, s[48:51], s69 offen offset:3072 sc1\n\t"
    "v_pk_mul_f32 v[228:229], v[84:85], v[84:85]\n\t"
    "v_pk_mul_f32 v[230:231], v[86:87], v[86:87]\n\t"
    "v_pk_mul_f32 v[232:233], v[92:93], v[92:93]\n\t"
    "v_pk_mul_f32 v[234:235], v[94:95], v[94:95]\n\t"
    "v_pk_fma_f32 v[220:221], v[228:229], v[214:215], v[212:213]\n\t"
    "v_pk_fma_f32 v[222:223], v[230:231], v[214:215], v[212:213]\n\t"
    "v_pk_fma_f32 v[224:225], v[232:233], v[214:215], v[212:213]\n\t"
    "v_pk_fma_f32 v[226:227], v[234:235], v[214:215], v[212:213]\n\t"
    "v_pk_mul_f32 v[220:221], v[220:221], v[84:85]\n\t"
    "v_pk_mul_f32 v[222:223], v[222:223], v[86:87]\n\t"
    "v_pk_mul_f32 v[224:225], v[224:225], v[92:93]\n\t"
    "v_pk_mul_f32 v[226:227], v[226:227], v[94:95]\n\t"
    "v_exp_f32 v220, v220\n\t"
    "v_exp_f32 v222, v222\n\t"
    "v_exp_f32 v224, v224\n\t"
    "v_exp_f32 v226, v226\n\t"
    "v_exp_f32 v221, v221\n\t"
    "v_exp_f32 v223, v223\n\t"
    "v_exp_f32 v225, v225\n\t"
    "v_exp_f32 v227, v227\n\t"
    "v_pk_mul_f32 v[116:117], v[116:117], v[84:85]\n\t"
    "v_pk_mul_f32 v[118:119], v[118:119], v[86:87]\n\t"
    "v_pk_mul_f32 v[124:125], v[124:125], v[92:93]\n\t"
    "v_pk_mul_f32 v[126:127], v[126:127], v[94:95]\n\t"
    "v_pk_add_f32 v[220:221], v[220:221], v[216:217]\n\t"
    "v_pk_add_f32 v[222:223], v[222:223], v[216:217]\n\t"
    "v_pk_add_f32 v[224:225], v[224:225], v[216:217]\n\t"
    "v_pk_add_f32 v[226:227], v[226:227], v[216:217]\n\t"
    "v_rcp_f32 v220, v220\n\t"
    "v_rcp_f32 v222, v222\n\t"
    "v_rcp_f32 v224, v224\n\t"
    "v_rcp_f32 v226, v226\n\t"
    "v_rcp_f32 v221, v221\n\t"
    "v_rcp_f32 v223, v223\n\t"
    "v_rcp_f32 v225, v225\n\t"
    "v_rcp_f32 v227, v227\n\t"
    "v_pk_mul_f32 v[116:117], v[116:117], v[220:221]\n\t"
    "v_pk_mul_f32 v[118:119], v[118:119], v[222:223]\n\t"
    "v_pk_mul_f32 v[124:125], v[124:125], v[224:225]\n\t"
    "v_pk_mul_f32 v[126:127], v[126:127], v[226:227]\n\t"
    "v_cvt_pk_bf16_f32 v240, v116, v117\n\t"
    "v_cvt_pk_bf16_f32 v241, v118, v119\n\t"
    "v_cvt_pk_bf16_f32 v242, v124, v125\n\t"
    "v_cvt_pk_bf16_f32 v243, v126, v127\n\t"
    "s_mov_b32 s69, 57344\n\t"
    "buffer_store_dwordx4 v[240:243], v197, s[48:51], s69 offen offset:3072 sc1\n\t"
    "s_waitcnt vmcnt(0)\n\t"
    "s_barrier\n\t"
      :
      : "s"(ws + WS_H2F), "s"(wg), "s"(wu), "s"(ws + WS_HID), "s"(mt128), "s"(idx), "s"(0u), "s"(wid), "s"(ldsb), "s"(wd), "s"(nt64)
      :
      "v0", "v1", "v2", "v3", "v4", "v5", "v6", "v7", "v8", "v9", "v10", "v11", "v12", "v13", "v14", "v15", "v16", "v17", "v18", "v19", "v20", "v21", "v22", "v23",
      "v24", "v25", "v26", "v27", "v28", "v29", "v30", "v31", "v32", "v33", "v34", "v35", "v36", "v37", "v38", "v39", "v40", "v41", "v42", "v43", "v44", "v45", "v46", "v47",
      "v48", "v49", "v50", "v51", "v52", "v53", "v54", "v55", "v56", "v57", "v58", "v59", "v60", "v61", "v62", "v63", "v64", "v65", "v66", "v67", "v68", "v69", "v70", "v71",
      "v72", "v73", "v74", "v75", "v76", "v77", "v78", "v79", "v80", "v81", "v82", "v83", "v84", "v85", "v86", "v87", "v88", "v89", "v90", "v91", "v92", "v93", "v94", "v95",
      "v96", "v97", "v98", "v99", "v100", "v101", "v102", "v103", "v104", "v105", "v106", "v107", "v108", "v109", "v110", "v111", "v112", "v113", "v114", "v115", "v116", "v117", "v118", "v119",
      "v120", "v121", "v122", "v123", "v124", "v125", "v126", "v127", "v128", "v129", "v130", "v131", "v132", "v133", "v134", "v135", "v136", "v137", "v138", "v139", "v140", "v141", "v142", "v143",
      "v144", "v145", "v146", "v147", "v148", "v149", "v150", "v151", "v152", "v153", "v154", "v155", "v156", "v157", "v158", "v159", "v160", "v161", "v162", "v163", "v164", "v165", "v166", "v167",
      "v168", "v169", "v170", "v171", "v172", "v173", "v174", "v175", "v176", "v177", "v178", "v179", "v180", "v181", "v182", "v183", "v184", "v185", "v186", "v187", "v188", "v189", "v190", "v191",
      "v192", "v193", "v194", "v195", "v196", "v197", "v198", "v199", "v200", "v201", "v202", "v203", "v204", "v205", "v206", "v207", "v208", "v209", "v210", "v211", "v212", "v213", "v214", "v215",
      "v216", "v217", "v218", "v219", "v220", "v221", "v222", "v223", "v224", "v225", "v226", "v227", "v228", "v229", "v230", "v231", "v232", "v233", "v234", "v235", "v236", "v237", "v238", "v239",
      "v240", "v241", "v242", "v243", "v244", "v245", "v246", "v247", "v248", "v249", "v250", "v251", "v252", "v253", "v254", "v255", "a0", "a1", "a2", "a3", "a4", "a5", "a6", "a7",
      "a8", "a9", "a10", "a11", "a12", "a13", "a14", "a15", "a16", "a17", "a18", "a19", "a20", "a21", "a22", "a23", "a24", "a25", "a26", "a27", "a28", "a29", "a30", "a31",
      "a32", "a33", "a34", "a35", "a36", "a37", "a38", "a39", "a40", "a41", "a42", "a43", "a44", "a45", "a46", "a47", "a48", "a49", "a50", "a51", "a52", "a53", "a54", "a55",
      "a56", "a57", "a58", "a59", "a60", "a61", "a62", "a63", "a64", "a65", "a66", "a67", "a68", "a69", "a70", "a71", "a72", "a73", "a74", "a75", "a76", "a77", "a78", "a79",
      "a80", "a81", "a82", "a83", "a84", "a85", "a86", "a87", "a88", "a89", "a90", "a91", "a92", "a93", "a94", "a95", "a96", "a97", "a98", "a99", "a100", "a101", "a102", "a103",
      "a104", "a105", "a106", "a107", "a108", "a109", "a110", "a111", "a112", "a113", "a114", "a115", "a116", "a117", "a118", "a119", "a120", "a121", "a122", "a123", "a124", "a125", "a126", "a127",
      "a128", "a129", "a130", "a131", "a132", "a133", "a134", "a135", "a136", "a137", "a138", "a139", "a140", "a141", "a142", "a143", "a144", "a145", "a146", "a147", "a148", "a149", "a150", "a151",
      "a152", "a153", "a154", "a155", "a156", "a157", "a158", "a159", "a160", "a161", "a162", "a163", "a164", "a165", "a166", "a167", "a168", "a169", "a170", "a171", "a172", "a173", "a174", "a175",
      "a176", "a177", "a178", "a179", "a180", "a181", "a182", "a183", "a184", "a185", "a186", "a187", "a188", "a189", "a190", "a191", "a192", "a193", "a194", "a195", "a196", "a197", "a198", "a199",
      "a200", "a201", "a202", "a203", "a204", "a205", "a206", "a207", "a208", "a209", "a210", "a211", "a212", "a213", "a214", "a215", "a216", "a217", "a218", "a219", "a220", "a221", "a222", "a223",
      "a224", "a225", "a226", "a227", "a228", "a229", "a230", "a231", "a232", "a233", "a234", "a235", "a236", "a237", "a238", "a239", "a240", "a241", "a242", "a243", "a244", "a245", "a246", "a247",
      "a248", "a249", "a250", "a251", "a252", "a253", "a254", "a255", "s36", "s37", "s38", "s39", "s40", "s41", "s42", "s43", "s44", "s45", "s46", "s47", "s48", "s49", "s50", "s51",
      "s52", "s53", "s54", "s55", "s56", "s57", "s58", "s59", "s60", "s61", "s62", "s63", "s64", "s65", "s66", "s67", "s68", "s69", "s70", "s71", "vcc", "scc", "memory"
    );
    seen = arrive_ret(cnt + 128, wid);
  } else { PRE_G3(G3P0); seen = 0; }
  if (wid != 0) PRE_G3(G3P1);
  wait_ge2(cnt + 128, 14, wid, seen);
  if (wid == 0) PRE_G3(G3P1);
  const unsigned tl = wid * 64 + lane_id();
  u32x4 xv[2]; f32x4 gd0, gd1;
  {
    const __amdgpu_buffer_rsrc_t rx = __builtin_amdgcn_make_buffer_rsrc((void*)(ws + WS_X1F), (short)0, 2 << 20, 0x00020000);
    const int p = tl & 7, u = p >> 2, g = p & 3, n1 = nt64 * 64 + 4 * (8 * u + g);
    gd0 = *(const f32x4*)&mod[2560 + n1]; gd1 = *(const f32x4*)&mod[2560 + n1 + 16];
#pragma unroll
    for (int rep = 0; rep < 2; ++rep) {
      const int row = mt64 * 64 + (tl >> 3) + 32 * rep;
      xv[rep] = __builtin_amdgcn_raw_buffer_load_b128(rx, ((((row >> 4) * 16 + 2 * nt64 + u) * 64) + g * 16 + (row & 15)) * 16, 0, 16);
    }
  }
  asm volatile(
    "v_mbcnt_lo_u32_b32 v96, -1, 0\n\t"
    "v_mbcnt_hi_u32_b32 v96, -1, v96\n\t"
    "v_lshlrev_b32 v97, 4, v96\n\t"
    "s_mul_i32 s44, %2, 224\n\t"
    "s_mul_i32 s45, %4, 14\n\t"
    "s_add_u32 s44, s44, s45\n\t"
    "s_lshl_b32 s44, s44, 10\n\t"
    "s_mov_b64 s[36:37], %0\n\t"
    "s_add_u32 s36, s36, s44\n\t"
    "s_addc_u32 s37, s37, 0\n\t"
    "s_and_b32 s37, s37, 0xffff\n\t"
    "s_mov_b32 s38, 186368\n\t"
    "s_mov_b32 s39, 0x20000\n\t"
    "s_mul_i32 s44, %4, 458752\n\t"
    "s_lshl_b32 s45, %3, 7\n\t"
    "s_add_u32 s44, s44, s45\n\t"
    "s_mov_b64 s[40:41], %1\n\t"
    "s_add_u32 s40, s40, s44\n\t"
    "s_addc_u32 s41, s41, 0\n\t"
    "s_and_b32 s41, s41, 0xffff\n\t"
    "s_mov_b32 s42, 457856\n\t"
    "s_mov_b32 s43, 0x20000\n\t"
    "v_lshrrev_b32 v101, 3, v96\n\t"
    "s_mov_b32 s44, 1024\n\t"
    "v_mul_lo_u32 v98, v101, s44\n\t"
    "v_bfe_u32 v102, v96, 4, 1\n\t"
    "v_lshlrev_b32 v102, 2, v102\n\t"
    "v_bfe_u32 v103, v96, 5, 1\n\t"
    "v_lshl_add_u32 v102, v103, 1, v102\n\t"
    "v_and_b32 v103, 7, v96\n\t"
    "v_xor_b32 v103, v103, v102\n\t"
    "v_lshl_add_u32 v98, v103, 4, v98\n\t"
    "s_mul_i32 s52, %4, 4096\n\t"
    "s_add_u32 s52, s52, %5\n\t"
    "s_mov_b32 s53, 0\n\t"
    "buffer_load_dwordx4 a[0:3], v97, s[36:39], s53 offen sc1\n\t"
    "s_mov_b32 s53, 57344\n\t"
    "buffer_load_dwordx4 a[4:7], v97, s[36:39], s53 offen sc1\n\t"
    "s_mov_b32 s53, 114688\n\t"
    "buffer_load_dwordx4 a[8:11], v97, s[36:39], s53 offen sc1\n\t"
    "s_mov_b32 s53, 172032\n\t"
    "buffer_load_dwordx4 a[12:15], v97, s[36:39], s53 offen sc1\n\t"
    "s_mov_b32 s53, 1024\n\t"
    "buffer_load_dwordx4 a[16:19], v97, s[36:39], s53 offen sc1\n\t"
    "s_mov_b32 s53, 58368\n\t"
    "buffer_load_dwordx4 a[20:23], v97, s[36:39], s53 offen sc1\n\t"
    "s_mov_b32 s53, 115712\n\t"
    "buffer_load_dwordx4 a[24:27], v97, s[36:39], s53 offen sc1\n\t"
    "s_mov_b32 s53, 173056\n\t"
    "buffer_load_dwordx4 a[28:31], v97, s[36:39], s53 offen sc1\n\t"
    "s_mov_b32 s53, 2048\n\t"
    "buffer_load_dwordx4 a[32:35], v97, s[36:39], s53 offen sc1\n\t"
    "s_mov_b32 s53, 59392\n\t"
    "buffer_load_dwordx4 a[36:39], v97, s[36:39], s53 offen sc1\n\t"
    "s_mov_b32 s53, 116736\n\t"
    "buffer_load_dwordx4 a[40:43], v97, s[36:39], s53 offen sc1\n\t"
    "s_mov_b32 s53, 174080\n\t"
    "buffer_load_dwordx4 a[44:47], v97, s[36:39], s53 offen sc1\n\t"
    "v_and_b32 v101, 1, v96\n\t"
    "v_lshlrev_b32 v99, 3, v101\n\t"
    "v_bfe_u32 v101, v96, 2, 3\n\t"
    "v_lshl_add_u32 v99, v101, 7, v99\n\t"
    "v_lshrrev_b32 v101, 5, v96\n\t"
    "v_lshl_add_u32 v99, v101, 10, v99\n\t"
    "v_add_u32 v99, s52, v99\n\t"
    "v_bfe_u32 v101, v96, 3, 1\n\t"
    "v_lshlrev_b32 v101, 2, v101\n\t"
    "v_bfe_u32 v102, v96, 4, 1\n\t"
    "v_lshl_add_u32 v101, v102, 1, v101\n\t"
    "v_bfe_u32 v102, v96, 1, 1\n\t"
    "v_xor_b32 v101, v101, v102\n\t"
    "v_xor_b32 v102, 6, v101\n\t"
    "v_lshl_add_u32 v109, v102, 4, v99\n\t"
    "v_xor_b32 v102, 4, v101\n\t"
    "v_lshl_add_u32 v108, v102, 4, v99\n\t"
    "v_xor_b32 v102, 2, v101\n\t"
    "v_lshl_add_u32 v100, v102, 4, v99\n\t"
    "v_xor_b32 v102, 0, v101\n\t"
    "v_lshl_add_u32 v99, v102, 4, v99\n\t"
    "v_add_u32 v112, 0x10000, v99\n\t"
    "v_add_u32 v113, 0x10000, v100\n\t"
    "v_add_u32 v114, 0x10000, v108\n\t"
    "v_add_u32 v115, 0x10000, v109\n\t"
    "v_add_u32 v116, 0x20000, v99\n\t"
    "v_add_u32 v117, 0x20000, v100\n\t"
    "v_add_u32 v118, 0x20000, v108\n\t"
    "v_add_u32 v119, 0x20000, v109\n\t"
    "ds_read_b64_tr_b16 v[64:65], v112 offset:16384\n\t"
    "ds_read_b64_tr_b16 v[66:67], v112 offset:18432\n\t"
    "ds_read_b64_tr_b16 v[68:69], v113 offset:16384\n\t"
    "ds_read_b64_tr_b16 v[70:71], v113 offset:18432\n\t"
    "ds_read_b64_tr_b16 v[72:73], v114 offset:16384\n\t"
    "ds_read_b64_tr_b16 v[74:75], v114 offset:18432\n\t"
    "ds_read_b64_tr_b16 v[76:77], v115 offset:16384\n\t"
    "ds_read_b64_tr_b16 v[78:79], v115 offset:18432\n\t"
    "s_waitcnt vmcnt(8) lgkmcnt(0)\n\t"
    "v_mfma_f32_16x16x32_bf16 v[0:3], v[64:67], a[0:3], 0\n\t"
    "ds_read_b64_tr_b16 v[80:81], v112 offset:32768\n\t"
    "s_mov_b32 s53, 4096\n\t"
    "v_mfma_f32_16x16x32_bf16 v[4:7], v[64:67], a[4:7], 0\n\t"
    "buffer_load_dwordx4 a[64:67], v97, s[36:39], s53 offen sc1\n\t"
    "v_mfma_f32_16x16x32_bf16 v[8:11], v[64:67], a[8:11], 0\n\t"
    "ds_read_b64_tr_b16 v[82:83], v112 offset:34816\n\t"
    "s_mov_b32 s53, 61440\n\t"
    "v_mfma_f32_16x16x32_bf16 v[12:15], v[64:67], a[12:15], 0\n\t"
    "buffer_load_dwordx4 a[68:71], v97, s[36:39], s53 offen sc1\n\t"
    "v_mfma_f32_16x16x32_bf16 v[16:19], v[68:71], a[0:3], 0\n\t"
    "ds_read_b64_tr_b16 v[84:85], v113 offset:32768\n\t"
    "s_mov_b32 s53, 118784\n\t"
    "v_mfma_f32_16x16x32_bf16 v[20:23], v[68:71], a[4:7], 0\n\t"
    "buffer_load_dwordx4 a[72:75], v97, s[36:39], s53 offen sc1\n\t"
    "v_mfma_f32_16x16x32_bf16 v[24:27], v[68:71], a[8:11], 0\n\t"
    "ds_read_b64_tr_b16 v[86:87], v113 offset:34816\n\t"
    "s_mov_b32 s53, 176128\n\t"
    "v_mfma_f32_16x16x32_bf16 v[28:31], v[68:71], a[12:15], 0\n\t"
    "buffer_load_dwordx4 a[76:79], v97, s[36:39], s53 offen sc1\n\t"
    "v_mfma_f32_16x16x32_bf16 v[32:35], v[72:75], a[0:3], 0\n\t"
    "ds_read_b64_tr_b16 v[88:89], v114 offset:32768\n\t"
    "s_mov_b32 s53, 3072\n\t"
    "v_mfma_f32_16x16x32_bf16 v[36:39], v[72:75], a[4:7], 0\n\t"
    "buffer_load_dwordx4 a[48:51], v97, s[36:39], s53 offen sc1\n\t"
    "v_mfma_f32_16x16x32_bf16 v[40:43], v[72:75], a[8:11], 0\n\t"
    "ds_read_b64_tr_b16 v[90:91], v114 offset:34816\n\t"
    "s_mov_b32 s53, 60416\n\t"
    "v_mfma_f32_16x16x32_bf16 v[44:47], v[72:75], a[12:15], 0\n\t"
    "buffer_load_dwordx4 a[52:55], v97, s[36:39], s53 offen sc1\n\t"
    "v_mfma_f32_16x16x32_bf16 v[48:51], v[76:79], a[0:3], 0\n\t"
    "ds_read_b64_tr_b16 v[92:93], v115 offset:32768\n\t"
    "s_mov_b32 s53, 117760\n\t"
    "v_mfma_f32_16x16x32_bf16 v[52:55], v[76:79], a[4:7], 0\n\t"
    "buffer_load_dwordx4 a[56:59], v97, s[36:39], s53 offen sc1\n\t"
    "v_mfma_f32_16x16x32_bf16 v[56:59], v[76:79], a[8:11], 0\n\t"
    "ds_read_b64_tr_b16 v[94:95], v115 offset:34816\n\t"
    "s_mov_b32 s53, 175104\n\t"
    "v_mfma_f32_16x16x32_bf16 v[60:63], v[76:79], a[12:15], 0\n\t"
    "buffer_load_dwordx4 a[60:63], v97, s[36:39], s53 offen sc1\n\t"
    "s_add_u32 m0, s52, 81920\n\t"
    "s_mov_b32 s53, 294912\n\t"
    "buffer_load_dwordx4 v98, s[40:43], s53 offen lds\n\t"
    "s_add_u32 m0, s52, 82944\n\t"
    "s_mov_b32 s53, 303104\n\t"
    "buffer_load_dwordx4 v98, s[40:43], s53 offen lds\n\t"
    "s_add_u32 m0, s52, 83968\n\t"
    "s_mov_b32 s53, 311296\n\t"
    "buffer_load_dwordx4 v98, s[40:43], s53 offen lds\n\t"
    "s_add_u32 m0, s52, 84992\n\t"
    "s_mov_b32 s53, 319488\n\t"
    "buffer_load_dwordx4 v98, s[40:43], s53 offen lds\n\t"
    "s_waitcnt vmcnt(16) lgkmcnt(0)\n\t"
    "v_mfma_f32_16x16x32_bf16 v[0:3], v[80:83], a[16:19], v[0:3]\n\t"
    "ds_read_b64_tr_b16 v[64:65], v112 offset:49152\n\t"
    "s_mov_b32 s53, 6144\n\t"
    "v_mfma_f32_16x16x32_bf16 v[4:7], v[80:83], a[20:23], v[4:7]\n\t"
    "buffer_load_dwordx4 a[96:99], v97, s[36:39], s53 offen sc1\n\t"
    "v_mfma_f32_16x16x32_bf16 v[8:11], v[80:83], a[24:27], v[8:11]\n\t"
    "ds_read_b64_tr_b16 v[66:67], v112 offset:51200\n\t"
    "s_mov_b32 s53, 63488\n\t"
    "v_mfma_f32_16x16x32_bf16 v[12:15], v[80:83], a[28:31], v[12:15]\n\t"
    "buffer_load_dwordx4 a[100:103], v97, s[36:39], s53 offen sc1\n\t"
    "v_mfma_f32_16x16x32_bf16 v[16:19], v[84:87], a[16:19], v[16:19]\n\t"
    "ds_read_b64_tr_b16 v[68:69], v113 offset:49152\n\t"
    "s_mov_b32 s53, 120832\n\t"
    "v_mfma_f32_16x16x32_bf16 v[20:23], v[84:87], a[20:23], v[20:23]\n\t"
    "buffer_load_dwordx4 a[104:107], v97, s[36:39], s53 offen sc1\n\t"
    "v_mfma_f32_16x16x32_bf16 v[24:27], v[84:87], a[24:27], v[24:27]\n\t"
    "ds_read_b64_tr_b16 v[70:71], v113 offset:51200\n\t"
    "s_mov_b32 s53, 178176\n\t"
    "v_mfma_f32_16x16x32_bf16 v[28:31], v[84:87], a[28:31], v[28:31]\n\t"
    "buffer_load_dwordx4 a[108:111], v97, s[36:39], s53 offen sc1\n\t"
    "v_mfma_f32_16x16x32_bf16 v[32:35], v[88:91], a[16:19], v[32:35]\n\t"
    "ds_read_b64_tr_b16 v[72:73], v114 offset:49152\n\t"
    "s_mov_b32 s53, 5120\n\t"
    "v_mfma_f32_16x16x32_bf16 v[36:39], v[88:91], a[20:23], v[36:39]\n\t"
    "buffer_load_dwordx4 a[80:83], v97, s[36:39], s53 offen sc1\n\t"
    "v_mfma_f32_16x16x32_bf16 v[40:43], v[88:91], a[24:27], v[40:43]\n\t"
    "ds_read_b64_tr_b16 v[74:75], v114 offset:51200\n\t"
    "s_mov_b32 s53, 62464\n\t"
    "v_mfma_f32_16x16x32_bf16 v[44:47], v[88:91], a[28:31], v[44:47]\n\t"
    "buffer_load_dwordx4 a[84:87], v97, s[36:39], s53 offen sc1\n\t"
    "v_mfma_f32_16x16x32_bf16 v[48:51], v[92:95], a[16:19], v[48:51]\n\t"
    "ds_read_b64_tr_b16 v[76:77], v115 offset:49152\n\t"
    "s_mov_b32 s53, 119808\n\t"
    "v_mfma_f32_16x16x32_bf16 v[52:55], v[92:95], a[20:23], v[52:55]\n\t"
    "buffer_load_dwordx4 a[88:91], v97, s[36:39], s53 offen sc1\n\t"
    "v_mfma_f32_16x16x32_bf16 v[56:59], v[92:95], a[24:27], v[56:59]\n\t"
    "ds_read_b64_tr_b16 v[78:79], v115 offset:51200\n\t"
    "s_mov_b32 s53, 177152\n\t"
    "v_mfma_f32_16x16x32_bf16 v[60:63], v[92:95], a[28:31], v[60:63]\n\t"
    "buffer_load_dwordx4 a[92:95], v97, s[36:39], s53 offen sc1\n\t"
    "s_add_u32 m0, s52, 98304\n\t"
    "s_mov_b32 s53, 327680\n\t"
    "buffer_load_dwordx4 v98, s[40:43], s53 offen lds\n\t"
    "s_add_u32 m0, s52, 99328\n\t"
    "s_mov_b32 s53, 335872\n\t"
    "buffer_load_dwordx4 v98, s[40:43], s53 offen lds\n\t"
    "s_add_u32 m0, s52, 100352\n\t"
    "s_mov_b32 s53, 344064\n\t"
    "buffer_load_dwordx4 v98, s[40:43], s53 offen lds\n\t"
    "s_add_u32 m0, s52, 101376\n\t"
    "s_mov_b32 s53, 352256\n\t"
    "buffer_load_dwordx4 v98, s[40:43], s53 offen lds\n\t"
    "s_waitcnt vmcnt(24) lgkmcnt(0)\n\t"
    "v_mfma_f32_16x16x32_bf16 v[0:3], v[64:67], a[32:35], v[0:3]\n\t"
    "ds_read_b64_tr_b16 v[80:81], v116 offset:0\n\t"
    "s_mov_b32 s53, 8192\n\t"
    "v_mfma_f32_16x16x32_bf16 v[4:7], v[64:67], a[36:39], v[4:7]\n\t"
    "buffer_load_dwordx4 a[128:131], v97, s[36:39], s53 offen sc1\n\t"
    "v_mfma_f32_16x16x32_bf16 v[8:11], v[64:67], a[40:43], v[8:11]\n\t"
    "ds_read_b64_tr_b16 v[82:83], v116 offset:2048\n\t"
    "s_mov_b32 s53, 65536\n\t"
    "v_mfma_f32_16x16x32_bf16 v[12:15], v[64:67], a[44:47], v[12:15]\n\t"
    "buffer_load_dwordx4 a[132:135], v97, s[36:39], s53 offen sc1\n\t"
    "v_mfma_f32_16x16x32_bf16 v[16:19], v[68:71], a[32:35], v[16:19]\n\t"
    "ds_read_b64_tr_b16 v[84:85], v117 offset:0\n\t"
    "s_mov_b32 s53, 122880\n\t"
    "v_mfma_f32_16x16x32_bf16 v[20:23], v[68:71], a[36:39], v[20:23]\n\t"
    "buffer_load_dwordx4 a[136:139], v97, s[36:39], s53 offen sc1\n\t"
    "v_mfma_f32_16x16x32_bf16 v[24:27], v[68:71], a[40:43], v[24:27]\n\t"
    "ds_read_b64_tr_b16 v[86:87], v117 offset:2048\n\t"
    "s_mov_b32 s53, 180224\n\t"
    "v_mfma_f32_16x16x32_bf16 v[28:31], v[68:71], a[44:47], v[28:31]\n\t"
    "buffer_load_dwordx4 a[140:143], v97, s[36:39], s53 offen sc1\n\t"
    "v_mfma_f32_16x16x32_bf16 v[32:35], v[72:75], a[32:35], v[32:35]\n\t"
    "ds_read_b64_tr_b16 v[88:89], v118 offset:0\n\t"
    "s_mov_b32 s53, 7168\n\t"
    "v_mfma_f32_16x16x32_bf16 v[36:39], v[72:75], a[36:39], v[36:39]\n\t"
    "buffer_load_dwordx4 a[112:115], v97, s[36:39], s53 offen sc1\n\t"
    "v_mfma_f32_16x16x32_bf16 v[40:43], v[72:75], a[40:43], v[40:43]\n\t"
    "ds_read_b64_tr_b16 v[90:91], v118 offset:2048\n\t"
    "s_mov_b32 s53, 64512\n\t"
    "v_mfma_f32_16x16x32_bf16 v[44:47], v[72:75], a[44:47], v[44:47]\n\t"
    "buffer_load_dwordx4 a[116:119], v97, s[36:39], s53 offen sc1\n\t"
    "v_mfma_f32_16x16x32_bf16 v[48:51], v[76:79], a[32:35], v[48:51]\n\t"
    "ds_read_b64_tr_b16 v[92:93], v119 offset:0\n\t"
    "s_mov_b32 s53, 121856\n\t"
    "v_mfma_f32_16x16x32_bf16 v[52:55], v[76:79], a[36:39], v[52:55]\n\t"
    "buffer_load_dwordx4 a[120:123], v97, s[36:39], s53 offen sc1\n\t"
    "v_mfma_f32_16x16x32_bf16 v[56:59], v[76:79], a[40:43], v[56:59]\n\t"
    "ds_read_b64_tr_b16 v[94:95], v119 offset:2048\n\t"
    "s_mov_b32 s53, 179200\n\t"
    "v_mfma_f32_16x16x32_bf16 v[60:63], v[76:79], a[44:47], v[60:63]\n\t"
    "buffer_load_dwordx4 a[124:127], v97, s[36:39], s53 offen sc1\n\t"
    "s_add_u32 m0, s52, 114688\n\t"
    "s_mov_b32 s53, 360448\n\t"
    "buffer_load_dwordx4 v98, s[40:43], s53 offen lds\n\t"
    "s_add_u32 m0, s52, 115712\n\t"
    "s_mov_b32 s53, 368640\n\t"
    "buffer_load_dwordx4 v98, s[40:43], s53 offen lds\n\t"
    "s_add_u32 m0, s52, 116736\n\t"
    "s_mov_b32 s53, 376832\n\t"
    "buffer_load_dwordx4 v98, s[40:43], s53 offen lds\n\t"
    "s_add_u32 m0, s52, 117760\n\t"
    "s_mov_b32 s53, 385024\n\t"
    "buffer_load_dwordx4 v98, s[40:43], s53 offen lds\n\t"
    "s_waitcnt vmcnt(28) lgkmcnt(0)\n\t"
    "v_mfma_f32_16x16x32_bf16 v[0:3], v[80:83], a[48:51], v[0:3]\n\t"
    "ds_read_b64_tr_b16 v[64:65], v99 offset:0\n\t"
    "s_mov_b32 s53, 10240\n\t"
    "v_mfma_f32_16x16x32_bf16 v[4:7], v[80:83], a[52:55], v[4:7]\n\t"
    "buffer_load_dwordx4 a[160:163], v97, s[36:39], s53 offen sc1\n\t"
    "v_mfma_f32_16x16x32_bf16 v[8:11], v[80:83], a[56:59], v[8:11]\n\t"
    "ds_read_b64_tr_b16 v[66:67], v99 offset:2048\n\t"
    "s_mov_b32 s53, 67584\n\t"
    "v_mfma_f32_16x16x32_bf16 v[12:15], v[80:83], a[60:63], v[12:15]\n\t"
    "buffer_load_dwordx4 a[164:167], v97, s[36:39], s53 offen sc1\n\t"
    "v_mfma_f32_16x16x32_bf16 v[16:19], v[84:87], a[48:51], v[16:19]\n\t"
    "ds_read_b64_tr_b16 v[68:69], v100 offset:0\n\t"
    "s_mov_b32 s53, 124928\n\t"
    "v_mfma_f32_16x16x32_bf16 v[20:23], v[84:87], a[52:55], v[20:23]\n\t"
    "buffer_load_dwordx4 a[168:171], v97, s[36:39], s53 offen sc1\n\t"
    "v_mfma_f32_16x16x32_bf16 v[24:27], v[84:87], a[56:59], v[24:27]\n\t"
    "ds_read_b64_tr_b16 v[70:71], v100 offset:2048\n\t"
    "s_mov_b32 s53, 182272\n\t"
    "v_mfma_f32_16x16x32_bf16 v[28:31], v[84:87], a[60:63], v[28:31]\n\t"
    "buffer_load_dwordx4 a[172:175], v97, s[36:39], s53 offen sc1\n\t"
    "v_mfma_f32_16x16x32_bf16 v[32:35], v[88:91], a[48:51], v[32:35]\n\t"
    "ds_read_b64_tr_b16 v[72:73], v108 offset:0\n\t"
    "s_mov_b32 s53, 9216\n\t"
    "v_mfma_f32_16x16x32_bf16 v[36:39], v[88:91], a[52:55], v[36:39]\n\t"
    "buffer_load_dwordx4 a[144:147], v97, s[36:39], s53 offen sc1\n\t"
    "v_mfma_f32_16x16x32_bf16 v[40:43], v[88:91], a[56:59], v[40:43]\n\t"
    "ds_read_b64_tr_b16 v[74:75], v108 offset:2048\n\t"
    "s_mov_b32 s53, 66560\n\t"
    "v_mfma_f32_16x16x32_bf16 v[44:47], v[88:91], a[60:63], v[44:47]\n\t"
    "buffer_load_dwordx4 a[148:151], v97, s[36:39], s53 offen sc1\n\t"
    "v_mfma_f32_16x16x32_bf16 v[48:51], v[92:95], a[48:51], v[48:51]\n\t"
    "ds_read_b64_tr_b16 v[76:77], v109 offset:0\n\t"
    "s_mov_b32 s53, 123904\n\t"
    "v_mfma_f32_16x16x32_bf16 v[52:55], v[92:95], a[52:55], v[52:55]\n\t"
    "buffer_load_dwordx4 a[152:155], v97, s[36:39], s53 offen sc1\n\t"
    "v_mfma_f32_16x16x32_bf16 v[56:59], v[92:95], a[56:59], v[56:59]\n\t"
    "ds_read_b64_tr_b16 v[78:79], v109 offset:2048\n\t"
    "s_mov_b32 s53, 181248\n\t"
    "v_mfma_f32_16x16x32_bf16 v[60:63], v[92:95], a[60:63], v[60:63]\n\t"
    "buffer_load_dwordx4 a[156:159], v97, s[36:39], s53 offen sc1\n\t"
    "s_add_u32 m0, s52, 131072\n\t"
    "s_mov_b32 s53, 393216\n\t"
    "buffer_load_dwordx4 v98, s[40:43], s53 offen lds\n\t"
    "s_add_u32 m0, s52, 132096\n\t"
    "s_mov_b32 s53, 401408\n\t"
    "buffer_load_dwordx4 v98, s[40:43], s53 offen lds\n\t"
    "s_add_u32 m0, s52, 133120\n\t"
    "s_mov_b32 s53, 409600\n\t"
    "buffer_load_dwordx4 v98, s[40:43], s53 offen lds\n\t"
    "s_add_u32 m0, s52, 134144\n\t"
    "s_mov_b32 s53, 417792\n\t"
    "buffer_load_dwordx4 v98, s[40:43], s53 offen lds\n\t"
    "s_waitcnt lgkmcnt(0)\n\t"
    "v_mfma_f32_16x16x32_bf16 v[0:3], v[64:67], a[64:67], v[0:3]\n\t"
    "ds_read_b64_tr_b16 v[80:81], v99 offset:16384\n\t"
    "s_mov_b32 s53, 12288\n\t"
    "v_mfma_f32_16x16x32_bf16 v[4:7], v[64:67], a[68:71], v[4:7]\n\t"
    "buffer_load_dwordx4 a[192:195], v97, s[36:39], s53 offen sc1\n\t"
    "v_mfma_f32_16x16x32_bf16 v[8:11], v[64:67], a[72:75], v[8:11]\n\t"
    "ds_read_b64_tr_b16 v[82:83], v99 offset:18432\n\t"
    "s_mov_b32 s53, 69632\n\t"
    "v_mfma_f32_16x16x32_bf16 v[12:15], v[64:67], a[76:79], v[12:15]\n\t"
    "buffer_load_dwordx4 a[196:199], v97, s[36:39], s53 offen sc1\n\t"
    "v_mfma_f32_16x16x32_bf16 v[16:19], v[68:71], a[64:67], v[16:19]\n\t"
    "ds_read_b64_tr_b16 v[84:85], v100 offset:16384\n\t"
    "s_mov_b32 s53, 126976\n\t"
    "v_mfma_f32_16x16x32_bf16 v[20:23], v[68:71], a[68:71], v[20:23]\n\t"
    "buffer_load_dwordx4 a[200:203], v97, s[36:39], s53 offen sc1\n\t"
    "v_mfma_f32_16x16x32_bf16 v[24:27], v[68:71], a[72:75], v[24:27]\n\t"
    "ds_read_b64_tr_b16 v[86:87], v100 offset:18432\n\t"
    "s_mov_b32 s53, 184320\n\t"
    "v_mfma_f32_16x16x32_bf16 v[28:31], v[68:71], a[76:79], v[28:31]\n\t"
    "buffer_load_dwordx4 a[204:207], v97, s[36:39], s53 offen sc1\n\t"
    "v_mfma_f32_16x16x32_bf16 v[32:35], v[72:75], a[64:67], v[32:35]\n\t"
    "ds_read_b64_tr_b16 v[88:89], v108 offset:16384\n\t"
    "s_mov_b32 s53, 11264\n\t"
    "v_mfma_f32_16x16x32_bf16 v[36:39], v[72:75], a[68:71], v[36:39]\n\t"
    "buffer_load_dwordx4 a[176:179], v97, s[36:39], s53 offen sc1\n\t"
    "v_mfma_f32_16x16x32_bf16 v[40:43], v[72:75], a[72:75], v[40:43]\n\t"
    "ds_read_b64_tr_b16 v[90:91], v108 offset:18432\n\t"
    "s_mov_b32 s53, 68608\n\t"
    "v_mfma_f32_16x16x32_bf16 v[44:47], v[72:75], a[76:79], v[44:47]\n\t"
    "buffer_load_dwordx4 a[180:183], v97, s[36:39], s53 offen sc1\n\t"
    "v_mfma_f32_16x16x32_bf16 v[48:51], v[76:79], a[64:67], v[48:51]\n\t"
    "ds_read_b64_tr_b16 v[92:93], v109 offset:16384\n\t"
    "s_mov_b32 s53, 125952\n\t"
    "v_mfma_f32_16x16x32_bf16 v[52:55], v[76:79], a[68:71], v[52:55]\n\t"
    "buffer_load_dwordx4 a[184:187], v97, s[36:39], s53 offen sc1\n\t"
    "v_mfma_f32_16x16x32_bf16 v[56:59], v[76:79], a[72:75], v[56:59]\n\t"
    "ds_read_b64_tr_b16 v[94:95], v109 offset:18432\n\t"
    "s_mov_b32 s53, 183296\n\t"
    "v_mfma_f32_16x16x32_bf16 v[60:63], v[76:79], a[76:79], v[60:63]\n\t"
    "buffer_load_dwordx4 a[188:191], v97, s[36:39], s53 offen sc1\n\t"
    "s_add_u32 m0, s52, 0\n\t"
    "s_mov_b32 s53, 425984\n\t"
    "buffer_load_dwordx4 v98, s[40:43], s53 offen lds\n\t"
    "s_add_u32 m0, s52, 1024\n\t"
    "s_mov_b32 s53, 434176\n\t"
    "buffer_load_dwordx4 v98, s[40:43], s53 offen lds\n\t"
    "s_add_u32 m0, s52, 2048\n\t"
    "s_mov_b32 s53, 442368\n\t"
    "buffer_load_dwordx4 v98, s[40:43], s53 offen lds\n\t"
    "s_add_u32 m0, s52, 3072\n\t"
    "s_mov_b32 s53, 450560\n\t"
    "buffer_load_dwordx4 v98, s[40:43], s53 offen lds\n\t"
    "s_waitcnt vmcnt(40) lgkmcnt(0)\n\t"
    "v_mfma_f32_16x16x32_bf16 v[0:3], v[80:83], a[80:83], v[0:3]\n\t"
    "ds_read_b64_tr_b16 v[64:65], v99 offset:32768\n\t"
    "s_mov_b32 s53, 13312\n\t"
    "v_mfma_f32_16x16x32_bf16 v[4:7], v[80:83], a[84:87], v[4:7]\n\t"
    "buffer_load_dwordx4 a[208:211], v97, s[36:39], s53 offen sc1\n\t"
    "v_mfma_f32_16x16x32_bf16 v[8:11], v[80:83], a[88:91], v[8:11]\n\t"
    "ds_read_b64_tr_b16 v[66:67], v99 offset:34816\n\t"
    "s_mov_b32 s53, 70656\n\t"
    "v_mfma_f32_16x16x32_bf16 v[12:15], v[80:83], a[92:95], v[12:15]\n\t"
    "buffer_load_dwordx4 a[212:215], v97, s[36:39], s53 offen sc1\n\t"
    "v_mfma_f32_16x16x32_bf16 v[16:19], v[84:87], a[80:83], v[16:19]\n\t"
    "ds_read_b64_tr_b16 v[68:69], v100 offset:32768\n\t"
    "s_mov_b32 s53, 128000\n\t"
    "v_mfma_f32_16x16x32_bf16 v[20:23], v[84:87], a[84:87], v[20:23]\n\t"
    "buffer_load_dwordx4 a[216:219], v97, s[36:39], s53 offen sc1\n\t"
    "v_mfma_f32_16x16x32_bf16 v[24:27], v[84:87], a[88:91], v[24:27]\n\t"
    "ds_read_b64_tr_b16 v[70:71], v100 offset:34816\n\t"
    "s_mov_b32 s53, 185344\n\t"
    "v_mfma_f32_16x16x32_bf16 v[28:31], v[84:87], a[92:95], v[28:31]\n\t"
    "buffer_load_dwordx4 a[220:223], v97, s[36:39], s53 offen sc1\n\t"
    "v_mfma_f32_16x16x32_bf16 v[32:35], v[88:91], a[80:83], v[32:35]\n\t"
    "ds_read_b64_tr_b16 v[72:73], v108 offset:32768\n\t"
    "v_mfma_f32_16x16x32_bf16 v[36:39], v[88:91], a[84:87], v[36:39]\n\t"
    "v_mfma_f32_16x16x32_bf16 v[40:43], v[88:91], a[88:91], v[40:43]\n\t"
    "ds_read_b64_tr_b16 v[74:75], v108 offset:34816\n\t"
    "v_mfma_f32_16x16x32_bf16 v[44:47], v[88:91], a[92:95], v[44:47]\n\t"
    "v_mfma_f32_16x16x32_bf16 v[48:51], v[92:95], a[80:83], v[48:51]\n\t"
    "ds_read_b64_tr_b16 v[76:77], v109 offset:32768\n\t"
    "v_mfma_f32_16x16x32_bf16 v[52:55], v[92:95], a[84:87], v[52:55]\n\t"
    "v_mfma_f32_16x16x32_bf16 v[56:59], v[92:95], a[88:91], v[56:59]\n\t"
    "ds_read_b64_tr_b16 v[78:79], v109 offset:34816\n\t"
    "v_mfma_f32_16x16x32_bf16 v[60:63], v[92:95], a[92:95], v[60:63]\n\t"
    "s_waitcnt lgkmcnt(0)\n\t"
    "v_mfma_f32_16x16x32_bf16 v[0:3], v[64:67], a[96:99], v[0:3]\n\t"
    "ds_read_b64_tr_b16 v[80:81], v99 offset:49152\n\t"
    "v_mfma_f32_16x16x32_bf16 v[4:7], v[64:67], a[100:103], v[4:7]\n\t"
    "v_mfma_f32_16x16x32_bf16 v[8:11], v[64:67], a[104:107], v[8:11]\n\t"
    "ds_read_b64_tr_b16 v[82:83], v99 offset:51200\n\t"
    "v_mfma_f32_16x16x32_bf16 v[12:15], v[64:67], a[108:111], v[12:15]\n\t"
    "v_mfma_f32_16x16x32_bf16 v[16:19], v[68:71], a[96:99], v[16:19]\n\t"
    "ds_read_b64_tr_b16 v[84:85], v100 offset:49152\n\t"
    "v_mfma_f32_16x16x32_bf16 v[20:23], v[68:71], a[100:103], v[20:23]\n\t"
    "v_mfma_f32_16x16x32_bf16 v[24:27], v[68:71], a[104:107], v[24:27]\n\t"
    "ds_read_b64_tr_b16 v[86:87], v100 offset:51200\n\t"
    "v_mfma_f32_16x16x32_bf16 v[28:31], v[68:71], a[108:111], v[28:31]\n\t"
    "v_mfma_f32_16x16x32_bf16 v[32:35], v[72:75], a[96:99], v[32:35]\n\t"
    "ds_read_b64_tr_b16 v[88:89], v108 offset:49152\n\t"
    "v_mfma_f32_16x16x32_bf16 v[36:39], v[72:75], a[100:103], v[36:39]\n\t"
    "v_mfma_f32_16x16x32_bf16 v[40:43], v[72:75], a[104:107], v[40:43]\n\t"
    "ds_read_b64_tr_b16 v[90:91], v108 offset:51200\n\t"
    "v_mfma_f32_16x16x32_bf16 v[44:47], v[72:75], a[108:111], v[44:47]\n\t"
    "v_mfma_f32_16x16x32_bf16 v[48:51], v[76:79], a[96:99], v[48:51]\n\t"
    "ds_read_b64_tr_b16 v[92:93], v109 offset:49152\n\t"
    "v_mfma_f32_16x16x32_bf16 v[52:55], v[76:79], a[100:103], v[52:55]\n\t"
    "v_mfma_f32_16x16x32_bf16 v[56:59], v[76:79], a[104:107], v[56:59]\n\t"
    "ds_read_b64_tr_b16 v[94:95], v109 offset:51200\n\t"
    "v_mfma_f32_16x16x32_bf16 v[60:63], v[76:79], a[108:111], v[60:63]\n\t"
    "s_waitcnt vmcnt(32) lgkmcnt(0)\n\t"
    "v_mfma_f32_16x16x32_bf16 v[0:3], v[80:83], a[112:115], v[0:3]\n\t"
    "ds_read_b64_tr_b16 v[64:65], v112 offset:0\n\t"
    "v_mfma_f32_16x16x32_bf16 v[4:7], v[80:83], a[116:119], v[4:7]\n\t"
    "v_mfma_f32_16x16x32_bf16 v[8:11], v[80:83], a[120:123], v[8:11]\n\t"
    "ds_read_b64_tr_b16 v[66:67], v112 offset:2048\n\t"
    "v_mfma_f32_16x16x32_bf16 v[12:15], v[80:83], a[124:127], v[12:15]\n\t"
    "v_mfma_f32_16x16x32_bf16 v[16:19], v[84:87], a[112:115], v[16:19]\n\t"
    "ds_read_b64_tr_b16 v[68:69], v113 offset:0\n\t"
    "v_mfma_f32_16x16x32_bf16 v[20:23], v[84:87], a[116:119], v[20:23]\n\t"
    "v_mfma_f32_16x16x32_bf16 v[24:27], v[84:87], a[120:123], v[24:27]\n\t"
    "ds_read_b64_tr_b16 v[70:71], v113 offset:2048\n\t"
    "v_mfma_f32_16x16x32_bf16 v[28:31], v[84:87], a[124:127], v[28:31]\n\t"
    "v_mfma_f32_16x16x32_bf16 v[32:35], v[88:91], a[112:115], v[32:35]\n\t"
    "ds_read_b64_tr_b16 v[72:73], v114 offset:0\n\t"
    "v_mfma_f32_16x16x32_bf16 v[36:39], v[88:91], a[116:119], v[36:39]\n\t"
    "v_mfma_f32_16x16x32_bf16 v[40:43], v[88:91], a[120:123], v[40:43]\n\t"
    "ds_read_b64_tr_b16 v[74:75], v114 offset:2048\n\t"
    "v_mfma_f32_16x16x32_bf16 v[44:47], v[88:91], a[124:127], v[44:47]\n\t"
    "v_mfma_f32_16x16x32_bf16 v[48:51], v[92:95], a[112:115], v[48:51]\n\t"
    "ds_read_b64_tr_b16 v[76:77], v115 offset:0\n\t"
    "v_mfma_f32_16x16x32_bf16 v[52:55], v[92:95], a[116:119], v[52:55]\n\t"
    "v_mfma_f32_16x16x32_bf16 v[56:59], v[92:95], a[120:123], v[56:59]\n\t"
    "ds_read_b64_tr_b16 v[78:79], v115 offset:2048\n\t"
    "v_mfma_f32_16x16x32_bf16 v[60:63], v[92:95], a[124:127], v[60:63]\n\t"
    "s_waitcnt lgkmcnt(0)\n\t"
    "v_mfma_f32_16x16x32_bf16 v[0:3], v[64:67], a[128:131], v[0:3]\n\t"
    "ds_read_b64_tr_b16 v[80:81], v112 offset:16384\n\t"
    "v_mfma_f32_16x16x32_bf16 v[4:7], v[64:67], a[132:135], v[4:7]\n\t"
    "v_mfma_f32_16x16x32_bf16 v[8:11], v[64:67], a[136:139], v[8:11]\n\t"
    "ds_read_b64_tr_b16 v[82:83], v112 offset:18432\n\t"
    "v_mfma_f32_16x16x32_bf16 v[12:15], v[64:67], a[140:143], v[12:15]\n\t"
    "v_mfma_f32_16x16x32_bf16 v[16:19], v[68:71], a[128:131], v[16:19]\n\t"
    "ds_read_b64_tr_b16 v[84:85], v113 offset:16384\n\t"
    "v_mfma_f32_16x16x32_bf16 v[20:23], v[68:71], a[132:135], v[20:23]\n\t"
    "v_mfma_f32_16x16x32_bf16 v[24:27], v[68:71], a[136:139], v[24:27]\n\t"
    "ds_read_b64_tr_b16 v[86:87], v113 offset:18432\n\t"
    "v_mfma_f32_16x16x32_bf16 v[28:31], v[68:71], a[140:143], v[28:31]\n\t"
    "v_mfma_f32_16x16x32_bf16 v[32:35], v[72:75], a[128:131], v[32:35]\n\t"
    "ds_read_b64_tr_b16 v[88:89], v114 offset:16384\n\t"
    "v_mfma_f32_16x16x32_bf16 v[36:39], v[72:75], a[132:135], v[36:39]\n\t"
    "v_mfma_f32_16x16x32_bf16 v[40:43], v[72:75], a[136:139], v[40:43]\n\t"
    "ds_read_b64_tr_b16 v[90:91], v114 offset:18432\n\t"
    "v_mfma_f32_16x16x32_bf16 v[44:47], v[72:75], a[140:143], v[44:47]\n\t"
    "v_mfma_f32_16x16x32_bf16 v[48:51], v[76:79], a[128:131], v[48:51]\n\t"
    "ds_read_b64_tr_b16 v[92:93], v115 offset:16384\n\t"
    "v_mfma_f32_16x16x32_bf16 v[52:55], v[76:79], a[132:135], v[52:55]\n\t"
    "v_mfma_f32_16x16x32_bf16 v[56:59], v[76:79], a[136:139], v[56:59]\n\t"
    "ds_read_b64_tr_b16 v[94:95], v115 offset:18432\n\t"
    "v_mfma_f32_16x16x32_bf16 v[60:63], v[76:79], a[140:143], v[60:63]\n\t"
    "s_waitcnt vmcnt(20) lgkmcnt(0)\n\t"
    "v_mfma_f32_16x16x32_bf16 v[0:3], v[80:83], a[144:147], v[0:3]\n\t"
    "ds_read_b64_tr_b16 v[64:65], v112 offset:32768\n\t"
    "v_mfma_f32_16x16x32_bf16 v[4:7], v[80:83], a[148:151], v[4:7]\n\t"
    "v_mfma_f32_16x16x32_bf16 v[8:11], v[80:83], a[152:155], v[8:11]\n\t"
    "ds_read_b64_tr_b16 v[66:67], v112 offset:34816\n\t"
    "v_mfma_f32_16x16x32_bf16 v[12:15], v[80:83], a[156:159], v[12:15]\n\t"
    "v_mfma_f32_16x16x32_bf16 v[16:19], v[84:87], a[144:147], v[16:19]\n\t"
    "ds_read_b64_tr_b16 v[68:69], v113 offset:32768\n\t"
    "v_mfma_f32_16x16x32_bf16 v[20:23], v[84:87], a[148:151], v[20:23]\n\t"
    "v_mfma_f32_16x16x32_bf16 v[24:27], v[84:87], a[152:155], v[24:27]\n\t"
    "ds_read_b64_tr_b16 v[70:71], v113 offset:34816\n\t"
    "v_mfma_f32_16x16x32_bf16 v[28:31], v[84:87], a[156:159], v[28:31]\n\t"
    "v_mfma_f32_16x16x32_bf16 v[32:35], v[88:91], a[144:147], v[32:35]\n\t"
    "ds_read_b64_tr_b16 v[72:73], v114 offset:32768\n\t"
    "v_mfma_f32_16x16x32_bf16 v[36:39], v[88:91], a[148:151], v[36:39]\n\t"
    "v_mfma_f32_16x16x32_bf16 v[40:43], v[88:91], a[152:155], v[40:43]\n\t"
    "ds_read_b64_tr_b16 v[74:75], v114 offset:34816\n\t"
    "v_mfma_f32_16x16x32_bf16 v[44:47], v[88:91], a[156:159], v[44:47]\n\t"
    "v_mfma_f32_16x16x32_bf16 v[48:51], v[92:95], a[144:147], v[48:51]\n\t"
    "ds_read_b64_tr_b16 v[76:77], v115 offset:32768\n\t"
    "v_mfma_f32_16x16x32_bf16 v[52:55], v[92:95], a[148:151], v[52:55]\n\t"
    "v_mfma_f32_16x16x32_bf16 v[56:59], v[92:95], a[152:155], v[56:59]\n\t"
    "ds_read_b64_tr_b16 v[78:79], v115 offset:34816\n\t"
    "v_mfma_f32_16x16x32_bf16 v[60:63], v[92:95], a[156:159], v[60:63]\n\t"
    "s_waitcnt lgkmcnt(0)\n\t"
    "v_mfma_f32_16x16x32_bf16 v[0:3], v[64:67], a[160:163], v[0:3]\n\t"
    "ds_read_b64_tr_b16 v[80:81], v112 offset:49152\n\t"
    "v_mfma_f32_16x16x32_bf16 v[4:7], v[64:67], a[164:167], v[4:7]\n\t"
    "v_mfma_f32_16x16x32_bf16 v[8:11], v[64:67], a[168:171], v[8:11]\n\t"
    "ds_read_b64_tr_b16 v[82:83], v112 offset:51200\n\t"
    "v_mfma_f32_16x16x32_bf16 v[12:15], v[64:67], a[172:175], v[12:15]\n\t"
    "v_mfma_f32_16x16x32_bf16 v[16:19], v[68:71], a[160:163], v[16:19]\n\t"
    "ds_read_b64_tr_b16 v[84:85], v113 offset:49152\n\t"
    "v_mfma_f32_16x16x32_bf16 v[20:23], v[68:71], a[164:167], v[20:23]\n\t"
    "v_mfma_f32_16x16x32_bf16 v[24:27], v[68:71], a[168:171], v[24:27]\n\t"
    "ds_read_b64_tr_b16 v[86:87], v113 offset:51200\n\t"
    "v_mfma_f32_16x16x32_bf16 v[28:31], v[68:71], a[172:175], v[28:31]\n\t"
    "v_mfma_f32_16x16x32_bf16 v[32:35], v[72:75], a[160:163], v[32:35]\n\t"
    "ds_read_b64_tr_b16 v[88:89], v114 offset:49152\n\t"
    "v_mfma_f32_16x16x32_bf16 v[36:39], v[72:75], a[164:167], v[36:39]\n\t"
    "v_mfma_f32_16x16x32_bf16 v[40:43], v[72:75], a[168:171], v[40:43]\n\t"
    "ds_read_b64_tr_b16 v[90:91], v114 offset:51200\n\t"
    "v_mfma_f32_16x16x32_bf16 v[44:47], v[72:75], a[172:175], v[44:47]\n\t"
    "v_mfma_f32_16x16x32_bf16 v[48:51], v[76:79], a[160:163], v[48:51]\n\t"
    "ds_read_b64_tr_b16 v[92:93], v115 offset:49152\n\t"
    "v_mfma_f32_16x16x32_bf16 v[52:55], v[76:79], a[164:167], v[52:55]\n\t"
    "v_mfma_f32_16x16x32_bf16 v[56:59], v[76:79], a[168:171], v[56:59]\n\t"
    "ds_read_b64_tr_b16 v[94:95], v115 offset:51200\n\t"
    "v_mfma_f32_16x16x32_bf16 v[60:63], v[76:79], a[172:175], v[60:63]\n\t"
    "s_waitcnt vmcnt(16)\n\t"
    "s_waitcnt vmcnt(8) lgkmcnt(0)\n\t"
    "v_mfma_f32_16x16x32_bf16 v[0:3], v[80:83], a[176:179], v[0:3]\n\t"
    "ds_read_b64_tr_b16 v[64:65], v116 offset:0\n\t"
    "v_mfma_f32_16x16x32_bf16 v[4:7], v[80:83], a[180:183], v[4:7]\n\t"
    "v_mfma_f32_16x16x32_bf16 v[8:11], v[80:83], a[184:187], v[8:11]\n\t"
    "ds_read_b64_tr_b16 v[66:67], v116 offset:2048\n\t"
    "v_mfma_f32_16x16x32_bf16 v[12:15], v[80:83], a[188:191], v[12:15]\n\t"
    "v_mfma_f32_16x16x32_bf16 v[16:19], v[84:87], a[176:179], v[16:19]\n\t"
    "ds_read_b64_tr_b16 v[68:69], v117 offset:0\n\t"
    "v_mfma_f32_16x16x32_bf16 v[20:23], v[84:87], a[180:183], v[20:23]\n\t"
    "v_mfma_f32_16x16x32_bf16 v[24:27], v[84:87], a[184:187], v[24:27]\n\t"
    "ds_read_b64_tr_b16 v[70:71], v117 offset:2048\n\t"
    "v_mfma_f32_16x16x32_bf16 v[28:31], v[84:87], a[188:191], v[28:31]\n\t"
    "v_mfma_f32_16x16x32_bf16 v[32:35], v[88:91], a[176:179], v[32:35]\n\t"
    "ds_read_b64_tr_b16 v[72:73], v118 offset:0\n\t"
    "v_mfma_f32_16x16x32_bf16 v[36:39], v[88:91], a[180:183], v[36:39]\n\t"
    "v_mfma_f32_16x16x32_bf16 v[40:43], v[88:91], a[184:187], v[40:43]\n\t"
    "ds_read_b64_tr_b16 v[74:75], v118 offset:2048\n\t"
    "v_mfma_f32_16x16x32_bf16 v[44:47], v[88:91], a[188:191], v[44:47]\n\t"
    "v_mfma_f32_16x16x32_bf16 v[48:51], v[92:95], a[176:179], v[48:51]\n\t"
    "ds_read_b64_tr_b16 v[76:77], v119 offset:0\n\t"
    "v_mfma_f32_16x16x32_bf16 v[52:55], v[92:95], a[180:183], v[52:55]\n\t"
    "v_mfma_f32_16x16x32_bf16 v[56:59], v[92:95], a[184:187], v[56:59]\n\t"
    "ds_read_b64_tr_b16 v[78:79], v119 offset:2048\n\t"
    "v_mfma_f32_16x16x32_bf16 v[60:63], v[92:95], a[188:191], v[60:63]\n\t"
    "s_waitcnt vmcnt(4)\n\t"
    "s_waitcnt lgkmcnt(0)\n\t"
    "v_mfma_f32_16x16x32_bf16 v[0:3], v[64:67], a[192:195], v[0:3]\n\t"
    "ds_read_b64_tr_b16 v[80:81], v99 offset:0\n\t"
    "v_mfma_f32_16x16x32_bf16 v[4:7], v[64:67], a[196:199], v[4:7]\n\t"
    "v_mfma_f32_16x16x32_bf16 v[8:11], v[64:67], a[200:203], v[8:11]\n\t"
    "ds_read_b64_tr_b16 v[82:83], v99 offset:2048\n\t"
    "v_mfma_f32_16x16x32_bf16 v[12:15], v[64:67], a[204:207], v[12:15]\n\t"
    "v_mfma_f32_16x16x32_bf16 v[16:19], v[68:71], a[192:195], v[16:19]\n\t"
    "ds_read_b64_tr_b16 v[84:85], v100 offset:0\n\t"
    "v_mfma_f32_16x16x32_bf16 v[20:23], v[68:71], a[196:199], v[20:23]\n\t"
    "v_mfma_f32_16x16x32_bf16 v[24:27], v[68:71], a[200:203], v[24:27]\n\t"
    "ds_read_b64_tr_b16 v[86:87], v100 offset:2048\n\t"
    "v_mfma_f32_16x16x32_bf16 v[28:31], v[68:71], a[204:207], v[28:31]\n\t"
    "v_mfma_f32_16x16x32_bf16 v[32:35], v[72:75], a[192:195], v[32:35]\n\t"
    "ds_read_b64_tr_b16 v[88:89], v108 offset:0\n\t"
    "v_mfma_f32_16x16x32_bf16 v[36:39], v[72:75], a[196:199], v[36:39]\n\t"
    "v_mfma_f32_16x16x32_bf16 v[40:43], v[72:75], a[200:203], v[40:43]\n\t"
    "ds_read_b64_tr_b16 v[90:91], v108 offset:2048\n\t"
    "v_mfma_f32_16x16x32_bf16 v[44:47], v[72:75], a[204:207], v[44:47]\n\t"
    "v_mfma_f32_16x16x32_bf16 v[48:51], v[76:79], a[192:195], v[48:51]\n\t"
    "ds_read_b64_tr_b16 v[92:93], v109 offset:0\n\t"
    "v_mfma_f32_16x16x32_bf16 v[52:55], v[76:79], a[196:199], v[52:55]\n\t"
    "v_mfma_f32_16x16x32_bf16 v[56:59], v[76:79], a[200:203], v[56:59]\n\t"
    "ds_read_b64_tr_b16 v[94:95], v109 offset:2048\n\t"
    "v_mfma_f32_16x16x32_bf16 v[60:63], v[76:79], a[204:207], v[60:63]\n\t"
    "s_waitcnt vmcnt(0) lgkmcnt(0)\n\t"
    "v_mfma_f32_16x16x32_bf16 v[0:3], v[80:83], a[208:211], v[0:3]\n\t"
    "v_mfma_f32_16x16x32_bf16 v[4:7], v[80:83], a[212:215], v[4:7]\n\t"
    "v_mfma_f32_16x16x32_bf16 v[8:11], v[80:83], a[216:219], v[8:11]\n\t"
    "v_mfma_f32_16x16x32_bf16 v[12:15], v[80:83], a[220:223], v[12:15]\n\t"
    "v_mfma_f32_16x16x32_bf16 v[16:19], v[84:87], a[208:211], v[16:19]\n\t"
    "v_mfma_f32_16x16x32_bf16 v[20:23], v[84:87], a[212:215], v[20:23]\n\t"
    "v_mfma_f32_16x16x32_bf16 v[24:27], v[84:87], a[216:219], v[24:27]\n\t"
    "v_mfma_f32_16x16x32_bf16 v[28:31], v[84:87], a[220:223], v[28:31]\n\t"
    "v_mfma_f32_16x16x32_bf16 v[32:35], v[88:91], a[208:211], v[32:35]\n\t"
    "v_mfma_f32_16x16x32_bf16 v[36:39], v[88:91], a[212:215], v[36:39]\n\t"
    "v_mfma_f32_16x16x32_bf16 v[40:43], v[88:91], a[216:219], v[40:43]\n\t"
    "v_mfma_f32_16x16x32_bf16 v[44:47], v[88:91], a[220:223], v[44:47]\n\t"
    "v_mfma_f32_16x16x32_bf16 v[48:51], v[92:95], a[208:211], v[48:51]\n\t"
    "v_mfma_f32_16x16x32_bf16 v[52:55], v[92:95], a[212:215], v[52:55]\n\t"
    "v_mfma_f32_16x16x32_bf16 v[56:59], v[92:95], a[216:219], v[56:59]\n\t"
    "v_mfma_f32_16x16x32_bf16 v[60:63], v[92:95], a[220:223], v[60:63]\n\t"
    "s_barrier\n\t"
    "v_and_b32 v101, 15, v96\n\t"
    "v_lshrrev_b32 v102, 4, v96\n\t"
    "v_xor_b32 v102, v102, v101\n\t"
    "v_lshlrev_b32 v101, 8, v101\n\t"
    "s_lshl_b32 s44, %4, 14\n\t"
    "s_add_u32 s44, s44, %5\n\t"
    "v_add_u32 v101, s44, v101\n\t"
    "v_xor_b32 v103, 0, v102\n\t"
    "v_lshl_add_u32 v104, v103, 4, v101\n\t"
    "v_xor_b32 v103, 4, v102\n\t"
    "v_lshl_add_u32 v105, v103, 4, v101\n\t"
    "v_xor_b32 v103, 8, v102\n\t"
    "v_lshl_add_u32 v106, v103, 4, v101\n\t"
    "v_xor_b32 v103, 12, v102\n\t"
    "v_lshl_add_u32 v107, v103, 4, v101\n\t"
    "ds_write_b128 v104, v[0:3] offset:0\n\t"
    "ds_write_b128 v104, v[4:7] offset:4096\n\t"
    "ds_write_b128 v104, v[8:11] offset:8192\n\t"
    "ds_write_b128 v104, v[12:15] offset:12288\n\t"
    "ds_write_b128 v105, v[16:19] offset:0\n\t"
    "ds_write_b128 v105, v[20:23] offset:4096\n\t"
    "ds_write_b128 v105, v[24:27] offset:8192\n\t"
    "ds_write_b128 v105, v[28:31] offset:12288\n\t"
    "ds_write_b128 v106, v[32:35] offset:0\n\t"
    "ds_write_b128 v106, v[36:39] offset:4096\n\t"
    "ds_write_b128 v106, v[40:43] offset:8192\n\t"
    "ds_write_b128 v106, v[44:47] offset:12288\n\t"
    "ds_write_b128 v107, v[48:51] offset:0\n\t"
    "ds_write_b128 v107, v[52:55] offset:4096\n\t"
    "ds_write_b128 v107, v[56:59] offset:8192\n\t"
    "ds_write_b128 v107, v[60:63] offset:12288\n\t"
    "s_waitcnt lgkmcnt(0)\n\t"
    "s_barrier\n\t"
    :
    : "s"(ws + WS_HID), "s"(wd), "s"(mt64), "s"(nt64), "s"(wid), "s"(ldsb)
    :
      "v0", "v1", "v2", "v3", "v4", "v5", "v6", "v7", "v8", "v9", "v10", "v11", "v12", "v13", "v14", "v15", "v16", "v17", "v18", "v19", "v20", "v21", "v22", "v23",
      "v24", "v25", "v26", "v27", "v28", "v29", "v30", "v31", "v32", "v33", "v34", "v35", "v36", "v37", "v38", "v39", "v40", "v41", "v42", "v43", "v44", "v45", "v46", "v47",
      "v48", "v49", "v50", "v51", "v52", "v53", "v54", "v55", "v56", "v57", "v58", "v59", "v60", "v61", "v62", "v63", "v64", "v65", "v66", "v67", "v68", "v69", "v70", "v71",
      "v72", "v73", "v74", "v75", "v76", "v77", "v78", "v79", "v80", "v81", "v82", "v83", "v84", "v85", "v86", "v87", "v88", "v89", "v90", "v91", "v92", "v93", "v94", "v95",
      "v96", "v97", "v98", "v99", "v100", "v101", "v102", "v103", "v104", "v105", "v106", "v107", "v108", "v109", "v110", "v111", "v112", "v113", "v114", "v115", "v116", "v117", "v118", "v119",
      "a0", "a1", "a2", "a3", "a4", "a5", "a6", "a7", "a8", "a9", "a10", "a11", "a12", "a13", "a14", "a15", "a16", "a17", "a18", "a19", "a20", "a21", "a22", "a23",
      "a24", "a25", "a26", "a27", "a28", "a29", "a30", "a31", "a32", "a33", "a34", "a35", "a36", "a37", "a38", "a39", "a40", "a41", "a42", "a43", "a44", "a45", "a46", "a47",
      "a48", "a49", "a50", "a51", "a52", "a53", "a54", "a55", "a56", "a57", "a58", "a59", "a60", "a61", "a62", "a63", "a64", "a65", "a66", "a67", "a68", "a69", "a70", "a71",
      "a72", "a73", "a74", "a75", "a76", "a77", "a78", "a79", "a80", "a81", "a82", "a83", "a84", "a85", "a86", "a87", "a88", "a89", "a90", "a91", "a92", "a93", "a94", "a95",
      "a96", "a97", "a98", "a99", "a100", "a101", "a102", "a103", "a104", "a105", "a106", "a107", "a108", "a109", "a110", "a111", "a112", "a113", "a114", "a115", "a116", "a117", "a118", "a119",
      "a120", "a121", "a122", "a123", "a124", "a125", "a126", "a127", "a128", "a129", "a130", "a131", "a132", "a133", "a134", "a135", "a136", "a137", "a138", "a139", "a140", "a141", "a142", "a143",
      "a144", "a145", "a146", "a147", "a148", "a149", "a150", "a151", "a152", "a153", "a154", "a155", "a156", "a157", "a158", "a159", "a160", "a161", "a162", "a163", "a164", "a165", "a166", "a167",
      "a168", "a169", "a170", "a171", "a172", "a173", "a174", "a175", "a176", "a177", "a178", "a179", "a180", "a181", "a182", "a183", "a184", "a185", "a186", "a187", "a188", "a189", "a190", "a191",
      "a192", "a193", "a194", "a195", "a196", "a197", "a198", "a199", "a200", "a201", "a202", "a203", "a204", "a205", "a206", "a207", "a208", "a209", "a210", "a211", "a212", "a213", "a214", "a215",
      "a216", "a217", "a218", "a219", "a220", "a221", "a222", "a223", "s36", "s37", "s38", "s39", "s40", "s41", "s42", "s43", "s44", "s45", "s46", "s47", "s48", "s49", "s50", "s51",
      "s52", "s53", "s54", "s55", "vcc", "scc", "memory"
  );
#pragma unroll
  for (int rep = 0; rep < 2; ++rep) {
    const int m = (tl >> 3) + 32 * rep, p = tl & 7;
    const int u = p >> 2, g = p & 3, c1 = 8 * u + g, c2 = c1 + 4;
    const int row = mt64 * 64 + m, n1 = nt64 * 64 + 4 * c1, n2 = n1 + 16;
    f32x4 a = {0.f, 0.f, 0.f, 0.f}, b = a;
#pragma unroll
    for (int w = 0; w < 4; ++w) {
      a += *(const f32x4*)(lds + w * 16384 + m * 256 + 16 * (c1 ^ (m & 15)));
      b += *(const f32x4*)(lds + w * 16384 + m * 256 + 16 * (c2 ^ (m & 15)));
    }
#pragma unroll
    for (int j = 0; j < 4; ++j) {
      a[j] = __uint_as_float((j & 1) ? (xv[rep][j >> 1] & 0xffff0000u) : (xv[rep][j >> 1] << 16)) + gd0[j] * a[j];
      b[j] = __uint_as_float((j & 1) ? (xv[rep][2 + (j >> 1)] & 0xffff0000u) : (xv[rep][2 + (j >> 1)] << 16)) + gd1[j] * b[j];
    }
    *(u32x2*)&out[row * D + n1] = (u32x2){cvtpk(a[0], a[1]), cvtpk(a[2], a[3])};
    *(u32x2*)&out[row * D + n2] = (u32x2){cvtpk(b[0], b[1]), cvtpk(b[2], b[3])};
  }
  asm volatile("code_last_mark:");
}

extern "C" void kernel_launch(void* const* d_in, const int* in_sizes, int n_in, void* d_out, int out_size, void* d_ws, size_t ws_size, hipStream_t stream) {
  const u16* x = (const u16*)d_in[0]; const u16* kc = (const u16*)d_in[1]; const u16* vc = (const u16*)d_in[2];
  const u16* wq = (const u16*)d_in[3]; const u16* wo = (const u16*)d_in[4];
  const u16* wg = (const u16*)d_in[6]; const u16* wu = (const u16*)d_in[7]; const u16* wd = (const u16*)d_in[8];
  const u16* c = (const u16*)d_in[9]; const u16* wmod = (const u16*)d_in[10];
  char* ws = (char*)d_ws;
  float* mod = (float*)(ws + WS_MOD); float* modtab = (float*)(ws + WS_TAB);
  u16* h = (u16*)(ws + WS_H);
  k_mod<<<512, 256, 0, stream>>>(c, wmod, mod, modtab, x, h, wq, kc, vc);
  k_main<<<256, 256, 0, stream>>>(ws, wo, x, wg, wu, wd, (u16*)d_out, wq, kc, vc);
}
```
